```python
import torch

def _softmax(x, axis=-1):
    return torch.softmax(x, dim=axis)

def _gelu(x):
    return torch.nn.functional.gelu(x, approximate='tanh')

def _silu(x):
    return torch.nn.functional.silu(x)

def _sigmoid(x):
    return torch.sigmoid(x)

def _layernorm(x, axis=-1, eps=1e-05):
    m = x.mean(dim=axis, keepdim=True)
    v = x.var(dim=axis, keepdim=True, unbiased=False)
    return (x - m) / torch.sqrt(v + eps)

def _scatter_add(tgt, idx, src, axis=0):
    return tgt.clone().index_add_(axis, idx, src)

def _segment_sum(src, seg, n):
    out = torch.zeros((n,) + src.shape[1:], dtype=src.dtype)
    return out.index_add_(0, seg, src)

def _pad(x, pw):
    flat = tuple((v for pair in reversed(pw) for v in pair))
    return torch.nn.functional.pad(x, flat)
kernel_name = 'adaLN_cond__spectral_gfnet__glu_gated_reglu__prompt'

def setup_inputs():

    _w = lambda a: (lambda t: t.to(torch.bfloat16) if t.is_floating_point() else t)(torch.as_tensor(a))

    def _A_setup():
        B, S, D = (1, 2048, 448)
        Df = D // 2 + 1
        return {'x': _w((lambda *s: torch.randn(*s, dtype=torch.bfloat16) * 0.05)(B, S, D)), 'w_r': _w((lambda *s: torch.randn(*s, dtype=torch.bfloat16) * 3.2)(S, Df) * 0.02), 'w_i': _w((lambda *s: torch.randn(*s, dtype=torch.bfloat16) * 3.2)(S, Df) * 0.02)}
    _A_in = _A_setup()

    def _B_setup():
        B, S, D, F = (1, 2048, 448, 1792)
        return {'x': _w((lambda *s: torch.randn(*s, dtype=torch.bfloat16) * 0.05)(B, S, D)), 'W_gate': _w((lambda *s: torch.randn(*s, dtype=torch.bfloat16) * 0.4)(D, F) * (2.0 / D) ** 0.5), 'W_up': _w((lambda *s: torch.randn(*s, dtype=torch.bfloat16) * 0.4)(D, F) * (2.0 / D) ** 0.5), 'W_down': _w((lambda *s: torch.randn(*s, dtype=torch.bfloat16) * 0.4)(F, D) * (1.0 / F) ** 0.5)}
    _B_in = _B_setup()
    return {'A_x': _A_in['x'], 'A_w_r': _A_in['w_r'], 'A_w_i': _A_in['w_i'], 'B_x': _B_in['x'], 'B_W_gate': _B_in['W_gate'], 'B_W_up': _B_in['W_up'], 'B_W_down': _B_in['W_down'], 'c': _w((lambda *s: torch.randn(*s, dtype=torch.bfloat16) * 0.1)(1, 448) * 8.0), 'w_mod': _w((lambda *s: torch.randn(*s, dtype=torch.bfloat16) * 0.1)(448, 6 * 448) * 448 ** (-0.5) * 8.0)}

def reference(A_x, A_w_r, A_w_i, B_x, B_W_gate, B_W_up, B_W_down, c, w_mod):

    def _A_ref(x, w_r, w_i):
        B, S, D = x.shape
        X = (lambda x, axes=(-2, -1): torch.fft.rfft2(x.float(), dim=axes))(x, axes=(-2, -1))
        W = w_r + 1j * w_i
        X = X * W
        y = (lambda x, s, axes=(-2, -1): torch.fft.irfft2(x, s=s, dim=axes).to(torch.bfloat16))(X, (S, D), axes=(-2, -1))
        return y

    def _B_ref(x, W_gate, W_up, W_down):
        gate = torch.einsum('bsd,df->bsf', x, W_gate)
        up = torch.einsum('bsd,df->bsf', x, W_up)
        gate = gate * (lambda x, dt: x.to(dt))(gate > 0, gate.dtype)
        h = gate * up
        return torch.einsum('bsf,fd->bsd', h, W_down)
    x = A_x
    mod = (_silu(c) @ w_mod).reshape(c.shape[0], 6, 1, -1)
    sa, ca, ga, sb, cb, gb = (mod[:, 0].clone(), mod[:, 1].clone(), mod[:, 2].clone(), mod[:, 3].clone(), mod[:, 4].clone(), mod[:, 5].clone())
    h = _layernorm(x, axis=-1) * (1 + ca) + sa
    x = x + ga * _A_ref(h, A_w_r, A_w_i)
    h = _layernorm(x, axis=-1) * (1 + cb) + sb
    x = x + gb * _B_ref(h, B_W_gate, B_W_up, B_W_down)
    return x
```

```cpp
#include <hip/hip_runtime.h>
#include <hip/hip_bf16.h>
typedef unsigned short u16;
__device__ __forceinline__ float b2f(u16 v){ return __uint_as_float((unsigned)v<<16);}
__device__ __forceinline__ u16 f2b(float f){ __hip_bfloat16 b = __float2bfloat16(f); return *(u16*)&b; }
__device__ __forceinline__ float rb(float f){ return b2f(f2b(f)); }
constexpr int S=2048, D=448, DF=225, F=1792;
__global__ void k_mod(const u16* c, const u16* wm, float* mod){
  int j = blockIdx.x*blockDim.x+threadIdx.x; if(j>=6*D) return;
  float a=0; for(int k=0;k<D;k++){ float cv=b2f(c[k]); float s=rb(cv/(1.f+__expf(-cv))); a+=s*b2f(wm[k*6*D+j]); }
  mod[j]=rb(a);
}
__global__ void k_ln(const u16* x, const float* mod, int so, int co, u16* h){
  int r=blockIdx.x, t=threadIdx.x; __shared__ float red[64];
  float v[7]; float s=0; for(int i=0;i<7;i++){ v[i]=b2f(x[r*D+t+64*i]); s+=v[i]; }
  for(int m=32;m;m>>=1) s+=__shfl_xor(s,m);
  float mean=s/D; float q=0; for(int i=0;i<7;i++){ float d=v[i]-mean; q+=d*d; }
  for(int m=32;m;m>>=1) q+=__shfl_xor(q,m);
  float var=rb(q/D); float sd=rb(sqrtf(rb(var+1e-5f))); float mb=rb(mean);
  for(int i=0;i<7;i++){ int d=t+64*i; float n=rb(rb(v[i]-mb)/sd); float g=rb(1.f+mod[co*D+d]); h[r*D+d]=f2b(rb(n*g)+mod[so*D+d]); }
}
__global__ void k_dftd(const u16* h, float2* X){
  int r=blockIdx.x, k=threadIdx.x; __shared__ float hs[D], cs[D], sn[D];
  for(int i=k;i<D;i+=blockDim.x){ hs[i]=b2f(h[r*D+i]); float s,c; sincospif(2.f*i/D,&s,&c); cs[i]=c; sn[i]=s; }
  __syncthreads(); if(k>=DF) return;
  float re=0,im=0; int idx=0; for(int d=0;d<D;d++){ re+=hs[d]*cs[idx]; im-=hs[d]*sn[idx]; idx+=k; if(idx>=D) idx-=D; }
  X[k*S+r]=make_float2(re,im);
}
__global__ void k_col(const float2* X, const u16* wr, const u16* wi, float2* Z){
  int k=blockIdx.x, t=threadIdx.x; __shared__ float2 a[S], b[S], tw[S];
  for(int i=t;i<S;i+=1024){ a[i]=X[k*S+i]; float s,c; sincospif(2.f*i/S,&s,&c); tw[i]=make_float2(c,s);} __syncthreads();
  for(int u=t;u<S;u+=1024){ float re=0,im=0; for(int s=0;s<S;s++){ float2 w=tw[(u*s)&(S-1)]; float2 v=a[s]; re+=v.x*w.x+v.y*w.y; im+=v.y*w.x-v.x*w.y; }
    float fr=b2f(wr[u*DF+k]), fi=b2f(wi[u*DF+k]); b[u]=make_float2(re*fr-im*fi, re*fi+im*fr);} __syncthreads();
  for(int s=t;s<S;s+=1024){ float re=0,im=0; for(int u=0;u<S;u++){ float2 w=tw[(u*s)&(S-1)]; float2 v=b[u]; re+=v.x*w.x-v.y*w.y; im+=v.y*w.x+v.x*w.y; }
    Z[s*DF+k]=make_float2(re/S,im/S);}
}
__global__ void k_idft(const float2* Z, const u16* x, const float* mod, u16* x2){
  int r=blockIdx.x, d=threadIdx.x; __shared__ float2 z[DF]; __shared__ float cs[D], sn[D];
  for(int i=d;i<D;i+=blockDim.x){ float s,c; sincospif(2.f*i/D,&s,&c); cs[i]=c; sn[i]=s; if(i<DF) z[i]=Z[r*DF+i]; }
  __syncthreads(); if(d>=D) return;
  float a=z[0].x + ((d&1)?-z[224].x:z[224].x); int idx=d; for(int k=1;k<224;k++){ a+=2.f*(z[k].x*cs[idx]-z[k].y*sn[idx]); idx+=d; if(idx>=D) idx-=D; }
  float y=rb(a/D); x2[r*D+d]=f2b(b2f(x[r*D+d])+rb(mod[2*D+d]*y));
}
__global__ void k_g1(const u16* h, const u16* wg, const u16* wu, u16* act){
  int r=blockIdx.x; __shared__ float hs[D]; for(int i=threadIdx.x;i<D;i+=blockDim.x) hs[i]=b2f(h[r*D+i]); __syncthreads();
  for(int f=threadIdx.x;f<F;f+=blockDim.x){ float g=0,u=0; for(int d=0;d<D;d++){ g+=hs[d]*b2f(wg[d*F+f]); u+=hs[d]*b2f(wu[d*F+f]); }
    g=rb(g); u=rb(u); act[r*F+f]=f2b(g>0?g*u:0.f*u); }
}
__global__ void k_g2(const u16* act, const u16* wd, const u16* x2, const float* mod, u16* out){
  int r=blockIdx.x; __shared__ float as[F]; for(int i=threadIdx.x;i<F;i+=blockDim.x) as[i]=b2f(act[r*F+i]); __syncthreads();
  int d=threadIdx.x; if(d>=D) return; float a=0; for(int f=0;f<F;f++) a+=as[f]*b2f(wd[f*D+d]);
  out[r*D+d]=f2b(b2f(x2[r*D+d])+rb(mod[5*D+d]*rb(a)));
}
extern "C" void kernel_launch(void* const* d_in, const int* in_sizes, int n_in, void* d_out, int out_size, void* d_ws, size_t ws_size, hipStream_t stream){
  const u16* x=(const u16*)d_in[0]; const u16* wr=(const u16*)d_in[1]; const u16* wi=(const u16*)d_in[2];
  const u16* wg=(const u16*)d_in[4]; const u16* wu=(const u16*)d_in[5]; const u16* wd=(const u16*)d_in[6]; const u16* c=(const u16*)d_in[7]; const u16* wm=(const u16*)d_in[8];
  char* ws=(char*)d_ws; float* mod=(float*)ws; u16* h=(u16*)(ws+(1<<20)); float2* X=(float2*)(ws+(4<<20)); float2* Z=(float2*)(ws+(8<<20)); u16* x2=(u16*)(ws+(12<<20)); u16* h2=(u16*)(ws+(14<<20)); u16* act=(u16*)(ws+(16<<20));
  k_mod<<<(6*D+63)/64,64,0,stream>>>(c,wm,mod);
  k_ln<<<S,64,0,stream>>>(x,mod,0,1,h);
  k_dftd<<<S,256,0,stream>>>(h,X);
  k_col<<<DF,1024,0,stream>>>(X,wr,wi,Z);
  k_idft<<<S,448,0,stream>>>(Z,x,mod,x2);
  k_ln<<<S,64,0,stream>>>(x2,mod,3,4,h2);
  k_g1<<<S,256,0,stream>>>(h2,wg,wu,act);
  k_g2<<<S,448,0,stream>>>(act,wd,x2,mod,(u16*)d_out);
}
```

```cpp
#include <hip/hip_runtime.h>

typedef unsigned short u16;
typedef unsigned int u32;
typedef unsigned long long u64;
typedef u32 v4u __attribute__((ext_vector_type(4)));
typedef u32 v2u __attribute__((ext_vector_type(2)));
typedef float v4f __attribute__((ext_vector_type(4)));
typedef float v2f __attribute__((ext_vector_type(2)));
typedef short v8s __attribute__((ext_vector_type(8)));
typedef short v4s __attribute__((ext_vector_type(4)));
typedef __bf16 v8b __attribute__((ext_vector_type(8)));
typedef __bf16 v2b __attribute__((ext_vector_type(2)));
#define LAS __attribute__((address_space(3)))
#define GAS __attribute__((address_space(1)))
#define DEV __device__ __forceinline__

constexpr int S = 2048, D = 448, DF = 225, F = 1792;

struct alignas(16) Chain { u16 v[2048]; };
struct Cos448 { u16 v[448]; };
constexpr double c_cos_t(double a) {
  double a2 = a * a, t = 1.0, s = 1.0;
  for (int i = 1; i <= 10; ++i) { t = -t * a2 / ((2 * i - 1) * (2 * i)); s += t; }
  return s;
}
constexpr double c_sin_t(double a) {
  double a2 = a * a, t = a, s = a;
  for (int i = 1; i <= 10; ++i) { t = -t * a2 / ((2 * i) * (2 * i + 1)); s += t; }
  return s;
}
constexpr double c_cos448(int m) {
  if (m > 224) m = 448 - m;
  double sg = 1.0;
  if (m > 112) { m = 224 - m; sg = -1.0; }
  const double w = 6.283185307179586476925286766559 / 448.0;
  return m == 112 ? 0.0 : (m <= 56 ? sg * c_cos_t(w * m) : sg * c_sin_t(w * (112 - m)));
}
constexpr u16 c_bf16(double d) {
  const u32 u = __builtin_bit_cast(u32, (float)d);
  return (u16)((u + 0x7FFFu + ((u >> 16) & 1u)) >> 16);
}
constexpr Cos448 mk_cos() { Cos448 c{}; for (int m = 0; m < 448; ++m) c.v[m] = c_bf16(c_cos448(m)); return c; }
constexpr Cos448 CTAB = mk_cos();
constexpr u16 bcos(int m) { return CTAB.v[m % 448]; }
constexpr u16 bsin(int m) { return CTAB.v[(m % 448 + 336) % 448]; }
constexpr u16 bneg(u16 b) { return b ? (u16)(b ^ 0x8000u) : b; }
constexpr u16 bscale(u16 b, int e) { return b ? (u16)(b + e * 0x80) : b; }

constexpr Chain mk_p1(int c) {
  int par = 0, tile = 0, sn = 0;
  if (c < 8) { par = 0; tile = c >> 1; sn = c & 1; }
  else if (c < 16) { par = 1; tile = (c - 8) >> 1; sn = c & 1; }
  else if (c < 23) { par = 0; tile = 4 + ((c - 16) >> 1); sn = c & 1; }
  else { par = 1; tile = 4 + ((c - 23) >> 1); sn = (c - 23) & 1; }
  Chain ch{};
  for (int s = 0; s < 4; ++s)
    for (int l = 0; l < 64; ++l) {
      const int kp = 16 * tile + (l & 15), k = 2 * kp + par;
      if (k > 224) continue;
      for (int j = 0; j < 8; ++j) {
        const int dl = 32 * s + 8 * (l >> 4) + j;
        if (dl > 112) continue;
        u16 b = sn ? bneg(bsin(k * dl)) : bcos(k * dl);
        if (dl == 0 || dl == 112) b = bscale(b, -1);
        ch.v[(s * 64 + l) * 8 + j] = b;
      }
    }
  return ch;
}
constexpr Chain mk_p3(int c) {
  const int tile = c == 28 ? 0 : c >> 2, type = c == 28 ? 0 : c & 3, par = type & 1, sn = type >> 1;
  Chain ch{};
  for (int s = 0; s < 4; ++s)
    for (int l = 0; l < 64; ++l) {
      int dl = 16 * tile + (l & 15);
      if (c == 28) { if (dl != 0) continue; dl = 112; }
      if (c == 3 && dl == 0) dl = 112;
      for (int j = 0; j < 8; ++j) {
        const int kp = 32 * s + 16 * (j >> 2) + 4 * (l >> 4) + (j & 3), k = 2 * kp + par;
        if (k > 224) continue;
        u16 b = sn ? bsin(k * dl) : bcos(k * dl);
        if (k != 0 && k != 224) b = bscale(b, 1);
        ch.v[(s * 64 + l) * 8 + j] = b;
      }
    }
  return ch;
}
constexpr Chain mk_p2(int c) {
  Chain ch{};
  for (int f = 0; f < 4; ++f)
    for (int l = 0; l < 64; ++l)
      for (int j = 0; j < 8; ++j) {
        const int comp = c < 8 ? (c & 1) : (f >> 1), s = c < 8 ? f : (f & 1);
        const int o = 16 * (c < 8 ? (c >> 1) : (c - 8)) + (l & 15), i = 16 * s + 4 * (l >> 4) + (j >> 1);
        const int m = (c < 8 ? 7 : 14) * ((i * o) % 64);
        u16 b = 0;
        if (comp == 0) b = (j & 1) ? bsin(m) : bcos(m);
        else b = (j & 1) ? bcos(m) : bneg(bsin(m));
        ch.v[(f * 64 + l) * 8 + j] = b;
      }
  return ch;
}
template <int I> constexpr Chain P1C = mk_p1(I);
template <int I> constexpr Chain P3C = mk_p3(I);
template <int I> constexpr Chain P2C = mk_p2(I);
#define L10(T, b) T<b>, T<b + 1>, T<b + 2>, T<b + 3>, T<b + 4>, T<b + 5>, T<b + 6>, T<b + 7>, T<b + 8>, T<b + 9>
#define L29(T) L10(T, 0), L10(T, 10), T<20>, T<21>, T<22>, T<23>, T<24>, T<25>, T<26>, T<27>, T<28>
constexpr int TAB_P1 = 0, TAB_P3 = 29, TAB_P2 = 58, TAB_N = 68;
static __device__ const Chain TAB[TAB_N] = {L29(P1C), L29(P3C), L10(P2C, 0)};

constexpr int WS_EPOCH = 0;
constexpr int WS_RDY = 4096;
constexpr int WS_WRDY = 4096 + 8192;
constexpr int WS_MOD = 32768, MOD_REP = 12288;
constexpr int T1_BLK = 226 * 64;
constexpr int WS_T1 = 1 << 18;
constexpr int WS_T2 = WS_T1 + 128 * T1_BLK;
constexpr int WS_WT = WS_T2 + 226 * 8192;
constexpr int WS_H2 = WS_WT + 113 * 16384;
constexpr int WS_X2 = WS_H2 + 128 * 14336;
constexpr int WS_WD = WS_X2 + 128 * 14336;
constexpr int WS_ACT = WS_WD + 7 * 56 * 4096;
constexpr int WS_END = WS_ACT + 128 * 57344;
constexpr int WS_DBG = 32 << 20;

constexpr int L_W1 = 0;
constexpr int L_B = 114688;
constexpr int XP = 912;
constexpr int L_XS = L_B;
constexpr int L_ST = L_XS + 16 * XP;
constexpr int L_M1 = L_ST + 128;
constexpr int L_AF = L_M1 + 4 * 1792;
constexpr int L_M2 = L_AF + 16384;
constexpr int L_EX = L_M2 + 3 * 1792;
constexpr int L_REND = L_EX + 512;
constexpr int ZPL = 242 * 32;
constexpr int L_CI = L_B;
constexpr int L_CX = L_CI + 2 * 8256;
constexpr int L_RED = L_CX + 16384;
constexpr int L_SZ = 163840;
static_assert(L_REND <= L_SZ && L_RED + 2048 <= L_SZ && 2 * ZPL <= 16384, "lds");

constexpr u32 SENT = 0xFFFFFFFFu;
constexpr int SPIN = 1 << 14;
constexpr int SC1 = 16;

typedef __amdgpu_buffer_rsrc_t rsrc_t;
DEV rsrc_t mkrs(const void* p, int bytes) { return __builtin_amdgcn_make_buffer_rsrc((void*)p, 0, bytes, 0x00020000); }
DEV v4u ld4(rsrc_t r, int off, int so = 0) { return __builtin_amdgcn_raw_buffer_load_b128(r, off, so, 0); }
DEV v2u ld2(rsrc_t r, int off) { return __builtin_amdgcn_raw_buffer_load_b64(r, off, 0, 0); }
DEV v4u ld4c(rsrc_t r, int off, int so = 0) { return __builtin_amdgcn_raw_buffer_load_b128(r, off, so, SC1); }
DEV v2u ld2c(rsrc_t r, int off) { return __builtin_amdgcn_raw_buffer_load_b64(r, off, 0, SC1); }
DEV u32 ld1c(rsrc_t r, int off) { return __builtin_amdgcn_raw_buffer_load_b32(r, off, 0, SC1); }
DEV void st4c(rsrc_t r, int off, v4u v, int so = 0) { __builtin_amdgcn_raw_buffer_store_b128(v, r, off + so, 0, SC1); }
DEV void st2c(rsrc_t r, int off, v2u v) { __builtin_amdgcn_raw_buffer_store_b64(v, r, off, 0, SC1); }
DEV float blo(u32 v) { return __builtin_bit_cast(float, v << 16); }
DEV float bhi(u32 v) { return __builtin_bit_cast(float, v & 0xFFFF0000u); }
DEV u32 pk(float lo, float hi) { return __builtin_bit_cast(u32, __builtin_convertvector(v2f{lo, hi}, v2b)); }
DEV u32 pks(float lo, float hi) { return min(pk(lo, hi), 0xFFFFFFFEu); }
DEV float rb(float f) { return blo(pk(f, 0.f)); }
DEV v4f mfma(v4u a, v4u b, v4f c) {
  return __builtin_amdgcn_mfma_f32_16x16x32_bf16(__builtin_bit_cast(v8b, a), __builtin_bit_cast(v8b, b), c, 0, 0, 0);
}
DEV u32 max4(v4u v) { return max(max(v.x, v.y), max(v.z, v.w)); }
DEV void drain() { asm volatile("s_waitcnt vmcnt(0)" ::: "memory"); }
DEV void cfence() { asm volatile("" ::: "memory"); }
DEV void bar() { asm volatile("s_waitcnt lgkmcnt(0)\n\ts_barrier" ::: "memory"); }
template <int CTRL> DEV float dpp(float v) {
  return __builtin_bit_cast(float, __builtin_amdgcn_update_dpp(0, __builtin_bit_cast(int, v), CTRL, 0xF, 0xF, true));
}
DEV float sum16(float v) {
  v += dpp<0x128>(v); v += dpp<0x124>(v); v += dpp<0x122>(v); v += dpp<0x121>(v); return v;
}
DEV float sumg(float v) {
  float w = v;
  asm volatile("s_nop 1\n\tv_permlane16_swap_b32 %0, %1" : "+v"(v), "+v"(w));
  v += w; w = v;
  asm volatile("s_nop 1\n\tv_permlane32_swap_b32 %0, %1" : "+v"(v), "+v"(w));
  return v + w;
}
DEV v4u lds4(const char LAS* L, int off) { return *(const LAS v4u*)(L + off); }
DEV v4f lds4f(const char LAS* L, int off) { return *(const LAS v4f*)(L + off); }
DEV v2u ldtr(const char LAS* L, int off) { return __builtin_bit_cast(v2u, __builtin_amdgcn_ds_read_tr16_b64_v4i16((LAS v4s*)(L + off))); }
DEV v4u ldtr2(const char LAS* L, int o0, int o1) { const v2u a = ldtr(L, o0), b = ldtr(L, o1); return v4u{a.x, a.y, b.x, b.y}; }
DEV void glds16(const void* gsrc, u32 lds_dst) {
  u32 keep;
  asm volatile("s_mov_b32 %0, m0\n\ts_mov_b32 m0, %2\n\ts_nop 0\n\tglobal_load_lds_dwordx4 %1, off\n\ts_mov_b32 m0, %0"
               : "=&s"(keep) : "v"(gsrc), "s"(lds_dst) : "memory");
}
DEV void light_poll(rsrc_t ws, int off) {
  for (int s = 0; s < SPIN; ++s) {
    cfence();
    const u32 v = ld1c(ws, off);
    if (__builtin_amdgcn_readfirstlane(v) != SENT) break;
    __builtin_amdgcn_s_sleep(2);
  }
}
DEV int rho_row(int rho) {
  const int m = (rho >> 4) & 3, dl = ((rho >> 6) << 4) | (rho & 15);
  int d = m == 0 ? dl : m == 1 ? 448 - dl : m == 2 ? 224 - dl : 224 + dl;
  if (dl == 0 && m == 1) d = 112;
  if (dl == 0 && m == 3) d = 336;
  return d;
}
DEV int row_rho(int d) {
  int m, dl;
  if (d < 112) { m = 0; dl = d; }
  else if (d == 112) { m = 1; dl = 0; }
  else if (d <= 224) { m = 2; dl = 224 - d; }
  else if (d < 336) { m = 3; dl = d - 224; }
  else if (d == 336) { m = 3; dl = 0; }
  else { m = 1; dl = 448 - d; }
  return ((dl >> 4) << 6) | (m << 4) | (dl & 15);
}
DEV int tok_row(int blk, int t) {
  const int n1 = 16 * (blk >> 5) + 4 * ((blk >> 2) & 3) + (t & 3), n2 = 16 * ((blk >> 4) & 1) + 4 * (blk & 3) + (t >> 2);
  return n2 + 32 * n1;
}

#define STAMP(i)

DEV void dma_w1(const u16* wg, const u16* wu, int slice, int wave, int lane, u32 lbase) {
#pragma unroll 4
  for (int i = 0; i < 28; ++i) {
    const int rho = (wave * 28 + i) * 4 + (lane >> 4), c = lane & 15;
    const int u = (c >> 1) ^ (rho & 7);
    const u16* src = (u < 4 ? wg : wu) + rho_row(rho) * F + slice * 64 + (u & 3) * 16 + (c & 1) * 8;
    glds16(src, lbase + L_W1 + (wave * 28 + i) * 1024);
  }
}

__global__ void __launch_bounds__(256, 1)
fused(const u16* __restrict__ xin, const u16* __restrict__ wr, const u16* __restrict__ wi, const u16* __restrict__ wg,
      const u16* __restrict__ wu, const u16* __restrict__ wd, const u16* __restrict__ cin, const u16* __restrict__ wm,
      u16* __restrict__ out, char* __restrict__ wsp) {
  __shared__ __attribute__((aligned(16))) char smem[L_SZ];
  char LAS* L = (char LAS*)smem;
  const u32 lbase = (u32)(size_t)L;
  const int tid = threadIdx.x, lane = tid & 63, wave = __builtin_amdgcn_readfirstlane(tid >> 6);
  const int g = lane >> 4, c16 = lane & 15, l16 = lane * 16;
  const int grp = blockIdx.x & 7, j = blockIdx.x >> 3;
  const rsrc_t ws = mkrs(wsp, WS_END);
  const rsrc_t tab = mkrs(TAB, TAB_N * 4096);
  STAMP(0);
  const bool isR = j >= 14 && j < 30;
  const bool isG = j < 28;
  u32 epoch = 0;

  if (isR) {
    const int blk = 16 * grp + (j - 14);
    const rsrc_t xr = mkrs(xin, S * D * 2);
    v2u xv[7];
    {
      const int off = tok_row(blk, 4 * wave + g) * (D * 2) + c16 * 8;
#pragma unroll
      for (int m = 0; m < 7; ++m) xv[m] = ld2(xr, off + m * 128);
    }
    epoch = ld1c(ws, WS_EPOCH);
    const int ch0 = TAB_P1 + (wave == 0 ? 0 : wave == 1 ? 8 : wave == 2 ? 16 : 23);
    v4u cf[8][4];
#pragma unroll
    for (int q = 0; q < 8; ++q)
#pragma unroll
      for (int s = 0; s < 4; ++s) cf[q][s] = ld4(tab, l16 + s * 1024, (ch0 + q) * 4096);
    {
      const v4u sv = {SENT, SENT, SENT, SENT};
#pragma unroll
      for (int m = 0; m < 4; ++m) {
        const int kk = (tid >> 2) + 64 * m;
        if (kk < 226) st4c(ws, WS_T2 + kk * 8192 + blk * 64 + (tid & 3) * 16, sv);
        if (tid + 256 * m < 896) st4c(ws, tid * 16, sv, WS_H2 + blk * 14336 + 4096 * m);
      }
    }
    {
      float f[28], s = 0.f;
#pragma unroll
      for (int m = 0; m < 7; ++m) {
        f[4 * m] = blo(xv[m].x); f[4 * m + 1] = bhi(xv[m].x); f[4 * m + 2] = blo(xv[m].y); f[4 * m + 3] = bhi(xv[m].y);
        s += (f[4 * m] + f[4 * m + 1]) + (f[4 * m + 2] + f[4 * m + 3]);
        *(LAS v2u*)(L + L_XS + (4 * wave + g) * XP + c16 * 8 + m * 128) = xv[m];
      }
      const float mean = sum16(s) * (1.f / D);
      float q = 0.f;
#pragma unroll
      for (int m = 0; m < 28; ++m) q += (f[m] - mean) * (f[m] - mean);
      const float rstd = __builtin_amdgcn_rsqf(sum16(q) * (1.f / D) + 1e-5f);
      if (c16 == 0) *(LAS v2f*)(L + L_ST + (4 * wave + g) * 8) = v2f{mean, rstd};
    }
    bar();
    float n[4][8];
    const int d0 = 8 * (4 * wave + g);
    {
      const v2f st = *(const LAS v2f*)(L + L_ST + c16 * 8);
      const char LAS* xrow = L + L_XS + c16 * XP;
      const v4u a = lds4(xrow, d0 * 2), e = lds4(xrow, (224 + d0) * 2);
#pragma unroll
      for (int i = 0; i < 4; ++i) {
        n[0][2 * i] = blo(a[i]); n[0][2 * i + 1] = bhi(a[i]);
        n[3][2 * i] = blo(e[i]); n[3][2 * i + 1] = bhi(e[i]);
      }
#pragma unroll
      for (int i = 0; i < 8; ++i) {
        const int db = (d0 + i == 0) ? 0 : 448 - d0 - i;
        n[1][i] = blo(*(const LAS u16*)(xrow + db * 2));
        n[2][i] = blo(*(const LAS u16*)(xrow + (224 - d0 - i) * 2));
      }
#pragma unroll
      for (int m = 0; m < 4; ++m)
#pragma unroll
        for (int i = 0; i < 8; ++i) n[m][i] = (n[m][i] - st.x) * st.y;
    }
    const u32 tag = epoch * 0x9E3779B1u + 0x7F4A7C15u;
    STAMP(1);
    {
      v4u mv;
      for (int s = 0; s < SPIN; ++s) {
        cfence();
        bool ok;
        if (tid < 224) { mv = ld4c(ws, WS_MOD + grp * MOD_REP + tid * 16); ok = mv.y == tag && mv.w == tag; }
        else ok = true;
        if (tid >= 192) {
          const int f = (tid - 192) * 2;
          const v4u rv = ld4c(ws, WS_RDY + grp * 1024 + f * 8);
          ok = ok && (f >= 113 || (rv.x == tag && rv.y == ~tag)) && (f + 1 >= 113 || (rv.z == tag && rv.w == ~tag));
        }
        if (__all(ok)) break;
        __builtin_amdgcn_s_sleep(1);
      }
      if (tid < 224) {
        const int kind = tid >= 112, d = 4 * (tid - 112 * kind);
        const float v[4] = {blo(mv.x), bhi(mv.x), blo(mv.z), bhi(mv.z)};
        const int base = L_M1 + (kind ? 0 : 2 * 1792);
        *(LAS v4f*)(L + base + d * 4) = v4f{v[0], v[1], v[2], v[3]};
#pragma unroll
        for (int e = 0; e < 4; ++e) *(LAS float*)(L + base + 1792 + ((d + e == 0) ? 0 : 448 - d - e) * 4) = v[e];
      }
    }
    bar();
    STAMP(2);
    {
      float h[4][8];
#pragma unroll
      for (int m = 0; m < 4; ++m) {
        const int o = ((m == 1 || m == 2) ? 1792 : 0) + (d0 + (m >= 2 ? 224 : 0)) * 4;
#pragma unroll
        for (int hh = 0; hh < 2; ++hh) {
          const v4f gv = lds4f(L, L_M1 + o + hh * 16), sv = lds4f(L, L_M1 + 2 * 1792 + o + hh * 16);
#pragma unroll
          for (int i = 0; i < 4; ++i) h[m][4 * hh + i] = n[m][4 * hh + i] * gv[i] + sv[i];
        }
      }
      v4u fr[4];
#pragma unroll
      for (int i = 0; i < 4; ++i) {
        float t[4][2];
#pragma unroll
        for (int e = 0; e < 2; ++e) {
          const int ii = 2 * i + e;
          const float p = h[0][ii] + h[1][ii], q = h[2][ii] + h[3][ii], u = h[0][ii] - h[1][ii], v = h[3][ii] - h[2][ii];
          t[0][e] = p + q; t[1][e] = u + v; t[2][e] = p - q; t[3][e] = u - v;
        }
#pragma unroll
        for (int m = 0; m < 4; ++m) fr[m][i] = pk(t[m][0], t[m][1]);
      }
#pragma unroll
      for (int m = 0; m < 4; ++m) *(LAS v4u*)(L + L_AF + (m * 4 + wave) * 1024 + lane * 16) = fr[m];
    }
    bar();
    {
      v4u a[2][4];
#pragma unroll
      for (int m = 0; m < 2; ++m)
#pragma unroll
        for (int s = 0; s < 4; ++s) a[m][s] = lds4(L, L_AF + (((wave & 1) * 2 + m) * 4 + s) * 1024 + lane * 16);
      const int kk0 = (wave & 1) * 114 + (wave >> 1) * 64;
#pragma unroll
      for (int q = 0; q < 4; ++q) {
        if (q == 3 && wave == 3) break;
        v4f re = {0.f, 0.f, 0.f, 0.f}, im = re;
#pragma unroll
        for (int s = 0; s < 4; ++s) re = mfma(a[0][s], cf[2 * q][s], re);
        if (q < 3 || wave < 2) {
#pragma unroll
          for (int s = 0; s < 4; ++s) im = mfma(a[1][s], cf[2 * q + 1][s], im);
        }
        const v4u o = {pks(re.x, im.x), pks(re.y, im.y), pks(re.z, im.z), pks(re.w, im.w)};
        if (q < 3 || wave < 2 || c16 < 2) st4c(ws, WS_T1 + blk * T1_BLK + (kk0 + 16 * q + c16) * 64 + g * 16, o);
      }
    }
    STAMP(3);
    const int t0 = wave == 0 ? 0 : 2 * wave - 1;
#pragma unroll
    for (int q = 0; q < 8; ++q) {
      const int chn = TAB_P3 + ((wave == 0 && q >= 4) ? 28 : 4 * t0 + q);
#pragma unroll
      for (int s = 0; s < 4; ++s) cf[q][s] = ld4(tab, l16 + s * 1024, chn * 4096);
    }
    {
      const v4u sv = {SENT, SENT, SENT, SENT};
#pragma unroll
      for (int m = 0; m < 14; ++m) st4c(ws, tid * 16, sv, WS_ACT + blk * 57344 + 4096 * m);
#pragma unroll
      for (int m = 0; m < 4; ++m) if (tid + 256 * m < 896) st4c(ws, tid * 16, sv, WS_X2 + blk * 14336 + 4096 * m);
    }
    if (isG) dma_w1(wg, wu, j, wave, lane, lbase);
    {
      v4u mv[2];
      for (int s = 0; s < SPIN; ++s) {
        cfence();
        mv[0] = ld4c(ws, WS_MOD + grp * MOD_REP + (224 + tid) * 16);
        mv[1] = ld4c(ws, WS_MOD + grp * MOD_REP + (224 + (tid < 80 ? 256 + tid : tid)) * 16);
        if (__all(mv[0].y == tag && mv[0].w == tag && mv[1].y == tag && mv[1].w == tag)) break;
        __builtin_amdgcn_s_sleep(4);
      }
#pragma unroll
      for (int r = 0; r < 2; ++r) {
        const int q = tid + 256 * r;
        if (q < 336) {
          const int kind = q / 112, d = 4 * (q - 112 * kind);
          const float sc = kind == 0 ? 1.f / (float)(S * D) : 1.f;
          const float v[4] = {blo(mv[r].x), bhi(mv[r].x), blo(mv[r].z), bhi(mv[r].z)};
#pragma unroll
          for (int e = 0; e < 4; ++e) *(LAS float*)(L + L_M2 + kind * 1792 + row_rho(d + e) * 4) = v[e] * sc;
        }
      }
    }
    float xo[2][4][4];
#pragma unroll
    for (int t = 0; t < 2; ++t)
#pragma unroll
      for (int m = 0; m < 4; ++m)
#pragma unroll
        for (int r = 0; r < 4; ++r)
          xo[t][m][r] = blo(*(const LAS u16*)(L + L_XS + c16 * XP + rho_row((t0 + (wave ? t : 0)) * 64 + m * 16 + 4 * g + r) * 2));
    bar();
    if (tid < 64) *(LAS v4u*)(L + L_AF + (tid >> 5) * ZPL + 226 * 32 + (tid & 31) * 16) = v4u{0u, 0u, 0u, 0u};
    STAMP(4);
    {
      light_poll(ws, WS_T2 + ((blk * 7 + wave * 57) % 226) * 8192 + blk * 64);
      v4u z[4];
      for (int s = 0; s < SPIN; ++s) {
        cfence();
        u32 mx = 0;
#pragma unroll
        for (int m = 0; m < 4; ++m) {
          const int kk = (tid >> 2) + 64 * m;
          z[m] = ld4c(ws, WS_T2 + (kk < 226 ? kk : kk - 64) * 8192 + blk * 64 + (tid & 3) * 16);
        }
#pragma unroll
        for (int m = 0; m < 4; ++m) mx = max(mx, max4(z[m]));
        if (__all(mx != SENT)) break;
      }
#pragma unroll
      for (int m = 0; m < 4; ++m) {
        const int kk = (tid >> 2) + 64 * m;
        if (kk < 226) {
          *(LAS v2u*)(L + L_AF + kk * 32 + (tid & 3) * 8) =
              v2u{__builtin_amdgcn_perm(z[m].y, z[m].x, 0x05040100u), __builtin_amdgcn_perm(z[m].w, z[m].z, 0x05040100u)};
          *(LAS v2u*)(L + L_AF + ZPL + kk * 32 + (tid & 3) * 8) =
              v2u{__builtin_amdgcn_perm(z[m].y, z[m].x, 0x07060302u), __builtin_amdgcn_perm(z[m].w, z[m].z, 0x07060302u)};
        }
      }
    }
    bar();
    STAMP(5);
    {
      v4f acc[2][4];
#pragma unroll
      for (int ty = 0; ty < 4; ++ty) {
        acc[0][ty] = v4f{0.f, 0.f, 0.f, 0.f}; acc[1][ty] = acc[0][ty];
#pragma unroll
        for (int s = 0; s < 4; ++s) {
          const int o = L_AF + (ty >> 1) * ZPL + ((ty & 1) * 114 + 32 * s) * 32 + lane * 8;
          const v4u zf = ldtr2(L, o, o + 512);
          acc[0][ty] = mfma(cf[ty][s], zf, acc[0][ty]);
          if (wave != 0 || ty == 0) acc[1][ty] = mfma(cf[4 + ty][s], zf, acc[1][ty]);
        }
      }
      const int nt = wave == 0 ? 1 : 2;
      float x2[2][4][4], sm = 0.f;
#pragma unroll
      for (int t = 0; t < 2; ++t) {
        if (t < nt) {
          const int ro = ((t0 + t) * 64 + 4 * g) * 4;
          v4f ga[4];
#pragma unroll
          for (int m = 0; m < 4; ++m) ga[m] = lds4f(L, L_M2 + ro + m * 64);
#pragma unroll
          for (int r = 0; r < 4; ++r) {
            const float A = acc[t][0][r] + acc[t][1][r], B = acc[t][0][r] - acc[t][1][r];
            const float P = acc[t][2][r] + acc[t][3][r], Q = acc[t][2][r] - acc[t][3][r];
            float y[4] = {A - P, A + P, B + Q, B - Q};
            if (t == 0 && r == 0 && wave == 0 && g == 0) {
              y[0] = A; y[2] = B; y[1] = acc[1][0][0] - acc[0][3][0]; y[3] = acc[1][0][0] + acc[0][3][0];
            }
#pragma unroll
            for (int m = 0; m < 4; ++m) x2[t][m][r] = xo[t][m][r] + ga[m][r] * y[m];
          }
#pragma unroll
          for (int m = 0; m < 4; ++m)
#pragma unroll
            for (int r = 0; r < 4; r += 2) {
              const u32 p = pk(x2[t][m][r], x2[t][m][r + 1]);
              x2[t][m][r] = blo(p); x2[t][m][r + 1] = bhi(p);
              sm += x2[t][m][r] + x2[t][m][r + 1];
            }
        }
      }
      const float mw = sumg(sm) * (wave == 0 ? 1.f / 64.f : 1.f / 128.f);
      float q = 0.f;
#pragma unroll
      for (int t = 0; t < 2; ++t)
        if (t < nt)
#pragma unroll
          for (int m = 0; m < 4; ++m)
#pragma unroll
            for (int r = 0; r < 4; ++r) q += (x2[t][m][r] - mw) * (x2[t][m][r] - mw);
      q = sumg(q);
      if (g == 0) *(LAS v2f*)(L + L_EX + (wave * 16 + c16) * 8) = v2f{mw, q};
      bar();
      float mean, rstd;
      {
        v2f e[4];
#pragma unroll
        for (int w = 0; w < 4; ++w) e[w] = *(const LAS v2f*)(L + L_EX + (w * 16 + c16) * 8);
        mean = (e[0].x + 2.f * (e[1].x + e[2].x + e[3].x)) * (1.f / 7.f);
        float m2 = e[0].y + e[1].y + e[2].y + e[3].y + 64.f * (e[0].x - mean) * (e[0].x - mean);
#pragma unroll
        for (int w = 1; w < 4; ++w) m2 += 128.f * (e[w].x - mean) * (e[w].x - mean);
        rstd = __builtin_amdgcn_rsqf(m2 * (1.f / D) + 1e-5f);
      }
#pragma unroll
      for (int t = 0; t < 2; ++t) {
        if (t < nt) {
          const int ro = ((t0 + t) * 64 + 4 * g) * 4;
#pragma unroll
          for (int a = 0; a < 2; ++a) {
            v4u o;
#pragma unroll
            for (int hh = 0; hh < 2; ++hh) {
              const int m = 2 * a + hh;
              const v4f sb = lds4f(L, L_M2 + 1792 + ro + m * 64), cb = lds4f(L, L_M2 + 2 * 1792 + ro + m * 64);
              float hv[4];
#pragma unroll
              for (int r = 0; r < 4; ++r) hv[r] = (x2[t][m][r] - mean) * rstd * cb[r] + sb[r];
              o[2 * hh] = pks(hv[0], hv[1]); o[2 * hh + 1] = pks(hv[2], hv[3]);
            }
            st4c(ws, l16 + a * 1024, o, WS_H2 + blk * 14336 + 2 * (t0 + t) * 1024);
          }
        }
      }
      STAMP(6);
#pragma unroll
      for (int t = 0; t < 2; ++t)
        if (t < nt)
#pragma unroll
          for (int m = 0; m < 4; ++m)
#pragma unroll
            for (int r = 0; r < 4; ++r)
              *(LAS u16*)(L + L_XS + c16 * XP + rho_row((t0 + t) * 64 + m * 16 + 4 * g + r) * 2) =
                  (u16)(__builtin_bit_cast(u32, x2[t][m][r]) >> 16);
      bar();
#pragma unroll
      for (int m = 0; m < 4; ++m) {
        const int ch = tid + 256 * m;
        if (ch < 896) {
          const int tk = ch / 56;
          v4u v = lds4(L, L_XS + tk * XP + (ch - tk * 56) * 16);
          v.x = min(v.x, 0xFFFFFFFEu); v.y = min(v.y, 0xFFFFFFFEu); v.z = min(v.z, 0xFFFFFFFEu); v.w = min(v.w, 0xFFFFFFFEu);
          st4c(ws, WS_X2 + blk * 14336 + ch * 16, v);
        }
      }
    }
    if (!isG) return;
  } else {
    const int idx = j >= 30 ? (j - 30) * 8 + grp : 16 + j * 8 + grp;
    const bool isC = j < 14 || idx == 0;
    const int pair = j < 14 ? grp * 14 + j : 112;
    v4u mw[7]; u16 cv[7];
    if (idx < 84) {
      const rsrc_t mr = mkrs(wm, D * 6 * D * 2);
#pragma unroll
      for (int i = 0; i < 7; ++i) mw[i] = ld4(mr, (((tid >> 2) + 64 * i) * (6 * D) + idx * 32 + (tid & 3) * 8) * 2);
#pragma unroll
      for (int i = 0; i < 7; ++i) cv[i] = cin[(tid >> 2) + 64 * i];
    }
    epoch = ld1c(ws, WS_EPOCH);
    v4u fw[4];
    {
      const rsrc_t rr = mkrs(wr, S * DF * 2), ri = mkrs(wi, S * DF * 2);
      fw[0] = ld4(rr, idx * 7200 + tid * 16);
      fw[1] = ld4(rr, idx * 7200 + (tid < 194 ? 256 + tid : tid) * 16);
      fw[2] = ld4(ri, idx * 7200 + tid * 16);
      fw[3] = ld4(ri, idx * 7200 + (tid < 194 ? 256 + tid : tid) * 16);
    }
    if (isC) {
      const v4u sv = {SENT, SENT, SENT, SENT};
#pragma unroll
      for (int q = 0; q < 4; ++q) st4c(ws, WS_T1 + (32 * wave + 8 * q + (lane >> 3)) * T1_BLK + pair * 128 + (lane & 7) * 16, sv);
    }
    v2u dw[8];
    const int unit = (grp * 14 + j) * 4 + wave;
    const bool tiles = j < 14 && unit < 392;
    if (tiles) {
      const rsrc_t dr = mkrs(wd, F * D * 2);
      const int ct = unit / 56, kap = unit - ct * 56;
#pragma unroll
      for (int e = 0; e < 8; ++e) dw[e] = ld2(dr, ((32 * kap + 16 * (e >> 2) + 4 * g + (e & 3)) * D + 64 * ct + 4 * c16) * 2);
    }
    const u32 tag = epoch * 0x9E3779B1u + 0x7F4A7C15u;
    if (idx < 84) {
      float a[8];
#pragma unroll
      for (int e = 0; e < 8; ++e) a[e] = 0.f;
#pragma unroll
      for (int i = 0; i < 7; ++i) {
        const float cc = blo(cv[i]), sl = rb(cc / (1.f + __expf(-cc)));
#pragma unroll
        for (int e = 0; e < 4; ++e) { a[2 * e] += sl * blo(mw[i][e]); a[2 * e + 1] += sl * bhi(mw[i][e]); }
      }
#pragma unroll
      for (int e = 0; e < 8; ++e) { a[e] += dpp<0x128>(a[e]); a[e] += dpp<0x124>(a[e]); }
      if (c16 < 4) {
        *(LAS v4f*)(L + L_RED + ((tid >> 4) * 32 + c16 * 8) * 4) = v4f{a[0], a[1], a[2], a[3]};
        *(LAS v4f*)(L + L_RED + ((tid >> 4) * 32 + c16 * 8 + 4) * 4) = v4f{a[4], a[5], a[6], a[7]};
      }
      bar();
      if (wave == 0) {
        v4f s = {0.f, 0.f, 0.f, 0.f};
#pragma unroll
        for (int r = 0; r < 16; ++r) s += lds4f(L, L_RED + (r * 32 + (lane & 7) * 4) * 4);
        const int kind = idx / 14;
        const float one = (kind == 1 || kind == 4) ? 1.f : 0.f;
        st4c(ws, WS_MOD + (lane >> 3) * MOD_REP + idx * 128 + (lane & 7) * 16,
             v4u{pk(rb(s.x) + one, rb(s.y) + one), tag, pk(rb(s.z) + one, rb(s.w) + one), tag});
      }
    }
    STAMP(1);
    *(LAS v4u*)(L + L_CX + tid * 16) = fw[0];
    *(LAS v4u*)(L + L_CX + 7200 + tid * 16) = fw[2];
    if (tid < 194) {
      *(LAS v4u*)(L + L_CX + (256 + tid) * 16) = fw[1];
      *(LAS v4u*)(L + L_CX + 7200 + (256 + tid) * 16) = fw[3];
    }
    drain();
    bar();
    if (isC && tid < 8) st2c(ws, WS_RDY + tid * 1024 + pair * 8, v2u{tag, ~tag});
#pragma unroll
    for (int m = 0; m < 4; ++m) {
      const int it = tid + 256 * m;
      if (it < 904) {
        const int p = it >> 3, ka = p < 57 ? 4 * p : 4 * (p - 57) + 1;
        v4u o;
#pragma unroll
        for (int e = 0; e < 2; ++e) {
          const char LAS* row = L + L_CX + (2 * (it & 7) + e) * 450 + ka * 2;
          const int kb = p == 56 ? 0 : 4;
          o[2 * e] = *(const LAS u16*)(row) | ((u32) * (const LAS u16*)(row + 7200) << 16);
          o[2 * e + 1] = *(const LAS u16*)(row + kb) | ((u32) * (const LAS u16*)(row + 7200 + kb) << 16);
        }
        st4c(ws, WS_WT + p * 16384 + idx * 128 + (it & 7) * 16, o);
      }
    }
    if (tiles) {
#pragma unroll
      for (int tt = 0; tt < 4; ++tt) {
        v4u o;
#pragma unroll
        for (int m = 0; m < 4; ++m)
          o[m] = __builtin_amdgcn_perm(dw[2 * m + 1][tt >> 1], dw[2 * m][tt >> 1], (tt & 1) ? 0x07060302u : 0x05040100u);
        st4c(ws, l16 + tt * 1024, o, WS_WD + unit * 4096);
      }
    }
    drain();
    bar();
    if (tid < 8) st2c(ws, WS_WRDY + tid * 1024 + idx * 8, v2u{tag, ~tag});
    if (!isC) return;
    STAMP(2);
    v4u c64[2][4], c32[2][2][2];
#pragma unroll
    for (int cp = 0; cp < 2; ++cp)
#pragma unroll
      for (int s = 0; s < 4; ++s) c64[cp][s] = ld4(tab, l16 + s * 1024, (TAB_P2 + wave * 2 + cp) * 4096);
#pragma unroll
    for (int t = 0; t < 2; ++t)
#pragma unroll
      for (int f = 0; f < 4; ++f) c32[t][f >> 1][f & 1] = ld4(tab, l16 + f * 1024, (TAB_P2 + 8 + t) * 4096);
    if (isG) dma_w1(wg, wu, j, wave, lane, lbase);
    float t1c[2][4], t1s[2][4], t2c[2][4], t2s[2][4];
#pragma unroll
    for (int t = 0; t < 2; ++t)
#pragma unroll
      for (int r = 0; r < 4; ++r) {
        const float a1 = (float)((16 * t + 4 * g + r) * (16 * wave + c16)) * (1.f / S);
        const float a2 = (float)((16 * t + c16) * (16 * wave + 4 * g + r)) * (1.f / S);
        t1c[t][r] = __builtin_amdgcn_cosf(a1); t1s[t][r] = __builtin_amdgcn_sinf(a1);
        t2c[t][r] = __builtin_amdgcn_cosf(a2); t2s[t][r] = __builtin_amdgcn_sinf(a2);
      }
    for (int s = 0; s < SPIN; ++s) {
      cfence();
      const v4u rv = ld4c(ws, WS_WRDY + grp * 1024 + lane * 16);
      if (__all(rv.x == tag && rv.y == ~tag && rv.z == tag && rv.w == ~tag)) break;
      __builtin_amdgcn_s_sleep(4);
    }
    v2u wf[2][4];
#pragma unroll
    for (int t = 0; t < 2; ++t)
#pragma unroll
      for (int r = 0; r < 4; ++r) wf[t][r] = ld2c(ws, WS_WT + pair * 16384 + (4 * (16 * t + 4 * g + r) + wave) * 128 + c16 * 8);
    STAMP(3);
    {
      light_poll(ws, WS_T1 + (32 * wave + (pair & 31)) * T1_BLK + pair * 128);
      v4u z[4];
      for (int s = 0; s < SPIN; ++s) {
        cfence();
        u32 mx = 0;
#pragma unroll
        for (int q = 0; q < 4; ++q) z[q] = ld4c(ws, WS_T1 + (32 * wave + 8 * q + (lane >> 3)) * T1_BLK + pair * 128 + (lane & 7) * 16);
#pragma unroll
        for (int q = 0; q < 4; ++q) mx = max(mx, max4(z[q]));
        if (__all(mx != SENT)) break;
      }
#pragma unroll
      for (int q = 0; q < 4; ++q)
        *(LAS v4u*)(L + L_CI + ((lane >> 2) & 1) * 8256 + (wave * 2 + (q >> 1)) * 1024 + ((8 * (q & 1) + (lane >> 3)) * 4 + (lane & 3)) * 16) = z[q];
    }
    bar();
    STAMP(4);
    {
      const v4f zero = {0.f, 0.f, 0.f, 0.f};
#pragma unroll
      for (int c = 0; c < 2; ++c) {
        v4u f2[2], f3[2];
#pragma unroll
        for (int mt = 0; mt < 2; ++mt) {
          v4f re = zero, im = zero;
#pragma unroll
          for (int s = 0; s < 4; ++s) {
            const v4u a = lds4(L, L_CI + c * 8256 + (s * 2 + mt) * 1024 + lane * 16);
            re = mfma(a, c64[0][s], re); im = mfma(a, c64[1][s], im);
          }
#pragma unroll
          for (int r = 0; r < 4; ++r)
            f2[mt][r] = pk(re[r] * t1c[mt][r] + im[r] * t1s[mt][r], im[r] * t1c[mt][r] - re[r] * t1s[mt][r]);
        }
#pragma unroll
        for (int ot = 0; ot < 2; ++ot) {
          v4f re = zero, im = zero;
#pragma unroll
          for (int s = 0; s < 2; ++s) { re = mfma(c32[ot][0][s], f2[s], re); im = mfma(c32[ot][1][s], f2[s], im); }
#pragma unroll
          for (int r = 0; r < 4; ++r) {
            const float fr = blo(wf[ot][r][c]), fi = bhi(wf[ot][r][c]);
            f3[ot][r] = pk(re[r] * fi + im[r] * fr, re[r] * fr - im[r] * fi);
          }
        }
#pragma unroll
        for (int nt = 0; nt < 2; ++nt) {
          v4f re = zero, im = zero;
#pragma unroll
          for (int s = 0; s < 2; ++s) { im = mfma(f3[s], c32[nt][0][s], im); re = mfma(f3[s], c32[nt][1][s], re); }
          v4u f4;
#pragma unroll
          for (int r = 0; r < 4; ++r)
            f4[r] = pk(re[r] * t2s[nt][r] + im[r] * t2c[nt][r], re[r] * t2c[nt][r] - im[r] * t2s[nt][r]);
          *(LAS v4u*)(L + L_CX + ((c * 2 + nt) * 4 + wave) * 1024 + lane * 16) = f4;
        }
      }
      bar();
#pragma unroll
      for (int c = 0; c < 2; ++c)
#pragma unroll
        for (int nt = 0; nt < 2; ++nt) {
          v4f re = zero, im = zero;
#pragma unroll
          for (int s = 0; s < 4; ++s) {
            const v4u b = lds4(L, L_CX + ((c * 2 + nt) * 4 + s) * 1024 + lane * 16);
            im = mfma(c64[0][s], b, im); re = mfma(c64[1][s], b, re);
          }
          const v4u o = {pks(re.x, im.x), pks(re.y, im.y), pks(re.z, im.z), pks(re.w, im.w)};
          st4c(ws, l16 + nt * 1024, o, WS_T2 + (2 * pair + c) * 8192 + wave * 2048);
        }
    }
    STAMP(5);
    if (!isG) return;
  }

  const u32 tag = epoch * 0x9E3779B1u + 0x7F4A7C15u;
  const int rs = j / 7, ct = j - rs * 7;
  drain();
  bar();
  STAMP(7);
  v4u wdn[14][4];
  {
    const int hb = WS_H2 + (16 * grp + 4 * wave) * 14336;
    light_poll(ws, WS_H2 + (16 * grp + 4 * wave) * 14336 + 13 * 1024);
    v4u h[14][2];
#pragma unroll
    for (int ks = 0; ks < 14; ++ks)
#pragma unroll
      for (int tt = 0; tt < 2; ++tt) h[ks][tt] = ld4c(ws, l16 + (ks & 3) * 1024, hb + tt * 14336 + (ks >> 2) * 4096);
    const int wbase = L_W1 + (lane >> 2) * 256 + ((lane >> 2) & 7) * 32 + (lane & 3) * 8;
    const int wdb = WS_WD + (ct * 56 + wave * 14) * 4096;
#pragma unroll
    for (int ps = 0; ps < 2; ++ps) {
      v4f acc[8][2];
#pragma unroll
      for (int u = 0; u < 8; ++u) { acc[u][0] = v4f{0.f, 0.f, 0.f, 0.f}; acc[u][1] = acc[u][0]; }
      v4u wfr[2][8];
#pragma unroll
      for (int u = 0; u < 8; ++u) wfr[0][u] = ldtr2(L, wbase ^ (u << 5), (wbase ^ (u << 5)) + 4096);
#pragma unroll
      for (int ks = 0; ks < 14; ++ks) {
        if (ks < 13) {
#pragma unroll
          for (int u = 0; u < 8; ++u)
            wfr[(ks + 1) & 1][u] = ldtr2(L, (wbase ^ (u << 5)) + (ks + 1) * 8192, (wbase ^ (u << 5)) + (ks + 1) * 8192 + 4096);
        }
        if (__builtin_expect(__any(max(max4(h[ks][0]), max4(h[ks][1])) == SENT), 0)) {
          for (int s = 0; s < SPIN; ++s) {
            cfence();
            h[ks][0] = ld4c(ws, l16, hb + (2 * ps) * 14336 + ks * 1024);
            h[ks][1] = ld4c(ws, l16, hb + (2 * ps + 1) * 14336 + ks * 1024);
            if (__all(max(max4(h[ks][0]), max4(h[ks][1])) != SENT)) break;
          }
        }
        if (ps == 0 && ks == 0) STAMP(8);
#pragma unroll
        for (int u = 0; u < 8; ++u)
#pragma unroll
          for (int tt = 0; tt < 2; ++tt) acc[u][tt] = mfma(wfr[ks & 1][u], h[ks][tt], acc[u][tt]);
        if (ps == 0) {
#pragma unroll
          for (int tt = 0; tt < 2; ++tt) h[ks][tt] = ld4c(ws, l16 + (ks & 3) * 1024, hb + (2 + tt) * 14336 + (ks >> 2) * 4096);
        }
#pragma unroll
        for (int e = 0; e < 2; ++e) {
          const int f = (ps * 14 + ks) * 2 + e;
          wdn[f >> 2][f & 3] = ld4c(ws, l16 + (f & 3) * 1024, wdb + (f >> 2) * 4096);
        }
        __builtin_amdgcn_sched_barrier(0);
      }
#pragma unroll
      for (int tt = 0; tt < 2; ++tt)
#pragma unroll
        for (int f = 0; f < 2; ++f) {
          v4u o;
#pragma unroll
          for (int hh = 0; hh < 2; ++hh) {
            const v4f gt = acc[2 * f + hh][tt], up = acc[4 + 2 * f + hh][tt];
            o[2 * hh] = pks(fmaxf(gt.x, 0.f) * up.x, fmaxf(gt.y, 0.f) * up.y);
            o[2 * hh + 1] = pks(fmaxf(gt.z, 0.f) * up.z, fmaxf(gt.w, 0.f) * up.w);
          }
          st4c(ws, l16 + f * 1024, o, WS_ACT + (16 * grp + 4 * wave + 2 * ps + tt) * 57344 + 2 * j * 1024);
        }
      __builtin_amdgcn_sched_barrier(0);
    }
  }
  bar();
  STAMP(9);
  {
    const int ab = WS_ACT + (16 * grp + 4 * rs) * 57344 + wave * 14 * 1024;
    v4u a[2][14][2];
#pragma unroll
    for (int ps = 0; ps < 2; ++ps)
#pragma unroll
      for (int ks = 0; ks < 14; ++ks)
#pragma unroll
        for (int tt = 0; tt < 2; ++tt)
          if (ps == 0 || ks < 3) a[ps][ks][tt] = ld4c(ws, l16 + (ks & 3) * 1024, ab + (2 * ps + tt) * 57344 + (ks >> 2) * 4096);
    v4f acc[4][4];
#pragma unroll
    for (int t = 0; t < 4; ++t)
#pragma unroll
      for (int n = 0; n < 4; ++n) acc[t][n] = v4f{0.f, 0.f, 0.f, 0.f};
#pragma unroll
    for (int ps = 0; ps < 2; ++ps)
#pragma unroll
      for (int ks = 0; ks < 14; ++ks) {
        if (__builtin_expect(__any(max(max4(a[ps][ks][0]), max4(a[ps][ks][1])) == SENT), 0)) {
          for (int s = 0; s < SPIN; ++s) {
            cfence();
            a[ps][ks][0] = ld4c(ws, l16, ab + (2 * ps) * 57344 + ks * 1024);
            a[ps][ks][1] = ld4c(ws, l16, ab + (2 * ps + 1) * 57344 + ks * 1024);
            if (__all(max(max4(a[ps][ks][0]), max4(a[ps][ks][1])) != SENT)) break;
          }
        }
#pragma unroll
        for (int tt = 0; tt < 2; ++tt)
#pragma unroll
          for (int n = 0; n < 4; ++n) acc[2 * ps + tt][n] = mfma(wdn[ks][n], a[ps][ks][tt], acc[2 * ps + tt][n]);
        if (ps == 0 && ks < 11) {
#pragma unroll
          for (int tt = 0; tt < 2; ++tt) a[1][3 + ks][tt] = ld4c(ws, l16 + ((3 + ks) & 3) * 1024, ab + (2 + tt) * 57344 + ((3 + ks) >> 2) * 4096);
        }
        __builtin_amdgcn_sched_barrier(0);
      }
    STAMP(10);
    const int blk = 16 * grp + 4 * rs + wave;
    const int xo = WS_X2 + blk * 14336 + c16 * 896 + ct * 128 + g * 32;
    const int go = WS_MOD + grp * MOD_REP + (70 + 2 * ct) * 128 + g * 64;
    v4u xv[2], gv[4];
    xv[0] = ld4c(ws, xo); xv[1] = ld4c(ws, xo + 16);
#pragma unroll
    for (int e = 0; e < 4; ++e) gv[e] = ld4c(ws, go + e * 16);
#pragma unroll
    for (int t = 0; t < 4; ++t)
      if (t != wave)
#pragma unroll
        for (int n = 0; n < 4; ++n) *(LAS v4f*)(L + ((t * 4 + wave) * 4 + n) * 1024 + lane * 16) = acc[t][n];
    bar();
    v4f r[4];
#pragma unroll
    for (int n = 0; n < 4; ++n) r[n] = wave == 0 ? acc[0][n] : wave == 1 ? acc[1][n] : wave == 2 ? acc[2][n] : acc[3][n];
#pragma unroll
    for (int w = 0; w < 4; ++w)
      if (w != wave)
#pragma unroll
        for (int n = 0; n < 4; ++n) r[n] += lds4f(L, ((wave * 4 + w) * 4 + n) * 1024 + lane * 16);
    for (int s = 0; s < SPIN; ++s) {
      bool ok = max(max4(xv[0]), max4(xv[1])) != SENT;
#pragma unroll
      for (int e = 0; e < 4; ++e) ok = ok && gv[e].y == tag && gv[e].w == tag;
      if (__all(ok)) break;
      cfence();
      xv[0] = ld4c(ws, xo); xv[1] = ld4c(ws, xo + 16);
#pragma unroll
      for (int e = 0; e < 4; ++e) gv[e] = ld4c(ws, go + e * 16);
    }
    v4u o[2];
#pragma unroll
    for (int q = 0; q < 4; ++q) {
      const u32 x01 = xv[q >> 1][2 * (q & 1)], x23 = xv[q >> 1][2 * (q & 1) + 1];
      o[q >> 1][2 * (q & 1)] = pk(blo(x01) + blo(gv[q].x) * r[0][q], bhi(x01) + bhi(gv[q].x) * r[1][q]);
      o[q >> 1][2 * (q & 1) + 1] = pk(blo(x23) + blo(gv[q].z) * r[2][q], bhi(x23) + bhi(gv[q].z) * r[3][q]);
    }
    v4u* op = (v4u*)(out + tok_row(blk, c16) * D + ct * 64 + g * 16);
    op[0] = o[0]; op[1] = o[1];
    if (blockIdx.x == 0 && wave == 0) {
      for (int s = 0; s < SPIN; ++s) {
        cfence();
        const v4u v = ld4c(ws, WS_MOD + (70 + (lane < 14 ? lane : 0)) * 128);
        if (__all(v.y == tag)) break;
      }
      if (lane == 0) __builtin_amdgcn_raw_buffer_store_b32(epoch + 1u, ws, WS_EPOCH, 0, SC1);
    }
  }
  STAMP(11);
}

extern "C" void kernel_launch(void* const* d_in, const int* in_sizes, int n_in, void* d_out, int out_size, void* d_ws,
                              size_t ws_size, hipStream_t stream) {
  fused<<<256, 256, 0, stream>>>((const u16*)d_in[0], (const u16*)d_in[1], (const u16*)d_in[2], (const u16*)d_in[4],
                                 (const u16*)d_in[5], (const u16*)d_in[6], (const u16*)d_in[7], (const u16*)d_in[8],
                                 (u16*)d_out, (char*)d_ws);
}
```

```cpp
#include <hip/hip_runtime.h>

typedef unsigned short u16;
typedef unsigned int u32;
typedef unsigned long long u64;
typedef u32 v4u __attribute__((ext_vector_type(4)));
typedef u32 v2u __attribute__((ext_vector_type(2)));
typedef float v4f __attribute__((ext_vector_type(4)));
typedef float v2f __attribute__((ext_vector_type(2)));
typedef short v8s __attribute__((ext_vector_type(8)));
typedef short v4s __attribute__((ext_vector_type(4)));
typedef __bf16 v8b __attribute__((ext_vector_type(8)));
typedef __bf16 v2b __attribute__((ext_vector_type(2)));
#define LAS __attribute__((address_space(3)))
#define GAS __attribute__((address_space(1)))
#define DEV __device__ __forceinline__

#define C_W1_EARLY 0
#define A_AT 11
#define F_AT 5
#define CODE_BYTES 45056
#define WD_EARLY 16
#define WD_MID 32
#define B_AT 9
constexpr int S = 2048, D = 448, DF = 225, F = 1792;

struct alignas(16) Chain { u16 v[2048]; };
struct Cos448 { u16 v[448]; };
constexpr double c_cos_t(double a) {
  double a2 = a * a, t = 1.0, s = 1.0;
  for (int i = 1; i <= 10; ++i) { t = -t * a2 / ((2 * i - 1) * (2 * i)); s += t; }
  return s;
}
constexpr double c_sin_t(double a) {
  double a2 = a * a, t = a, s = a;
  for (int i = 1; i <= 10; ++i) { t = -t * a2 / ((2 * i) * (2 * i + 1)); s += t; }
  return s;
}
constexpr double c_cos448(int m) {
  if (m > 224) m = 448 - m;
  double sg = 1.0;
  if (m > 112) { m = 224 - m; sg = -1.0; }
  const double w = 6.283185307179586476925286766559 / 448.0;
  return m == 112 ? 0.0 : (m <= 56 ? sg * c_cos_t(w * m) : sg * c_sin_t(w * (112 - m)));
}
constexpr u16 c_bf16(double d) {
  const u32 u = __builtin_bit_cast(u32, (float)d);
  return (u16)((u + 0x7FFFu + ((u >> 16) & 1u)) >> 16);
}
constexpr Cos448 mk_cos() { Cos448 c{}; for (int m = 0; m < 448; ++m) c.v[m] = c_bf16(c_cos448(m)); return c; }
constexpr Cos448 CTAB = mk_cos();
constexpr u16 bcos(int m) { return CTAB.v[m % 448]; }
constexpr u16 bsin(int m) { return CTAB.v[(m % 448 + 336) % 448]; }
constexpr u16 bneg(u16 b) { return b ? (u16)(b ^ 0x8000u) : b; }
constexpr u16 bscale(u16 b, int e) { return b ? (u16)(b + e * 0x80) : b; }

constexpr Chain mk_p1(int c) {
  const int tile = c >> 2, par = c & 1, sn = (c >> 1) & 1;
  Chain ch{};
  for (int s = 0; s < 4; ++s)
    for (int l = 0; l < 64; ++l) {
      const int kp = 16 * tile + (l & 15), k = 2 * kp + par;
      if (k > 224) continue;
      for (int j = 0; j < 8; ++j) {
        const int dl = 32 * s + 8 * (l >> 4) + j;
        if (dl > 112) continue;
        u16 b = sn ? bneg(bsin(k * dl)) : bcos(k * dl);
        if (dl == 0 || dl == 112) b = bscale(b, -1);
        ch.v[(s * 64 + l) * 8 + j] = b;
      }
    }
  return ch;
}
constexpr Chain mk_p3(int c) {
  const int tile = c >> 1, sn = c & 1;
  Chain ch{};
  for (int s = 0; s < 4; ++s)
    for (int l = 0; l < 64; ++l) {
      int dl = 16 * tile + (l & 15);
      if (c == 1 && dl == 0) dl = 112;
      for (int j = 0; j < 8; ++j) {
        const int k = 2 * (32 * s + 8 * (l >> 4) + j) + 1;
        if (k < 224) ch.v[(s * 64 + l) * 8 + j] = sn ? bsin(k * dl) : bcos(k * dl);
      }
    }
  return ch;
}
constexpr Chain mk_p2(int c) {
  Chain ch{};
  for (int f = 0; f < 4; ++f)
    for (int l = 0; l < 64; ++l)
      for (int j = 0; j < 8; ++j) {
        const int comp = c < 8 ? (c & 1) : (f >> 1), s = c < 8 ? f : (f & 1);
        const int o = 16 * (c < 8 ? (c >> 1) : (c - 8)) + (l & 15), i = 16 * s + 4 * (l >> 4) + (j >> 1);
        const int m = (c < 8 ? 7 : 14) * ((i * o) % 64);
        u16 b = 0;
        if (comp == 0) b = (j & 1) ? bsin(m) : bcos(m);
        else b = (j & 1) ? bcos(m) : bneg(bsin(m));
        ch.v[(f * 64 + l) * 8 + j] = b;
      }
  return ch;
}
template <int I> constexpr Chain P1C = mk_p1(I);
template <int I> constexpr Chain P3C = mk_p3(I);
template <int I> constexpr Chain P2C = mk_p2(I);
#define L10(T, b) T<b>, T<b + 1>, T<b + 2>, T<b + 3>, T<b + 4>, T<b + 5>, T<b + 6>, T<b + 7>, T<b + 8>, T<b + 9>
#define L29(T) L10(T, 0), L10(T, 10), T<20>, T<21>, T<22>, T<23>, T<24>, T<25>, T<26>, T<27>, T<28>
constexpr int TAB_P1 = 0, TAB_P3 = 29, TAB_P2 = 43, TAB_N = 53;
static __device__ const Chain TAB[TAB_N] = {L29(P1C), L10(P3C, 0), P3C<10>, P3C<11>, P3C<12>, P3C<13>, L10(P2C, 0)};

constexpr int WS_EPOCH = 0;
constexpr int WS_RDY = 4096;
constexpr int WS_WRDY = 4096 + 8192;
constexpr int WS_DRDY = 4096 + 16384;
constexpr int WS_XRDY = 131072 + 65536;
constexpr int WS_ARDY = 131072;
constexpr int WS_MOD = 32768, MOD_REP = 12288;
constexpr int T1_BLK = 226 * 64;
constexpr int WS_T1 = 1 << 18;
constexpr int WS_T2 = WS_T1 + 128 * T1_BLK;
constexpr int WS_WT = WS_T2 + 226 * 8192;
constexpr int WS_H2 = WS_WT + 113 * 16384;
constexpr int WS_X2 = WS_H2 + 128 * 14336;
constexpr int WS_WD = WS_X2 + 128 * 14336;
constexpr int WS_ACT = WS_WD + 7 * 56 * 4096;
constexpr int WS_END = WS_ACT + 128 * 57344;
constexpr int WS_DBG = 32 << 20;

constexpr int L_W1 = 0;
constexpr int L_B = 114688;
constexpr int XP = 912;
constexpr int L_XS = L_B;
constexpr int L_ST = L_XS + 16 * XP;
constexpr int L_M1 = L_ST + 128;
constexpr int L_AF = L_M1 + 4 * 1792;
constexpr int L_M2 = L_AF + 16384;
constexpr int L_EX = L_M2 + 3 * 1792;
constexpr int L_REND = L_EX + 512;
constexpr int ZPL = 8192;
constexpr int L_CI = L_B;
constexpr int L_CX = L_CI + 2 * 8256;
constexpr int L_RED = L_CX + 16384;
constexpr int L_DBG = 163840 - 1024;
constexpr int L_SZ = 163840;
static_assert(L_REND <= L_DBG && L_RED + 2048 <= L_DBG && 2 * ZPL <= 16384, "lds");

constexpr u32 SENT = 0xFFFFFFFFu;
constexpr int SPIN = 1 << 14;
constexpr int SC1 = 16;
constexpr int OOB = 0x7FFF0000;

typedef __amdgpu_buffer_rsrc_t rsrc_t;
DEV rsrc_t mkrs(const void* p, int bytes) { return __builtin_amdgcn_make_buffer_rsrc((void*)p, 0, bytes, 0x00020000); }
DEV v4u ld4(rsrc_t r, int off, int so = 0) { return __builtin_amdgcn_raw_buffer_load_b128(r, off, so, 0); }
DEV v2u ld2(rsrc_t r, int off, int so = 0) { return __builtin_amdgcn_raw_buffer_load_b64(r, off, so, 0); }
DEV v4u ld4c(rsrc_t r, int off, int so = 0) { return __builtin_amdgcn_raw_buffer_load_b128(r, off, so, SC1); }
DEV v2u ld2c(rsrc_t r, int off, int so = 0) { return __builtin_amdgcn_raw_buffer_load_b64(r, off, so, SC1); }
DEV u32 ld1c(rsrc_t r, int off, int so = 0) { return __builtin_amdgcn_raw_buffer_load_b32(r, off, so, SC1); }
DEV void st4c(rsrc_t r, int off, v4u v) { __builtin_amdgcn_raw_buffer_store_b128(v, r, off, 0, SC1); }
DEV void st2c(rsrc_t r, int off, v2u v) { __builtin_amdgcn_raw_buffer_store_b64(v, r, off, 0, SC1); }
DEV float blo(u32 v) { return __builtin_bit_cast(float, v << 16); }
DEV float bhi(u32 v) { return __builtin_bit_cast(float, v & 0xFFFF0000u); }
DEV v2f bun(u32 v) { return v2f{blo(v), bhi(v)}; }
DEV u32 pk(float lo, float hi) { return __builtin_bit_cast(u32, __builtin_convertvector(v2f{lo, hi}, v2b)); }
DEV u32 pk2(v2f v) { return __builtin_bit_cast(u32, __builtin_convertvector(v, v2b)); }
DEV float rb(float f) { return blo(pk(f, 0.f)); }
DEV v2f lo2(v4f v) { return __builtin_shufflevector(v, v, 0, 1); }
DEV v2f hi2(v4f v) { return __builtin_shufflevector(v, v, 2, 3); }
DEV v2f fma2(v2f a, v2f b, v2f c) { return __builtin_elementwise_fma(a, b, c); }
DEV v4f fma4(v4f a, v4f b, v4f c) { return __builtin_elementwise_fma(a, b, c); }
DEV v4f splat(float f) { return v4f{f, f, f, f}; }
DEV v4f mfma(v4u a, v4u b, v4f c) {
  return __builtin_amdgcn_mfma_f32_16x16x32_bf16(__builtin_bit_cast(v8b, a), __builtin_bit_cast(v8b, b), c, 0, 0, 0);
}
DEV void mfa(v4f& c, v4u a, v4u b) { asm volatile("s_nop 1\n\tv_mfma_f32_16x16x32_bf16 %0, %1, %2, %0" : "+v"(c) : "v"(a), "v"(b)); }
DEV void mf0(v4f& c, v4u a, v4u b) { asm volatile("s_nop 1\n\tv_mfma_f32_16x16x32_bf16 %0, %1, %2, 0" : "=&v"(c) : "v"(a), "v"(b)); }
template <int N> DEV void mfence(v4f& a, v4f& b) { asm volatile("s_nop %2" : "+v"(a), "+v"(b) : "i"(N)); }
template <int N> DEV void mfence(v4f& a, v4f& b, v4f& c, v4f& d) { asm volatile("s_nop %4" : "+v"(a), "+v"(b), "+v"(c), "+v"(d) : "i"(N)); }
template <class T> DEV void pin(T& v) { asm volatile("" : "+v"(v)); }
DEV int fresh(int off) { asm volatile("" : "+v"(off) :: "memory"); return off; }
DEV bool wall(bool ok) { return __builtin_amdgcn_ballot_w64(!ok) == 0; }
DEV bool wany(bool b) { return __builtin_amdgcn_ballot_w64(b) != 0; }
DEV void sb() { __builtin_amdgcn_sched_barrier(0); }
DEV u32 max4(v4u v) { return max(max(v.x, v.y), max(v.z, v.w)); }
DEV void drain() { asm volatile("s_waitcnt vmcnt(0)" ::: "memory"); }
DEV void cfence() { asm volatile("" ::: "memory"); }
DEV void bar() { asm volatile("s_waitcnt lgkmcnt(0)\n\ts_barrier" ::: "memory"); }
template <int CTRL> DEV float dpp(float v) {
  return __builtin_bit_cast(float, __builtin_amdgcn_update_dpp(0, __builtin_bit_cast(int, v), CTRL, 0xF, 0xF, true));
}
DEV float sum16(float v) {
  v += dpp<0x128>(v); v += dpp<0x124>(v); v += dpp<0x122>(v); v += dpp<0x121>(v); return v;
}
DEV void sumg2(float& a, float& b) {
  float c = a, d = b;
  asm volatile("s_nop 1\n\tv_permlane16_swap_b32 %0, %1\n\tv_permlane16_swap_b32 %2, %3" : "+v"(a), "+v"(c), "+v"(b), "+v"(d));
  a += c; b += d; c = a; d = b;
  asm volatile("s_nop 1\n\tv_permlane32_swap_b32 %0, %1\n\tv_permlane32_swap_b32 %2, %3" : "+v"(a), "+v"(c), "+v"(b), "+v"(d));
  a += c; b += d;
}
DEV v4u lds4(const char LAS* L, int off) { return *(const LAS v4u*)(L + off); }
DEV v4f lds4f(const char LAS* L, int off) { return *(const LAS v4f*)(L + off); }
DEV v2u ldtr(const char LAS* L, int off) { return __builtin_bit_cast(v2u, __builtin_amdgcn_ds_read_tr16_b64_v4i16((LAS v4s*)(L + off))); }
DEV v4u ldtr2(const char LAS* L, int o0, int o1) { const v2u a = ldtr(L, o0), b = ldtr(L, o1); return v4u{a.x, a.y, b.x, b.y}; }
DEV void dma16(rsrc_t r, int voff, int soff, u32 lds_dst) {
  u32 keep;
  asm volatile("s_mov_b32 %0, m0\n\ts_mov_b32 m0, %1\n\ts_nop 0\n\tbuffer_load_dwordx4 %2, %3, %4 offen lds\n\ts_mov_b32 m0, %0"
               : "=&s"(keep) : "s"(lds_dst), "v"(voff), "s"(r), "s"(soff) : "memory");
}
DEV void light_poll(rsrc_t ws, int off) {
  _Pragma("nounroll") for (int s = 0; s < SPIN; ++s) {
    if (wall(ld1c(ws, fresh(off)) != SENT)) break;
    __builtin_amdgcn_s_sleep(2);
  }
}
DEV int rho_row(int rho) {
  const int m = (rho >> 4) & 3, dl = ((rho >> 6) << 4) | (rho & 15);
  int d = m == 0 ? dl : m == 1 ? 448 - dl : m == 2 ? 224 - dl : 224 + dl;
  if (dl == 0 && m == 1) d = 112;
  if (dl == 0 && m == 3) d = 336;
  return d;
}
DEV int row_rho(int d) {
  const int m = d < 112 ? 0 : d == 112 ? 1 : d <= 224 ? 2 : d <= 336 ? 3 : 1;
  const int dl = d < 112 ? d : d == 112 ? 0 : d <= 224 ? 224 - d : d < 336 ? d - 224 : d == 336 ? 0 : 448 - d;
  return ((dl >> 4) << 6) | (m << 4) | (dl & 15);
}
DEV int tok_row(int blk, int t) {
  const int n1 = 16 * (blk >> 5) + 4 * ((blk >> 2) & 3) + (t & 3), n2 = 16 * ((blk >> 4) & 1) + 4 * (blk & 3) + (t >> 2);
  return n2 + 32 * n1;
}
template <bool SWAP> DEV v4u cmul_pack(v4f a, v4f b, v4f c, v4f s) {
  const v4f re = fma4(b, s, a * c), im = fma4(b, c, -(a * s));
  return SWAP ? v4u{pk(im.x, re.x), pk(im.y, re.y), pk(im.z, re.z), pk(im.w, re.w)}
              : v4u{pk(re.x, im.x), pk(re.y, im.y), pk(re.z, im.z), pk(re.w, im.w)};
}

#define STAMP(i)
#define CSTAMP(i)
#define SLOWCNT
#define DUMP()

DEV void dma_w1(const u16* wg, const u16* wu, int slice, int wave, int lane, u32 lbase) {
  const rsrc_t rg = mkrs(wg, D * F * 2), ru = mkrs(wu, D * F * 2);
  const int r = lane >> 3, ch = ((((lane & 7) >> 1) ^ (lane >> 4)) & 3) * 32 + (lane & 1) * 16 + slice * 128;
  const int vp = r * (F * 2) + ch, vn = (7 - r) * (F * 2) + ch;
#pragma unroll
  for (int n = 0; n < 4; ++n) {
    const int ks = wave + 4 * n;
    if (n == 3 && wave >= 2) break;
    const int t16 = (ks >> 1) * 16, a = ks & 1;
#pragma unroll
    for (int q = 0; q < 4; ++q) {
      const int dl0 = t16 + 8 * (q & 1);
      const bool pos = (q >> 1) == a;
      const int d0 = q < 2 ? (a ? 224 - dl0 - 7 : dl0) : (a ? 224 + dl0 : 448 - dl0 - 7);
      int vo = pos ? vp : vn, so = d0 * (F * 2);
      if (n == 0 && q == 2) {
        vo += so; so = 0;
        if (wave < 2 && lane < 8) vo = (wave ? 336 : 112) * (F * 2) + ch;
      }
      dma16(rg, vo, so, lbase + L_W1 + ks * 8192 + q * 1024);
      dma16(ru, vo, so, lbase + L_W1 + ks * 8192 + 4096 + q * 1024);
    }
  }
}
DEV void cvt_w1(char LAS* L, int wave, int lane) {
  const int base = (lane >> 2) * 128 + ((lane >> 3) & 3) * 32 + (lane & 3) * 8;
#pragma unroll
  for (int n = 0; n < 4; ++n) {
    if (n == 3 && wave >= 2) break;
    const int reg = L_W1 + (wave + 4 * n) * 8192;
    v4u f[8];
#pragma unroll
    for (int u = 0; u < 8; ++u) {
      const int o = reg + (u >> 2) * 4096 + (base ^ ((u & 3) << 5));
      f[u] = ldtr2(L, o, o + 2048);
    }
    cfence();
#pragma unroll
    for (int u = 0; u < 8; ++u) pin(f[u]);
#pragma unroll
    for (int u = 0; u < 8; ++u) *(LAS v4u*)(L + reg + u * 1024 + lane * 16) = f[u];
    cfence();
  }
}

struct ModLoad { u32 cv[7]; v4u mw[7]; };
DEV void mod_load(ModLoad& m, const u16* cin, const u16* wm, int q, int tid) {
  const rsrc_t cr = mkrs(cin, q >= 0 ? D * 2 : 0), mr = mkrs(wm, q >= 0 ? D * 6 * D * 2 : 0);
#pragma unroll
  for (int i = 0; i < 7; ++i) m.cv[i] = __builtin_amdgcn_raw_buffer_load_b16(cr, (tid >> 2) * 2 + i * 128, 0, 0);
#pragma unroll
  for (int i = 0; i < 7; ++i) m.mw[i] = ld4(mr, ((tid >> 2) * (6 * D) + q * 32 + (tid & 3) * 8) * 2, i * 64 * 6 * D * 2);
}
DEV void mod_part(ModLoad& m, char LAS* L, int tid) {
  float sl[7];
#pragma unroll
  for (int i = 0; i < 7; ++i) { const float cc = blo(m.cv[i]); sl[i] = rb(cc * __builtin_amdgcn_rcpf(1.f + __expf(-cc))); pin(sl[i]); }
  v2f a[4];
#pragma unroll
  for (int e = 0; e < 4; ++e) a[e] = v2f{0.f, 0.f};
#pragma unroll
  for (int i = 0; i < 7; ++i)
#pragma unroll
    for (int e = 0; e < 4; ++e) a[e] = fma2(bun(m.mw[i][e]), v2f{sl[i], sl[i]}, a[e]);
#pragma unroll
  for (int e = 0; e < 4; ++e) {
    a[e].x += dpp<0x128>(a[e].x); a[e].y += dpp<0x128>(a[e].y);
    a[e].x += dpp<0x124>(a[e].x); a[e].y += dpp<0x124>(a[e].y);
  }
  if ((tid & 15) < 4) {
    *(LAS v4f*)(L + L_RED + ((tid >> 4) * 32 + (tid & 15) * 8) * 4) = v4f{a[0].x, a[0].y, a[1].x, a[1].y};
    *(LAS v4f*)(L + L_RED + ((tid >> 4) * 32 + (tid & 15) * 8 + 4) * 4) = v4f{a[2].x, a[2].y, a[3].x, a[3].y};
  }
}
DEV void mod_publish(char LAS* L, rsrc_t ws, int q, int lane, u32 tag) {
  v4f s = {0.f, 0.f, 0.f, 0.f};
#pragma unroll
  for (int r = 0; r < 16; ++r) s += lds4f(L, L_RED + (r * 32 + (lane & 7) * 4) * 4);
  const float one = (q / 14 == 1 || q / 14 == 4) ? 1.f : 0.f;
  st4c(ws, WS_MOD + (lane >> 3) * MOD_REP + q * 128 + (lane & 7) * 16,
       v4u{pk(rb(s.x) + one, rb(s.y) + one), tag, pk(rb(s.z) + one, rb(s.w) + one), tag});
}
DEV u32 ld1(rsrc_t r, int off) { return __builtin_amdgcn_raw_buffer_load_b32(r, off, 0, 0); }
DEV u32 mktag(u32 epoch) { return epoch * 0x9E3779B1u + 0x7F4A7C15u; }

DEV void x2_flush(char LAS* L, rsrc_t ws, int blk, int tid) {
#pragma unroll
  for (int m = 0; m < 4; ++m) {
    const int ch = min(tid + 256 * m, 895), tk = ch / 56;
    st4c(ws, tid + 256 * m < 896 ? WS_X2 + blk * 14336 + ch * 16 : OOB, lds4(L, L_XS + tk * XP + (ch - tk * 56) * 16));
  }
}
DEV bool flags_ok(v4u rv, u32 tag) { return rv.x == tag && rv.y == ~tag && rv.z == tag && rv.w == ~tag; }
DEV void wait_flags(rsrc_t ws, int off, u32 tag) {
  _Pragma("nounroll") for (int s = 0; s < SPIN; ++s) {
    const v2u rv = ld2c(ws, fresh(off));
    if (wall(rv.x == tag && rv.y == ~tag)) break;
    __builtin_amdgcn_s_sleep(1);
  }
}

__global__ void __launch_bounds__(256, 1)
fused(const u16* __restrict__ xin, const u16* __restrict__ wr, const u16* __restrict__ wi, const u16* __restrict__ wg,
      const u16* __restrict__ wu, const u16* __restrict__ wd, const u16* __restrict__ cin, const u16* __restrict__ wm,
      u16* __restrict__ out, char* wsp) {
  __shared__ __attribute__((aligned(16))) char smem[L_SZ];
  char LAS* L = (char LAS*)smem;
  const u32 lbase = (u32)(size_t)L;
  const int tid = threadIdx.x, lane = tid & 63, wave = __builtin_amdgcn_readfirstlane(tid >> 6);
  const int g = lane >> 4, c16 = lane & 15, l16 = lane * 16;
  const int grp = blockIdx.x & 7, j = blockIdx.x >> 3;
  u64 pc;
  asm volatile("s_getpc_b64 %0" : "=s"(pc));
  STAMP(0);
  asm volatile("" :: "s"(xin), "s"(wr), "s"(wi), "s"(wg), "s"(wu), "s"(wd), "s"(cin), "s"(wm), "s"(out), "s"(wsp));
  const rsrc_t ws = mkrs(wsp, WS_END);
  const rsrc_t tab = mkrs(TAB, TAB_N * 4096);
  const rsrc_t code = mkrs((const void*)(pc & ~127ull), CODE_BYTES);
  const bool isR = j >= 14 && j < 30;
  const bool isG = j < 28;
  const int gk = j / 7, ct = j - gk * 7;
  const int tile = 4 * (grp >> 2) + gk, slice = 7 * (grp & 3) + ct;
  const int wdb = WS_WD + (ct * 56 + wave * 14) * 4096;
  const v4u sv = {SENT, SENT, SENT, SENT};
  int lb = L_B + l16;
  pin(lb);
  const char LAS* LB = L + lb - L_B;
  const int pfo = ((j * 4 + wave) * 14 + lane) * 128, pfc = ((j * 4 + wave) * 4 + lane) * 128;
  v4u wdn[14][4];
  u32 epoch, tag;

  if (isR) {
    const int blk = 16 * grp + (j - 14);
    v2u xv[7];
    {
      const rsrc_t xr = mkrs(xin, S * D * 2);
      const int off = tok_row(blk, 4 * wave + g) * (D * 2) + c16 * 8;
#pragma unroll
      for (int m = 0; m < 7; ++m) xv[m] = ld2(xr, off + m * 128);
    }
    u32 pf0 = ld1(tab, lane < 14 ? pfo : OOB), pf1 = ld1(code, lane < 4 ? pfc : OOB);
    epoch = ld1c(ws, WS_EPOCH);
    sb();
    const int t0 = wave == 0 ? 0 : 2 * wave - 1;
    v4u cf[8][4];
#define CF_LOAD(q) { const int chn = TAB_P1 + ((wave == 0 && q >= 4) ? 28 : 4 * t0 + q), vo = (q < 5 || wave != 0) ? l16 : OOB; \
      _Pragma("unroll") for (int s = 0; s < 4; ++s) cf[q][s] = ld4(tab, vo + s * 1024, chn * 4096); sb(); }
    CF_LOAD(0) CF_LOAD(1)
    {
      v2f f[14], s2 = {0.f, 0.f};
#pragma unroll
      for (int m = 0; m < 7; ++m) {
        f[2 * m] = bun(xv[m].x); f[2 * m + 1] = bun(xv[m].y);
        s2 += f[2 * m] + f[2 * m + 1];
        *(LAS v2u*)(L + L_XS + (4 * wave + g) * XP + c16 * 8 + m * 128) = xv[m];
      }
      STAMP(21);
      sb();
      CF_LOAD(2)
      const float mean = sum16(s2.x + s2.y) * (1.f / D);
      v2f q2 = {0.f, 0.f};
#pragma unroll
      for (int m = 0; m < 14; ++m) { const v2f d = f[m] - mean; q2 = fma2(d, d, q2); }
      pin(q2);
      sb();
      CF_LOAD(3)
      const float rstd = __builtin_amdgcn_rsqf(sum16(q2.x + q2.y) * (1.f / D) + 1e-5f);
      if (c16 == 0) *(LAS v2f*)(L + L_ST + (4 * wave + g) * 8) = v2f{mean, rstd};
    }
    pin(pf0); pin(pf1);
    epoch = __builtin_amdgcn_readfirstlane(epoch);
    tag = mktag(epoch);
    STAMP(22);
    bar();
    v4f n[4][2];
    const int d0 = 8 * (4 * wave + g);
    const v2f st = *(const LAS v2f*)(L + L_ST + c16 * 8);
    {
      int xr_ = L_XS + c16 * XP + d0 * 2, xb_ = L_XS + c16 * XP + (216 - d0) * 2, x0_ = d0 == 0 ? xr_ : xb_ + 464;
      pin(xr_); pin(xb_); pin(x0_);
      const v4u a = lds4(L + xr_, 0), e = lds4(L + xr_, 448), c = lds4(L + xb_, 0), b = lds4(L + xb_, 448);
      const u32 c0 = *(const LAS u16*)(L + xb_ + 16), b0 = *(const LAS u16*)(L + x0_);
      sb();
      CF_LOAD(4)
#pragma unroll
      for (int i = 0; i < 8; ++i) {
        n[0][i >> 2][i & 3] = (i & 1) ? bhi(a[i >> 1]) : blo(a[i >> 1]);
        n[3][i >> 2][i & 3] = (i & 1) ? bhi(e[i >> 1]) : blo(e[i >> 1]);
        n[1][i >> 2][i & 3] = i == 0 ? blo(b0) : (i & 1) ? bhi(b[(8 - i) >> 1]) : blo(b[(8 - i) >> 1]);
        n[2][i >> 2][i & 3] = i == 0 ? blo(c0) : (i & 1) ? bhi(c[(8 - i) >> 1]) : blo(c[(8 - i) >> 1]);
      }
      const float nm = -st.x * st.y;
#pragma unroll
      for (int m = 0; m < 4; ++m) {
#pragma unroll
        for (int h = 0; h < 2; ++h) { n[m][h] = fma4(n[m][h], splat(st.y), splat(nm)); pin(n[m][h]); }
        sb();
        if (m == 0) CF_LOAD(5)
        if (m == 1) CF_LOAD(6)
        if (m == 2) CF_LOAD(7)
      }
    }
#pragma unroll
    for (int m = 0; m < 4; ++m) {
      const int kk = (tid >> 2) + 64 * m;
      st4c(ws, kk < 226 ? WS_T2 + kk * 8192 + blk * 64 + (tid & 3) * 16 : OOB, sv);
      st4c(ws, tid + 256 * m < 896 ? WS_H2 + blk * 14336 + 4096 * m + tid * 16 : OOB, sv);
    }
    sb();
    STAMP(1);
    int mb = L_M1 + d0 * 4;
    pin(mb);
    {
      const int kind = tid >= 112, d = 4 * (tid - 112 * kind);
      int wb = L_M1 + (kind ? 0 : 2 * 1792) + d * 4, wrv = L_M1 + (kind ? 0 : 2 * 1792) + 1792 + (445 - d) * 4;
      int wr0 = d == 0 ? wrv + 12 - 1792 : wrv + 12;
      const int mo = tid < 224 ? WS_MOD + grp * MOD_REP + tid * 16 : OOB;
      pin(wb); pin(wrv); pin(wr0);
      if (wave >= 2) wait_flags(ws, WS_RDY + grp * 1024 + min(tid - 128, 112) * 8, tag);
      STAMP(23);
      v4u mv;
      _Pragma("nounroll") for (int s = 0; s < SPIN; ++s) {
        mv = ld4c(ws, fresh(mo));
        if (wall(tid >= 224 || (mv.y == tag && mv.w == tag))) break;
        __builtin_amdgcn_s_sleep(1);
      }
      STAMP(2);
      if (tid < 224) {
        const v4f v = {blo(mv.x), bhi(mv.x), blo(mv.z), bhi(mv.z)};
        *(LAS v4f*)(L + wb) = v;
        *(LAS float*)(L + wr0) = v.x;
        *(LAS float*)(L + wrv + 8) = v.y;
        *(LAS float*)(L + wrv + 4) = v.z;
        *(LAS float*)(L + wrv) = v.w;
      }
    }
    bar();
    {
      v4u fr[4];
#pragma unroll
      for (int hh = 0; hh < 2; ++hh) {
        v4f h[4];
#pragma unroll
        for (int m = 0; m < 4; ++m) {
          const int o = ((m == 1 || m == 2) ? 1792 : 0) + (m >= 2 ? 896 : 0) + hh * 16;
          h[m] = fma4(n[m][hh], lds4f(L + mb, o), lds4f(L + mb, 2 * 1792 + o));
        }
        const v4f p = h[0] + h[1], q = h[2] + h[3], u = h[0] - h[1], v = h[3] - h[2];
        const v4f t[4] = {p + q, p - q, u + v, u - v};
#pragma unroll
        for (int m = 0; m < 4; ++m) { fr[m][2 * hh] = pk2(lo2(t[m])); fr[m][2 * hh + 1] = pk2(hi2(t[m])); }
      }
#pragma unroll
      for (int m = 0; m < 4; ++m) *(LAS v4u*)(LB + wave * 1024 + L_AF + m * 4096) = fr[m];
    }
    bar();
    {
      v4u a[4][4];
#pragma unroll
      for (int p = 0; p < 2; ++p)
#pragma unroll
        for (int s = 0; s < 4; ++s) { a[p][s] = lds4(LB, L_AF + (p * 4 + s) * 1024); a[2 + p][s] = lds4(LB, L_AF + ((2 + p) * 4 + s) * 1024); }
      const int o1 = WS_T1 + blk * T1_BLK + (16 * t0 + c16) * 64 + g * 16;
      const int o2 = wave != 0 ? o1 + 1024 : c16 < 2 ? o1 + 112 * 64 : OOB, o3 = wave != 0 ? o1 + 1024 + 114 * 64 : OOB;
      v4f re[4], im[4];
#pragma unroll
      for (int q = 0; q < 4; ++q) {
        const int p = q >> 1, t = q & 1;
        mf0(re[q], a[p][0], cf[4 * t + p][0]); mf0(im[q], a[2 + p][0], cf[4 * t + 2 + p][0]);
        if (q > 0) {
          mfence<1>(re[q - 1], im[q - 1]);
          st4c(ws, q == 1 ? o1 : q == 2 ? o2 : o1 + 114 * 64,
               v4u{pk(re[q - 1].x, im[q - 1].x), pk(re[q - 1].y, im[q - 1].y), pk(re[q - 1].z, im[q - 1].z), pk(re[q - 1].w, im[q - 1].w)});
          sb();
        }
#pragma unroll
        for (int s = 1; s < 4; ++s) { mfa(re[q], a[p][s], cf[4 * t + p][s]); mfa(im[q], a[2 + p][s], cf[4 * t + 2 + p][s]); }
      }
      mfence<7>(re[3], im[3]);
      st4c(ws, o3, v4u{pk(re[3].x, im[3].x), pk(re[3].y, im[3].y), pk(re[3].z, im[3].z), pk(re[3].w, im[3].w)});
    }
    STAMP(3);
    sb();
#pragma unroll
    for (int q = 0; q < 4; ++q) {
      const int vo = (q < 2 || wave != 0) ? l16 : OOB;
#pragma unroll
      for (int s = 0; s < 4; ++s) cf[2 * q + 1][s] = ld4(tab, vo + s * 1024, (TAB_P3 + 2 * t0 + q) * 4096);
    }
    if (isG) dma_w1(wg, wu, slice, wave, lane, lbase);
    sb();
    v4f xo[2][4];
    int xa = L_XS + c16 * XP + (16 * t0 + 4 * g) * 2, xd = L_XS + c16 * XP + (192 - 16 * t0 - 4 * g) * 2;
    int xs1 = tid < 16 ? L_XS + c16 * XP + 224 : xd + 512, xs3 = tid < 16 ? L_XS + c16 * XP + 672 : xa + 448;
    pin(xa); pin(xd); pin(xs1); pin(xs3);
#define XOFF(t, m, r) ((t) == 0 && (r) == 0 && (m) == 1 ? xs1 : (t) == 0 && (r) == 0 && (m) == 3 ? xs3 : \
    (m) == 0 ? xa + 32 * (t) + 2 * (r) : (m) == 3 ? xa + 448 + 32 * (t) + 2 * (r) : (m) == 1 ? xd + 512 - 32 * (t) - 2 * (r) : xd + 64 - 32 * (t) - 2 * (r))
#pragma unroll
    for (int t = 0; t < 2; ++t)
#pragma unroll
      for (int m = 0; m < 4; ++m) {
#pragma unroll
        for (int r = 0; r < 4; ++r) xo[t][m][r] = blo(*(const LAS u16*)(L + XOFF(t, m, r)));
        xo[t][m] -= st.x;
        pin(xo[t][m]);
      }
    {
      v4u mv[2];
      _Pragma("nounroll") for (int s = 0; s < SPIN; ++s) {
        mv[0] = ld4c(ws, fresh(WS_MOD + grp * MOD_REP + (224 + tid) * 16));
        mv[1] = ld4c(ws, fresh(WS_MOD + grp * MOD_REP + (224 + (tid < 80 ? 256 + tid : tid)) * 16));
        const v4u rv = ld4c(ws, fresh(WS_DRDY + grp * 1024 + min(lane, 55) * 16));
        if (wall(mv[0].y == tag && mv[0].w == tag && mv[1].y == tag && mv[1].w == tag && flags_ok(rv, tag))) break;
        __builtin_amdgcn_s_sleep(4);
      }
      STAMP(12);
#pragma unroll
      for (int f = 0; f < WD_EARLY; ++f) wdn[f >> 2][f & 3] = ld4c(ws, (isG ? l16 : OOB) + (f & 3) * 1024, wdb + (f >> 2) * 4096);
      sb();
#pragma unroll
      for (int r = 0; r < 2; ++r) {
        const int q = tid + 256 * r;
        if (q < 336) {
          const int kind = q / 112, d = 4 * (q - 112 * kind);
          const float sc = kind == 0 ? 2.f / (float)(S * D) : 1.f;
          const int r0 = row_rho(d), r1 = row_rho(d + 1), dr = d < 112 || (d >= 224 && d < 336) ? 4 : -4;
          char LAS* p = L + L_M2 + kind * 1792;
          *(LAS float*)(p + r0 * 4) = blo(mv[r].x) * sc;
          *(LAS float*)(p + r1 * 4) = bhi(mv[r].x) * sc;
          *(LAS float*)(p + r1 * 4 + dr) = blo(mv[r].z) * sc;
          *(LAS float*)(p + r1 * 4 + 2 * dr) = bhi(mv[r].z) * sc;
        }
      }
    }
    STAMP(24);
    if (isG) cvt_w1(L, wave, lane);
    bar();
    *(LAS v4u*)(L + L_AF + (tid >> 6) * 4096 + 3072 + l16) = v4u{0u, 0u, 0u, 0u};
    STAMP(4);
    int zr = L_AF + lane * 8, ro = L_M2 + (t0 * 64 + 4 * g) * 4, ro1 = wave == 0 ? ro : ro + 256, ex = L_EX + c16 * 8;
    pin(zr); pin(ro); pin(ro1); pin(ex);
    {
      int zo = WS_T2 + (tid >> 2) * 8192 + blk * 64 + (tid & 3) * 16, zo3 = tid < 136 ? zo : zo - 64 * 8192;
      int zl[4];
#pragma unroll
      for (int m = 0; m < 4; ++m) {
        const int kk = (tid >> 2) + 64 * (m == 3 && tid >= 136 ? 2 : m), kq = kk < 114 ? kk : kk + 14;
        zl[m] = L_AF + ((kq & ~31) + ((kq & 4) << 2) + ((kq & 24) >> 1) + (kq & 3)) * 32 + (tid & 3) * 8;
        pin(zl[m]);
      }
      pin(zo); pin(zo3);
      bar();
      light_poll(ws, WS_T2 + ((blk * 7 + wave * 57) % 226) * 8192 + blk * 64);
      STAMP(13);
      v4u z[4];
      _Pragma("nounroll") for (int s = 0; s < SPIN; ++s) {
        zo = fresh(zo); zo3 = fresh(zo3);
#pragma unroll
        for (int m = 0; m < 4; ++m) z[m] = ld4c(ws, m < 3 ? zo : zo3, m * 64 * 8192);
        if (wall(max(max(max4(z[0]), max4(z[1])), max(max4(z[2]), max4(z[3]))) != SENT)) break;
      }
      STAMP(14);
#pragma unroll
      for (int m = 0; m < 4; ++m) {
        char LAS* p = L + zl[m];
        *(LAS v2u*)(p) = v2u{__builtin_amdgcn_perm(z[m].y, z[m].x, 0x05040100u), __builtin_amdgcn_perm(z[m].w, z[m].z, 0x05040100u)};
        *(LAS v2u*)(p + ZPL) = v2u{__builtin_amdgcn_perm(z[m].y, z[m].x, 0x07060302u), __builtin_amdgcn_perm(z[m].w, z[m].z, 0x07060302u)};
      }
    }
    bar();
    STAMP(5);
    {
      v4u zf[4][4];
#pragma unroll
      for (int ty = 0; ty < 4; ++ty)
#pragma unroll
        for (int s = 0; s < 4; ++s) zf[ty][s] = ldtr2(L + zr, ty * 4096 + s * 1024, ty * 4096 + s * 1024 + 512);
      v4f acc[2][4], x2[2][4], gav[2][4];
      v2f s1 = {0.f, 0.f}, s2 = {0.f, 0.f};
#define P3M(t, ty) { mf0(acc[t][ty], cf[4 * t + ty][0], zf[ty][0]); \
      _Pragma("unroll") for (int s = 1; s < 4; ++s) mfa(acc[t][ty], cf[4 * t + ty][s], zf[ty][s]); }
#define P3X(t, m, yv) { x2[t][m] = fma4(gav[t][m], yv, xo[t][m]); \
      if (t == 0 || wave != 0) { s1 += lo2(x2[t][m]) + hi2(x2[t][m]); \
        s2 = fma2(lo2(x2[t][m]), lo2(x2[t][m]), s2); s2 = fma2(hi2(x2[t][m]), hi2(x2[t][m]), s2); } }
      P3M(1, 0) P3M(1, 1) P3M(1, 2) P3M(1, 3)
#pragma unroll
      for (int m = 0; m < 4; ++m) { gav[0][m] = lds4f(L + ro, m * 64); gav[1][m] = lds4f(L + ro1, m * 64); }
      sb();
      P3M(0, 0)
      mfence<0>(acc[1][0], acc[1][1], acc[1][2], acc[1][3]);
      const v4f A1 = acc[1][0] + acc[1][1], B1 = acc[1][0] - acc[1][1], P1 = acc[1][3] - acc[1][2], Q1 = acc[1][2] + acc[1][3];
      sb();
      P3M(0, 1)
      P3X(1, 0, A1 - P1) P3X(1, 1, A1 + P1)
      sb();
      P3M(0, 2)
      P3X(1, 2, B1 - Q1) P3X(1, 3, B1 + Q1)
      sb();
      P3M(0, 3)
      mfence<7>(acc[0][0], acc[0][1], acc[0][2], acc[0][3]);
      {
        const v4f A = acc[0][0] + acc[0][1], B = acc[0][0] - acc[0][1], P = acc[0][3] - acc[0][2], Q = acc[0][2] + acc[0][3];
        v4f y[4] = {A - P, A + P, B - Q, B + Q};
        const bool sp = tid < 16;
        y[0].x = sp ? A.x : y[0].x; y[2].x = sp ? B.x : y[2].x;
        y[1].x = sp ? acc[1][0].x - acc[0][3].x : y[1].x; y[3].x = sp ? acc[1][0].x + acc[0][3].x : y[3].x;
        P3X(0, 0, y[0]) P3X(0, 1, y[1]) P3X(0, 2, y[2]) P3X(0, 3, y[3])
      }
      float sm = s1.x + s1.y, sq = s2.x + s2.y;
      sumg2(sm, sq);
      if (g == 0) *(LAS v2f*)(L + ex + wave * 128) = v2f{sm, sq};
      v4f sbv[2][4], cbv[2][4];
#pragma unroll
      for (int m = 0; m < 4; ++m) {
        sbv[0][m] = lds4f(L + ro, 1792 + m * 64); cbv[0][m] = lds4f(L + ro, 2 * 1792 + m * 64);
        sbv[1][m] = lds4f(L + ro1, 1792 + m * 64); cbv[1][m] = lds4f(L + ro1, 2 * 1792 + m * 64);
      }
      bar();
      v2f e = *(const LAS v2f*)(L + ex);
#pragma unroll
      for (int w = 1; w < 4; ++w) e += *(const LAS v2f*)(L + ex + w * 128);
      const float mc = e.x * (1.f / D), rstd = __builtin_amdgcn_rsqf(fmaxf(e.y * (1.f / D) - mc * mc, 0.f) + 1e-5f), nb = -mc * rstd;
      const int ho = WS_H2 + blk * 14336 + 2 * t0 * 1024 + l16, ho1 = wave == 0 ? OOB : ho + 2048;
#pragma unroll
      for (int t = 0; t < 2; ++t)
#pragma unroll
        for (int a = 0; a < 2; ++a) {
          v4u o;
#pragma unroll
          for (int hh = 0; hh < 2; ++hh) {
            const int m = 2 * a + hh;
            const v4f hv = fma4(fma4(x2[t][m], splat(rstd), splat(nb)), cbv[t][m], sbv[t][m]);
            o[2 * hh] = pk2(lo2(hv)); o[2 * hh + 1] = pk2(hi2(hv));
          }
          st4c(ws, (t ? ho1 : ho) + a * 1024, o);
        }
      STAMP(6);
      sb();
#pragma unroll
      for (int f = WD_EARLY; f < WD_MID; ++f) wdn[f >> 2][f & 3] = ld4c(ws, (isG ? l16 : OOB) + (f & 3) * 1024, wdb + (f >> 2) * 4096);
      sb();
#pragma unroll
      for (int t = 0; t < 2; ++t)
        if (t == 0 || wave != 0)
#pragma unroll
          for (int m = 0; m < 4; ++m)
#pragma unroll
            for (int r = 0; r < 4; r += 2) {
              const u32 p = pk(x2[t][m][r] + st.x, x2[t][m][r + 1] + st.x);
              *(LAS u16*)(L + XOFF(t, m, r)) = (u16)p;
              *(LAS u16*)(L + XOFF(t, m, r + 1)) = (u16)(p >> 16);
            }
      sb();
    }
    if (!isG) {
      bar();
      x2_flush(L, ws, blk, tid);
      drain();
      if (lane == 0) st2c(ws, WS_XRDY + blk * 64 + wave * 8, v2u{tag, ~tag});
      DUMP(); return;
    }
  } else {
    const int sj = j >= 30 ? j - 30 : j + 2;
    const int idx = sj * 8 + grp;
    const bool isC = j < 14 || idx == 0;
    const int pair = j < 14 ? grp * 14 + j : 112;
    const int lc = grp + 8 * (sj >> 1);
    const int qu = sj < 4 && lc < 14 ? 2 * lc + (sj & 1) : -1;
    const int qn = sj >= 4 && sj < 11 ? 28 + (sj - 4) * 8 + grp : -1;
    const int t1o = WS_T1 + (32 * wave + (lane >> 3)) * T1_BLK + pair * 128 + (lane & 7) * 16;
#pragma unroll
    for (int q = 0; q < 4; ++q) st4c(ws, isC ? t1o + 8 * q * T1_BLK : OOB, sv);
    ModLoad ml;
    epoch = ld1c(ws, WS_EPOCH);
    mod_load(ml, cin, wm, qu, tid);
    u32 pf0, pf1, pf2;
    {
      const rsrc_t mr = mkrs(wm, D * 6 * D * 2);
      const int row = (sj * 4 + wave) * 7 + (lane >> 1);
      pf0 = ld1(mr, lane < 14 && (grp < 6 || !(lane & 1)) ? row * (6 * D * 2) + (grp + 8 * (lane & 1)) * 128 : OOB);
      pf1 = ld1(tab, lane < 14 ? pfo : OOB); pf2 = ld1(code, lane < 4 ? pfc : OOB);
    }
    v4u fw[4]; v2u dw[2][8];
    {
      const rsrc_t rr = mkrs(wr, S * DF * 2), ri = mkrs(wi, S * DF * 2);
      const int o2 = tid < 194 ? 4096 + tid * 16 : OOB;
      fw[0] = ld4(rr, tid * 16, idx * 7200); fw[1] = ld4(rr, o2, idx * 7200);
      fw[2] = ld4(ri, tid * 16, idx * 7200); fw[3] = ld4(ri, o2, idx * 7200);
    }
    sb();
    epoch = __builtin_amdgcn_readfirstlane(epoch);
    tag = mktag(epoch);
    sb();
    if (qu >= 0) mod_part(ml, L, tid);
    bar();
    if (wave == 1 && lane < 8) st2c(ws, isC ? WS_RDY + lane * 1024 + pair * 8 : OOB, v2u{tag, ~tag});
    if (wave == 0 && qu >= 0) mod_publish(L, ws, qu, lane, tag);
    STAMP(1);
    sb();
    mod_load(ml, cin, wm, qn, tid);
    const int tw = (sj < 4 ? sj - 2 : sj - 9) * 8 + grp, unit = tw * 8 + wave * 2;
    const bool tiles = j < 14 && qn < 0 && tw < 49;
    {
      const rsrc_t dr = mkrs(wd, tiles ? F * D * 2 : 0);
      const int uc = unit / 56, kap = unit - uc * 56;
#pragma unroll
      for (int e = 0; e < 16; ++e)
        dw[e >> 3][e & 7] = ld2(dr, ((32 * kap + 4 * g) * D + 64 * uc + 4 * c16) * 2 + (e & 3) * D * 2, 16 * (e >> 2) * D * 2);
    }
    sb();
    pin(pf0); pin(pf1); pin(pf2);
    *(LAS v4u*)(L + L_CX + tid * 16) = fw[0];
    *(LAS v4u*)(L + L_CX + 7200 + tid * 16) = fw[2];
    if (tid < 194) {
      *(LAS v4u*)(L + L_CX + (256 + tid) * 16) = fw[1];
      *(LAS v4u*)(L + L_CX + 7200 + (256 + tid) * 16) = fw[3];
    }
    bar();
#pragma unroll
    for (int m = 0; m < 4; ++m) {
      const int p = min((tid >> 3) + 32 * m, 112), ka = p < 57 ? 4 * p : 4 * (p - 57) + 1;
      v4u o;
#pragma unroll
      for (int e = 0; e < 2; ++e) {
        const char LAS* row = L + L_CX + (2 * (tid & 7) + e) * 450 + ka * 2;
        const int kb = p == 56 ? 0 : 4;
        o[2 * e] = *(const LAS u16*)(row) | ((u32) * (const LAS u16*)(row + 7200) << 16);
        o[2 * e + 1] = *(const LAS u16*)(row + kb) | ((u32) * (const LAS u16*)(row + 7200 + kb) << 16);
      }
      st4c(ws, tid + 256 * m < 904 ? WS_WT + p * 16384 + idx * 128 + (tid & 7) * 16 : OOB, o);
    }
    STAMP(20);
    sb();
    v4f t1c[2], t1s[2], t2c[2], t2s[2];
#pragma unroll
    for (int t = 0; t < 2; ++t) {
#pragma unroll
      for (int r = 0; r < 4; ++r) {
        const float a1 = (float)((16 * t + 4 * g + r) * (16 * wave + c16)) * (1.f / S);
        const float a2 = (float)((16 * t + c16) * (16 * wave + 4 * g + r)) * (1.f / S);
        t1c[t][r] = __builtin_amdgcn_cosf(a1); t1s[t][r] = __builtin_amdgcn_sinf(a1);
        t2c[t][r] = __builtin_amdgcn_cosf(a2); t2s[t][r] = -__builtin_amdgcn_sinf(a2);
      }
      pin(t1c[t]); pin(t1s[t]); pin(t2c[t]); pin(t2s[t]);
    }
    if (qn >= 0) mod_part(ml, L, tid);
    drain();
    bar();
    if (wave == 1 && lane < 8) st2c(ws, WS_WRDY + lane * 1024 + idx * 8, v2u{tag, ~tag});
    if (wave == 0 && qn >= 0) mod_publish(L, ws, qn, lane, tag);
    STAMP(21);
#pragma unroll
    for (int tt = 0; tt < 8; ++tt) {
      v4u o;
#pragma unroll
      for (int m = 0; m < 4; ++m)
        o[m] = __builtin_amdgcn_perm(dw[tt >> 2][2 * m + 1][(tt & 3) >> 1], dw[tt >> 2][2 * m][(tt & 3) >> 1], (tt & 1) ? 0x07060302u : 0x05040100u);
      st4c(ws, tiles ? WS_WD + unit * 4096 + tt * 1024 + l16 : OOB, o);
    }
    sb();
    {
      const rsrc_t rg = mkrs(wg, D * F * 2), ru = mkrs(wu, D * F * 2);
      const int r7 = lane / 7, po = lane < 49 ? ((sj * 4 + wave) * 7 + r7) * (F * 2) + ((grp & 3) * 7 + lane - r7 * 7) * 128 : OOB;
      pf0 = ld1(rg, po); pf1 = ld1(ru, po);
    }
    if (!isC) { pin(pf0); pin(pf1); DUMP(); return; }
    STAMP(2);
    v4u c64[2][4], c32[2][2][2];
#pragma unroll
    for (int cp = 0; cp < 2; ++cp)
#pragma unroll
      for (int s = 0; s < 4; ++s) c64[cp][s] = ld4(tab, l16 + s * 1024, (TAB_P2 + wave * 2 + cp) * 4096);
#pragma unroll
    for (int t = 0; t < 2; ++t)
#pragma unroll
      for (int f = 0; f < 4; ++f) c32[t][f >> 1][f & 1] = ld4(tab, l16 + f * 1024, (TAB_P2 + 8 + t) * 4096);
    _Pragma("nounroll") for (int s = 0; s < SPIN; ++s) {
      const v4u rv = ld4c(ws, fresh(WS_WRDY + grp * 1024 + l16));
      if (wall(flags_ok(rv, tag))) break;
      __builtin_amdgcn_s_sleep(2);
    }
    pin(pf0); pin(pf1);
    STAMP(12);
    v2u wf[2][4];
#pragma unroll
    for (int t = 0; t < 2; ++t)
#pragma unroll
      for (int r = 0; r < 4; ++r) wf[t][r] = ld2c(ws, WS_WT + pair * 16384 + (16 * g + wave) * 128 + c16 * 8 + r * 512, t * 8192);
    bar();
    if (tid < 8) st2c(ws, j < 14 ? WS_DRDY + tid * 1024 + pair * 8 : OOB, v2u{tag, ~tag});
    v4f fc[2][2], fs[2][2];
#pragma unroll
    for (int c = 0; c < 2; ++c)
#pragma unroll
      for (int t = 0; t < 2; ++t) {
#pragma unroll
        for (int r = 0; r < 4; ++r) { fc[c][t][r] = blo(wf[t][r][c]); fs[c][t][r] = -bhi(wf[t][r][c]); }
        pin(fc[c][t]); pin(fs[c][t]);
      }
    STAMP(3);
    {
      int wl = L_CI + ((lane >> 2) & 1) * 8256 + wave * 2048 + ((lane >> 3) * 4 + (lane & 3)) * 16, lo = t1o;
      pin(wl); pin(lo);
      light_poll(ws, WS_T1 + (32 * wave + (pair & 31)) * T1_BLK + pair * 128);
      STAMP(13);
      v4u z[4];
      _Pragma("nounroll") for (int s = 0; s < SPIN; ++s) {
        lo = fresh(lo);
#pragma unroll
        for (int q = 0; q < 4; ++q) z[q] = ld4c(ws, lo, 8 * q * T1_BLK);
        if (wall(max(max(max4(z[0]), max4(z[1])), max(max4(z[2]), max4(z[3]))) != SENT)) break;
      }
      STAMP(14);
#pragma unroll
      for (int q = 0; q < 4; ++q) *(LAS v4u*)(L + wl + q * 512) = z[q];
    }
    bar();
    STAMP(4);
    {
      v4u a[2][2][4];
#pragma unroll
      for (int c = 0; c < 2; ++c)
#pragma unroll
        for (int mt = 0; mt < 2; ++mt)
#pragma unroll
          for (int s = 0; s < 4; ++s) a[c][mt][s] = lds4(LB, L_CI + c * 8256 + (s * 2 + mt) * 1024);
      v4f re[2][2], im[2][2];
      v4u f2[2][2], f3[2][2];
      char LAS* xw = L + lb + wave * 1024 - L_B;
#define S1H(c, mt) { mf0(re[c][mt], a[c][mt][0], c64[0][0]); mf0(im[c][mt], a[c][mt][0], c64[1][0]); }
#define S1T(c, mt) { _Pragma("unroll") for (int s = 1; s < 4; ++s) { mfa(re[c][mt], a[c][mt][s], c64[0][s]); mfa(im[c][mt], a[c][mt][s], c64[1][s]); } }
#define S2(c, t) { mf0(re[c][t], c32[t][0][0], f2[c][0]); mf0(im[c][t], c32[t][1][0], f2[c][0]); \
      mfa(re[c][t], c32[t][0][1], f2[c][1]); mfa(im[c][t], c32[t][1][1], f2[c][1]); }
#define S3(c, t) { mf0(im[c][t], f3[c][0], c32[t][0][0]); mf0(re[c][t], f3[c][0], c32[t][1][0]); \
      mfa(im[c][t], f3[c][1], c32[t][0][1]); mfa(re[c][t], f3[c][1], c32[t][1][1]); }
#define TW1(c, t) { f2[c][t] = cmul_pack<false>(re[c][t], im[c][t], t1c[t], t1s[t]); pin(f2[c][t]); }
#define FLT(c, t) { f3[c][t] = cmul_pack<true>(re[c][t], im[c][t], fc[c][t], fs[c][t]); pin(f3[c][t]); }
#define TW2(c, t) { *(LAS v4u*)(xw + L_CX + (c * 2 + t) * 4096) = cmul_pack<true>(re[c][t], im[c][t], t2c[t], t2s[t]); }
#define F2(c, t) mfence<0>(re[c][t], im[c][t]);
      S1H(0, 0) S1T(0, 0) S1H(0, 1) S1T(0, 1) sb();
      F2(0, 0) S1H(1, 0) S1T(1, 0) TW1(0, 0) sb();
      F2(0, 1) S1H(1, 1) S1T(1, 1) TW1(0, 1) sb();
      F2(1, 0) S2(0, 0) S2(0, 1) TW1(1, 0) sb();
      F2(1, 1) TW1(1, 1) sb();
      F2(0, 0) S2(1, 0) FLT(0, 0) sb();
      F2(0, 1) S2(1, 1) FLT(0, 1) sb();
      F2(1, 0) S3(0, 0) FLT(1, 0) sb();
      F2(1, 1) S3(0, 1) FLT(1, 1) sb();
      F2(0, 0) S3(1, 0) TW2(0, 0) sb();
      F2(0, 1) S3(1, 1) TW2(0, 1) sb();
      F2(1, 0) TW2(1, 0) sb();
      F2(1, 1) TW2(1, 1)
      bar();
#pragma unroll
      for (int c = 0; c < 2; ++c)
#pragma unroll
        for (int nt = 0; nt < 2; ++nt)
#pragma unroll
          for (int s = 0; s < 4; ++s) a[c][nt][s] = lds4(LB, L_CX + ((c * 2 + nt) * 4 + s) * 1024);
      const int o2 = WS_T2 + 2 * pair * 8192 + wave * 2048 + l16;
#define S4H(c, t) { mf0(im[c][t], c64[0][0], a[c][t][0]); mf0(re[c][t], c64[1][0], a[c][t][0]); }
#define S4T(c, t) { _Pragma("unroll") for (int s = 1; s < 4; ++s) { mfa(im[c][t], c64[0][s], a[c][t][s]); mfa(re[c][t], c64[1][s], a[c][t][s]); } }
#define OUT(c, t) st4c(ws, o2 + c * 8192 + t * 1024, \
      v4u{pk(re[c][t].x, im[c][t].x), pk(re[c][t].y, im[c][t].y), pk(re[c][t].z, im[c][t].z), pk(re[c][t].w, im[c][t].w)});
      S4H(0, 0) S4T(0, 0) S4H(0, 1) mfence<1>(re[0][0], im[0][0]); OUT(0, 0) sb();
      S4T(0, 1) S4H(1, 0) mfence<1>(re[0][1], im[0][1]); OUT(0, 1) sb();
      S4T(1, 0) S4H(1, 1) mfence<1>(re[1][0], im[1][0]); OUT(1, 0) sb();
      S4T(1, 1) mfence<7>(re[1][1], im[1][1]); OUT(1, 1)
    }
    STAMP(5);
    sb();
    if (!isG) { DUMP(); return; }
    dma_w1(wg, wu, slice, wave, lane, lbase);
    _Pragma("nounroll") for (int s = 0; s < SPIN; ++s) {
      const v4u rv = ld4c(ws, fresh(WS_DRDY + grp * 1024 + min(lane, 55) * 16));
      if (wall(flags_ok(rv, tag))) break;
      __builtin_amdgcn_s_sleep(2);
    }
#pragma unroll
    for (int f = 0; f < WD_MID; ++f) wdn[f >> 2][f & 3] = ld4c(ws, l16 + (f & 3) * 1024, wdb + (f >> 2) * 4096);
    sb();
    cvt_w1(L, wave, lane);
    bar();
  }

  STAMP(7);
  const int blk0 = 16 * tile + 4 * (grp & 3);
  const int ab = WS_ACT + blk0 * 57344 + wave * 14 * 1024;
  const int fo = WS_ARDY + (tile * 4 + (grp & 3)) * 512 + (7 * wave + min(lane, 6)) * 8;
  v4u h[14][2];
  {
    const int hb = WS_H2 + (16 * tile + 4 * wave) * 14336;
    const int fw_ = WS_ARDY + (tile * 4 + wave) * 512 + slice * 8;
    int w2 = l16 + 57344;
    pin(w2);
    light_poll(ws, hb + (lane & 1) * 14336 + 13 * 1024);
    STAMP(15);
#pragma unroll
    for (int ks = 0; ks < 14; ++ks)
#pragma unroll
      for (int tt = 0; tt < 2; ++tt) h[ks][tt] = ld4c(ws, l16 + (ks & 3) * 1024, hb + tt * 14336 + (ks >> 2) * 4096);
    v4u wfr[2][8];
#pragma unroll
    for (int u = 0; u < 8; ++u) wfr[0][u] = lds4(L + l16, L_W1 + u * 1024);
    sb();
#define BAD2(x0, x1) wany(max(max(max(max(x0.x, x0.y), x0.z), max(max(x0.w, x1.x), x1.y)), max(x1.z, x1.w)) == SENT)
#define RETRY2(x0, x1, o0, o1) \
      _Pragma("nounroll") for (int s_ = 0; s_ < SPIN; ++s_) { \
        const int lf_ = fresh(l16); SLOWCNT \
        x0 = ld4c(ws, lf_, o0); x1 = ld4c(ws, lf_, o1); \
        if (wall(max(max4(x0), max4(x1)) != SENT)) break; \
      }
    if (__builtin_expect(BAD2(h[0][0], h[0][1]), 0)) { RETRY2(h[0][0], h[0][1], hb, hb + 14336) }
    STAMP(8); CSTAMP(28);
    v4f acc[2][8][2];
    v2u fa;
    const int ao = WS_ACT + (16 * tile + 4 * wave) * 57344 + 2 * slice * 1024 + l16;
#define ACT_OUT(ps, tt, f) { v4u o; \
      _Pragma("unroll") for (int hh = 0; hh < 2; ++hh) { \
        const v4f av = __builtin_elementwise_max(acc[ps][2 * f + hh][tt], splat(0.f)) * acc[ps][4 + 2 * f + hh][tt]; \
        o[2 * hh] = pk2(lo2(av)); o[2 * hh + 1] = pk2(hi2(av)); } \
      st4c(ws, ao + (2 * ps + tt) * 57344 + f * 1024, o); }
#pragma unroll
    for (int ps = 0; ps < 2; ++ps) {
#pragma unroll
      for (int u = 0; u < 8; ++u) { acc[ps][u][0] = v4f{0.f, 0.f, 0.f, 0.f}; acc[ps][u][1] = acc[ps][u][0]; }
#pragma unroll
      for (int ks = 0; ks < 14; ++ks) {
        const int cur = ks & 1, nk = ks == 13 ? 0 : ks + 1;
        const bool more = ps == 0 || ks < 13;
        __builtin_amdgcn_s_waitcnt(0xC07F);
#pragma unroll
        for (int u = 0; u < 8; ++u) {
          acc[ps][u][0] = mfma(wfr[cur][u], h[ks][0], acc[ps][u][0]);
          if (more) wfr[cur ^ 1][u] = nk < 7 ? lds4(L + l16, L_W1 + nk * 8192 + u * 1024) : lds4(L + w2, L_W1 + (nk - 7) * 8192 + u * 1024);
        }
        const bool bad = more && BAD2(h[nk][0], h[nk][1]);
#pragma unroll
        for (int u = 0; u < 8; ++u) acc[ps][u][1] = mfma(wfr[cur][u], h[ks][1], acc[ps][u][1]);
        if (ps == 1 && ks < 4) ACT_OUT(0, (ks >> 1), (ks & 1))
#pragma unroll
        for (int u = 0; u < 8; ++u) {
          __builtin_amdgcn_sched_group_barrier(0x8, 1, 0);
          __builtin_amdgcn_sched_group_barrier(0x100, 1, 0);
          if (ps == 1 && ks < 4) __builtin_amdgcn_sched_group_barrier(0x2, 1, 0);
        }
#pragma unroll
        for (int u = 0; u < 8; ++u) {
          __builtin_amdgcn_sched_group_barrier(0x8, 1, 0);
          if (ps == 1 && ks < 4) __builtin_amdgcn_sched_group_barrier(0x2, 4, 0);
          else __builtin_amdgcn_sched_group_barrier(0x2, 1, 0);
        }
        sb();
        if (ps == 0) {
#pragma unroll
          for (int tt = 0; tt < 2; ++tt) h[ks][tt] = ld4c(ws, l16 + (ks & 3) * 1024, hb + (2 + tt) * 14336 + (ks >> 2) * 4096);
          if (ks == 8 && isR) { asm volatile("s_barrier" ::: "memory"); x2_flush(L, ws, 16 * grp + j - 14, tid); }
        } else {
          if (ks == 6) {
            asm volatile("s_waitcnt vmcnt(%0)" :: "i"(WD_MID + 24 <= 56 ? 12 : WD_MID + 12 >= 56 ? 0 : 44 - WD_MID) : "memory");
            if (lane < 2) st2c(ws, lane == 0 ? fw_ : isR ? WS_XRDY + (16 * grp + j - 14) * 64 + wave * 8 : OOB, v2u{tag, ~tag});
          }
#pragma unroll
          for (int e = 0; e < 4; ++e) {
            const int f = WD_MID + 4 * ks + e;
            if (f < 56) wdn[f >> 2][f & 3] = ld4c(ws, l16 + (f & 3) * 1024, wdb + (f >> 2) * 4096);
          }
          if (ks == A_AT - 1) fa = ld2c(ws, fo);
          if (ks == A_AT && !wall(fa.x == tag && fa.y == ~tag)) wait_flags(ws, fo, tag);
          if (ks >= A_AT) {
#pragma unroll
            for (int k2 = (ks == A_AT ? 0 : ks); k2 <= ks; ++k2)
#pragma unroll
              for (int tt = 0; tt < 2; ++tt) h[k2][tt] = ld4c(ws, l16 + (k2 & 3) * 1024, ab + tt * 57344 + (k2 >> 2) * 4096);
          }
        }
        if (__builtin_expect(bad, 0)) {
          const int pb = (ks == 13 ? 2 : 2 * ps) * 14336 + nk * 1024;
          RETRY2(h[nk][0], h[nk][1], hb + pb, hb + pb + 14336)
        }
        if (ps == 0 && ks == 13) { STAMP(16); CSTAMP(29); }
        sb();
      }
    }
    ACT_OUT(1, 0, 0) ACT_OUT(1, 0, 1) ACT_OUT(1, 1, 0) ACT_OUT(1, 1, 1)
    CSTAMP(30);
    sb();
    STAMP(9);
    const int blk = blk0 + wave;
    const int xo = WS_X2 + blk * 14336 + c16 * 896 + ct * 128 + g * 32;
    const int go = WS_MOD + grp * MOD_REP + (70 + 2 * ct) * 128 + g * 64;
    v4u a[14][2];
    v4f ac2[4][4];
#pragma unroll
    for (int t = 0; t < 4; ++t)
#pragma unroll
      for (int n = 0; n < 4; ++n) ac2[t][n] = v4f{0.f, 0.f, 0.f, 0.f};
    v4u xv[2], gv[4];
#pragma unroll
    for (int ks = 0; ks < 14; ++ks) {
#pragma unroll
      for (int tt = 0; tt < 2; ++tt)
#pragma unroll
        for (int n = 0; n < 4; ++n) ac2[tt][n] = mfma(wdn[ks][n], h[ks][tt], ac2[tt][n]);
      sb();
      if (ks == F_AT) {
        drain();
        if (lane == 0) st2c(ws, fw_ + 256, v2u{tag, ~tag});
        fa = ld2c(ws, WS_XRDY + blk * 64 + (lane & 3) * 8);
#pragma unroll
        for (int e = 0; e < 4; ++e) gv[e] = ld4c(ws, go + e * 16);
      }
      sb();
    }
    STAMP(17);
    if (!wall(fa.x == tag && fa.y == ~tag)) wait_flags(ws, WS_XRDY + blk * 64 + (lane & 3) * 8, tag);
    xv[0] = ld4c(ws, xo); xv[1] = ld4c(ws, xo + 16);
    wait_flags(ws, fo + 256, tag);
    STAMP(18);
#pragma unroll
    for (int ks = 0; ks < 14; ++ks)
#pragma unroll
      for (int tt = 0; tt < 2; ++tt) a[ks][tt] = ld4c(ws, l16 + (ks & 3) * 1024, ab + (2 + tt) * 57344 + (ks >> 2) * 4096);
    sb();
    asm volatile("s_barrier" ::: "memory");
#pragma unroll
    for (int t = 0; t < 2; ++t)
      if (t != wave)
#pragma unroll
        for (int n = 0; n < 4; ++n) *(LAS v4f*)(L + l16 + wave * 4096 + (t * 16 + n) * 1024) = ac2[t][n];
    sb();
#pragma unroll
    for (int ks = 0; ks < 14; ++ks) {
#pragma unroll
      for (int tt = 0; tt < 2; ++tt)
#pragma unroll
        for (int n = 0; n < 4; ++n) ac2[2 + tt][n] = mfma(wdn[ks][n], a[ks][tt], ac2[2 + tt][n]);
      sb();
    }
    STAMP(10);
#pragma unroll
    for (int t = 2; t < 4; ++t)
      if (t != wave)
#pragma unroll
        for (int n = 0; n < 4; ++n) *(LAS v4f*)(L + l16 + wave * 4096 + (t * 16 + n) * 1024) = ac2[t][n];
    bar();
    v4f r[4];
#pragma unroll
    for (int n = 0; n < 4; ++n) r[n] = wave == 0 ? ac2[0][n] : wave == 1 ? ac2[1][n] : wave == 2 ? ac2[2][n] : ac2[3][n];
#pragma unroll
    for (int w = 1; w < 4; ++w)
#pragma unroll
      for (int n = 0; n < 4; ++n) r[n] += lds4f(L + l16 + (wave * 4 + ((wave + w) & 3)) * 4096, n * 1024);
    _Pragma("nounroll") for (int s = 0; s < SPIN; ++s) {
      bool ok = true;
#pragma unroll
      for (int e = 0; e < 4; ++e) ok = ok && gv[e].y == tag && gv[e].w == tag;
      if (wall(ok)) break;
      const int gf = fresh(go);
#pragma unroll
      for (int e = 0; e < 4; ++e) gv[e] = ld4c(ws, gf + e * 16);
    }
    v4u o[2];
#pragma unroll
    for (int q = 0; q < 4; ++q) {
      const u32 x01 = xv[q >> 1][2 * (q & 1)], x23 = xv[q >> 1][2 * (q & 1) + 1];
      o[q >> 1][2 * (q & 1)] = pk2(fma2(bun(gv[q].x), v2f{r[0][q], r[1][q]}, bun(x01)));
      o[q >> 1][2 * (q & 1) + 1] = pk2(fma2(bun(gv[q].z), v2f{r[2][q], r[3][q]}, bun(x23)));
    }
    v4u* op = (v4u*)(out + tok_row(blk, c16) * D + ct * 64 + g * 16);
    op[0] = o[0]; op[1] = o[1];
    if (blockIdx.x == 0 && wave == 0) {
      _Pragma("nounroll") for (int s = 0; s < SPIN; ++s) {
        const v4u v = ld4c(ws, fresh(WS_MOD + (70 + (lane < 14 ? lane : 0)) * 128));
        if (wall(v.y == tag)) break;
      }
      if (lane == 0) __builtin_amdgcn_raw_buffer_store_b32(epoch + 1u, ws, WS_EPOCH, 0, SC1);
    }
  }
  STAMP(11);
  DUMP();
}

extern "C" void kernel_launch(void* const* d_in, const int* in_sizes, int n_in, void* d_out, int out_size, void* d_ws,
                              size_t ws_size, hipStream_t stream) {
  fused<<<256, 256, 0, stream>>>((const u16*)d_in[0], (const u16*)d_in[1], (const u16*)d_in[2], (const u16*)d_in[4],
                                 (const u16*)d_in[5], (const u16*)d_in[6], (const u16*)d_in[7], (const u16*)d_in[8],
                                 (u16*)d_out, (char*)d_ws);
}
```

```cpp
#include <hip/hip_runtime.h>

typedef unsigned short u16;
typedef unsigned int u32;
typedef unsigned long long u64;
typedef u32 v4u __attribute__((ext_vector_type(4)));
typedef u32 v2u __attribute__((ext_vector_type(2)));
typedef float v4f __attribute__((ext_vector_type(4)));
typedef float v2f __attribute__((ext_vector_type(2)));
typedef short v8s __attribute__((ext_vector_type(8)));
typedef short v4s __attribute__((ext_vector_type(4)));
typedef __bf16 v8b __attribute__((ext_vector_type(8)));
typedef __bf16 v2b __attribute__((ext_vector_type(2)));
#define LAS __attribute__((address_space(3)))
#define GAS __attribute__((address_space(1)))
#define DEV __device__ __forceinline__

#define C_W1_EARLY 0
#define CLR_DELAY 0
#define W_DELAY 0
#define CLR2_DELAY 0
#define PF_POS 2
#define SL1 1
#define SL2 2
#define SL4 4
#define X_AFTER 8
#define PF_AUX 2
#define W_LATE 1
#define W1_PF 1
#define DMA_MOD ""
#define RF(ks) ((ks) <= 5 ? 0 : (3 * ((ks) - 5) < 2 * ((ks) - 1) ? 3 * ((ks) - 5) : 2 * ((ks) - 1)))
#define AF(ks) ((ks) <= A_AT + 1 ? 0 : (6 * ((ks) - A_AT - 1) < 2 * ((ks) - 1) ? 6 * ((ks) - A_AT - 1) : 2 * ((ks) - 1)))
#define WD_PER 3
#define A_FIRST 10
#define B_FIRST 12
#define A_AT 9
#define F_AT 4
#define CODE_BYTES 45056
#define WD_EARLY 8
#define WD_MID 16
#define B_AT 9
constexpr int S = 2048, D = 448, DF = 225, F = 1792;

struct alignas(16) Chain { u16 v[2048]; };
struct Cos448 { u16 v[448]; };
constexpr double c_cos_t(double a) {
  double a2 = a * a, t = 1.0, s = 1.0;
  for (int i = 1; i <= 10; ++i) { t = -t * a2 / ((2 * i - 1) * (2 * i)); s += t; }
  return s;
}
constexpr double c_sin_t(double a) {
  double a2 = a * a, t = a, s = a;
  for (int i = 1; i <= 10; ++i) { t = -t * a2 / ((2 * i) * (2 * i + 1)); s += t; }
  return s;
}
constexpr double c_cos448(int m) {
  if (m > 224) m = 448 - m;
  double sg = 1.0;
  if (m > 112) { m = 224 - m; sg = -1.0; }
  const double w = 6.283185307179586476925286766559 / 448.0;
  return m == 112 ? 0.0 : (m <= 56 ? sg * c_cos_t(w * m) : sg * c_sin_t(w * (112 - m)));
}
constexpr u16 c_bf16(double d) {
  const u32 u = __builtin_bit_cast(u32, (float)d);
  return (u16)((u + 0x7FFFu + ((u >> 16) & 1u)) >> 16);
}
constexpr Cos448 mk_cos() { Cos448 c{}; for (int m = 0; m < 448; ++m) c.v[m] = c_bf16(c_cos448(m)); return c; }
constexpr Cos448 CTAB = mk_cos();
constexpr u16 bcos(int m) { return CTAB.v[m % 448]; }
constexpr u16 bsin(int m) { return CTAB.v[(m % 448 + 336) % 448]; }
constexpr u16 bneg(u16 b) { return b ? (u16)(b ^ 0x8000u) : b; }
constexpr u16 bscale(u16 b, int e) { return b ? (u16)(b + e * 0x80) : b; }

constexpr Chain mk_p1(int c) {
  const int tile = c >> 2, par = c & 1, sn = (c >> 1) & 1;
  Chain ch{};
  for (int s = 0; s < 4; ++s)
    for (int l = 0; l < 64; ++l) {
      const int kp = 16 * tile + (l & 15), k = 2 * kp + par;
      if (k > 224) continue;
      for (int j = 0; j < 8; ++j) {
        const int dl = 32 * s + 8 * (l >> 4) + j;
        if (dl > 112) continue;
        u16 b = sn ? bneg(bsin(k * dl)) : bcos(k * dl);
        if (dl == 0 || dl == 112) b = bscale(b, -1);
        ch.v[(s * 64 + l) * 8 + j] = b;
      }
    }
  return ch;
}
constexpr Chain mk_p3(int c) {
  const int tile = c >> 1, sn = c & 1;
  Chain ch{};
  for (int s = 0; s < 4; ++s)
    for (int l = 0; l < 64; ++l) {
      int dl = 16 * tile + (l & 15);
      if (c == 1 && dl == 0) dl = 112;
      for (int j = 0; j < 8; ++j) {
        const int k = 2 * (32 * s + 8 * (l >> 4) + j) + 1;
        if (k < 224) ch.v[(s * 64 + l) * 8 + j] = sn ? bsin(k * dl) : bcos(k * dl);
      }
    }
  return ch;
}
constexpr Chain mk_p2(int c) {
  Chain ch{};
  for (int f = 0; f < 4; ++f)
    for (int l = 0; l < 64; ++l)
      for (int j = 0; j < 8; ++j) {
        const int comp = c < 8 ? (c & 1) : (f >> 1), s = c < 8 ? f : (f & 1);
        const int o = 16 * (c < 8 ? (c >> 1) : (c - 8)) + (l & 15), i = 16 * s + 4 * (l >> 4) + (j >> 1);
        const int m = (c < 8 ? 7 : 14) * ((i * o) % 64);
        u16 b = 0;
        if (comp == 0) b = (j & 1) ? bsin(m) : bcos(m);
        else b = (j & 1) ? bcos(m) : bneg(bsin(m));
        ch.v[(f * 64 + l) * 8 + j] = b;
      }
  return ch;
}
template <int I> constexpr Chain P1C = mk_p1(I);
template <int I> constexpr Chain P3C = mk_p3(I);
template <int I> constexpr Chain P2C = mk_p2(I);
#define L10(T, b) T<b>, T<b + 1>, T<b + 2>, T<b + 3>, T<b + 4>, T<b + 5>, T<b + 6>, T<b + 7>, T<b + 8>, T<b + 9>
#define L29(T) L10(T, 0), L10(T, 10), T<20>, T<21>, T<22>, T<23>, T<24>, T<25>, T<26>, T<27>, T<28>
constexpr int TAB_P1 = 0, TAB_P3 = 29, TAB_P2 = 43, TAB_N = 53;
static __device__ const Chain TAB[TAB_N] = {L29(P1C), L10(P3C, 0), P3C<10>, P3C<11>, P3C<12>, P3C<13>, L10(P2C, 0)};

constexpr int WS_EPOCH = 0;
constexpr int WS_RDY = 4096;
constexpr int WS_HRDY = 4096 + 8192;
constexpr int WS_DRDY = 4096 + 16384;
constexpr int WS_XRDY = 131072 + 65536;
constexpr int WS_ARDY = 131072;
constexpr int WS_MOD = 32768, MOD_REP = 12288;
constexpr int T1_BLK = 226 * 64;
constexpr int WS_T1 = 1 << 18;
constexpr int WS_WT = WS_T1 + 128 * T1_BLK;
constexpr int WS_H2 = WS_WT + 113 * 16384;
constexpr int WS_X2 = WS_H2 + 128 * 14336;
constexpr int WS_WD = WS_X2 + 128 * 14336;
constexpr int WS_ACT = WS_WD + 7 * 56 * 4096;
constexpr int WS_END = WS_ACT + 128 * 57344;
constexpr int WS_DBG = 32 << 20;

constexpr int L_W1 = 0;
constexpr int L_B = 114688;
constexpr int XP = 912;
constexpr int L_XS = L_B;
constexpr int L_ST = L_XS + 16 * XP;
constexpr int L_M1 = L_ST + 128;
constexpr int L_AF = L_M1 + 4 * 1792;
constexpr int L_M2 = L_AF + 16384;
constexpr int L_EX = L_M2 + 3 * 1792;
constexpr int L_REND = L_EX + 512;
constexpr int ZPL = 8192;
constexpr int L_CI = L_B;
constexpr int L_CX = L_CI + 2 * 8256;
constexpr int L_RED = L_CX + 16384;
constexpr int L_DBG = 163840 - 1024;
constexpr int L_SZ = 163840;
static_assert(L_REND <= L_DBG && L_RED + 2048 <= L_DBG && 2 * ZPL <= 16384, "lds");

constexpr u32 SENT = 0xFFFFFFFFu;
constexpr int SPIN = 1 << 14;
constexpr int SC1 = 16;
constexpr int OOB = 0x7FFF0000;

typedef __amdgpu_buffer_rsrc_t rsrc_t;
DEV rsrc_t mkrs(const void* p, int bytes) { return __builtin_amdgcn_make_buffer_rsrc((void*)p, 0, bytes, 0x00020000); }
DEV v4u ld4(rsrc_t r, int off, int so = 0) { return __builtin_amdgcn_raw_buffer_load_b128(r, off, so, 0); }
DEV v2u ld2(rsrc_t r, int off, int so = 0) { return __builtin_amdgcn_raw_buffer_load_b64(r, off, so, 0); }
DEV v4u ld4c(rsrc_t r, int off, int so = 0) { return __builtin_amdgcn_raw_buffer_load_b128(r, off, so, SC1); }
DEV v2u ld2c(rsrc_t r, int off, int so = 0) { return __builtin_amdgcn_raw_buffer_load_b64(r, off, so, SC1); }
DEV u32 ld1c(rsrc_t r, int off, int so = 0) { return __builtin_amdgcn_raw_buffer_load_b32(r, off, so, SC1); }
DEV void st4c(rsrc_t r, int off, v4u v) { __builtin_amdgcn_raw_buffer_store_b128(v, r, off, 0, SC1); }
DEV void st2c(rsrc_t r, int off, v2u v) { __builtin_amdgcn_raw_buffer_store_b64(v, r, off, 0, SC1); }
DEV float blo(u32 v) { return __builtin_bit_cast(float, v << 16); }
DEV float bhi(u32 v) { return __builtin_bit_cast(float, v & 0xFFFF0000u); }
DEV v2f bun(u32 v) { return v2f{blo(v), bhi(v)}; }
DEV u32 pk(float lo, float hi) { return __builtin_bit_cast(u32, __builtin_convertvector(v2f{lo, hi}, v2b)); }
DEV u32 pk2(v2f v) { return __builtin_bit_cast(u32, __builtin_convertvector(v, v2b)); }
DEV float rb(float f) { return blo(pk(f, 0.f)); }
DEV v2f lo2(v4f v) { return __builtin_shufflevector(v, v, 0, 1); }
DEV v2f hi2(v4f v) { return __builtin_shufflevector(v, v, 2, 3); }
DEV v2f fma2(v2f a, v2f b, v2f c) { return __builtin_elementwise_fma(a, b, c); }
DEV v4f fma4(v4f a, v4f b, v4f c) { return __builtin_elementwise_fma(a, b, c); }
DEV v4f splat(float f) { return v4f{f, f, f, f}; }
DEV v4f mfma(v4u a, v4u b, v4f c) {
  return __builtin_amdgcn_mfma_f32_16x16x32_bf16(__builtin_bit_cast(v8b, a), __builtin_bit_cast(v8b, b), c, 0, 0, 0);
}
DEV void mfa(v4f& c, v4u a, v4u b) { asm volatile("s_nop 1\n\tv_mfma_f32_16x16x32_bf16 %0, %1, %2, %0" : "+v"(c) : "v"(a), "v"(b)); }
DEV void mf0(v4f& c, v4u a, v4u b) { asm volatile("s_nop 1\n\tv_mfma_f32_16x16x32_bf16 %0, %1, %2, 0" : "=&v"(c) : "v"(a), "v"(b)); }
template <int N> DEV void mfence(v4f& a, v4f& b) { asm volatile("s_nop %2" : "+v"(a), "+v"(b) : "i"(N)); }
template <int N> DEV void mfence(v4f& a, v4f& b, v4f& c, v4f& d) { asm volatile("s_nop %4" : "+v"(a), "+v"(b), "+v"(c), "+v"(d) : "i"(N)); }
template <class T> DEV void pin(T& v) { asm volatile("" : "+v"(v)); }
DEV int fresh(int off) { asm volatile("" : "+v"(off) :: "memory"); return off; }
DEV bool wall(bool ok) { return __builtin_amdgcn_ballot_w64(!ok) == 0; }
DEV bool wany(bool b) { return __builtin_amdgcn_ballot_w64(b) != 0; }
DEV void sb() { __builtin_amdgcn_sched_barrier(0); }
DEV u32 max4(v4u v) { return max(max(v.x, v.y), max(v.z, v.w)); }
DEV void drain() { asm volatile("s_waitcnt vmcnt(0)" ::: "memory"); }
DEV void cfence() { asm volatile("" ::: "memory"); }
DEV void bar() { asm volatile("s_waitcnt lgkmcnt(0)\n\ts_barrier" ::: "memory"); }
template <int CTRL> DEV float dpp(float v) {
  return __builtin_bit_cast(float, __builtin_amdgcn_update_dpp(0, __builtin_bit_cast(int, v), CTRL, 0xF, 0xF, true));
}
DEV float sum16(float v) {
  v += dpp<0x128>(v); v += dpp<0x124>(v); v += dpp<0x122>(v); v += dpp<0x121>(v); return v;
}
DEV void sumg2(float& a, float& b) {
  float c = a, d = b;
  asm volatile("s_nop 1\n\tv_permlane16_swap_b32 %0, %1\n\tv_permlane16_swap_b32 %2, %3" : "+v"(a), "+v"(c), "+v"(b), "+v"(d));
  a += c; b += d; c = a; d = b;
  asm volatile("s_nop 1\n\tv_permlane32_swap_b32 %0, %1\n\tv_permlane32_swap_b32 %2, %3" : "+v"(a), "+v"(c), "+v"(b), "+v"(d));
  a += c; b += d;
}
DEV v4u lds4(const char LAS* L, int off) { return *(const LAS v4u*)(L + off); }
DEV v4f lds4f(const char LAS* L, int off) { return *(const LAS v4f*)(L + off); }
DEV v2u ldtr(const char LAS* L, int off) { return __builtin_bit_cast(v2u, __builtin_amdgcn_ds_read_tr16_b64_v4i16((LAS v4s*)(L + off))); }
DEV v4u ldtr2(const char LAS* L, int o0, int o1) { const v2u a = ldtr(L, o0), b = ldtr(L, o1); return v4u{a.x, a.y, b.x, b.y}; }
DEV void dma16(rsrc_t r, int voff, int soff, u32 lds_dst) {
  u32 keep;
  asm volatile("s_mov_b32 %0, m0\n\ts_mov_b32 m0, %1\n\ts_nop 0\n\tbuffer_load_dwordx4 %2, %3, %4 offen" DMA_MOD " lds\n\ts_mov_b32 m0, %0"
               : "=&s"(keep) : "s"(lds_dst), "v"(voff), "s"(r), "s"(soff) : "memory");
}
DEV void light_poll(rsrc_t ws, int off) {
  _Pragma("nounroll") for (int s = 0; s < SPIN; ++s) {
    if (wall(ld1c(ws, fresh(off)) != SENT)) break;
    __builtin_amdgcn_s_sleep(SL2);
  }
}
DEV int tok_row(int blk, int t) {
  const int n1 = 16 * (blk >> 5) + 4 * ((blk >> 2) & 3) + (t & 3), n2 = 16 * ((blk >> 4) & 1) + 4 * (blk & 3) + (t >> 2);
  return n2 + 32 * n1;
}
template <bool SWAP> DEV v4u cmul_pack(v4f a, v4f b, v4f c, v4f s) {
  const v4f re = fma4(b, s, a * c), im = fma4(b, c, -(a * s));
  return SWAP ? v4u{pk(im.x, re.x), pk(im.y, re.y), pk(im.z, re.z), pk(im.w, re.w)}
              : v4u{pk(re.x, im.x), pk(re.y, im.y), pk(re.z, im.z), pk(re.w, im.w)};
}

#define STAMP(i)
#define CSTAMP(i)
#define SLOWCNT
#define DUMP()

DEV void dma_w1(const u16* wg, const u16* wu, int slice, int wave, int lane, u32 lbase) {
  const rsrc_t rg = mkrs(wg, D * F * 2), ru = mkrs(wu, D * F * 2);
  const int r = lane >> 3, ch = ((((lane & 7) >> 1) ^ (lane >> 4)) & 3) * 32 + (lane & 1) * 16 + slice * 128;
  const int vp = r * (F * 2) + ch, vn = (7 - r) * (F * 2) + ch;
#pragma unroll
  for (int n = 0; n < 4; ++n) {
    const int ks = wave + 4 * n;
    if (n == 3 && wave >= 2) break;
    const int t16 = (ks >> 1) * 16, a = ks & 1;
#pragma unroll
    for (int q = 0; q < 4; ++q) {
      const int dl0 = t16 + 8 * (q & 1);
      const bool pos = (q >> 1) == a;
      const int d0 = q < 2 ? (a ? 224 - dl0 - 7 : dl0) : (a ? 224 + dl0 : 448 - dl0 - 7);
      int vo = pos ? vp : vn, so = d0 * (F * 2);
      if (n == 0 && q == 2) {
        vo += so; so = 0;
        if (wave < 2 && lane < 8) vo = (wave ? 336 : 112) * (F * 2) + ch;
      }
      dma16(rg, vo, so, lbase + L_W1 + ks * 8192 + q * 1024);
      dma16(ru, vo, so, lbase + L_W1 + ks * 8192 + 4096 + q * 1024);
    }
  }
}
struct ModLoad { u32 cv[7]; v4u mw[7]; };
DEV void mod_load(ModLoad& m, const u16* cin, const u16* wm, int q, int tid) {
  const rsrc_t cr = mkrs(cin, q >= 0 ? D * 2 : 0), mr = mkrs(wm, q >= 0 ? D * 6 * D * 2 : 0);
#pragma unroll
  for (int i = 0; i < 7; ++i) m.cv[i] = __builtin_amdgcn_raw_buffer_load_b16(cr, (tid >> 2) * 2 + i * 128, 0, 0);
#pragma unroll
  for (int i = 0; i < 7; ++i) m.mw[i] = __builtin_amdgcn_raw_buffer_load_b128(mr, ((tid >> 2) * (6 * D) + q * 32 + (tid & 3) * 8) * 2, i * 64 * 6 * D * 2, PF_AUX);
}
DEV void mod_part(ModLoad& m, char LAS* L, int tid) {
  float sl[7];
#pragma unroll
  for (int i = 0; i < 7; ++i) { const float cc = blo(m.cv[i]); sl[i] = rb(cc * __builtin_amdgcn_rcpf(1.f + __expf(-cc))); pin(sl[i]); }
  v2f a[4];
#pragma unroll
  for (int e = 0; e < 4; ++e) a[e] = v2f{0.f, 0.f};
#pragma unroll
  for (int i = 0; i < 7; ++i)
#pragma unroll
    for (int e = 0; e < 4; ++e) a[e] = fma2(bun(m.mw[i][e]), v2f{sl[i], sl[i]}, a[e]);
#pragma unroll
  for (int e = 0; e < 4; ++e) {
    a[e].x += dpp<0x128>(a[e].x); a[e].y += dpp<0x128>(a[e].y);
    a[e].x += dpp<0x124>(a[e].x); a[e].y += dpp<0x124>(a[e].y);
  }
  if ((tid & 15) < 4) {
    *(LAS v4f*)(L + L_RED + ((tid >> 4) * 32 + (tid & 15) * 8) * 4) = v4f{a[0].x, a[0].y, a[1].x, a[1].y};
    *(LAS v4f*)(L + L_RED + ((tid >> 4) * 32 + (tid & 15) * 8 + 4) * 4) = v4f{a[2].x, a[2].y, a[3].x, a[3].y};
  }
}
DEV void mod_publish(char LAS* L, rsrc_t ws, int q, int lane, u32 tag) {
  v4f s = {0.f, 0.f, 0.f, 0.f};
#pragma unroll
  for (int r = 0; r < 16; ++r) s += lds4f(L, L_RED + (r * 32 + (lane & 7) * 4) * 4);
  const float one = (q / 14 == 1 || q / 14 == 4) ? 1.f : 0.f;
  st4c(ws, WS_MOD + (lane >> 3) * MOD_REP + q * 128 + (lane & 7) * 16,
       v4u{pk(rb(s.x) + one, rb(s.y) + one), tag, pk(rb(s.z) + one, rb(s.w) + one), tag});
}
DEV u32 ld1(rsrc_t r, int off) { return __builtin_amdgcn_raw_buffer_load_b32(r, off, 0, 0); }
DEV u32 mktag(u32 epoch) { return epoch * 0x9E3779B1u + 0x7F4A7C15u; }

DEV void x2_flush(char LAS* L, rsrc_t ws, int blk, int tid) {
#pragma unroll
  for (int m = 0; m < 4; ++m) {
    const int ch = min(tid + 256 * m, 895), tk = ch / 56;
    st4c(ws, tid + 256 * m < 896 ? WS_X2 + blk * 14336 + ch * 16 : OOB, lds4(L, L_XS + tk * XP + (ch - tk * 56) * 16));
  }
}
DEV bool flags_ok(v4u rv, u32 tag) { return rv.x == tag && rv.y == ~tag && rv.z == tag && rv.w == ~tag; }
DEV void wait_flags(rsrc_t ws, int off, u32 tag) {
  _Pragma("nounroll") for (int s = 0; s < SPIN; ++s) {
    const v2u rv = ld2c(ws, fresh(off));
    if (wall(rv.x == tag && rv.y == ~tag)) break;
    __builtin_amdgcn_s_sleep(SL1);
  }
}

__global__ void __launch_bounds__(256, 1)
fused(const u16* __restrict__ xin, const u16* __restrict__ wr, const u16* __restrict__ wi, const u16* __restrict__ wg,
      const u16* __restrict__ wu, const u16* __restrict__ wd, const u16* __restrict__ cin, const u16* __restrict__ wm,
      u16* __restrict__ out, char* wsp) {
  __shared__ __attribute__((aligned(16))) char smem[L_SZ];
  char LAS* L = (char LAS*)smem;
  const u32 lbase = (u32)(size_t)L;
  const int tid = threadIdx.x, lane = tid & 63, wave = __builtin_amdgcn_readfirstlane(tid >> 6);
  const int g = lane >> 4, c16 = lane & 15, l16 = lane * 16;
  const int grp = blockIdx.x & 7, j = blockIdx.x >> 3;
  u64 pc;
  asm volatile("s_getpc_b64 %0" : "=s"(pc));
  STAMP(0);
  asm volatile("" :: "s"(xin), "s"(wr), "s"(wi), "s"(wg), "s"(wu), "s"(wd), "s"(cin), "s"(wm), "s"(out), "s"(wsp));
  const rsrc_t ws = mkrs(wsp, WS_END);
  const rsrc_t tab = mkrs(TAB, TAB_N * 4096);
  const rsrc_t code = mkrs((const void*)(pc & ~127ull), CODE_BYTES);
  const bool isR = j >= 14 && j < 30;
  const bool isG = j < 28;
  const int gk = j / 7, ct = j - gk * 7;
  const int tile = 4 * (grp >> 2) + gk, slice = 7 * (grp & 3) + ct;
  const int wdb = WS_WD + (ct * 56 + wave * 14) * 4096;
  const v4u sv = {SENT, SENT, SENT, SENT};
  int lb = L_B + l16;
  pin(lb);
  const char LAS* LB = L + lb - L_B;
  const int pfo = ((j * 4 + wave) * 14 + lane) * 128, pfc = ((j * 4 + wave) * 4 + lane) * 128;
  u32 pfa = ld1(tab, lane < 14 ? pfo : OOB), pfb = ld1(code, lane < 4 ? pfc : OOB);
  v4u wdn[14][4];
  u32 epoch, tag;

  if (isR) {
    const int blk = 16 * grp + (j - 14);
    epoch = ld1c(ws, WS_EPOCH);
    const int t0 = wave == 0 ? 0 : 2 * wave - 1;
    v4u cf[8][4];
#define CF_LOAD(q) { const int chn = TAB_P1 + ((wave == 0 && q >= 4) ? 28 : 4 * t0 + q), vo = (q < 5 || wave != 0) ? l16 : OOB; \
      _Pragma("unroll") for (int s = 0; s < 4; ++s) cf[q][s] = ld4(tab, vo + s * 1024, chn * 4096); sb(); }
#pragma unroll
    for (int q = 0; q < X_AFTER; ++q) CF_LOAD(q)
    v2u xv[7];
    {
      const rsrc_t xr = mkrs(xin, S * D * 2);
      const int off = tok_row(blk, 4 * wave + g) * (D * 2) + c16 * 8;
#pragma unroll
      for (int m = 0; m < 7; ++m) xv[m] = ld2(xr, off + m * 128);
    }
    typedef u32 v3u __attribute__((ext_vector_type(3)));
    v2u fa_[2], fb_[2]; v3u fc_[2], fd_[2];
    {
      const rsrc_t rr = mkrs(wr, S * DF * 2), ri = mkrs(wi, S * DF * 2);
      const int fo_ = 16 * wave + c16 < 57 ? 2 * g * 450 + (16 * wave + c16) * 8 : OOB;
#pragma unroll
      for (int hh = 0; hh < 2; ++hh) {
        const int so = blk * 7200 + hh * 3600;
        fa_[hh] = ld2(rr, fo_, so); fb_[hh] = ld2(ri, fo_, so);
        fc_[hh] = __builtin_amdgcn_raw_buffer_load_b96(rr, fo_ + 448, so, 0); fd_[hh] = __builtin_amdgcn_raw_buffer_load_b96(ri, fo_ + 448, so, 0);
      }
    }
    sb();
#pragma unroll
    for (int q = X_AFTER; q < 8; ++q) CF_LOAD(q)
    STAMP(23);
    {
      v2f f[14], s2 = {0.f, 0.f};
#pragma unroll
      for (int m = 0; m < 7; ++m) {
        f[2 * m] = bun(xv[m].x); f[2 * m + 1] = bun(xv[m].y);
        s2 += f[2 * m] + f[2 * m + 1];
        *(LAS v2u*)(L + L_XS + (4 * wave + g) * XP + c16 * 8 + m * 128) = xv[m];
      }
      STAMP(21);
      sb();
      {
        const int q = 16 * wave + c16, o = WS_WT + q * 16384 + blk * 128 + g * 16;
#pragma unroll
        for (int hh = 0; hh < 2; ++hh) {
          const v2u a = fa_[hh], b = fb_[hh]; const v3u c = fc_[hh], d = fd_[hh];
          st4c(ws, q < 57 ? o + hh * 64 : OOB, v4u{__builtin_amdgcn_perm(b.x, a.x, 0x05040100u), __builtin_amdgcn_perm(b.y, a.y, 0x05040100u),
                                                   __builtin_amdgcn_perm(d.x, c.x, 0x07060302u), __builtin_amdgcn_perm(d.y, c.y, 0x07060302u)});
          st4c(ws, q < 56 ? o + hh * 64 + 57 * 16384 : OOB,
               v4u{__builtin_amdgcn_perm(b.x, a.x, 0x07060302u), __builtin_amdgcn_perm(b.y, a.y, 0x07060302u),
                   __builtin_amdgcn_perm(d.y, c.y, 0x05040100u), __builtin_amdgcn_perm(d.z, c.z, 0x05040100u)});
        }
      }
      sb();
      const float mean = sum16(s2.x + s2.y) * (1.f / D);
      v2f q2 = {0.f, 0.f};
#pragma unroll
      for (int m = 0; m < 14; ++m) { const v2f d = f[m] - mean; q2 = fma2(d, d, q2); }
      const float rstd = __builtin_amdgcn_rsqf(sum16(q2.x + q2.y) * (1.f / D) + 1e-5f);
      if (c16 == 0) *(LAS v2f*)(L + L_ST + (4 * wave + g) * 8) = v2f{mean, rstd};
    }
    pin(pfa); pin(pfb);
    epoch = __builtin_amdgcn_readfirstlane(epoch);
    tag = mktag(epoch);
    STAMP(22);
    bar();
    v4f n[4][2];
    const int d0 = 8 * (4 * wave + g);
    const v2f st = *(const LAS v2f*)(L + L_ST + c16 * 8);
    {
      int xr_ = L_XS + c16 * XP + d0 * 2, xb_ = L_XS + c16 * XP + (216 - d0) * 2, x0_ = d0 == 0 ? xr_ : xb_ + 464;
      pin(xr_); pin(xb_); pin(x0_);
      const v4u a = lds4(L + xr_, 0), e = lds4(L + xr_, 448), c = lds4(L + xb_, 0), b = lds4(L + xb_, 448);
      const u32 c0 = *(const LAS u16*)(L + xb_ + 16), b0 = *(const LAS u16*)(L + x0_);
#pragma unroll
      for (int i = 0; i < 8; ++i) {
        n[0][i >> 2][i & 3] = (i & 1) ? bhi(a[i >> 1]) : blo(a[i >> 1]);
        n[3][i >> 2][i & 3] = (i & 1) ? bhi(e[i >> 1]) : blo(e[i >> 1]);
        n[1][i >> 2][i & 3] = i == 0 ? blo(b0) : (i & 1) ? bhi(b[(8 - i) >> 1]) : blo(b[(8 - i) >> 1]);
        n[2][i >> 2][i & 3] = i == 0 ? blo(c0) : (i & 1) ? bhi(c[(8 - i) >> 1]) : blo(c[(8 - i) >> 1]);
      }
      const float nm = -st.x * st.y;
#pragma unroll
      for (int m = 0; m < 4; ++m) {
#pragma unroll
        for (int h = 0; h < 2; ++h) { n[m][h] = fma4(n[m][h], splat(st.y), splat(nm)); pin(n[m][h]); }
      }
    }
    sb();
    STAMP(1);
    int mb = L_M1 + d0 * 4;
    pin(mb);
    v4u xs[4];
    int to[4];
    to[0] = WS_T1 + blk * T1_BLK + (16 * t0 + c16) * 64 + g * 16;
    to[1] = wave != 0 ? to[0] + 1024 : c16 < 2 ? to[0] + 112 * 64 : OOB;
    to[2] = to[0] + 114 * 64;
    to[3] = wave != 0 ? to[0] + 1024 + 114 * 64 : OOB;
#pragma unroll
    for (int q = 0; q < 4; ++q) pin(to[q]);

    {
      const int kind = tid >= 112, d = 4 * (tid - 112 * kind);
      int wb = L_M1 + (kind ? 0 : 2 * 1792) + d * 4, wrv = L_M1 + (kind ? 0 : 2 * 1792) + 1792 + (445 - d) * 4;
      int wr0 = d == 0 ? wrv + 12 - 1792 : wrv + 12;
      const int mo = tid < 224 ? WS_MOD + grp * MOD_REP + tid * 16 : OOB;
      pin(wb); pin(wrv); pin(wr0);
      const int ro = WS_RDY + grp * 1024 + min(tid >> 1, 112) * 8;
      v4u mv;
      _Pragma("nounroll") for (int s = 0; s < SPIN; ++s) {
        mv = ld4c(ws, fresh(mo));
        const v2u rv = ld2c(ws, fresh(ro));
        if (wall((tid >= 224 || (mv.y == tag && mv.w == tag)) && rv.x == tag && rv.y == ~tag)) break;
        __builtin_amdgcn_s_sleep(SL1);
      }
      STAMP(2);
      if (tid < 224) {
        const v4f v = {blo(mv.x), bhi(mv.x), blo(mv.z), bhi(mv.z)};
        *(LAS v4f*)(L + wb) = v;
        *(LAS float*)(L + wr0) = v.x;
        *(LAS float*)(L + wrv + 8) = v.y;
        *(LAS float*)(L + wrv + 4) = v.z;
        *(LAS float*)(L + wrv) = v.w;
      }
    }
    bar();
    {
      v4u fr[4];
#pragma unroll
      for (int hh = 0; hh < 2; ++hh) {
        v4f h[4];
#pragma unroll
        for (int m = 0; m < 4; ++m) {
          const int o = ((m == 1 || m == 2) ? 1792 : 0) + (m >= 2 ? 896 : 0) + hh * 16;
          h[m] = fma4(n[m][hh], lds4f(L + mb, o), lds4f(L + mb, 2 * 1792 + o));
        }
        const v4f p = h[0] + h[1], q = h[2] + h[3], u = h[0] - h[1], v = h[3] - h[2];
        const v4f t[4] = {p + q, p - q, u + v, u - v};
#pragma unroll
        for (int m = 0; m < 4; ++m) { fr[m][2 * hh] = pk2(lo2(t[m])); fr[m][2 * hh + 1] = pk2(hi2(t[m])); }
      }
#pragma unroll
      for (int m = 0; m < 4; ++m) *(LAS v4u*)(LB + wave * 1024 + L_AF + m * 4096) = fr[m];
    }
    bar();
    {
      v4u a[4][4];
#pragma unroll
      for (int p = 0; p < 2; ++p)
#pragma unroll
        for (int s = 0; s < 4; ++s) { a[p][s] = lds4(LB, L_AF + (p * 4 + s) * 1024); a[2 + p][s] = lds4(LB, L_AF + ((2 + p) * 4 + s) * 1024); }
      v4f re[4], im[4];
#pragma unroll
      for (int q = 0; q < 4; ++q) {
        const int p = q >> 1, t = q & 1;
        mf0(re[q], a[p][0], cf[4 * t + p][0]); mf0(im[q], a[2 + p][0], cf[4 * t + 2 + p][0]);
        if (q > 0) {
          mfence<1>(re[q - 1], im[q - 1]);
          xs[q - 1] = v4u{pk(re[q - 1].x, im[q - 1].x), pk(re[q - 1].y, im[q - 1].y), pk(re[q - 1].z, im[q - 1].z), pk(re[q - 1].w, im[q - 1].w)};
          st4c(ws, to[q - 1], xs[q - 1]);
          sb();
        }
#pragma unroll
        for (int s = 1; s < 4; ++s) { mfa(re[q], a[p][s], cf[4 * t + p][s]); mfa(im[q], a[2 + p][s], cf[4 * t + 2 + p][s]); }
      }
      mfence<7>(re[3], im[3]);
      xs[3] = v4u{pk(re[3].x, im[3].x), pk(re[3].y, im[3].y), pk(re[3].z, im[3].z), pk(re[3].w, im[3].w)};
      st4c(ws, to[3], xs[3]);
    }
    STAMP(3);
    sb();
    sb();
    if (isG) dma_w1(wg, wu, slice, wave, lane, lbase);
#pragma unroll
    for (int q = 0; q < 4; ++q) {
      const int vo = (q < 2 || wave != 0) ? l16 : OOB;
#pragma unroll
      for (int s = 0; s < 4; ++s) cf[2 * q + 1][s] = ld4(tab, vo + s * 1024, (TAB_P3 + 2 * t0 + q) * 4096);
    }
#pragma unroll
    for (int m = 0; m < 4; ++m)
      st4c(ws, tid + 256 * m < 896 ? WS_H2 + blk * 14336 + 4096 * m + tid * 16 : OOB, sv);
    sb();
    STAMP(20);
    v4f xo[2][4];
    int xa = L_XS + c16 * XP + (16 * t0 + 4 * g) * 2, xd = L_XS + c16 * XP + (192 - 16 * t0 - 4 * g) * 2;
    int xs1 = tid < 16 ? L_XS + c16 * XP + 224 : xd + 512, xs3 = tid < 16 ? L_XS + c16 * XP + 672 : xa + 448;
    pin(xa); pin(xd); pin(xs1); pin(xs3);
#define XOFF(t, m, r) ((t) == 0 && (r) == 0 && (m) == 1 ? xs1 : (t) == 0 && (r) == 0 && (m) == 3 ? xs3 : \
    (m) == 0 ? xa + 32 * (t) + 2 * (r) : (m) == 3 ? xa + 448 + 32 * (t) + 2 * (r) : (m) == 1 ? xd + 512 - 32 * (t) - 2 * (r) : xd + 64 - 32 * (t) - 2 * (r))
    {
      v2u w[2][4]; u32 e[2][2], e3;
#pragma unroll
      for (int t = 0; t < 2; ++t) {
        w[t][0] = *(const LAS v2u*)(L + xa + 32 * t); w[t][3] = *(const LAS v2u*)(L + xa + 448 + 32 * t);
        w[t][1] = *(const LAS v2u*)(L + xd + 504 - 32 * t); w[t][2] = *(const LAS v2u*)(L + xd + 56 - 32 * t);
        e[t][0] = *(const LAS u16*)(L + XOFF(t, 1, 0)); e[t][1] = *(const LAS u16*)(L + XOFF(t, 2, 0));
      }
      e3 = *(const LAS u16*)(L + xs3);
      sb();
#pragma unroll
      for (int t = 0; t < 2; ++t) {
        xo[t][0] = v4f{blo(w[t][0].x), bhi(w[t][0].x), blo(w[t][0].y), bhi(w[t][0].y)};
        xo[t][3] = v4f{t == 0 ? blo(e3) : blo(w[t][3].x), bhi(w[t][3].x), blo(w[t][3].y), bhi(w[t][3].y)};
        xo[t][1] = v4f{blo(e[t][0]), bhi(w[t][1].y), blo(w[t][1].y), bhi(w[t][1].x)};
        xo[t][2] = v4f{blo(e[t][1]), bhi(w[t][2].y), blo(w[t][2].y), bhi(w[t][2].x)};
#pragma unroll
        for (int m = 0; m < 4; ++m) { xo[t][m] -= st.x; pin(xo[t][m]); }
      }
    }
    int m0_[2], m1_[2], md_[2];
#pragma unroll
    for (int r = 0; r < 2; ++r) {
      const int q = r == 0 || tid < 80 ? tid + 256 * r : tid;
      const int kind = (q * 586) >> 16, d = 4 * (q - 112 * kind);
      const int reg = (d >= 112) + (d >= 224) + (d >= 336), cc = ((reg + 1) >> 1) * 224, m1 = (0x1320 >> (4 * reg)) & 3;
      const int d1 = abs(d + 1 - cc), d0_ = abs(d - cc), edge = d == 112 * reg && reg > 0;
      m1_[r] = L_M2 + kind * 1792 + (((d1 >> 4) << 6) | (m1 << 4) | (d1 & 15)) * 4;
      m0_[r] = L_M2 + kind * 1792 + (edge ? reg << 4 : ((d0_ >> 4) << 6) | (m1 << 4) | (d0_ & 15)) * 4;
      md_[r] = (reg & 1) ? -4 : 4;
      pin(m0_[r]); pin(m1_[r]); pin(md_[r]);
    }
    {
      v4u mv[2];
      _Pragma("nounroll") for (int s = 0; s < SPIN; ++s) {
        mv[0] = ld4c(ws, fresh(WS_MOD + grp * MOD_REP + (224 + tid) * 16));
        mv[1] = ld4c(ws, fresh(WS_MOD + grp * MOD_REP + (224 + (tid < 80 ? 256 + tid : tid)) * 16));
        const v2u rv = ld2c(ws, fresh(WS_DRDY + grp * 1024 + min(lane, 48) * 8));
        if (wall(mv[0].y == tag && mv[0].w == tag && mv[1].y == tag && mv[1].w == tag && rv.x == tag && rv.y == ~tag)) break;
        __builtin_amdgcn_s_sleep(SL4);
      }
      if (lane == 0) st2c(ws, WS_HRDY + blk * 32 + wave * 8, v2u{tag, ~tag});
      STAMP(12);
#pragma unroll
      for (int f = 0; f < WD_MID; ++f) wdn[f >> 2][f & 3] = ld4c(ws, (isG ? l16 : OOB) + (f & 3) * 1024, wdb + (f >> 2) * 4096);
      sb();
#pragma unroll
      for (int r = 0; r < 2; ++r) {
        const float sc = m0_[r] < L_M2 + 1792 ? 2.f / (float)(S * D) : 1.f;
        *(LAS float*)(L + m0_[r]) = blo(mv[r].x) * sc;
        *(LAS float*)(L + m1_[r]) = bhi(mv[r].x) * sc;
        *(LAS float*)(L + m1_[r] + md_[r]) = blo(mv[r].z) * sc;
        *(LAS float*)(L + m1_[r] + 2 * md_[r]) = bhi(mv[r].z) * sc;
      }
    }
    bar();
    *(LAS v4u*)(L + L_AF + (tid >> 6) * 4096 + 3072 + l16) = v4u{0u, 0u, 0u, 0u};
    STAMP(4);
    int zr = L_AF + lane * 8, ro = L_M2 + (t0 * 64 + 4 * g) * 4, ro1 = wave == 0 ? ro : ro + 256, ex = L_EX + c16 * 8;
    pin(zr); pin(ro); pin(ro1); pin(ex);
    {
      int zl[4];
#pragma unroll
      for (int m = 0; m < 4; ++m) {
        const int kq = (m >> 1) * 128 + (to[m] == OOB ? 127 : wave == 0 && m == 1 ? 112 + c16 : 16 * (t0 + (m & 1)) + c16);
        zl[m] = L_AF + ((kq & ~31) + ((kq & 4) << 2) + ((kq & 24) >> 1) + (kq & 3)) * 32 + g * 8;
        pin(zl[m]);
        if (to[m] == OOB) xs[m] = sv;
      }
      bar();
      if (isG) wait_flags(ws, WS_HRDY + (16 * tile + 4 * wave) * 32 + c16 * 8, tag);
      {
        const int src = (blk * 5 + wave * 13) & 63;
        const int po = __builtin_amdgcn_readlane(to[(0)], 0) + (src & 15) * 64 + (src >> 4) * 16;
        const u32 old = __builtin_amdgcn_ds_bpermute(src * 4, xs[0].x);
        _Pragma("nounroll") for (int s = 0; s < SPIN; ++s) {
          if (wall(ld1c(ws, fresh(po)) != old)) break;
          __builtin_amdgcn_s_sleep(SL2);
        }
      }
      STAMP(13);
      v4u z[4];
      _Pragma("nounroll") for (int s = 0; s < SPIN; ++s) {
        u32 mn = 1;
#pragma unroll
        for (int m = 0; m < 4; ++m) z[m] = ld4c(ws, fresh(to[m]));
#pragma unroll
        for (int m = 0; m < 4; ++m) { const v4u d = z[m] ^ xs[m]; mn = min(min(mn, d.x), min(min(d.y, d.z), d.w)); }
        if (wall(mn != 0)) break;
      }
      STAMP(14);
#pragma unroll
      for (int m = 0; m < 4; ++m) {
        char LAS* p = L + zl[m];
        *(LAS v2u*)(p) = v2u{__builtin_amdgcn_perm(z[m].y, z[m].x, 0x05040100u), __builtin_amdgcn_perm(z[m].w, z[m].z, 0x05040100u)};
        *(LAS v2u*)(p + ZPL) = v2u{__builtin_amdgcn_perm(z[m].y, z[m].x, 0x07060302u), __builtin_amdgcn_perm(z[m].w, z[m].z, 0x07060302u)};
      }
    }
    bar();
    STAMP(5);
    {
      v4u zf[4][4];
#pragma unroll
      for (int ty = 0; ty < 4; ++ty)
#pragma unroll
        for (int s = 0; s < 4; ++s) zf[ty][s] = ldtr2(L + zr, ty * 4096 + s * 1024, ty * 4096 + s * 1024 + 512);
      v4f acc[2][4], x2[2][4], gav[2][4];
      v2f s1 = {0.f, 0.f}, s2 = {0.f, 0.f};
#define P3M(t, ty) { mf0(acc[t][ty], cf[4 * t + ty][0], zf[ty][0]); \
      _Pragma("unroll") for (int s = 1; s < 4; ++s) mfa(acc[t][ty], cf[4 * t + ty][s], zf[ty][s]); }
#define P3X(t, m, yv) { x2[t][m] = fma4(gav[t][m], yv, xo[t][m]); \
      if (t == 0 || wave != 0) { s1 += lo2(x2[t][m]) + hi2(x2[t][m]); \
        s2 = fma2(lo2(x2[t][m]), lo2(x2[t][m]), s2); s2 = fma2(hi2(x2[t][m]), hi2(x2[t][m]), s2); } }
      P3M(1, 0) P3M(1, 1) P3M(1, 2) P3M(1, 3)
#pragma unroll
      for (int m = 0; m < 4; ++m) { gav[0][m] = lds4f(L + ro, m * 64); gav[1][m] = lds4f(L + ro1, m * 64); }
      sb();
      P3M(0, 0)
      mfence<0>(acc[1][0], acc[1][1], acc[1][2], acc[1][3]);
      const v4f A1 = acc[1][0] + acc[1][1], B1 = acc[1][0] - acc[1][1], P1 = acc[1][3] - acc[1][2], Q1 = acc[1][2] + acc[1][3];
      sb();
      P3M(0, 1)
      P3X(1, 0, A1 - P1) P3X(1, 1, A1 + P1)
      sb();
      P3M(0, 2)
      P3X(1, 2, B1 - Q1) P3X(1, 3, B1 + Q1)
      sb();
      P3M(0, 3)
      mfence<7>(acc[0][0], acc[0][1], acc[0][2], acc[0][3]);
      {
        const v4f A = acc[0][0] + acc[0][1], B = acc[0][0] - acc[0][1], P = acc[0][3] - acc[0][2], Q = acc[0][2] + acc[0][3];
        v4f y[4] = {A - P, A + P, B - Q, B + Q};
        const bool sp = tid < 16;
        y[0].x = sp ? A.x : y[0].x; y[2].x = sp ? B.x : y[2].x;
        y[1].x = sp ? acc[1][0].x - acc[0][3].x : y[1].x; y[3].x = sp ? acc[1][0].x + acc[0][3].x : y[3].x;
        P3X(0, 0, y[0]) P3X(0, 1, y[1]) P3X(0, 2, y[2]) P3X(0, 3, y[3])
      }
      float sm = s1.x + s1.y, sq = s2.x + s2.y;
      sumg2(sm, sq);
      if (g == 0) *(LAS v2f*)(L + ex + wave * 128) = v2f{sm, sq};
      v4f sbv[2][4], cbv[2][4];
#pragma unroll
      for (int m = 0; m < 4; ++m) {
        sbv[0][m] = lds4f(L + ro, 1792 + m * 64); cbv[0][m] = lds4f(L + ro, 2 * 1792 + m * 64);
        sbv[1][m] = lds4f(L + ro1, 1792 + m * 64); cbv[1][m] = lds4f(L + ro1, 2 * 1792 + m * 64);
      }
      bar();
      v2f e = *(const LAS v2f*)(L + ex);
#pragma unroll
      for (int w = 1; w < 4; ++w) e += *(const LAS v2f*)(L + ex + w * 128);
      const float mc = e.x * (1.f / D), rstd = __builtin_amdgcn_rsqf(fmaxf(e.y * (1.f / D) - mc * mc, 0.f) + 1e-5f), nb = -mc * rstd;
      const int ho = WS_H2 + blk * 14336 + 2 * t0 * 1024 + l16, ho1 = wave == 0 ? OOB : ho + 2048;
#pragma unroll
      for (int t = 0; t < 2; ++t)
#pragma unroll
        for (int a = 0; a < 2; ++a) {
          v4u o;
#pragma unroll
          for (int hh = 0; hh < 2; ++hh) {
            const int m = 2 * a + hh;
            const v4f hv = fma4(fma4(x2[t][m], splat(rstd), splat(nb)), cbv[t][m], sbv[t][m]);
            o[2 * hh] = pk2(lo2(hv)); o[2 * hh + 1] = pk2(hi2(hv));
          }
          st4c(ws, (t ? ho1 : ho) + a * 1024, o);
        }
      STAMP(6);
      sb();
#pragma unroll
      for (int t = 0; t < 2; ++t)
        if (t == 0 || wave != 0)
#pragma unroll
          for (int m = 0; m < 4; ++m)
#pragma unroll
            for (int r = 0; r < 4; r += 2) {
              const u32 p = pk(x2[t][m][r] + st.x, x2[t][m][r + 1] + st.x);
              *(LAS u16*)(L + XOFF(t, m, r)) = (u16)p;
              *(LAS u16*)(L + XOFF(t, m, r + 1)) = (u16)(p >> 16);
            }
      sb();
    }
    bar();
    if (!isG) {
      x2_flush(L, ws, blk, tid);
      drain();
      if (lane == 0) st2c(ws, WS_XRDY + blk * 64 + wave * 8, v2u{tag, ~tag});
      DUMP(); return;
    }
  } else {
    const int sj = j >= 30 ? j - 30 : j + 2;
    const int idx = sj * 8 + grp;
    const bool isC = j < 14 || idx == 0;
    const int pair = j < 14 ? grp * 14 + j : 112;
    const int lc = grp + 8 * (sj >> 1);
    const int qu = sj < 4 && lc < 14 ? 2 * lc + (sj & 1) : -1;
    const int t1o = WS_T1 + (32 * wave + (lane >> 3)) * T1_BLK + pair * 128 + (lane & 7) * 16;
    ModLoad ml;
    epoch = ld1c(ws, WS_EPOCH);
    if (qu >= 0) mod_load(ml, cin, wm, qu, tid);
#pragma unroll
    for (int q = 0; q < 4; ++q) st4c(ws, isC ? t1o + 8 * q * T1_BLK : OOB, sv);
    u32 pf0, pf1, pf2;
    {
      const rsrc_t mr = mkrs(wm, D * 6 * D * 2), xr = mkrs(xin, S * D * 2), fr_ = mkrs((wave & 1) ? wi : wr, S * DF * 2);
      const int row = (sj * 4 + wave) * 7 + (lane >> 1), l7 = lane / 7;
      pf0 = __builtin_amdgcn_raw_buffer_load_b32(mr, lane < 14 && (grp < 6 || !(lane & 1)) ? row * (6 * D * 2) + (grp + 8 * (lane & 1)) * 128 : OOB, 0, PF_AUX);
      pf1 = __builtin_amdgcn_raw_buffer_load_b32(xr, lane < 28 ? tok_row(16 * grp + sj, 4 * wave + l7) * (D * 2) + (lane - 7 * l7) * 128 : OOB, 0, PF_AUX);
    }
    sb();
    epoch = __builtin_amdgcn_readfirstlane(epoch);
    tag = mktag(epoch);
    if (qu >= 0) mod_part(ml, L, tid);
    asm volatile("s_waitcnt vmcnt(%0)" :: "i"(W_LATE ? 2 : 3) : "memory");
    { const rsrc_t fr_ = mkrs((wave & 1) ? wi : wr, S * DF * 2);
      pf2 = __builtin_amdgcn_raw_buffer_load_b32(fr_, lane < 29 ? (((16 * grp + sj) * 7200) & ~127) + ((wave >> 1) * 29 + lane) * 128 : OOB, 0, PF_AUX);
    }
    bar();
    if (wave == 0 && qu >= 0) mod_publish(L, ws, qu, lane, tag);
    if (wave == 1 && lane < 8) st2c(ws, isC ? WS_RDY + lane * 1024 + pair * 8 : OOB, v2u{tag, ~tag});
    STAMP(1);
    pin(pf0); pin(pf1); pin(pf2); pin(pfa); pin(pfb);
    const int qn = sj >= 4 && sj < 10 && grp < 7 ? 28 + grp * 6 + (sj - 4) : !isC && idx < 15 ? 69 + idx : -1;
    const int tw = sj >= 10 ? (sj - 10) * 8 + grp : sj == 4 && grp == 7 ? 48 : -1;
    v2u dw[2][8];
    if (qn >= 0) mod_load(ml, cin, wm, qn, tid);
    if (tw >= 0) {
      const rsrc_t dr = mkrs(wd, F * D * 2);
      const int u0 = tw * 4 + wave, uc = u0 / 28, kap = 2 * (u0 - uc * 28);
#pragma unroll
      for (int e = 0; e < 16; ++e)
        dw[e >> 3][e & 7] = __builtin_amdgcn_raw_buffer_load_b64(dr, (4 * g * D + 4 * c16) * 2 + (e & 3) * D * 2, ((32 * kap + 16 * (e >> 2)) * D + 64 * uc) * 2, PF_AUX);
    }
    sb();
    {
      const rsrc_t rg = mkrs(wg, D * F * 2), ru = mkrs(wu, D * F * 2);
      const int r7 = lane / 7, po = lane < 56 && j < 14 ? ((j * 4 + wave) * 8 + r7) * (F * 2) + ((grp & 3) * 7 + lane - r7 * 7) * 128 : OOB;
      pf0 = ld1(rg, po); pf1 = ld1(ru, po);
    }
    sb();
    STAMP(20);
    if (tw >= 0) {
#pragma unroll
      for (int tt = 0; tt < 8; ++tt) {
        v4u o;
#pragma unroll
        for (int m = 0; m < 4; ++m)
          o[m] = __builtin_amdgcn_perm(dw[tt >> 2][2 * m + 1][(tt & 3) >> 1], dw[tt >> 2][2 * m][(tt & 3) >> 1], (tt & 1) ? 0x07060302u : 0x05040100u);
        st4c(ws, WS_WD + (tw * 4 + wave) * 8192 + tt * 1024 + l16, o);
      }
    }
    if (qn >= 0) {
      bar();
      mod_part(ml, L, tid);
      bar();
      if (wave == 0) mod_publish(L, ws, qn, lane, tag);
    }
    STAMP(21);
    if (!isC) { DUMP(); return; }
    v4u c64[2][4], c32[2][2][2];
#pragma unroll
    for (int cp = 0; cp < 2; ++cp)
#pragma unroll
      for (int s = 0; s < 4; ++s) c64[cp][s] = ld4(tab, l16 + s * 1024, (TAB_P2 + wave * 2 + cp) * 4096);
#pragma unroll
    for (int t = 0; t < 2; ++t)
#pragma unroll
      for (int f = 0; f < 4; ++f) c32[t][f >> 1][f & 1] = ld4(tab, l16 + f * 1024, (TAB_P2 + 8 + t) * 4096);
    sb();
    if (tw >= 0) {
      asm volatile("s_waitcnt vmcnt(16)" ::: "memory");
      bar();
      if (tid < 8) st2c(ws, WS_DRDY + tid * 1024 + tw * 8, v2u{tag, ~tag});
    }
    v4f t1c[2], t1s[2], t2c[2], t2s[2];
#pragma unroll
    for (int t = 0; t < 2; ++t) {
#pragma unroll
      for (int r = 0; r < 4; ++r) {
        const float a1 = (float)((16 * t + 4 * g + r) * (16 * wave + c16)) * (1.f / S);
        const float a2 = (float)((16 * t + c16) * (16 * wave + 4 * g + r)) * (1.f / S);
        t1c[t][r] = __builtin_amdgcn_cosf(a1); t1s[t][r] = __builtin_amdgcn_sinf(a1);
        t2c[t][r] = __builtin_amdgcn_cosf(a2); t2s[t][r] = -__builtin_amdgcn_sinf(a2);
      }
      pin(t1c[t]); pin(t1s[t]); pin(t2c[t]); pin(t2s[t]);
    }
    STAMP(2);
    int wfo = WS_WT + pair * 16384 + (16 * g + wave) * 128 + c16 * 8;
    pin(wfo);
    v2u wf[2][4];
    {
      int wl = L_CI + ((lane >> 2) & 1) * 8256 + wave * 2048 + ((lane >> 3) * 4 + (lane & 3)) * 16, lo = t1o;
      pin(wl); pin(lo);
      light_poll(ws, WS_T1 + (32 * wave + (pair & 31)) * T1_BLK + pair * 128);
      pin(pf0); pin(pf1);
      STAMP(13);
      v4u z[4];
      _Pragma("nounroll") for (int s = 0; s < SPIN; ++s) {
        lo = fresh(lo);
#pragma unroll
        for (int q = 0; q < 4; ++q) z[q] = ld4c(ws, lo, 8 * q * T1_BLK);
        if (wall(max(max(max4(z[0]), max4(z[1])), max(max4(z[2]), max4(z[3]))) != SENT)) break;
      }
      STAMP(14);
#pragma unroll
      for (int q = 0; q < 4; ++q) *(LAS v4u*)(L + wl + q * 512) = z[q];
#pragma unroll
      for (int t = 0; t < 2; ++t)
#pragma unroll
        for (int r = 0; r < 4; ++r) wf[t][r] = ld2c(ws, wfo + r * 512, t * 8192);
    }
    bar();
    STAMP(4);
    {
      v4u a[2][2][4];
#pragma unroll
      for (int c = 0; c < 2; ++c)
#pragma unroll
        for (int mt = 0; mt < 2; ++mt)
#pragma unroll
          for (int s = 0; s < 4; ++s) a[c][mt][s] = lds4(LB, L_CI + c * 8256 + (s * 2 + mt) * 1024);
      v4u old[2][2];
#pragma unroll
      for (int c = 0; c < 2; ++c)
#pragma unroll
        for (int mt = 0; mt < 2; ++mt) old[c][mt] = lds4(LB + wave * 2048, L_CI + c * 8256 + mt * 1024);
      v4f re[2][2], im[2][2];
      v4u f2[2][2], f3[2][2];
      char LAS* xw = L + lb + wave * 1024 - L_B;
#define S1H(c, mt) { mf0(re[c][mt], a[c][mt][0], c64[0][0]); mf0(im[c][mt], a[c][mt][0], c64[1][0]); }
#define S1T(c, mt) { _Pragma("unroll") for (int s = 1; s < 4; ++s) { mfa(re[c][mt], a[c][mt][s], c64[0][s]); mfa(im[c][mt], a[c][mt][s], c64[1][s]); } }
#define S2(c, t) { mf0(re[c][t], c32[t][0][0], f2[c][0]); mf0(im[c][t], c32[t][1][0], f2[c][0]); \
      mfa(re[c][t], c32[t][0][1], f2[c][1]); mfa(im[c][t], c32[t][1][1], f2[c][1]); }
#define S3(c, t) { mf0(im[c][t], f3[c][0], c32[t][0][0]); mf0(re[c][t], f3[c][0], c32[t][1][0]); \
      mfa(im[c][t], f3[c][1], c32[t][0][1]); mfa(re[c][t], f3[c][1], c32[t][1][1]); }
#define TW1(c, t) { f2[c][t] = cmul_pack<false>(re[c][t], im[c][t], t1c[t], t1s[t]); pin(f2[c][t]); }
#define FLT(c, t) { const v4f fr_ = {blo(wf[t][0][c]), blo(wf[t][1][c]), blo(wf[t][2][c]), blo(wf[t][3][c])}, \
      fi_ = {bhi(wf[t][0][c]), bhi(wf[t][1][c]), bhi(wf[t][2][c]), bhi(wf[t][3][c])}; \
      f3[c][t] = cmul_pack<true>(re[c][t], im[c][t], fr_, -fi_); pin(f3[c][t]); }
#define TW2(c, t) { *(LAS v4u*)(xw + L_CX + (c * 2 + t) * 4096) = cmul_pack<true>(re[c][t], im[c][t], t2c[t], t2s[t]); }
#define F2(c, t) mfence<0>(re[c][t], im[c][t]);
      S1H(0, 0) S1T(0, 0) S1H(0, 1) S1T(0, 1) sb();
      F2(0, 0) S1H(1, 0) S1T(1, 0) TW1(0, 0) sb();
      F2(0, 1) S1H(1, 1) S1T(1, 1) TW1(0, 1) sb();
      F2(1, 0) S2(0, 0) S2(0, 1) TW1(1, 0) sb();
      F2(1, 1) TW1(1, 1) sb();
      F2(0, 0) S2(1, 0) FLT(0, 0) sb();
      F2(0, 1) S2(1, 1) FLT(0, 1) sb();
      F2(1, 0) S3(0, 0) FLT(1, 0) sb();
      F2(1, 1) S3(0, 1) FLT(1, 1) sb();
      F2(0, 0) S3(1, 0) TW2(0, 0) sb();
      F2(0, 1) S3(1, 1) TW2(0, 1) sb();
      F2(1, 0) TW2(1, 0) sb();
      F2(1, 1) TW2(1, 1)
      bar();
#pragma unroll
      for (int c = 0; c < 2; ++c)
#pragma unroll
        for (int nt = 0; nt < 2; ++nt)
#pragma unroll
          for (int s = 0; s < 4; ++s) a[c][nt][s] = lds4(LB, L_CX + ((c * 2 + nt) * 4 + s) * 1024);
      const int o2 = WS_T1 + (32 * wave + (lane >> 2)) * T1_BLK + pair * 128 + (lane & 3) * 16;
#define S4H(c, t) { mf0(im[c][t], c64[0][0], a[c][t][0]); mf0(re[c][t], c64[1][0], a[c][t][0]); }
#define S4T(c, t) { _Pragma("unroll") for (int s = 1; s < 4; ++s) { mfa(im[c][t], c64[0][s], a[c][t][s]); mfa(re[c][t], c64[1][s], a[c][t][s]); } }
#define NEQ(x, y) ((x) == (y) ? (x) ^ 1u : (x))
#define OUT(c, t) { const v4u o = {pk(re[c][t].x, im[c][t].x), pk(re[c][t].y, im[c][t].y), pk(re[c][t].z, im[c][t].z), pk(re[c][t].w, im[c][t].w)}; \
      st4c(ws, o2 + c * 64 + t * 16 * T1_BLK, v4u{NEQ(o.x, old[c][t].x), NEQ(o.y, old[c][t].y), NEQ(o.z, old[c][t].z), NEQ(o.w, old[c][t].w)}); }
      S4H(0, 0) S4T(0, 0) S4H(0, 1) mfence<1>(re[0][0], im[0][0]); OUT(0, 0) sb();
      S4T(0, 1) S4H(1, 0) mfence<1>(re[0][1], im[0][1]); OUT(0, 1) sb();
      S4T(1, 0) S4H(1, 1) mfence<1>(re[1][0], im[1][0]); OUT(1, 0) sb();
      S4T(1, 1) mfence<7>(re[1][1], im[1][1]); OUT(1, 1)
    }
    STAMP(5);
    sb();
    if (!isG) { DUMP(); return; }
    dma_w1(wg, wu, slice, wave, lane, lbase);
    wait_flags(ws, WS_DRDY + grp * 1024 + min(lane, 48) * 8, tag);
#pragma unroll
    for (int f = 0; f < WD_MID; ++f) wdn[f >> 2][f & 3] = ld4c(ws, l16 + (f & 3) * 1024, wdb + (f >> 2) * 4096);
    sb();
    wait_flags(ws, WS_HRDY + (16 * tile + 4 * wave) * 32 + c16 * 8, tag);
    bar();
  }

  STAMP(7);
  const int blk0 = 16 * tile + 4 * (grp & 3);
  const int ab = WS_ACT + blk0 * 57344 + wave * 14 * 1024;
  const int fo = WS_ARDY + (tile * 4 + (grp & 3)) * 512 + (7 * wave + min(lane, 6)) * 8;
  v4u h[14][2];
  {
    const int hb = WS_H2 + (16 * tile + 4 * wave) * 14336;
    const int fw_ = WS_ARDY + (tile * 4 + wave) * 512 + slice * 8;
    int tb[2][4];
#pragma unroll
    for (int u = 0; u < 8; ++u) {
      tb[u >> 2][u & 3] = (((lane >> 2) * 128 + ((lane >> 3) & 3) * 32 + (lane & 3) * 8) ^ ((u & 3) << 5)) + (u >> 2) * 57344;
      pin(tb[u >> 2][u & 3]);
    }
#define W_RD(dst, k, u) dst = ldtr2(L + tb[k >= 7][u & 3], (k % 7) * 8192 + (u >> 2) * 4096, (k % 7) * 8192 + (u >> 2) * 4096 + 2048)
#define H_LD(k, tt, base) h[k][tt] = ld4c(ws, l16 + (k & 3) * 1024, base + (k >> 2) * 4096)
    v4u wfr[2][8];
#pragma unroll
    for (int u = 0; u < 8; ++u) W_RD(wfr[0][u], 0, u);
    light_poll(ws, hb + (lane & 1) * 14336 + 13 * 1024);
    STAMP(15);
#pragma unroll
    for (int e = 0; e < 8; ++e) H_LD((e >> 1), (e & 1), hb + (e & 1) * 14336);
    sb();
    if (isR) x2_flush(L, ws, 16 * grp + j - 14, tid);
    sb();
#define BAD2(x0, x1) wany(max(max(max(max(x0.x, x0.y), x0.z), max(max(x0.w, x1.x), x1.y)), max(x1.z, x1.w)) == SENT)
#define RETRY2(x0, x1, o0, o1) \
      _Pragma("nounroll") for (int s_ = 0; s_ < SPIN; ++s_) { \
        const int lf_ = fresh(l16); SLOWCNT \
        x0 = ld4c(ws, lf_, o0); x1 = ld4c(ws, lf_, o1); \
        if (wall(max(max4(x0), max4(x1)) != SENT)) break; \
      }
    if (__builtin_expect(BAD2(h[0][0], h[0][1]), 0)) { RETRY2(h[0][0], h[0][1], hb, hb + 14336) }
    STAMP(8); CSTAMP(28);
    v4f acc[2][8][2];
    v2u fa;
    const int ao = WS_ACT + (16 * tile + 4 * wave) * 57344 + 2 * slice * 1024 + l16;
#define ACT_OUT(ps, tt, f) { v4u o; \
      _Pragma("unroll") for (int hh = 0; hh < 2; ++hh) { \
        const v4f av = __builtin_elementwise_max(acc[ps][2 * f + hh][tt], splat(0.f)) * acc[ps][4 + 2 * f + hh][tt]; \
        o[2 * hh] = pk2(lo2(av)); o[2 * hh + 1] = pk2(hi2(av)); } \
      st4c(ws, ao + (2 * ps + tt) * 57344 + f * 1024, o); }
#pragma unroll
    for (int ps = 0; ps < 2; ++ps) {
#pragma unroll
      for (int u = 0; u < 8; ++u) { acc[ps][u][0] = v4f{0.f, 0.f, 0.f, 0.f}; acc[ps][u][1] = acc[ps][u][0]; }
#pragma unroll
      for (int ks = 0; ks < 14; ++ks) {
        const int cur = ks & 1, nk = ks == 13 ? 0 : ks + 1;
        const bool more = ps == 0 || ks < 13, epi = ps == 1 && ks < 4;
        __builtin_amdgcn_s_waitcnt(0xC07F);
#pragma unroll
        for (int u = 0; u < 8; ++u) {
          acc[ps][u][0] = mfma(wfr[cur][u], h[ks][0], acc[ps][u][0]);
          if (more) W_RD(wfr[cur ^ 1][u], nk, u);
        }
        if (ps == 0) {
#pragma unroll
          for (int e = 8 + 4 * ks; e < 12 + 4 * ks; ++e)
            if (e < 28) H_LD((e >> 1), (e & 1), hb + (e & 1) * 14336);
#pragma unroll
          for (int e = 0; e < 28; ++e)
            if (e >= RF(ks) && e < RF(ks + 1)) H_LD((e >> 1), (e & 1), hb + (2 + (e & 1)) * 14336);
        } else {
          if (ks == 0) { H_LD(13, 0, hb + 2 * 14336); H_LD(13, 1, hb + 3 * 14336); }
#pragma unroll
          for (int e = 0; e < WD_PER; ++e) {
            const int f = WD_MID + WD_PER * ks + e;
            if (f < 56) wdn[f >> 2][f & 3] = ld4c(ws, l16 + (f & 3) * 1024, wdb + (f >> 2) * 4096);
          }
          if (ks == 12) fa = ld2c(ws, fo);
        }
        const bool bad = more && BAD2(h[nk][0], h[nk][1]);
#pragma unroll
        for (int u = 0; u < 8; ++u) acc[ps][u][1] = mfma(wfr[cur][u], h[ks][1], acc[ps][u][1]);
        if (epi) ACT_OUT(0, (ks >> 1), (ks & 1))
#pragma unroll
        for (int u = 0; u < 16; ++u) {
          __builtin_amdgcn_sched_group_barrier(0x8, 1, 0);
          if (u < 4) __builtin_amdgcn_sched_group_barrier(0x100, 2, 0);
          else if (u < 12) __builtin_amdgcn_sched_group_barrier(0x100, 1, 0);
          if (u >= 4 && u < 12) __builtin_amdgcn_sched_group_barrier(0x20, 1, 0);
          if (epi) __builtin_amdgcn_sched_group_barrier(0x2, 3, 0);
          else if (u >= 10) __builtin_amdgcn_sched_group_barrier(0x2, 1, 0);
        }
        sb();
        if (ps == 1 && ks == 6) {
          static_assert(WD_MID + 7 * WD_PER <= 56, "the count below");
          asm volatile("s_waitcnt vmcnt(%0)" :: "i"(3 * WD_PER) : "memory");
          if (lane < 2) st2c(ws, lane == 0 ? fw_ : isR ? WS_XRDY + (16 * grp + j - 14) * 64 + wave * 8 : OOB, v2u{tag, ~tag});
        }
        if (__builtin_expect(bad, 0)) {
          const int pb = (ks == 13 ? 2 : 2 * ps) * 14336 + nk * 1024;
          RETRY2(h[nk][0], h[nk][1], hb + pb, hb + pb + 14336)
        }
        if (ps == 0 && ks == 13) { STAMP(16); CSTAMP(29); }
        sb();
      }
    }
    STAMP(27);
    if (!wall(fa.x == tag && fa.y == ~tag)) wait_flags(ws, fo, tag);
    STAMP(26);
#pragma unroll
    for (int e = 0; e < A_FIRST; ++e) H_LD((e >> 1), (e & 1), ab + (e & 1) * 57344);
    sb();
    ACT_OUT(1, 0, 0) ACT_OUT(1, 0, 1) ACT_OUT(1, 1, 0) ACT_OUT(1, 1, 1)
    CSTAMP(30);
    sb();
    STAMP(9);
    const int blk = blk0 + (wave >> 1);
    const int xo = WS_X2 + blk * 14336 + c16 * 896 + ct * 128 + g * 32 + (wave & 1) * 16;
    const int go = WS_MOD + grp * MOD_REP + (70 + 2 * ct) * 128 + g * 64 + (wave & 1) * 32;
    const int xf = WS_XRDY + (blk + (lane & 4 ? 2 : 0)) * 64 + (lane & 3) * 8;
    v4u a[14][2];
    v4f ac2[4][4];
#pragma unroll
    for (int t = 0; t < 4; ++t)
#pragma unroll
      for (int n = 0; n < 4; ++n) ac2[t][n] = v4f{0.f, 0.f, 0.f, 0.f};
    v4u xv[2], gv[2];
#pragma unroll
    for (int ks = 0; ks < 14; ++ks) {
#pragma unroll
      for (int tt = 0; tt < 2; ++tt)
#pragma unroll
        for (int n = 0; n < 4; ++n) ac2[tt][n] = mfma(wdn[ks][n], h[ks][tt], ac2[tt][n]);
#pragma unroll
      for (int e = A_FIRST + 3 * ks; e < A_FIRST + 3 * ks + 3; ++e)
        if (e < 28) H_LD((e >> 1), (e & 1), ab + (e & 1) * 57344);
#pragma unroll
      for (int u = 0; u < 4; ++u) {
        __builtin_amdgcn_sched_group_barrier(0x8, 2, 0);
        __builtin_amdgcn_sched_group_barrier(0x20, 1, 0);
      }
      sb();
      if (ks == F_AT) {
        static_assert(A_FIRST + 3 * F_AT + 3 <= 28, "the count below");
        asm volatile("s_waitcnt vmcnt(%0)" :: "i"(3 * F_AT + 3) : "memory");
        if (lane == 0) st2c(ws, fw_ + 256, v2u{tag, ~tag});
      }
      if (ks == 7) {
        fa = ld2c(ws, xf);
        gv[0] = ld4c(ws, go); gv[1] = ld4c(ws, go + 16);
      }
      if (ks == 11) {
        if (!wall(fa.x == tag && fa.y == ~tag)) wait_flags(ws, xf, tag);
        xv[0] = ld4c(ws, xo); xv[1] = ld4c(ws, xo + 2 * 14336);
      }
      sb();
    }
    STAMP(17);
    int rw = lane * 8 + wave * 2048, rr_ = lane * 8 + wave * 8192;
    pin(rw); pin(rr_);
#define RED_WR(ph) _Pragma("unroll") for (int dd = 0; dd < 4; ++dd) if (dd != wave) \
      _Pragma("unroll") for (int n = 0; n < 4; ++n) \
        *(LAS v2f*)(L + rw + ph * 32768 + dd * 8192 + n * 512) = (dd & 1) ? hi2(ac2[2 * ph + (dd >> 1)][n]) : lo2(ac2[2 * ph + (dd >> 1)][n]);
#define RED_FIN(ph) { v2f r[4]; \
      _Pragma("unroll") for (int n = 0; n < 4; ++n) { \
        const v4f own = wave < 2 ? ac2[2 * ph][n] : ac2[2 * ph + 1][n]; r[n] = (wave & 1) ? hi2(own) : lo2(own); } \
      _Pragma("unroll") for (int w = 1; w < 4; ++w) \
        _Pragma("unroll") for (int n = 0; n < 4; ++n) r[n] += *(const LAS v2f*)(L + rr_ + ((wave + w) & 3) * 2048 + ph * 32768 + n * 512); \
      v4u o; \
      _Pragma("unroll") for (int q = 0; q < 2; ++q) { \
        o[2 * q] = pk2(fma2(bun(gv[q].x), v2f{r[0][q], r[1][q]}, bun(xv[ph][2 * q]))); \
        o[2 * q + 1] = pk2(fma2(bun(gv[q].z), v2f{r[2][q], r[3][q]}, bun(xv[ph][2 * q + 1]))); } \
      *(v4u*)(out + tok_row(blk + 2 * ph, c16) * D + ct * 64 + g * 16 + (wave & 1) * 8) = o; }
    fa = ld2c(ws, fo + 256);
    asm volatile("s_barrier" ::: "memory");
    RED_WR(0)
    _Pragma("nounroll") for (int s = 0; s < SPIN; ++s) {
      if (wall(gv[0].y == tag && gv[0].w == tag && gv[1].y == tag && gv[1].w == tag)) break;
      const int gf = fresh(go);
      gv[0] = ld4c(ws, gf); gv[1] = ld4c(ws, gf + 16);
    }
#define A_LD(e) a[(e) >> 1][(e) & 1] = ld4c(ws, l16 + (((e) >> 1) & 3) * 1024, ab + (2 + ((e) & 1)) * 57344 + ((e) >> 3) * 4096)
    if (!wall(fa.x == tag && fa.y == ~tag)) wait_flags(ws, fo + 256, tag);
    STAMP(18);
#pragma unroll
    for (int e = 0; e < B_FIRST; ++e) A_LD(e);
    sb();
    bar();
    RED_FIN(0)
    sb();
#pragma unroll
    for (int ks = 0; ks < 14; ++ks) {
#pragma unroll
      for (int tt = 0; tt < 2; ++tt)
#pragma unroll
        for (int n = 0; n < 4; ++n) ac2[2 + tt][n] = mfma(wdn[ks][n], a[ks][tt], ac2[2 + tt][n]);
#pragma unroll
      for (int e = B_FIRST + 3 * ks; e < B_FIRST + 3 * ks + 3; ++e)
        if (e < 28) A_LD(e);
#pragma unroll
      for (int u = 0; u < 4; ++u) {
        __builtin_amdgcn_sched_group_barrier(0x8, 2, 0);
        __builtin_amdgcn_sched_group_barrier(0x20, 1, 0);
      }
      sb();
    }
    STAMP(10);
    RED_WR(1)
    bar();
    RED_FIN(1)
    if (blockIdx.x == 0 && wave == 0) {
      _Pragma("nounroll") for (int s = 0; s < SPIN; ++s) {
        const v4u v = ld4c(ws, fresh(WS_MOD + (70 + (lane < 14 ? lane : 0)) * 128));
        if (wall(v.y == tag)) break;
      }
      if (lane == 0) __builtin_amdgcn_raw_buffer_store_b32(epoch + 1u, ws, WS_EPOCH, 0, SC1);
    }
  }
  STAMP(11);
  DUMP();
}

extern "C" void kernel_launch(void* const* d_in, const int* in_sizes, int n_in, void* d_out, int out_size, void* d_ws,
                              size_t ws_size, hipStream_t stream) {
  fused<<<256, 256, 0, stream>>>((const u16*)d_in[0], (const u16*)d_in[1], (const u16*)d_in[2], (const u16*)d_in[4],
                                 (const u16*)d_in[5], (const u16*)d_in[6], (const u16*)d_in[7], (const u16*)d_in[8],
                                 (u16*)d_out, (char*)d_ws);
}
```

```cpp
#define PADI 8
#define PADB 40
#define PADT 2000
#include <hip/hip_runtime.h>

typedef unsigned short u16;
typedef unsigned int u32;
typedef unsigned long long u64;
typedef u32 v4u __attribute__((ext_vector_type(4)));
typedef u32 v2u __attribute__((ext_vector_type(2)));
typedef float v4f __attribute__((ext_vector_type(4)));
typedef float v2f __attribute__((ext_vector_type(2)));
typedef short v8s __attribute__((ext_vector_type(8)));
typedef short v4s __attribute__((ext_vector_type(4)));
typedef __bf16 v8b __attribute__((ext_vector_type(8)));
typedef __bf16 v2b __attribute__((ext_vector_type(2)));
#define LAS __attribute__((address_space(3)))
#define GAS __attribute__((address_space(1)))
#define DEV __device__ __forceinline__

#define C_W1_EARLY 0
#define CLR_DELAY 0
#define W_DELAY 0
#define CLR2_DELAY 0
#define PF_POS 2
#define SL1 1
#define SL2 2
#define SL4 4
#define X_AFTER 8
#define PF_AUX 2
#define W_LATE 1
#define W1_PF 1
#define DMA_MOD ""
#define RF(ks) ((ks) <= 5 ? 0 : (3 * ((ks) - 5) < 2 * ((ks) - 1) ? 3 * ((ks) - 5) : 2 * ((ks) - 1)))
#define AF(ks) ((ks) <= A_AT + 1 ? 0 : (6 * ((ks) - A_AT - 1) < 2 * ((ks) - 1) ? 6 * ((ks) - A_AT - 1) : 2 * ((ks) - 1)))
#define WD_PER 3
#define A_FIRST 10
#define B_FIRST 12
#define A_AT 9
#define F_AT 4
#define CODE_BYTES 45056
#define WD_EARLY 8
#define WD_MID 16
#define B_AT 9
constexpr int S = 2048, D = 448, DF = 225, F = 1792;

struct alignas(16) Chain { u16 v[2048]; };
struct Cos448 { u16 v[448]; };
constexpr double c_cos_t(double a) {
  double a2 = a * a, t = 1.0, s = 1.0;
  for (int i = 1; i <= 10; ++i) { t = -t * a2 / ((2 * i - 1) * (2 * i)); s += t; }
  return s;
}
constexpr double c_sin_t(double a) {
  double a2 = a * a, t = a, s = a;
  for (int i = 1; i <= 10; ++i) { t = -t * a2 / ((2 * i) * (2 * i + 1)); s += t; }
  return s;
}
constexpr double c_cos448(int m) {
  if (m > 224) m = 448 - m;
  double sg = 1.0;
  if (m > 112) { m = 224 - m; sg = -1.0; }
  const double w = 6.283185307179586476925286766559 / 448.0;
  return m == 112 ? 0.0 : (m <= 56 ? sg * c_cos_t(w * m) : sg * c_sin_t(w * (112 - m)));
}
constexpr u16 c_bf16(double d) {
  const u32 u = __builtin_bit_cast(u32, (float)d);
  return (u16)((u + 0x7FFFu + ((u >> 16) & 1u)) >> 16);
}
constexpr Cos448 mk_cos() { Cos448 c{}; for (int m = 0; m < 448; ++m) c.v[m] = c_bf16(c_cos448(m)); return c; }
constexpr Cos448 CTAB = mk_cos();
constexpr u16 bcos(int m) { return CTAB.v[m % 448]; }
constexpr u16 bsin(int m) { return CTAB.v[(m % 448 + 336) % 448]; }
constexpr u16 bneg(u16 b) { return b ? (u16)(b ^ 0x8000u) : b; }
constexpr u16 bscale(u16 b, int e) { return b ? (u16)(b + e * 0x80) : b; }

constexpr Chain mk_p1(int c) {
  const int tile = c >> 2, par = c & 1, sn = (c >> 1) & 1;
  Chain ch{};
  for (int s = 0; s < 4; ++s)
    for (int l = 0; l < 64; ++l) {
      const int kp = 16 * tile + (l & 15), k = 2 * kp + par;
      if (k > 224) continue;
      for (int j = 0; j < 8; ++j) {
        const int dl = 32 * s + 8 * (l >> 4) + j;
        if (dl > 112) continue;
        u16 b = sn ? bneg(bsin(k * dl)) : bcos(k * dl);
        if (dl == 0 || dl == 112) b = bscale(b, -1);
        ch.v[(s * 64 + l) * 8 + j] = b;
      }
    }
  return ch;
}
constexpr Chain mk_p3(int c) {
  const int tile = c >> 1, sn = c & 1;
  Chain ch{};
  for (int s = 0; s < 4; ++s)
    for (int l = 0; l < 64; ++l) {
      int dl = 16 * tile + (l & 15);
      if (c == 1 && dl == 0) dl = 112;
      for (int j = 0; j < 8; ++j) {
        const int k = 2 * (32 * s + 8 * (l >> 4) + j) + 1;
        if (k < 224) ch.v[(s * 64 + l) * 8 + j] = sn ? bsin(k * dl) : bcos(k * dl);
      }
    }
  return ch;
}
constexpr Chain mk_p2(int c) {
  Chain ch{};
  for (int f = 0; f < 4; ++f)
    for (int l = 0; l < 64; ++l)
      for (int j = 0; j < 8; ++j) {
        const int comp = c < 8 ? (c & 1) : (f >> 1), s = c < 8 ? f : (f & 1);
        const int o = 16 * (c < 8 ? (c >> 1) : (c - 8)) + (l & 15), i = 16 * s + 4 * (l >> 4) + (j >> 1);
        const int m = (c < 8 ? 7 : 14) * ((i * o) % 64);
        u16 b = 0;
        if (comp == 0) b = (j & 1) ? bsin(m) : bcos(m);
        else b = (j & 1) ? bcos(m) : bneg(bsin(m));
        ch.v[(f * 64 + l) * 8 + j] = b;
      }
  return ch;
}
template <int I> constexpr Chain P1C = mk_p1(I);
template <int I> constexpr Chain P3C = mk_p3(I);
template <int I> constexpr Chain P2C = mk_p2(I);
#define L10(T, b) T<b>, T<b + 1>, T<b + 2>, T<b + 3>, T<b + 4>, T<b + 5>, T<b + 6>, T<b + 7>, T<b + 8>, T<b + 9>
#define L29(T) L10(T, 0), L10(T, 10), T<20>, T<21>, T<22>, T<23>, T<24>, T<25>, T<26>, T<27>, T<28>
constexpr int TAB_P1 = 0, TAB_P3 = 29, TAB_P2 = 43, TAB_N = 53;
static __device__ const Chain TAB[TAB_N] = {L29(P1C), L10(P3C, 0), P3C<10>, P3C<11>, P3C<12>, P3C<13>, L10(P2C, 0)};

constexpr int WS_EPOCH = 0;
constexpr int WS_RDY = 4096;
constexpr int WS_HRDY = 4096 + 8192;
constexpr int WS_DRDY = 4096 + 16384;
constexpr int WS_XRDY = 131072 + 65536;
constexpr int WS_ARDY = 131072;
constexpr int WS_MOD = 32768, MOD_REP = 12288;
constexpr int T1_BLK = 226 * 64;
constexpr int WS_T1 = 1 << 18;
constexpr int WS_WT = WS_T1 + 128 * T1_BLK;
constexpr int WS_H2 = WS_WT + 113 * 16384;
constexpr int WS_X2 = WS_H2 + 128 * 14336;
constexpr int WS_WD = WS_X2 + 128 * 14336;
constexpr int WS_ACT = WS_WD + 7 * 56 * 4096;
constexpr int WS_END = WS_ACT + 128 * 57344;
constexpr int WS_DBG = 32 << 20;

constexpr int L_W1 = 0;
constexpr int L_B = 114688;
constexpr int XP = 912;
constexpr int L_XS = L_B;
constexpr int L_ST = L_XS + 16 * XP;
constexpr int L_M1 = L_ST + 128;
constexpr int L_AF = L_M1 + 4 * 1792;
constexpr int L_M2 = L_AF + 16384;
constexpr int L_EX = L_M2 + 3 * 1792;
constexpr int L_REND = L_EX + 512;
constexpr int ZPL = 8192;
constexpr int L_CI = L_B;
constexpr int L_CX = L_CI + 2 * 8256;
constexpr int L_RED = L_CX + 16384;
constexpr int L_DBG = 163840 - 1024;
constexpr int L_SZ = 163840;
static_assert(L_REND <= L_DBG && L_RED + 2048 <= L_DBG && 2 * ZPL <= 16384, "lds");

constexpr u32 SENT = 0xFFFFFFFFu;
constexpr int SPIN = 1 << 14;
constexpr int SC1 = 16;
constexpr int OOB = 0x7FFF0000;

typedef __amdgpu_buffer_rsrc_t rsrc_t;
DEV rsrc_t mkrs(const void* p, int bytes) { return __builtin_amdgcn_make_buffer_rsrc((void*)p, 0, bytes, 0x00020000); }
DEV v4u ld4(rsrc_t r, int off, int so = 0) { return __builtin_amdgcn_raw_buffer_load_b128(r, off, so, 0); }
DEV v2u ld2(rsrc_t r, int off, int so = 0) { return __builtin_amdgcn_raw_buffer_load_b64(r, off, so, 0); }
DEV v4u ld4c(rsrc_t r, int off, int so = 0) { return __builtin_amdgcn_raw_buffer_load_b128(r, off, so, SC1); }
DEV v2u ld2c(rsrc_t r, int off, int so = 0) { return __builtin_amdgcn_raw_buffer_load_b64(r, off, so, SC1); }
DEV u32 ld1c(rsrc_t r, int off, int so = 0) { return __builtin_amdgcn_raw_buffer_load_b32(r, off, so, SC1); }
DEV void st4c(rsrc_t r, int off, v4u v) { __builtin_amdgcn_raw_buffer_store_b128(v, r, off, 0, SC1); }
DEV void st2c(rsrc_t r, int off, v2u v) { __builtin_amdgcn_raw_buffer_store_b64(v, r, off, 0, SC1); }
DEV float blo(u32 v) { return __builtin_bit_cast(float, v << 16); }
DEV float bhi(u32 v) { return __builtin_bit_cast(float, v & 0xFFFF0000u); }
DEV v2f bun(u32 v) { return v2f{blo(v), bhi(v)}; }
DEV u32 pk(float lo, float hi) { return __builtin_bit_cast(u32, __builtin_convertvector(v2f{lo, hi}, v2b)); }
DEV u32 pk2(v2f v) { return __builtin_bit_cast(u32, __builtin_convertvector(v, v2b)); }
DEV float rb(float f) { return blo(pk(f, 0.f)); }
DEV v2f lo2(v4f v) { return __builtin_shufflevector(v, v, 0, 1); }
DEV v2f hi2(v4f v) { return __builtin_shufflevector(v, v, 2, 3); }
DEV v2f fma2(v2f a, v2f b, v2f c) { return __builtin_elementwise_fma(a, b, c); }
DEV v4f fma4(v4f a, v4f b, v4f c) { return __builtin_elementwise_fma(a, b, c); }
DEV v4f splat(float f) { return v4f{f, f, f, f}; }
DEV v4f mfma(v4u a, v4u b, v4f c) {
  return __builtin_amdgcn_mfma_f32_16x16x32_bf16(__builtin_bit_cast(v8b, a), __builtin_bit_cast(v8b, b), c, 0, 0, 0);
}
DEV void mfa(v4f& c, v4u a, v4u b) { asm volatile("s_nop 1\n\tv_mfma_f32_16x16x32_bf16 %0, %1, %2, %0" : "+v"(c) : "v"(a), "v"(b)); }
DEV void mf0(v4f& c, v4u a, v4u b) { asm volatile("s_nop 1\n\tv_mfma_f32_16x16x32_bf16 %0, %1, %2, 0" : "=&v"(c) : "v"(a), "v"(b)); }
template <int N> DEV void mfence(v4f& a, v4f& b) { asm volatile("s_nop %2" : "+v"(a), "+v"(b) : "i"(N)); }
template <int N> DEV void mfence(v4f& a, v4f& b, v4f& c, v4f& d) { asm volatile("s_nop %4" : "+v"(a), "+v"(b), "+v"(c), "+v"(d) : "i"(N)); }
template <class T> DEV void pin(T& v) { asm volatile("" : "+v"(v)); }
DEV int fresh(int off) { asm volatile("" : "+v"(off) :: "memory"); return off; }
DEV bool wall(bool ok) { return __builtin_amdgcn_ballot_w64(!ok) == 0; }
DEV bool wany(bool b) { return __builtin_amdgcn_ballot_w64(b) != 0; }
DEV void sb() { __builtin_amdgcn_sched_barrier(0); }
DEV u32 max4(v4u v) { return max(max(v.x, v.y), max(v.z, v.w)); }
DEV void drain() { asm volatile("s_waitcnt vmcnt(0)" ::: "memory"); }
DEV void cfence() { asm volatile("" ::: "memory"); }
DEV void bar() { asm volatile("s_waitcnt lgkmcnt(0)\n\ts_barrier" ::: "memory"); }
template <int CTRL> DEV float dpp(float v) {
  return __builtin_bit_cast(float, __builtin_amdgcn_update_dpp(0, __builtin_bit_cast(int, v), CTRL, 0xF, 0xF, true));
}
DEV float sum16(float v) {
  v += dpp<0x128>(v); v += dpp<0x124>(v); v += dpp<0x122>(v); v += dpp<0x121>(v); return v;
}
DEV void sumg2(float& a, float& b) {
  float c = a, d = b;
  asm volatile("s_nop 1\n\tv_permlane16_swap_b32 %0, %1\n\tv_permlane16_swap_b32 %2, %3" : "+v"(a), "+v"(c), "+v"(b), "+v"(d));
  a += c; b += d; c = a; d = b;
  asm volatile("s_nop 1\n\tv_permlane32_swap_b32 %0, %1\n\tv_permlane32_swap_b32 %2, %3" : "+v"(a), "+v"(c), "+v"(b), "+v"(d));
  a += c; b += d;
}
DEV v4u lds4(const char LAS* L, int off) { return *(const LAS v4u*)(L + off); }
DEV v4f lds4f(const char LAS* L, int off) { return *(const LAS v4f*)(L + off); }
DEV v2u ldtr(const char LAS* L, int off) { return __builtin_bit_cast(v2u, __builtin_amdgcn_ds_read_tr16_b64_v4i16((LAS v4s*)(L + off))); }
DEV v4u ldtr2(const char LAS* L, int o0, int o1) { const v2u a = ldtr(L, o0), b = ldtr(L, o1); return v4u{a.x, a.y, b.x, b.y}; }
DEV void dma16(rsrc_t r, int voff, int soff, u32 lds_dst) {
  u32 keep;
  asm volatile("s_mov_b32 %0, m0\n\ts_mov_b32 m0, %1\n\ts_nop 0\n\tbuffer_load_dwordx4 %2, %3, %4 offen" DMA_MOD " lds\n\ts_mov_b32 m0, %0"
               : "=&s"(keep) : "s"(lds_dst), "v"(voff), "s"(r), "s"(soff) : "memory");
}
DEV void light_poll(rsrc_t ws, int off) {
  _Pragma("nounroll") for (int s = 0; s < SPIN; ++s) {
    if (wall(ld1c(ws, fresh(off)) != SENT)) break;
    __builtin_amdgcn_s_sleep(SL2);
  }
}
DEV int tok_row(int blk, int t) {
  const int n1 = 16 * (blk >> 5) + 4 * ((blk >> 2) & 3) + (t & 3), n2 = 16 * ((blk >> 4) & 1) + 4 * (blk & 3) + (t >> 2);
  return n2 + 32 * n1;
}
template <bool SWAP> DEV v4u cmul_pack(v4f a, v4f b, v4f c, v4f s) {
  const v4f re = fma4(b, s, a * c), im = fma4(b, c, -(a * s));
  return SWAP ? v4u{pk(im.x, re.x), pk(im.y, re.y), pk(im.z, re.z), pk(im.w, re.w)}
              : v4u{pk(re.x, im.x), pk(re.y, im.y), pk(re.z, im.z), pk(re.w, im.w)};
}

#define STAMP(i) if ((i) == PADI) padt = __builtin_amdgcn_s_memrealtime();
#define CSTAMP(i)
#define SLOWCNT
#define DUMP()

DEV void dma_w1(const u16* wg, const u16* wu, int slice, int wave, int lane, u32 lbase) {
  const rsrc_t rg = mkrs(wg, D * F * 2), ru = mkrs(wu, D * F * 2);
  const int r = lane >> 3, ch = ((((lane & 7) >> 1) ^ (lane >> 4)) & 3) * 32 + (lane & 1) * 16 + slice * 128;
  const int vp = r * (F * 2) + ch, vn = (7 - r) * (F * 2) + ch;
#pragma unroll
  for (int n = 0; n < 4; ++n) {
    const int ks = wave + 4 * n;
    if (n == 3 && wave >= 2) break;
    const int t16 = (ks >> 1) * 16, a = ks & 1;
#pragma unroll
    for (int q = 0; q < 4; ++q) {
      const int dl0 = t16 + 8 * (q & 1);
      const bool pos = (q >> 1) == a;
      const int d0 = q < 2 ? (a ? 224 - dl0 - 7 : dl0) : (a ? 224 + dl0 : 448 - dl0 - 7);
      int vo = pos ? vp : vn, so = d0 * (F * 2);
      if (n == 0 && q == 2) {
        vo += so; so = 0;
        if (wave < 2 && lane < 8) vo = (wave ? 336 : 112) * (F * 2) + ch;
      }
      dma16(rg, vo, so, lbase + L_W1 + ks * 8192 + q * 1024);
      dma16(ru, vo, so, lbase + L_W1 + ks * 8192 + 4096 + q * 1024);
    }
  }
}
struct ModLoad { u32 cv[7]; v4u mw[7]; };
DEV void mod_load(ModLoad& m, const u16* cin, const u16* wm, int q, int tid) {
  const rsrc_t cr = mkrs(cin, q >= 0 ? D * 2 : 0), mr = mkrs(wm, q >= 0 ? D * 6 * D * 2 : 0);
#pragma unroll
  for (int i = 0; i < 7; ++i) m.cv[i] = __builtin_amdgcn_raw_buffer_load_b16(cr, (tid >> 2) * 2 + i * 128, 0, 0);
#pragma unroll
  for (int i = 0; i < 7; ++i) m.mw[i] = __builtin_amdgcn_raw_buffer_load_b128(mr, ((tid >> 2) * (6 * D) + q * 32 + (tid & 3) * 8) * 2, i * 64 * 6 * D * 2, PF_AUX);
}
DEV void mod_part(ModLoad& m, char LAS* L, int tid) {
  float sl[7];
#pragma unroll
  for (int i = 0; i < 7; ++i) { const float cc = blo(m.cv[i]); sl[i] = rb(cc * __builtin_amdgcn_rcpf(1.f + __expf(-cc))); pin(sl[i]); }
  v2f a[4];
#pragma unroll
  for (int e = 0; e < 4; ++e) a[e] = v2f{0.f, 0.f};
#pragma unroll
  for (int i = 0; i < 7; ++i)
#pragma unroll
    for (int e = 0; e < 4; ++e) a[e] = fma2(bun(m.mw[i][e]), v2f{sl[i], sl[i]}, a[e]);
#pragma unroll
  for (int e = 0; e < 4; ++e) {
    a[e].x += dpp<0x128>(a[e].x); a[e].y += dpp<0x128>(a[e].y);
    a[e].x += dpp<0x124>(a[e].x); a[e].y += dpp<0x124>(a[e].y);
  }
  if ((tid & 15) < 4) {
    *(LAS v4f*)(L + L_RED + ((tid >> 4) * 32 + (tid & 15) * 8) * 4) = v4f{a[0].x, a[0].y, a[1].x, a[1].y};
    *(LAS v4f*)(L + L_RED + ((tid >> 4) * 32 + (tid & 15) * 8 + 4) * 4) = v4f{a[2].x, a[2].y, a[3].x, a[3].y};
  }
}
DEV void mod_publish(char LAS* L, rsrc_t ws, int q, int lane, u32 tag) {
  v4f s = {0.f, 0.f, 0.f, 0.f};
#pragma unroll
  for (int r = 0; r < 16; ++r) s += lds4f(L, L_RED + (r * 32 + (lane & 7) * 4) * 4);
  const float one = (q / 14 == 1 || q / 14 == 4) ? 1.f : 0.f;
  st4c(ws, WS_MOD + (lane >> 3) * MOD_REP + q * 128 + (lane & 7) * 16,
       v4u{pk(rb(s.x) + one, rb(s.y) + one), tag, pk(rb(s.z) + one, rb(s.w) + one), tag});
}
DEV u32 ld1(rsrc_t r, int off) { return __builtin_amdgcn_raw_buffer_load_b32(r, off, 0, 0); }
DEV u32 mktag(u32 epoch) { return epoch * 0x9E3779B1u + 0x7F4A7C15u; }

DEV void x2_flush(char LAS* L, rsrc_t ws, int blk, int tid) {
#pragma unroll
  for (int m = 0; m < 4; ++m) {
    const int ch = min(tid + 256 * m, 895), tk = ch / 56;
    st4c(ws, tid + 256 * m < 896 ? WS_X2 + blk * 14336 + ch * 16 : OOB, lds4(L, L_XS + tk * XP + (ch - tk * 56) * 16));
  }
}
DEV bool flags_ok(v4u rv, u32 tag) { return rv.x == tag && rv.y == ~tag && rv.z == tag && rv.w == ~tag; }
DEV void wait_flags(rsrc_t ws, int off, u32 tag) {
  _Pragma("nounroll") for (int s = 0; s < SPIN; ++s) {
    const v2u rv = ld2c(ws, fresh(off));
    if (wall(rv.x == tag && rv.y == ~tag)) break;
    __builtin_amdgcn_s_sleep(SL1);
  }
}

__global__ void __launch_bounds__(256, 1)
fused(const u16* __restrict__ xin, const u16* __restrict__ wr, const u16* __restrict__ wi, const u16* __restrict__ wg,
      const u16* __restrict__ wu, const u16* __restrict__ wd, const u16* __restrict__ cin, const u16* __restrict__ wm,
      u16* __restrict__ out, char* wsp) {
  __shared__ __attribute__((aligned(16))) char smem[L_SZ];
  char LAS* L = (char LAS*)smem;
  const u32 lbase = (u32)(size_t)L;
  const int tid = threadIdx.x, lane = tid & 63, wave = __builtin_amdgcn_readfirstlane(tid >> 6);
  const int g = lane >> 4, c16 = lane & 15, l16 = lane * 16;
  const int grp = blockIdx.x & 7, j = blockIdx.x >> 3;
  u64 pc, padt = 0;
  asm volatile("s_getpc_b64 %0" : "=s"(pc));
  STAMP(0);
  asm volatile("" :: "s"(xin), "s"(wr), "s"(wi), "s"(wg), "s"(wu), "s"(wd), "s"(cin), "s"(wm), "s"(out), "s"(wsp));
  const rsrc_t ws = mkrs(wsp, WS_END);
  const rsrc_t tab = mkrs(TAB, TAB_N * 4096);
  const rsrc_t code = mkrs((const void*)(pc & ~127ull), CODE_BYTES);
  const bool isR = j >= 14 && j < 30;
  const bool isG = j < 28;
  const int gk = j / 7, ct = j - gk * 7;
  const int tile = 4 * (grp >> 2) + gk, slice = 7 * (grp & 3) + ct;
  const int wdb = WS_WD + (ct * 56 + wave * 14) * 4096;
  const v4u sv = {SENT, SENT, SENT, SENT};
  int lb = L_B + l16;
  pin(lb);
  const char LAS* LB = L + lb - L_B;
  const int pfo = ((j * 4 + wave) * 14 + lane) * 128, pfc = ((j * 4 + wave) * 4 + lane) * 128;
  u32 pfa = ld1(tab, lane < 14 ? pfo : OOB), pfb = ld1(code, lane < 4 ? pfc : OOB);
  v4u wdn[14][4];
  u32 epoch, tag;

  if (isR) {
    const int blk = 16 * grp + (j - 14);
    epoch = ld1c(ws, WS_EPOCH);
    const int t0 = wave == 0 ? 0 : 2 * wave - 1;
    v4u cf[8][4];
#define CF_LOAD(q) { const int chn = TAB_P1 + ((wave == 0 && q >= 4) ? 28 : 4 * t0 + q), vo = (q < 5 || wave != 0) ? l16 : OOB; \
      _Pragma("unroll") for (int s = 0; s < 4; ++s) cf[q][s] = ld4(tab, vo + s * 1024, chn * 4096); sb(); }
#pragma unroll
    for (int q = 0; q < X_AFTER; ++q) CF_LOAD(q)
    v2u xv[7];
    {
      const rsrc_t xr = mkrs(xin, S * D * 2);
      const int off = tok_row(blk, 4 * wave + g) * (D * 2) + c16 * 8;
#pragma unroll
      for (int m = 0; m < 7; ++m) xv[m] = ld2(xr, off + m * 128);
    }
    typedef u32 v3u __attribute__((ext_vector_type(3)));
    v2u fa_[2], fb_[2]; v3u fc_[2], fd_[2];
    {
      const rsrc_t rr = mkrs(wr, S * DF * 2), ri = mkrs(wi, S * DF * 2);
      const int fo_ = 16 * wave + c16 < 57 ? 2 * g * 450 + (16 * wave + c16) * 8 : OOB;
#pragma unroll
      for (int hh = 0; hh < 2; ++hh) {
        const int so = blk * 7200 + hh * 3600;
        fa_[hh] = ld2(rr, fo_, so); fb_[hh] = ld2(ri, fo_, so);
        fc_[hh] = __builtin_amdgcn_raw_buffer_load_b96(rr, fo_ + 448, so, 0); fd_[hh] = __builtin_amdgcn_raw_buffer_load_b96(ri, fo_ + 448, so, 0);
      }
    }
    sb();
#pragma unroll
    for (int q = X_AFTER; q < 8; ++q) CF_LOAD(q)
    STAMP(23);
    {
      v2f f[14], s2 = {0.f, 0.f};
#pragma unroll
      for (int m = 0; m < 7; ++m) {
        f[2 * m] = bun(xv[m].x); f[2 * m + 1] = bun(xv[m].y);
        s2 += f[2 * m] + f[2 * m + 1];
        *(LAS v2u*)(L + L_XS + (4 * wave + g) * XP + c16 * 8 + m * 128) = xv[m];
      }
      STAMP(21);
      sb();
      {
        const int q = 16 * wave + c16, o = WS_WT + q * 16384 + blk * 128 + g * 16;
#pragma unroll
        for (int hh = 0; hh < 2; ++hh) {
          const v2u a = fa_[hh], b = fb_[hh]; const v3u c = fc_[hh], d = fd_[hh];
          st4c(ws, q < 57 ? o + hh * 64 : OOB, v4u{__builtin_amdgcn_perm(b.x, a.x, 0x05040100u), __builtin_amdgcn_perm(b.y, a.y, 0x05040100u),
                                                   __builtin_amdgcn_perm(d.x, c.x, 0x07060302u), __builtin_amdgcn_perm(d.y, c.y, 0x07060302u)});
          st4c(ws, q < 56 ? o + hh * 64 + 57 * 16384 : OOB,
               v4u{__builtin_amdgcn_perm(b.x, a.x, 0x07060302u), __builtin_amdgcn_perm(b.y, a.y, 0x07060302u),
                   __builtin_amdgcn_perm(d.y, c.y, 0x05040100u), __builtin_amdgcn_perm(d.z, c.z, 0x05040100u)});
        }
      }
      sb();
      const float mean = sum16(s2.x + s2.y) * (1.f / D);
      v2f q2 = {0.f, 0.f};
#pragma unroll
      for (int m = 0; m < 14; ++m) { const v2f d = f[m] - mean; q2 = fma2(d, d, q2); }
      const float rstd = __builtin_amdgcn_rsqf(sum16(q2.x + q2.y) * (1.f / D) + 1e-5f);
      if (c16 == 0) *(LAS v2f*)(L + L_ST + (4 * wave + g) * 8) = v2f{mean, rstd};
    }
    pin(pfa); pin(pfb);
    epoch = __builtin_amdgcn_readfirstlane(epoch);
    tag = mktag(epoch);
    STAMP(22);
    bar();
    v4f n[4][2];
    const int d0 = 8 * (4 * wave + g);
    const v2f st = *(const LAS v2f*)(L + L_ST + c16 * 8);
    {
      int xr_ = L_XS + c16 * XP + d0 * 2, xb_ = L_XS + c16 * XP + (216 - d0) * 2, x0_ = d0 == 0 ? xr_ : xb_ + 464;
      pin(xr_); pin(xb_); pin(x0_);
      const v4u a = lds4(L + xr_, 0), e = lds4(L + xr_, 448), c = lds4(L + xb_, 0), b = lds4(L + xb_, 448);
      const u32 c0 = *(const LAS u16*)(L + xb_ + 16), b0 = *(const LAS u16*)(L + x0_);
#pragma unroll
      for (int i = 0; i < 8; ++i) {
        n[0][i >> 2][i & 3] = (i & 1) ? bhi(a[i >> 1]) : blo(a[i >> 1]);
        n[3][i >> 2][i & 3] = (i & 1) ? bhi(e[i >> 1]) : blo(e[i >> 1]);
        n[1][i >> 2][i & 3] = i == 0 ? blo(b0) : (i & 1) ? bhi(b[(8 - i) >> 1]) : blo(b[(8 - i) >> 1]);
        n[2][i >> 2][i & 3] = i == 0 ? blo(c0) : (i & 1) ? bhi(c[(8 - i) >> 1]) : blo(c[(8 - i) >> 1]);
      }
      const float nm = -st.x * st.y;
#pragma unroll
      for (int m = 0; m < 4; ++m) {
#pragma unroll
        for (int h = 0; h < 2; ++h) { n[m][h] = fma4(n[m][h], splat(st.y), splat(nm)); pin(n[m][h]); }
      }
    }
    sb();
    STAMP(1);
    int mb = L_M1 + d0 * 4;
    pin(mb);
    v4u xs[4];
    int to[4];
    to[0] = WS_T1 + blk * T1_BLK + (16 * t0 + c16) * 64 + g * 16;
    to[1] = wave != 0 ? to[0] + 1024 : c16 < 2 ? to[0] + 112 * 64 : OOB;
    to[2] = to[0] + 114 * 64;
    to[3] = wave != 0 ? to[0] + 1024 + 114 * 64 : OOB;
#pragma unroll
    for (int q = 0; q < 4; ++q) pin(to[q]);

    {
      const int kind = tid >= 112, d = 4 * (tid - 112 * kind);
      int wb = L_M1 + (kind ? 0 : 2 * 1792) + d * 4, wrv = L_M1 + (kind ? 0 : 2 * 1792) + 1792 + (445 - d) * 4;
      int wr0 = d == 0 ? wrv + 12 - 1792 : wrv + 12;
      const int mo = tid < 224 ? WS_MOD + grp * MOD_REP + tid * 16 : OOB;
      pin(wb); pin(wrv); pin(wr0);
      const int ro = WS_RDY + grp * 1024 + min(tid >> 1, 112) * 8;
      v4u mv;
      _Pragma("nounroll") for (int s = 0; s < SPIN; ++s) {
        mv = ld4c(ws, fresh(mo));
        const v2u rv = ld2c(ws, fresh(ro));
        if (wall((tid >= 224 || (mv.y == tag && mv.w == tag)) && rv.x == tag && rv.y == ~tag)) break;
        __builtin_amdgcn_s_sleep(SL1);
      }
      STAMP(2);
      if (tid < 224) {
        const v4f v = {blo(mv.x), bhi(mv.x), blo(mv.z), bhi(mv.z)};
        *(LAS v4f*)(L + wb) = v;
        *(LAS float*)(L + wr0) = v.x;
        *(LAS float*)(L + wrv + 8) = v.y;
        *(LAS float*)(L + wrv + 4) = v.z;
        *(LAS float*)(L + wrv) = v.w;
      }
    }
    bar();
    {
      v4u fr[4];
#pragma unroll
      for (int hh = 0; hh < 2; ++hh) {
        v4f h[4];
#pragma unroll
        for (int m = 0; m < 4; ++m) {
          const int o = ((m == 1 || m == 2) ? 1792 : 0) + (m >= 2 ? 896 : 0) + hh * 16;
          h[m] = fma4(n[m][hh], lds4f(L + mb, o), lds4f(L + mb, 2 * 1792 + o));
        }
        const v4f p = h[0] + h[1], q = h[2] + h[3], u = h[0] - h[1], v = h[3] - h[2];
        const v4f t[4] = {p + q, p - q, u + v, u - v};
#pragma unroll
        for (int m = 0; m < 4; ++m) { fr[m][2 * hh] = pk2(lo2(t[m])); fr[m][2 * hh + 1] = pk2(hi2(t[m])); }
      }
#pragma unroll
      for (int m = 0; m < 4; ++m) *(LAS v4u*)(LB + wave * 1024 + L_AF + m * 4096) = fr[m];
    }
    bar();
    {
      v4u a[4][4];
#pragma unroll
      for (int p = 0; p < 2; ++p)
#pragma unroll
        for (int s = 0; s < 4; ++s) { a[p][s] = lds4(LB, L_AF + (p * 4 + s) * 1024); a[2 + p][s] = lds4(LB, L_AF + ((2 + p) * 4 + s) * 1024); }
      v4f re[4], im[4];
#pragma unroll
      for (int q = 0; q < 4; ++q) {
        const int p = q >> 1, t = q & 1;
        mf0(re[q], a[p][0], cf[4 * t + p][0]); mf0(im[q], a[2 + p][0], cf[4 * t + 2 + p][0]);
        if (q > 0) {
          mfence<1>(re[q - 1], im[q - 1]);
          xs[q - 1] = v4u{pk(re[q - 1].x, im[q - 1].x), pk(re[q - 1].y, im[q - 1].y), pk(re[q - 1].z, im[q - 1].z), pk(re[q - 1].w, im[q - 1].w)};
          st4c(ws, to[q - 1], xs[q - 1]);
          sb();
        }
#pragma unroll
        for (int s = 1; s < 4; ++s) { mfa(re[q], a[p][s], cf[4 * t + p][s]); mfa(im[q], a[2 + p][s], cf[4 * t + 2 + p][s]); }
      }
      mfence<7>(re[3], im[3]);
      xs[3] = v4u{pk(re[3].x, im[3].x), pk(re[3].y, im[3].y), pk(re[3].z, im[3].z), pk(re[3].w, im[3].w)};
      st4c(ws, to[3], xs[3]);
    }
    STAMP(3);
    sb();
    sb();
    if (isG) dma_w1(wg, wu, slice, wave, lane, lbase);
#pragma unroll
    for (int q = 0; q < 4; ++q) {
      const int vo = (q < 2 || wave != 0) ? l16 : OOB;
#pragma unroll
      for (int s = 0; s < 4; ++s) cf[2 * q + 1][s] = ld4(tab, vo + s * 1024, (TAB_P3 + 2 * t0 + q) * 4096);
    }
#pragma unroll
    for (int m = 0; m < 4; ++m)
      st4c(ws, tid + 256 * m < 896 ? WS_H2 + blk * 14336 + 4096 * m + tid * 16 : OOB, sv);
    sb();
    STAMP(20);
    v4f xo[2][4];
    int xa = L_XS + c16 * XP + (16 * t0 + 4 * g) * 2, xd = L_XS + c16 * XP + (192 - 16 * t0 - 4 * g) * 2;
    int xs1 = tid < 16 ? L_XS + c16 * XP + 224 : xd + 512, xs3 = tid < 16 ? L_XS + c16 * XP + 672 : xa + 448;
    pin(xa); pin(xd); pin(xs1); pin(xs3);
#define XOFF(t, m, r) ((t) == 0 && (r) == 0 && (m) == 1 ? xs1 : (t) == 0 && (r) == 0 && (m) == 3 ? xs3 : \
    (m) == 0 ? xa + 32 * (t) + 2 * (r) : (m) == 3 ? xa + 448 + 32 * (t) + 2 * (r) : (m) == 1 ? xd + 512 - 32 * (t) - 2 * (r) : xd + 64 - 32 * (t) - 2 * (r))
    {
      v2u w[2][4]; u32 e[2][2], e3;
#pragma unroll
      for (int t = 0; t < 2; ++t) {
        w[t][0] = *(const LAS v2u*)(L + xa + 32 * t); w[t][3] = *(const LAS v2u*)(L + xa + 448 + 32 * t);
        w[t][1] = *(const LAS v2u*)(L + xd + 504 - 32 * t); w[t][2] = *(const LAS v2u*)(L + xd + 56 - 32 * t);
        e[t][0] = *(const LAS u16*)(L + XOFF(t, 1, 0)); e[t][1] = *(const LAS u16*)(L + XOFF(t, 2, 0));
      }
      e3 = *(const LAS u16*)(L + xs3);
      sb();
#pragma unroll
      for (int t = 0; t < 2; ++t) {
        xo[t][0] = v4f{blo(w[t][0].x), bhi(w[t][0].x), blo(w[t][0].y), bhi(w[t][0].y)};
        xo[t][3] = v4f{t == 0 ? blo(e3) : blo(w[t][3].x), bhi(w[t][3].x), blo(w[t][3].y), bhi(w[t][3].y)};
        xo[t][1] = v4f{blo(e[t][0]), bhi(w[t][1].y), blo(w[t][1].y), bhi(w[t][1].x)};
        xo[t][2] = v4f{blo(e[t][1]), bhi(w[t][2].y), blo(w[t][2].y), bhi(w[t][2].x)};
#pragma unroll
        for (int m = 0; m < 4; ++m) { xo[t][m] -= st.x; pin(xo[t][m]); }
      }
    }
    int m0_[2], m1_[2], md_[2];
#pragma unroll
    for (int r = 0; r < 2; ++r) {
      const int q = r == 0 || tid < 80 ? tid + 256 * r : tid;
      const int kind = (q * 586) >> 16, d = 4 * (q - 112 * kind);
      const int reg = (d >= 112) + (d >= 224) + (d >= 336), cc = ((reg + 1) >> 1) * 224, m1 = (0x1320 >> (4 * reg)) & 3;
      const int d1 = abs(d + 1 - cc), d0_ = abs(d - cc), edge = d == 112 * reg && reg > 0;
      m1_[r] = L_M2 + kind * 1792 + (((d1 >> 4) << 6) | (m1 << 4) | (d1 & 15)) * 4;
      m0_[r] = L_M2 + kind * 1792 + (edge ? reg << 4 : ((d0_ >> 4) << 6) | (m1 << 4) | (d0_ & 15)) * 4;
      md_[r] = (reg & 1) ? -4 : 4;
      pin(m0_[r]); pin(m1_[r]); pin(md_[r]);
    }
    {
      v4u mv[2];
      _Pragma("nounroll") for (int s = 0; s < SPIN; ++s) {
        mv[0] = ld4c(ws, fresh(WS_MOD + grp * MOD_REP + (224 + tid) * 16));
        mv[1] = ld4c(ws, fresh(WS_MOD + grp * MOD_REP + (224 + (tid < 80 ? 256 + tid : tid)) * 16));
        const v2u rv = ld2c(ws, fresh(WS_DRDY + grp * 1024 + min(lane, 48) * 8));
        if (wall(mv[0].y == tag && mv[0].w == tag && mv[1].y == tag && mv[1].w == tag && rv.x == tag && rv.y == ~tag)) break;
        __builtin_amdgcn_s_sleep(SL4);
      }
      if (lane == 0) st2c(ws, WS_HRDY + blk * 32 + wave * 8, v2u{tag, ~tag});
      STAMP(12);
#pragma unroll
      for (int f = 0; f < WD_MID; ++f) wdn[f >> 2][f & 3] = ld4c(ws, (isG ? l16 : OOB) + (f & 3) * 1024, wdb + (f >> 2) * 4096);
      sb();
#pragma unroll
      for (int r = 0; r < 2; ++r) {
        const float sc = m0_[r] < L_M2 + 1792 ? 2.f / (float)(S * D) : 1.f;
        *(LAS float*)(L + m0_[r]) = blo(mv[r].x) * sc;
        *(LAS float*)(L + m1_[r]) = bhi(mv[r].x) * sc;
        *(LAS float*)(L + m1_[r] + md_[r]) = blo(mv[r].z) * sc;
        *(LAS float*)(L + m1_[r] + 2 * md_[r]) = bhi(mv[r].z) * sc;
      }
    }
    bar();
    *(LAS v4u*)(L + L_AF + (tid >> 6) * 4096 + 3072 + l16) = v4u{0u, 0u, 0u, 0u};
    STAMP(4);
    int zr = L_AF + lane * 8, ro = L_M2 + (t0 * 64 + 4 * g) * 4, ro1 = wave == 0 ? ro : ro + 256, ex = L_EX + c16 * 8;
    pin(zr); pin(ro); pin(ro1); pin(ex);
    {
      int zl[4];
#pragma unroll
      for (int m = 0; m < 4; ++m) {
        const int kq = (m >> 1) * 128 + (to[m] == OOB ? 127 : wave == 0 && m == 1 ? 112 + c16 : 16 * (t0 + (m & 1)) + c16);
        zl[m] = L_AF + ((kq & ~31) + ((kq & 4) << 2) + ((kq & 24) >> 1) + (kq & 3)) * 32 + g * 8;
        pin(zl[m]);
        if (to[m] == OOB) xs[m] = sv;
      }
      bar();
      if (isG) wait_flags(ws, WS_HRDY + (16 * tile + 4 * wave) * 32 + c16 * 8, tag);
      {
        const int src = (blk * 5 + wave * 13) & 63;
        const int po = __builtin_amdgcn_readlane(to[(0)], 0) + (src & 15) * 64 + (src >> 4) * 16;
        const u32 old = __builtin_amdgcn_ds_bpermute(src * 4, xs[0].x);
        _Pragma("nounroll") for (int s = 0; s < SPIN; ++s) {
          if (wall(ld1c(ws, fresh(po)) != old)) break;
          __builtin_amdgcn_s_sleep(SL2);
        }
      }
      STAMP(13);
      v4u z[4];
      _Pragma("nounroll") for (int s = 0; s < SPIN; ++s) {
        u32 mn = 1;
#pragma unroll
        for (int m = 0; m < 4; ++m) z[m] = ld4c(ws, fresh(to[m]));
#pragma unroll
        for (int m = 0; m < 4; ++m) { const v4u d = z[m] ^ xs[m]; mn = min(min(mn, d.x), min(min(d.y, d.z), d.w)); }
        if (wall(mn != 0)) break;
      }
      STAMP(14);
#pragma unroll
      for (int m = 0; m < 4; ++m) {
        char LAS* p = L + zl[m];
        *(LAS v2u*)(p) = v2u{__builtin_amdgcn_perm(z[m].y, z[m].x, 0x05040100u), __builtin_amdgcn_perm(z[m].w, z[m].z, 0x05040100u)};
        *(LAS v2u*)(p + ZPL) = v2u{__builtin_amdgcn_perm(z[m].y, z[m].x, 0x07060302u), __builtin_amdgcn_perm(z[m].w, z[m].z, 0x07060302u)};
      }
    }
    bar();
    STAMP(5);
    {
      v4u zf[4][4];
#pragma unroll
      for (int ty = 0; ty < 4; ++ty)
#pragma unroll
        for (int s = 0; s < 4; ++s) zf[ty][s] = ldtr2(L + zr, ty * 4096 + s * 1024, ty * 4096 + s * 1024 + 512);
      v4f acc[2][4], x2[2][4], gav[2][4];
      v2f s1 = {0.f, 0.f}, s2 = {0.f, 0.f};
#define P3M(t, ty) { mf0(acc[t][ty], cf[4 * t + ty][0], zf[ty][0]); \
      _Pragma("unroll") for (int s = 1; s < 4; ++s) mfa(acc[t][ty], cf[4 * t + ty][s], zf[ty][s]); }
#define P3X(t, m, yv) { x2[t][m] = fma4(gav[t][m], yv, xo[t][m]); \
      if (t == 0 || wave != 0) { s1 += lo2(x2[t][m]) + hi2(x2[t][m]); \
        s2 = fma2(lo2(x2[t][m]), lo2(x2[t][m]), s2); s2 = fma2(hi2(x2[t][m]), hi2(x2[t][m]), s2); } }
      P3M(1, 0) P3M(1, 1) P3M(1, 2) P3M(1, 3)
#pragma unroll
      for (int m = 0; m < 4; ++m) { gav[0][m] = lds4f(L + ro, m * 64); gav[1][m] = lds4f(L + ro1, m * 64); }
      sb();
      P3M(0, 0)
      mfence<0>(acc[1][0], acc[1][1], acc[1][2], acc[1][3]);
      const v4f A1 = acc[1][0] + acc[1][1], B1 = acc[1][0] - acc[1][1], P1 = acc[1][3] - acc[1][2], Q1 = acc[1][2] + acc[1][3];
      sb();
      P3M(0, 1)
      P3X(1, 0, A1 - P1) P3X(1, 1, A1 + P1)
      sb();
      P3M(0, 2)
      P3X(1, 2, B1 - Q1) P3X(1, 3, B1 + Q1)
      sb();
      P3M(0, 3)
      mfence<7>(acc[0][0], acc[0][1], acc[0][2], acc[0][3]);
      {
        const v4f A = acc[0][0] + acc[0][1], B = acc[0][0] - acc[0][1], P = acc[0][3] - acc[0][2], Q = acc[0][2] + acc[0][3];
        v4f y[4] = {A - P, A + P, B - Q, B + Q};
        const bool sp = tid < 16;
        y[0].x = sp ? A.x : y[0].x; y[2].x = sp ? B.x : y[2].x;
        y[1].x = sp ? acc[1][0].x - acc[0][3].x : y[1].x; y[3].x = sp ? acc[1][0].x + acc[0][3].x : y[3].x;
        P3X(0, 0, y[0]) P3X(0, 1, y[1]) P3X(0, 2, y[2]) P3X(0, 3, y[3])
      }
      float sm = s1.x + s1.y, sq = s2.x + s2.y;
      sumg2(sm, sq);
      if (g == 0) *(LAS v2f*)(L + ex + wave * 128) = v2f{sm, sq};
      v4f sbv[2][4], cbv[2][4];
#pragma unroll
      for (int m = 0; m < 4; ++m) {
        sbv[0][m] = lds4f(L + ro, 1792 + m * 64); cbv[0][m] = lds4f(L + ro, 2 * 1792 + m * 64);
        sbv[1][m] = lds4f(L + ro1, 1792 + m * 64); cbv[1][m] = lds4f(L + ro1, 2 * 1792 + m * 64);
      }
      bar();
      v2f e = *(const LAS v2f*)(L + ex);
#pragma unroll
      for (int w = 1; w < 4; ++w) e += *(const LAS v2f*)(L + ex + w * 128);
      const float mc = e.x * (1.f / D), rstd = __builtin_amdgcn_rsqf(fmaxf(e.y * (1.f / D) - mc * mc, 0.f) + 1e-5f), nb = -mc * rstd;
      const int ho = WS_H2 + blk * 14336 + 2 * t0 * 1024 + l16, ho1 = wave == 0 ? OOB : ho + 2048;
#pragma unroll
      for (int t = 0; t < 2; ++t)
#pragma unroll
        for (int a = 0; a < 2; ++a) {
          v4u o;
#pragma unroll
          for (int hh = 0; hh < 2; ++hh) {
            const int m = 2 * a + hh;
            const v4f hv = fma4(fma4(x2[t][m], splat(rstd), splat(nb)), cbv[t][m], sbv[t][m]);
            o[2 * hh] = pk2(lo2(hv)); o[2 * hh + 1] = pk2(hi2(hv));
          }
          st4c(ws, (t ? ho1 : ho) + a * 1024, o);
        }
      STAMP(6);
      sb();
#pragma unroll
      for (int t = 0; t < 2; ++t)
        if (t == 0 || wave != 0)
#pragma unroll
          for (int m = 0; m < 4; ++m)
#pragma unroll
            for (int r = 0; r < 4; r += 2) {
              const u32 p = pk(x2[t][m][r] + st.x, x2[t][m][r + 1] + st.x);
              *(LAS u16*)(L + XOFF(t, m, r)) = (u16)p;
              *(LAS u16*)(L + XOFF(t, m, r + 1)) = (u16)(p >> 16);
            }
      sb();
    }
    bar();
    if (!isG) {
      x2_flush(L, ws, blk, tid);
      drain();
      if (lane == 0) st2c(ws, WS_XRDY + blk * 64 + wave * 8, v2u{tag, ~tag});
      DUMP(); return;
    }
  } else {
    const int sj = j >= 30 ? j - 30 : j + 2;
    const int idx = sj * 8 + grp;
    const bool isC = j < 14 || idx == 0;
    const int pair = j < 14 ? grp * 14 + j : 112;
    const int lc = grp + 8 * (sj >> 1);
    const int qu = sj < 4 && lc < 14 ? 2 * lc + (sj & 1) : -1;
    const int t1o = WS_T1 + (32 * wave + (lane >> 3)) * T1_BLK + pair * 128 + (lane & 7) * 16;
    ModLoad ml;
    epoch = ld1c(ws, WS_EPOCH);
    if (qu >= 0) mod_load(ml, cin, wm, qu, tid);
#pragma unroll
    for (int q = 0; q < 4; ++q) st4c(ws, isC ? t1o + 8 * q * T1_BLK : OOB, sv);
    u32 pf0, pf1, pf2;
    {
      const rsrc_t mr = mkrs(wm, D * 6 * D * 2), xr = mkrs(xin, S * D * 2), fr_ = mkrs((wave & 1) ? wi : wr, S * DF * 2);
      const int row = (sj * 4 + wave) * 7 + (lane >> 1), l7 = lane / 7;
      pf0 = __builtin_amdgcn_raw_buffer_load_b32(mr, lane < 14 && (grp < 6 || !(lane & 1)) ? row * (6 * D * 2) + (grp + 8 * (lane & 1)) * 128 : OOB, 0, PF_AUX);
      pf1 = __builtin_amdgcn_raw_buffer_load_b32(xr, lane < 28 ? tok_row(16 * grp + sj, 4 * wave + l7) * (D * 2) + (lane - 7 * l7) * 128 : OOB, 0, PF_AUX);
    }
    sb();
    epoch = __builtin_amdgcn_readfirstlane(epoch);
    tag = mktag(epoch);
    if (qu >= 0) mod_part(ml, L, tid);
    asm volatile("s_waitcnt vmcnt(%0)" :: "i"(W_LATE ? 2 : 3) : "memory");
    { const rsrc_t fr_ = mkrs((wave & 1) ? wi : wr, S * DF * 2);
      pf2 = __builtin_amdgcn_raw_buffer_load_b32(fr_, lane < 29 ? (((16 * grp + sj) * 7200) & ~127) + ((wave >> 1) * 29 + lane) * 128 : OOB, 0, PF_AUX);
    }
    bar();
    if (wave == 0 && qu >= 0) mod_publish(L, ws, qu, lane, tag);
    if (wave == 1 && lane < 8) st2c(ws, isC ? WS_RDY + lane * 1024 + pair * 8 : OOB, v2u{tag, ~tag});
    STAMP(1);
    pin(pf0); pin(pf1); pin(pf2); pin(pfa); pin(pfb);
    const int qn = sj >= 4 && sj < 10 && grp < 7 ? 28 + grp * 6 + (sj - 4) : !isC && idx < 15 ? 69 + idx : -1;
    const int tw = sj >= 10 ? (sj - 10) * 8 + grp : sj == 4 && grp == 7 ? 48 : -1;
    v2u dw[2][8];
    if (qn >= 0) mod_load(ml, cin, wm, qn, tid);
    if (tw >= 0) {
      const rsrc_t dr = mkrs(wd, F * D * 2);
      const int u0 = tw * 4 + wave, uc = u0 / 28, kap = 2 * (u0 - uc * 28);
#pragma unroll
      for (int e = 0; e < 16; ++e)
        dw[e >> 3][e & 7] = __builtin_amdgcn_raw_buffer_load_b64(dr, (4 * g * D + 4 * c16) * 2 + (e & 3) * D * 2, ((32 * kap + 16 * (e >> 2)) * D + 64 * uc) * 2, PF_AUX);
    }
    sb();
    {
      const rsrc_t rg = mkrs(wg, D * F * 2), ru = mkrs(wu, D * F * 2);
      const int r7 = lane / 7, po = lane < 56 && j < 14 ? ((j * 4 + wave) * 8 + r7) * (F * 2) + ((grp & 3) * 7 + lane - r7 * 7) * 128 : OOB;
      pf0 = ld1(rg, po); pf1 = ld1(ru, po);
    }
    sb();
    STAMP(20);
    if (tw >= 0) {
#pragma unroll
      for (int tt = 0; tt < 8; ++tt) {
        v4u o;
#pragma unroll
        for (int m = 0; m < 4; ++m)
          o[m] = __builtin_amdgcn_perm(dw[tt >> 2][2 * m + 1][(tt & 3) >> 1], dw[tt >> 2][2 * m][(tt & 3) >> 1], (tt & 1) ? 0x07060302u : 0x05040100u);
        st4c(ws, WS_WD + (tw * 4 + wave) * 8192 + tt * 1024 + l16, o);
      }
    }
    if (qn >= 0) {
      bar();
      mod_part(ml, L, tid);
      bar();
      if (wave == 0) mod_publish(L, ws, qn, lane, tag);
    }
    STAMP(21);
    if (!isC) { DUMP(); return; }
    v4u c64[2][4], c32[2][2][2];
#pragma unroll
    for (int cp = 0; cp < 2; ++cp)
#pragma unroll
      for (int s = 0; s < 4; ++s) c64[cp][s] = ld4(tab, l16 + s * 1024, (TAB_P2 + wave * 2 + cp) * 4096);
#pragma unroll
    for (int t = 0; t < 2; ++t)
#pragma unroll
      for (int f = 0; f < 4; ++f) c32[t][f >> 1][f & 1] = ld4(tab, l16 + f * 1024, (TAB_P2 + 8 + t) * 4096);
    sb();
    if (tw >= 0) {
      asm volatile("s_waitcnt vmcnt(16)" ::: "memory");
      bar();
      if (tid < 8) st2c(ws, WS_DRDY + tid * 1024 + tw * 8, v2u{tag, ~tag});
    }
    v4f t1c[2], t1s[2], t2c[2], t2s[2];
#pragma unroll
    for (int t = 0; t < 2; ++t) {
#pragma unroll
      for (int r = 0; r < 4; ++r) {
        const float a1 = (float)((16 * t + 4 * g + r) * (16 * wave + c16)) * (1.f / S);
        const float a2 = (float)((16 * t + c16) * (16 * wave + 4 * g + r)) * (1.f / S);
        t1c[t][r] = __builtin_amdgcn_cosf(a1); t1s[t][r] = __builtin_amdgcn_sinf(a1);
        t2c[t][r] = __builtin_amdgcn_cosf(a2); t2s[t][r] = -__builtin_amdgcn_sinf(a2);
      }
      pin(t1c[t]); pin(t1s[t]); pin(t2c[t]); pin(t2s[t]);
    }
    STAMP(2);
    int wfo = WS_WT + pair * 16384 + (16 * g + wave) * 128 + c16 * 8;
    pin(wfo);
    v2u wf[2][4];
    {
      int wl = L_CI + ((lane >> 2) & 1) * 8256 + wave * 2048 + ((lane >> 3) * 4 + (lane & 3)) * 16, lo = t1o;
      pin(wl); pin(lo);
      light_poll(ws, WS_T1 + (32 * wave + (pair & 31)) * T1_BLK + pair * 128);
      pin(pf0); pin(pf1);
      STAMP(13);
      v4u z[4];
      _Pragma("nounroll") for (int s = 0; s < SPIN; ++s) {
        lo = fresh(lo);
#pragma unroll
        for (int q = 0; q < 4; ++q) z[q] = ld4c(ws, lo, 8 * q * T1_BLK);
        if (wall(max(max(max4(z[0]), max4(z[1])), max(max4(z[2]), max4(z[3]))) != SENT)) break;
      }
      STAMP(14);
#pragma unroll
      for (int q = 0; q < 4; ++q) *(LAS v4u*)(L + wl + q * 512) = z[q];
#pragma unroll
      for (int t = 0; t < 2; ++t)
#pragma unroll
        for (int r = 0; r < 4; ++r) wf[t][r] = ld2c(ws, wfo + r * 512, t * 8192);
    }
    bar();
    STAMP(4);
    {
      v4u a[2][2][4];
#pragma unroll
      for (int c = 0; c < 2; ++c)
#pragma unroll
        for (int mt = 0; mt < 2; ++mt)
#pragma unroll
          for (int s = 0; s < 4; ++s) a[c][mt][s] = lds4(LB, L_CI + c * 8256 + (s * 2 + mt) * 1024);
      v4u old[2][2];
#pragma unroll
      for (int c = 0; c < 2; ++c)
#pragma unroll
        for (int mt = 0; mt < 2; ++mt) old[c][mt] = lds4(LB + wave * 2048, L_CI + c * 8256 + mt * 1024);
      v4f re[2][2], im[2][2];
      v4u f2[2][2], f3[2][2];
      char LAS* xw = L + lb + wave * 1024 - L_B;
#define S1H(c, mt) { mf0(re[c][mt], a[c][mt][0], c64[0][0]); mf0(im[c][mt], a[c][mt][0], c64[1][0]); }
#define S1T(c, mt) { _Pragma("unroll") for (int s = 1; s < 4; ++s) { mfa(re[c][mt], a[c][mt][s], c64[0][s]); mfa(im[c][mt], a[c][mt][s], c64[1][s]); } }
#define S2(c, t) { mf0(re[c][t], c32[t][0][0], f2[c][0]); mf0(im[c][t], c32[t][1][0], f2[c][0]); \
      mfa(re[c][t], c32[t][0][1], f2[c][1]); mfa(im[c][t], c32[t][1][1], f2[c][1]); }
#define S3(c, t) { mf0(im[c][t], f3[c][0], c32[t][0][0]); mf0(re[c][t], f3[c][0], c32[t][1][0]); \
      mfa(im[c][t], f3[c][1], c32[t][0][1]); mfa(re[c][t], f3[c][1], c32[t][1][1]); }
#define TW1(c, t) { f2[c][t] = cmul_pack<false>(re[c][t], im[c][t], t1c[t], t1s[t]); pin(f2[c][t]); }
#define FLT(c, t) { const v4f fr_ = {blo(wf[t][0][c]), blo(wf[t][1][c]), blo(wf[t][2][c]), blo(wf[t][3][c])}, \
      fi_ = {bhi(wf[t][0][c]), bhi(wf[t][1][c]), bhi(wf[t][2][c]), bhi(wf[t][3][c])}; \
      f3[c][t] = cmul_pack<true>(re[c][t], im[c][t], fr_, -fi_); pin(f3[c][t]); }
#define TW2(c, t) { *(LAS v4u*)(xw + L_CX + (c * 2 + t) * 4096) = cmul_pack<true>(re[c][t], im[c][t], t2c[t], t2s[t]); }
#define F2(c, t) mfence<0>(re[c][t], im[c][t]);
      S1H(0, 0) S1T(0, 0) S1H(0, 1) S1T(0, 1) sb();
      F2(0, 0) S1H(1, 0) S1T(1, 0) TW1(0, 0) sb();
      F2(0, 1) S1H(1, 1) S1T(1, 1) TW1(0, 1) sb();
      F2(1, 0) S2(0, 0) S2(0, 1) TW1(1, 0) sb();
      F2(1, 1) TW1(1, 1) sb();
      F2(0, 0) S2(1, 0) FLT(0, 0) sb();
      F2(0, 1) S2(1, 1) FLT(0, 1) sb();
      F2(1, 0) S3(0, 0) FLT(1, 0) sb();
      F2(1, 1) S3(0, 1) FLT(1, 1) sb();
      F2(0, 0) S3(1, 0) TW2(0, 0) sb();
      F2(0, 1) S3(1, 1) TW2(0, 1) sb();
      F2(1, 0) TW2(1, 0) sb();
      F2(1, 1) TW2(1, 1)
      bar();
#pragma unroll
      for (int c = 0; c < 2; ++c)
#pragma unroll
        for (int nt = 0; nt < 2; ++nt)
#pragma unroll
          for (int s = 0; s < 4; ++s) a[c][nt][s] = lds4(LB, L_CX + ((c * 2 + nt) * 4 + s) * 1024);
      const int o2 = WS_T1 + (32 * wave + (lane >> 2)) * T1_BLK + pair * 128 + (lane & 3) * 16;
#define S4H(c, t) { mf0(im[c][t], c64[0][0], a[c][t][0]); mf0(re[c][t], c64[1][0], a[c][t][0]); }
#define S4T(c, t) { _Pragma("unroll") for (int s = 1; s < 4; ++s) { mfa(im[c][t], c64[0][s], a[c][t][s]); mfa(re[c][t], c64[1][s], a[c][t][s]); } }
#define NEQ(x, y) ((x) == (y) ? (x) ^ 1u : (x))
#define OUT(c, t) { const v4u o = {pk(re[c][t].x, im[c][t].x), pk(re[c][t].y, im[c][t].y), pk(re[c][t].z, im[c][t].z), pk(re[c][t].w, im[c][t].w)}; \
      st4c(ws, o2 + c * 64 + t * 16 * T1_BLK, v4u{NEQ(o.x, old[c][t].x), NEQ(o.y, old[c][t].y), NEQ(o.z, old[c][t].z), NEQ(o.w, old[c][t].w)}); }
      S4H(0, 0) S4T(0, 0) S4H(0, 1) mfence<1>(re[0][0], im[0][0]); OUT(0, 0) sb();
      S4T(0, 1) S4H(1, 0) mfence<1>(re[0][1], im[0][1]); OUT(0, 1) sb();
      S4T(1, 0) S4H(1, 1) mfence<1>(re[1][0], im[1][0]); OUT(1, 0) sb();
      S4T(1, 1) mfence<7>(re[1][1], im[1][1]); OUT(1, 1)
    }
    STAMP(5);
    sb();
    if (!isG) { DUMP(); return; }
    dma_w1(wg, wu, slice, wave, lane, lbase);
    wait_flags(ws, WS_DRDY + grp * 1024 + min(lane, 48) * 8, tag);
#pragma unroll
    for (int f = 0; f < WD_MID; ++f) wdn[f >> 2][f & 3] = ld4c(ws, l16 + (f & 3) * 1024, wdb + (f >> 2) * 4096);
    sb();
    wait_flags(ws, WS_HRDY + (16 * tile + 4 * wave) * 32 + c16 * 8, tag);
    bar();
  }

  STAMP(7);
  const int blk0 = 16 * tile + 4 * (grp & 3);
  const int ab = WS_ACT + blk0 * 57344 + wave * 14 * 1024;
  const int fo = WS_ARDY + (tile * 4 + (grp & 3)) * 512 + (7 * wave + min(lane, 6)) * 8;
  v4u h[14][2];
  {
    const int hb = WS_H2 + (16 * tile + 4 * wave) * 14336;
    const int fw_ = WS_ARDY + (tile * 4 + wave) * 512 + slice * 8;
    int tb[2][4];
#pragma unroll
    for (int u = 0; u < 8; ++u) {
      tb[u >> 2][u & 3] = (((lane >> 2) * 128 + ((lane >> 3) & 3) * 32 + (lane & 3) * 8) ^ ((u & 3) << 5)) + (u >> 2) * 57344;
      pin(tb[u >> 2][u & 3]);
    }
#define W_RD(dst, k, u) dst = ldtr2(L + tb[k >= 7][u & 3], (k % 7) * 8192 + (u >> 2) * 4096, (k % 7) * 8192 + (u >> 2) * 4096 + 2048)
#define H_LD(k, tt, base) h[k][tt] = ld4c(ws, l16 + (k & 3) * 1024, base + (k >> 2) * 4096)
    v4u wfr[2][8];
#pragma unroll
    for (int u = 0; u < 8; ++u) W_RD(wfr[0][u], 0, u);
    light_poll(ws, hb + (lane & 1) * 14336 + 13 * 1024);
    STAMP(15);
#pragma unroll
    for (int e = 0; e < 8; ++e) H_LD((e >> 1), (e & 1), hb + (e & 1) * 14336);
    sb();
    if (isR) x2_flush(L, ws, 16 * grp + j - 14, tid);
    sb();
#define BAD2(x0, x1) wany(max(max(max(max(x0.x, x0.y), x0.z), max(max(x0.w, x1.x), x1.y)), max(x1.z, x1.w)) == SENT)
#define RETRY2(x0, x1, o0, o1) \
      _Pragma("nounroll") for (int s_ = 0; s_ < SPIN; ++s_) { \
        const int lf_ = fresh(l16); SLOWCNT \
        x0 = ld4c(ws, lf_, o0); x1 = ld4c(ws, lf_, o1); \
        if (wall(max(max4(x0), max4(x1)) != SENT)) break; \
      }
    if (__builtin_expect(BAD2(h[0][0], h[0][1]), 0)) { RETRY2(h[0][0], h[0][1], hb, hb + 14336) }
    STAMP(8); CSTAMP(28);
    v4f acc[2][8][2];
    v2u fa;
    const int ao = WS_ACT + (16 * tile + 4 * wave) * 57344 + 2 * slice * 1024 + l16;
#define ACT_OUT(ps, tt, f) { v4u o; \
      _Pragma("unroll") for (int hh = 0; hh < 2; ++hh) { \
        const v4f av = __builtin_elementwise_max(acc[ps][2 * f + hh][tt], splat(0.f)) * acc[ps][4 + 2 * f + hh][tt]; \
        o[2 * hh] = pk2(lo2(av)); o[2 * hh + 1] = pk2(hi2(av)); } \
      st4c(ws, ao + (2 * ps + tt) * 57344 + f * 1024, o); }
#pragma unroll
    for (int ps = 0; ps < 2; ++ps) {
#pragma unroll
      for (int u = 0; u < 8; ++u) { acc[ps][u][0] = v4f{0.f, 0.f, 0.f, 0.f}; acc[ps][u][1] = acc[ps][u][0]; }
#pragma unroll
      for (int ks = 0; ks < 14; ++ks) {
        const int cur = ks & 1, nk = ks == 13 ? 0 : ks + 1;
        const bool more = ps == 0 || ks < 13, epi = ps == 1 && ks < 4;
        __builtin_amdgcn_s_waitcnt(0xC07F);
#pragma unroll
        for (int u = 0; u < 8; ++u) {
          acc[ps][u][0] = mfma(wfr[cur][u], h[ks][0], acc[ps][u][0]);
          if (more) W_RD(wfr[cur ^ 1][u], nk, u);
        }
        if (ps == 0) {
#pragma unroll
          for (int e = 8 + 4 * ks; e < 12 + 4 * ks; ++e)
            if (e < 28) H_LD((e >> 1), (e & 1), hb + (e & 1) * 14336);
#pragma unroll
          for (int e = 0; e < 28; ++e)
            if (e >= RF(ks) && e < RF(ks + 1)) H_LD((e >> 1), (e & 1), hb + (2 + (e & 1)) * 14336);
        } else {
          if (ks == 0) { H_LD(13, 0, hb + 2 * 14336); H_LD(13, 1, hb + 3 * 14336); }
#pragma unroll
          for (int e = 0; e < WD_PER; ++e) {
            const int f = WD_MID + WD_PER * ks + e;
            if (f < 56) wdn[f >> 2][f & 3] = ld4c(ws, l16 + (f & 3) * 1024, wdb + (f >> 2) * 4096);
          }
          if (ks == 12) fa = ld2c(ws, fo);
        }
        const bool bad = more && BAD2(h[nk][0], h[nk][1]);
#pragma unroll
        for (int u = 0; u < 8; ++u) acc[ps][u][1] = mfma(wfr[cur][u], h[ks][1], acc[ps][u][1]);
        if (epi) ACT_OUT(0, (ks >> 1), (ks & 1))
#pragma unroll
        for (int u = 0; u < 16; ++u) {
          __builtin_amdgcn_sched_group_barrier(0x8, 1, 0);
          if (u < 4) __builtin_amdgcn_sched_group_barrier(0x100, 2, 0);
          else if (u < 12) __builtin_amdgcn_sched_group_barrier(0x100, 1, 0);
          if (u >= 4 && u < 12) __builtin_amdgcn_sched_group_barrier(0x20, 1, 0);
          if (epi) __builtin_amdgcn_sched_group_barrier(0x2, 3, 0);
          else if (u >= 10) __builtin_amdgcn_sched_group_barrier(0x2, 1, 0);
        }
        sb();
        if (ps == 1 && ks == 6) {
          static_assert(WD_MID + 7 * WD_PER <= 56, "the count below");
          asm volatile("s_waitcnt vmcnt(%0)" :: "i"(3 * WD_PER) : "memory");
          if (lane < 2) st2c(ws, lane == 0 ? fw_ : isR ? WS_XRDY + (16 * grp + j - 14) * 64 + wave * 8 : OOB, v2u{tag, ~tag});
        }
        if (__builtin_expect(bad, 0)) {
          const int pb = (ks == 13 ? 2 : 2 * ps) * 14336 + nk * 1024;
          RETRY2(h[nk][0], h[nk][1], hb + pb, hb + pb + 14336)
        }
        if (ps == 0 && ks == 13) { STAMP(16); CSTAMP(29); }
        sb();
      }
    }
    STAMP(27);
    if (!wall(fa.x == tag && fa.y == ~tag)) wait_flags(ws, fo, tag);
    STAMP(26);
#pragma unroll
    for (int e = 0; e < A_FIRST; ++e) H_LD((e >> 1), (e & 1), ab + (e & 1) * 57344);
    sb();
    ACT_OUT(1, 0, 0) ACT_OUT(1, 0, 1) ACT_OUT(1, 1, 0) ACT_OUT(1, 1, 1)
    CSTAMP(30);
    sb();
    STAMP(9);
    const int blk = blk0 + (wave >> 1);
    const int xo = WS_X2 + blk * 14336 + c16 * 896 + ct * 128 + g * 32 + (wave & 1) * 16;
    const int go = WS_MOD + grp * MOD_REP + (70 + 2 * ct) * 128 + g * 64 + (wave & 1) * 32;
    const int xf = WS_XRDY + (blk + (lane & 4 ? 2 : 0)) * 64 + (lane & 3) * 8;
    v4u a[14][2];
    v4f ac2[4][4];
#pragma unroll
    for (int t = 0; t < 4; ++t)
#pragma unroll
      for (int n = 0; n < 4; ++n) ac2[t][n] = v4f{0.f, 0.f, 0.f, 0.f};
    v4u xv[2], gv[2];
#pragma unroll
    for (int ks = 0; ks < 14; ++ks) {
#pragma unroll
      for (int tt = 0; tt < 2; ++tt)
#pragma unroll
        for (int n = 0; n < 4; ++n) ac2[tt][n] = mfma(wdn[ks][n], h[ks][tt], ac2[tt][n]);
#pragma unroll
      for (int e = A_FIRST + 3 * ks; e < A_FIRST + 3 * ks + 3; ++e)
        if (e < 28) H_LD((e >> 1), (e & 1), ab + (e & 1) * 57344);
#pragma unroll
      for (int u = 0; u < 4; ++u) {
        __builtin_amdgcn_sched_group_barrier(0x8, 2, 0);
        __builtin_amdgcn_sched_group_barrier(0x20, 1, 0);
      }
      sb();
      if (ks == F_AT) {
        static_assert(A_FIRST + 3 * F_AT + 3 <= 28, "the count below");
        asm volatile("s_waitcnt vmcnt(%0)" :: "i"(3 * F_AT + 3) : "memory");
        if (lane == 0) st2c(ws, fw_ + 256, v2u{tag, ~tag});
      }
      if (ks == 7) {
        fa = ld2c(ws, xf);
        gv[0] = ld4c(ws, go); gv[1] = ld4c(ws, go + 16);
      }
      if (ks == 11) {
        if (!wall(fa.x == tag && fa.y == ~tag)) wait_flags(ws, xf, tag);
        xv[0] = ld4c(ws, xo); xv[1] = ld4c(ws, xo + 2 * 14336);
      }
      sb();
    }
    STAMP(17);
    int rw = lane * 8 + wave * 2048, rr_ = lane * 8 + wave * 8192;
    pin(rw); pin(rr_);
#define RED_WR(ph) _Pragma("unroll") for (int dd = 0; dd < 4; ++dd) if (dd != wave) \
      _Pragma("unroll") for (int n = 0; n < 4; ++n) \
        *(LAS v2f*)(L + rw + ph * 32768 + dd * 8192 + n * 512) = (dd & 1) ? hi2(ac2[2 * ph + (dd >> 1)][n]) : lo2(ac2[2 * ph + (dd >> 1)][n]);
#define RED_FIN(ph) { v2f r[4]; \
      _Pragma("unroll") for (int n = 0; n < 4; ++n) { \
        const v4f own = wave < 2 ? ac2[2 * ph][n] : ac2[2 * ph + 1][n]; r[n] = (wave & 1) ? hi2(own) : lo2(own); } \
      _Pragma("unroll") for (int w = 1; w < 4; ++w) \
        _Pragma("unroll") for (int n = 0; n < 4; ++n) r[n] += *(const LAS v2f*)(L + rr_ + ((wave + w) & 3) * 2048 + ph * 32768 + n * 512); \
      v4u o; \
      _Pragma("unroll") for (int q = 0; q < 2; ++q) { \
        o[2 * q] = pk2(fma2(bun(gv[q].x), v2f{r[0][q], r[1][q]}, bun(xv[ph][2 * q]))); \
        o[2 * q + 1] = pk2(fma2(bun(gv[q].z), v2f{r[2][q], r[3][q]}, bun(xv[ph][2 * q + 1]))); } \
      *(v4u*)(out + tok_row(blk + 2 * ph, c16) * D + ct * 64 + g * 16 + (wave & 1) * 8) = o; }
    fa = ld2c(ws, fo + 256);
    asm volatile("s_barrier" ::: "memory");
    RED_WR(0)
    _Pragma("nounroll") for (int s = 0; s < SPIN; ++s) {
      if (wall(gv[0].y == tag && gv[0].w == tag && gv[1].y == tag && gv[1].w == tag)) break;
      const int gf = fresh(go);
      gv[0] = ld4c(ws, gf); gv[1] = ld4c(ws, gf + 16);
    }
#define A_LD(e) a[(e) >> 1][(e) & 1] = ld4c(ws, l16 + (((e) >> 1) & 3) * 1024, ab + (2 + ((e) & 1)) * 57344 + ((e) >> 3) * 4096)
    if (!wall(fa.x == tag && fa.y == ~tag)) wait_flags(ws, fo + 256, tag);
    STAMP(18);
#pragma unroll
    for (int e = 0; e < B_FIRST; ++e) A_LD(e);
    sb();
    bar();
    RED_FIN(0)
    sb();
#pragma unroll
    for (int ks = 0; ks < 14; ++ks) {
#pragma unroll
      for (int tt = 0; tt < 2; ++tt)
#pragma unroll
        for (int n = 0; n < 4; ++n) ac2[2 + tt][n] = mfma(wdn[ks][n], a[ks][tt], ac2[2 + tt][n]);
#pragma unroll
      for (int e = B_FIRST + 3 * ks; e < B_FIRST + 3 * ks + 3; ++e)
        if (e < 28) A_LD(e);
#pragma unroll
      for (int u = 0; u < 4; ++u) {
        __builtin_amdgcn_sched_group_barrier(0x8, 2, 0);
        __builtin_amdgcn_sched_group_barrier(0x20, 1, 0);
      }
      sb();
    }
    STAMP(10);
    RED_WR(1)
    bar();
    RED_FIN(1)
    if (blockIdx.x == 0 && wave == 0) {
      _Pragma("nounroll") for (int s = 0; s < SPIN; ++s) {
        const v4u v = ld4c(ws, fresh(WS_MOD + (70 + (lane < 14 ? lane : 0)) * 128));
        if (wall(v.y == tag)) break;
      }
      if (lane == 0) __builtin_amdgcn_raw_buffer_store_b32(epoch + 1u, ws, WS_EPOCH, 0, SC1);
    }
  }
  if (blockIdx.x == PADB) { while (__builtin_amdgcn_s_memrealtime() < padt + PADT) __builtin_amdgcn_s_sleep(1); }
  DUMP();
}

extern "C" void kernel_launch(void* const* d_in, const int* in_sizes, int n_in, void* d_out, int out_size, void* d_ws,
                              size_t ws_size, hipStream_t stream) {
  fused<<<256, 256, 0, stream>>>((const u16*)d_in[0], (const u16*)d_in[1], (const u16*)d_in[2], (const u16*)d_in[4],
                                 (const u16*)d_in[5], (const u16*)d_in[6], (const u16*)d_in[7], (const u16*)d_in[8],
                                 (u16*)d_out, (char*)d_ws);
}
```

```cpp
#include <hip/hip_runtime.h>

typedef unsigned short u16;
typedef unsigned int u32;
typedef unsigned long long u64;
typedef u32 v4u __attribute__((ext_vector_type(4)));
typedef u32 v2u __attribute__((ext_vector_type(2)));
typedef float v4f __attribute__((ext_vector_type(4)));
typedef float v2f __attribute__((ext_vector_type(2)));
typedef short v8s __attribute__((ext_vector_type(8)));
typedef short v4s __attribute__((ext_vector_type(4)));
typedef __bf16 v8b __attribute__((ext_vector_type(8)));
typedef __bf16 v2b __attribute__((ext_vector_type(2)));
#define LAS __attribute__((address_space(3)))
#define GAS __attribute__((address_space(1)))
#define DEV __device__ __forceinline__

#define C_W1_EARLY 0
#define CLR_DELAY 0
#define W_DELAY 0
#define CLR2_DELAY 0
#define PF_POS 2
#define SL1 1
#define SL2 2
#define SL4 4
#define X_AFTER 8
#define PF_AUX 2
#define W_LATE 1
#define W1_PF 1
#define DMA_MOD ""
#define RF(ks) ((ks) <= 5 ? 0 : (3 * ((ks) - 5) < 2 * ((ks) - 1) ? 3 * ((ks) - 5) : 2 * ((ks) - 1)))
#define AF(ks) ((ks) <= A_AT + 1 ? 0 : (6 * ((ks) - A_AT - 1) < 2 * ((ks) - 1) ? 6 * ((ks) - A_AT - 1) : 2 * ((ks) - 1)))
#define WD_PER 3
#define A_FIRST 10
#define B_FIRST 12
#define A_AT 9
#define F_AT 4
#define CODE_BYTES 45056
#define WD_EARLY 8
#define WD_MID 16
#define B_AT 9
constexpr int S = 2048, D = 448, DF = 225, F = 1792;

struct alignas(16) Chain { u16 v[2048]; };
struct Cos448 { u16 v[448]; };
constexpr double c_cos_t(double a) {
  double a2 = a * a, t = 1.0, s = 1.0;
  for (int i = 1; i <= 10; ++i) { t = -t * a2 / ((2 * i - 1) * (2 * i)); s += t; }
  return s;
}
constexpr double c_sin_t(double a) {
  double a2 = a * a, t = a, s = a;
  for (int i = 1; i <= 10; ++i) { t = -t * a2 / ((2 * i) * (2 * i + 1)); s += t; }
  return s;
}
constexpr double c_cos448(int m) {
  if (m > 224) m = 448 - m;
  double sg = 1.0;
  if (m > 112) { m = 224 - m; sg = -1.0; }
  const double w = 6.283185307179586476925286766559 / 448.0;
  return m == 112 ? 0.0 : (m <= 56 ? sg * c_cos_t(w * m) : sg * c_sin_t(w * (112 - m)));
}
constexpr u16 c_bf16(double d) {
  const u32 u = __builtin_bit_cast(u32, (float)d);
  return (u16)((u + 0x7FFFu + ((u >> 16) & 1u)) >> 16);
}
constexpr Cos448 mk_cos() { Cos448 c{}; for (int m = 0; m < 448; ++m) c.v[m] = c_bf16(c_cos448(m)); return c; }
constexpr Cos448 CTAB = mk_cos();
constexpr u16 bcos(int m) { return CTAB.v[m % 448]; }
constexpr u16 bsin(int m) { return CTAB.v[(m % 448 + 336) % 448]; }
constexpr u16 bneg(u16 b) { return b ? (u16)(b ^ 0x8000u) : b; }
constexpr u16 bscale(u16 b, int e) { return b ? (u16)(b + e * 0x80) : b; }

constexpr Chain mk_p1(int c) {
  const int tile = c >> 2, par = c & 1, sn = (c >> 1) & 1;
  Chain ch{};
  for (int s = 0; s < 4; ++s)
    for (int l = 0; l < 64; ++l) {
      const int kp = 16 * tile + (l & 15), k = 2 * kp + par;
      if (k > 224) continue;
      for (int j = 0; j < 8; ++j) {
        const int dl = 32 * s + 8 * (l >> 4) + j;
        if (dl > 112) continue;
        u16 b = sn ? bneg(bsin(k * dl)) : bcos(k * dl);
        if (dl == 0 || dl == 112) b = bscale(b, -1);
        ch.v[(s * 64 + l) * 8 + j] = b;
      }
    }
  return ch;
}
constexpr Chain mk_p3(int c) {
  const int tile = c >> 1, sn = c & 1;
  Chain ch{};
  for (int s = 0; s < 4; ++s)
    for (int l = 0; l < 64; ++l) {
      int dl = 16 * tile + (l & 15);
      if (c == 1 && dl == 0) dl = 112;
      for (int j = 0; j < 8; ++j) {
        const int k = 2 * (32 * s + 8 * (l >> 4) + j) + 1;
        if (k < 224) ch.v[(s * 64 + l) * 8 + j] = sn ? bsin(k * dl) : bcos(k * dl);
      }
    }
  return ch;
}
constexpr Chain mk_p2(int c) {
  Chain ch{};
  for (int f = 0; f < 4; ++f)
    for (int l = 0; l < 64; ++l)
      for (int j = 0; j < 8; ++j) {
        const int comp = c < 8 ? (c & 1) : (f >> 1), s = c < 8 ? f : (f & 1);
        const int o = 16 * (c < 8 ? (c >> 1) : (c - 8)) + (l & 15), i = 16 * s + 4 * (l >> 4) + (j >> 1);
        const int m = (c < 8 ? 7 : 14) * ((i * o) % 64);
        u16 b = 0;
        if (comp == 0) b = (j & 1) ? bsin(m) : bcos(m);
        else b = (j & 1) ? bcos(m) : bneg(bsin(m));
        ch.v[(f * 64 + l) * 8 + j] = b;
      }
  return ch;
}
template <int I> constexpr Chain P1C = mk_p1(I);
template <int I> constexpr Chain P3C = mk_p3(I);
template <int I> constexpr Chain P2C = mk_p2(I);
#define L10(T, b) T<b>, T<b + 1>, T<b + 2>, T<b + 3>, T<b + 4>, T<b + 5>, T<b + 6>, T<b + 7>, T<b + 8>, T<b + 9>
#define L29(T) L10(T, 0), L10(T, 10), T<20>, T<21>, T<22>, T<23>, T<24>, T<25>, T<26>, T<27>, T<28>
constexpr int TAB_P1 = 0, TAB_P3 = 29, TAB_P2 = 43, TAB_N = 53;
static __device__ const Chain TAB[TAB_N] = {L29(P1C), L10(P3C, 0), P3C<10>, P3C<11>, P3C<12>, P3C<13>, L10(P2C, 0)};

constexpr int WS_EPOCH = 0;
constexpr int WS_RDY = 4096;
constexpr int WS_HRDY = 4096 + 8192;
constexpr int WS_DRDY = 4096 + 16384;
constexpr int WS_XRDY = 131072 + 65536;
constexpr int WS_ARDY = 131072;
constexpr int WS_MOD = 32768, MOD_REP = 12288;
constexpr int T1_BLK = 226 * 64;
constexpr int WS_T1 = 1 << 18;
constexpr int WS_WT = WS_T1 + 128 * T1_BLK;
constexpr int WS_H2 = WS_WT + 113 * 16384;
constexpr int WS_X2 = WS_H2 + 128 * 14336;
constexpr int WS_WD = WS_X2 + 128 * 14336;
constexpr int WS_ACT = WS_WD + 7 * 56 * 4096;
constexpr int WS_END = WS_ACT + 128 * 57344;
constexpr int WS_DBG = 32 << 20;

constexpr int L_W1 = 0;
constexpr int L_B = 114688;
constexpr int XP = 912;
constexpr int L_XS = L_B;
constexpr int L_ST = L_XS + 16 * XP;
constexpr int L_M1 = L_ST + 128;
constexpr int L_AF = L_M1 + 4 * 1792;
constexpr int L_M2 = L_AF + 16384;
constexpr int L_EX = L_M2 + 3 * 1792;
constexpr int L_REND = L_EX + 512;
constexpr int ZPL = 8192;
constexpr int L_CI = L_B;
constexpr int L_CX = L_CI + 2 * 8256;
constexpr int L_RED = L_CX + 16384;
constexpr int L_DBG = 163840 - 1024;
constexpr int L_SZ = 163840;
static_assert(L_REND <= L_DBG && L_RED + 2048 <= L_DBG && 2 * ZPL <= 16384, "lds");

constexpr u32 SENT = 0xFFFFFFFFu;
constexpr int SPIN = 1 << 14;
constexpr int SC1 = 16;
constexpr int OOB = 0x7FFF0000;

typedef __amdgpu_buffer_rsrc_t rsrc_t;
DEV rsrc_t mkrs(const void* p, int bytes) { return __builtin_amdgcn_make_buffer_rsrc((void*)p, 0, bytes, 0x00020000); }
DEV v4u ld4(rsrc_t r, int off, int so = 0) { return __builtin_amdgcn_raw_buffer_load_b128(r, off, so, 0); }
DEV v2u ld2(rsrc_t r, int off, int so = 0) { return __builtin_amdgcn_raw_buffer_load_b64(r, off, so, 0); }
DEV v4u ld4c(rsrc_t r, int off, int so = 0) { return __builtin_amdgcn_raw_buffer_load_b128(r, off, so, SC1); }
DEV v2u ld2c(rsrc_t r, int off, int so = 0) { return __builtin_amdgcn_raw_buffer_load_b64(r, off, so, SC1); }
DEV u32 ld1c(rsrc_t r, int off, int so = 0) { return __builtin_amdgcn_raw_buffer_load_b32(r, off, so, SC1); }
DEV void st4c(rsrc_t r, int off, v4u v) { __builtin_amdgcn_raw_buffer_store_b128(v, r, off, 0, SC1); }
DEV void st2c(rsrc_t r, int off, v2u v) { __builtin_amdgcn_raw_buffer_store_b64(v, r, off, 0, SC1); }
DEV float blo(u32 v) { return __builtin_bit_cast(float, v << 16); }
DEV float bhi(u32 v) { return __builtin_bit_cast(float, v & 0xFFFF0000u); }
DEV v2f bun(u32 v) { return v2f{blo(v), bhi(v)}; }
DEV u32 pk(float lo, float hi) { return __builtin_bit_cast(u32, __builtin_convertvector(v2f{lo, hi}, v2b)); }
DEV u32 pk2(v2f v) { return __builtin_bit_cast(u32, __builtin_convertvector(v, v2b)); }
DEV float rb(float f) { return blo(pk(f, 0.f)); }
DEV v2f lo2(v4f v) { return __builtin_shufflevector(v, v, 0, 1); }
DEV v2f hi2(v4f v) { return __builtin_shufflevector(v, v, 2, 3); }
DEV v2f fma2(v2f a, v2f b, v2f c) { return __builtin_elementwise_fma(a, b, c); }
DEV v4f fma4(v4f a, v4f b, v4f c) { return __builtin_elementwise_fma(a, b, c); }
DEV v4f splat(float f) { return v4f{f, f, f, f}; }
DEV v4f mfma(v4u a, v4u b, v4f c) {
  return __builtin_amdgcn_mfma_f32_16x16x32_bf16(__builtin_bit_cast(v8b, a), __builtin_bit_cast(v8b, b), c, 0, 0, 0);
}
DEV void mfa(v4f& c, v4u a, v4u b) { asm volatile("s_nop 1\n\tv_mfma_f32_16x16x32_bf16 %0, %1, %2, %0" : "+v"(c) : "v"(a), "v"(b)); }
DEV void mf0(v4f& c, v4u a, v4u b) { asm volatile("s_nop 1\n\tv_mfma_f32_16x16x32_bf16 %0, %1, %2, 0" : "=&v"(c) : "v"(a), "v"(b)); }
template <int N> DEV void mfence(v4f& a, v4f& b) { asm volatile("s_nop %2" : "+v"(a), "+v"(b) : "i"(N)); }
template <int N> DEV void mfence(v4f& a, v4f& b, v4f& c, v4f& d) { asm volatile("s_nop %4" : "+v"(a), "+v"(b), "+v"(c), "+v"(d) : "i"(N)); }
template <class T> DEV void pin(T& v) { asm volatile("" : "+v"(v)); }
DEV int fresh(int off) { asm volatile("" : "+v"(off) :: "memory"); return off; }
DEV bool wall(bool ok) { return __builtin_amdgcn_ballot_w64(!ok) == 0; }
DEV bool wany(bool b) { return __builtin_amdgcn_ballot_w64(b) != 0; }
DEV void sb() { __builtin_amdgcn_sched_barrier(0); }
DEV u32 max4(v4u v) { return max(max(v.x, v.y), max(v.z, v.w)); }
DEV void drain() { asm volatile("s_waitcnt vmcnt(0)" ::: "memory"); }
DEV void cfence() { asm volatile("" ::: "memory"); }
DEV void bar() { asm volatile("s_waitcnt lgkmcnt(0)\n\ts_barrier" ::: "memory"); }
template <int CTRL> DEV float dpp(float v) {
  return __builtin_bit_cast(float, __builtin_amdgcn_update_dpp(0, __builtin_bit_cast(int, v), CTRL, 0xF, 0xF, true));
}
DEV float sum16(float v) {
  v += dpp<0x128>(v); v += dpp<0x124>(v); v += dpp<0x122>(v); v += dpp<0x121>(v); return v;
}
DEV void sumg2(float& a, float& b) {
  float c = a, d = b;
  asm volatile("s_nop 1\n\tv_permlane16_swap_b32 %0, %1\n\tv_permlane16_swap_b32 %2, %3" : "+v"(a), "+v"(c), "+v"(b), "+v"(d));
  a += c; b += d; c = a; d = b;
  asm volatile("s_nop 1\n\tv_permlane32_swap_b32 %0, %1\n\tv_permlane32_swap_b32 %2, %3" : "+v"(a), "+v"(c), "+v"(b), "+v"(d));
  a += c; b += d;
}
DEV v4u lds4(const char LAS* L, int off) { return *(const LAS v4u*)(L + off); }
DEV v4f lds4f(const char LAS* L, int off) { return *(const LAS v4f*)(L + off); }
DEV v2u ldtr(const char LAS* L, int off) { return __builtin_bit_cast(v2u, __builtin_amdgcn_ds_read_tr16_b64_v4i16((LAS v4s*)(L + off))); }
DEV v4u ldtr2(const char LAS* L, int o0, int o1) { const v2u a = ldtr(L, o0), b = ldtr(L, o1); return v4u{a.x, a.y, b.x, b.y}; }
DEV void dma16(rsrc_t r, int voff, int soff, u32 lds_dst) {
  u32 keep;
  asm volatile("s_mov_b32 %0, m0\n\ts_mov_b32 m0, %1\n\ts_nop 0\n\tbuffer_load_dwordx4 %2, %3, %4 offen" DMA_MOD " lds\n\ts_mov_b32 m0, %0"
               : "=&s"(keep) : "s"(lds_dst), "v"(voff), "s"(r), "s"(soff) : "memory");
}
DEV void light_poll(rsrc_t ws, int off) {
  _Pragma("nounroll") for (int s = 0; s < SPIN; ++s) {
    if (wall(ld1c(ws, fresh(off)) != SENT)) break;
    __builtin_amdgcn_s_sleep(SL2);
  }
}
DEV int tok_row(int blk, int t) {
  const int n1 = 16 * (blk >> 5) + 4 * ((blk >> 2) & 3) + (t & 3), n2 = 16 * ((blk >> 4) & 1) + 4 * (blk & 3) + (t >> 2);
  return n2 + 32 * n1;
}
template <bool SWAP> DEV v4u cmul_pack(v4f a, v4f b, v4f c, v4f s) {
  const v4f re = fma4(b, s, a * c), im = fma4(b, c, -(a * s));
  return SWAP ? v4u{pk(im.x, re.x), pk(im.y, re.y), pk(im.z, re.z), pk(im.w, re.w)}
              : v4u{pk(re.x, im.x), pk(re.y, im.y), pk(re.z, im.z), pk(re.w, im.w)};
}

#define STAMP(i)
#define CSTAMP(i)
#define SLOWCNT
#define DUMP()

DEV void dma_w1(const u16* wg, const u16* wu, int slice, int wave, int lane, u32 lbase) {
  const rsrc_t rg = mkrs(wg, D * F * 2), ru = mkrs(wu, D * F * 2);
  const int r = lane >> 3, ch = ((((lane & 7) >> 1) ^ (lane >> 4)) & 3) * 32 + (lane & 1) * 16 + slice * 128;
  const int vp = r * (F * 2) + ch, vn = (7 - r) * (F * 2) + ch;
#pragma unroll
  for (int n = 0; n < 4; ++n) {
    const int ks = wave + 4 * n;
    if (n == 3 && wave >= 2) break;
    const int t16 = (ks >> 1) * 16, a = ks & 1;
#pragma unroll
    for (int q = 0; q < 4; ++q) {
      const int dl0 = t16 + 8 * (q & 1);
      const bool pos = (q >> 1) == a;
      const int d0 = q < 2 ? (a ? 224 - dl0 - 7 : dl0) : (a ? 224 + dl0 : 448 - dl0 - 7);
      int vo = pos ? vp : vn, so = d0 * (F * 2);
      if (n == 0 && q == 2) {
        vo += so; so = 0;
        if (wave < 2 && lane < 8) vo = (wave ? 336 : 112) * (F * 2) + ch;
      }
      dma16(rg, vo, so, lbase + L_W1 + ks * 8192 + q * 1024);
      dma16(ru, vo, so, lbase + L_W1 + ks * 8192 + 4096 + q * 1024);
    }
  }
}
struct ModLoad { u32 cv[7]; v4u mw[7]; };
DEV void mod_load(ModLoad& m, const u16* cin, const u16* wm, int q, int tid) {
  const rsrc_t cr = mkrs(cin, q >= 0 ? D * 2 : 0), mr = mkrs(wm, q >= 0 ? D * 6 * D * 2 : 0);
#pragma unroll
  for (int i = 0; i < 7; ++i) m.cv[i] = __builtin_amdgcn_raw_buffer_load_b16(cr, (tid >> 2) * 2 + i * 128, 0, 0);
#pragma unroll
  for (int i = 0; i < 7; ++i) m.mw[i] = __builtin_amdgcn_raw_buffer_load_b128(mr, ((tid >> 2) * (6 * D) + q * 32 + (tid & 3) * 8) * 2, i * 64 * 6 * D * 2, PF_AUX);
}
DEV void mod_part(ModLoad& m, char LAS* L, int tid) {
  float sl[7];
#pragma unroll
  for (int i = 0; i < 7; ++i) { const float cc = blo(m.cv[i]); sl[i] = rb(cc * __builtin_amdgcn_rcpf(1.f + __expf(-cc))); pin(sl[i]); }
  v2f a[4];
#pragma unroll
  for (int e = 0; e < 4; ++e) a[e] = v2f{0.f, 0.f};
#pragma unroll
  for (int i = 0; i < 7; ++i)
#pragma unroll
    for (int e = 0; e < 4; ++e) a[e] = fma2(bun(m.mw[i][e]), v2f{sl[i], sl[i]}, a[e]);
#pragma unroll
  for (int e = 0; e < 4; ++e) {
    a[e].x += dpp<0x128>(a[e].x); a[e].y += dpp<0x128>(a[e].y);
    a[e].x += dpp<0x124>(a[e].x); a[e].y += dpp<0x124>(a[e].y);
  }
  if ((tid & 15) < 4) {
    *(LAS v4f*)(L + L_RED + ((tid >> 4) * 32 + (tid & 15) * 8) * 4) = v4f{a[0].x, a[0].y, a[1].x, a[1].y};
    *(LAS v4f*)(L + L_RED + ((tid >> 4) * 32 + (tid & 15) * 8 + 4) * 4) = v4f{a[2].x, a[2].y, a[3].x, a[3].y};
  }
}
DEV void mod_publish(char LAS* L, rsrc_t ws, int q, int lane, u32 tag) {
  v4f s = {0.f, 0.f, 0.f, 0.f};
#pragma unroll
  for (int r = 0; r < 16; ++r) s += lds4f(L, L_RED + (r * 32 + (lane & 7) * 4) * 4);
  const float one = (q / 14 == 1 || q / 14 == 4) ? 1.f : 0.f;
  st4c(ws, WS_MOD + (lane >> 3) * MOD_REP + q * 128 + (lane & 7) * 16,
       v4u{pk(rb(s.x) + one, rb(s.y) + one), tag, pk(rb(s.z) + one, rb(s.w) + one), tag});
}
DEV u32 ld1(rsrc_t r, int off) { return __builtin_amdgcn_raw_buffer_load_b32(r, off, 0, 0); }
DEV u32 mktag(u32 epoch) { return epoch * 0x9E3779B1u + 0x7F4A7C15u; }

DEV void x2_flush(char LAS* L, rsrc_t ws, int blk, int tid) {
#pragma unroll
  for (int m = 0; m < 4; ++m) {
    const int ch = min(tid + 256 * m, 895), tk = ch / 56;
    st4c(ws, tid + 256 * m < 896 ? WS_X2 + blk * 14336 + ch * 16 : OOB, lds4(L, L_XS + tk * XP + (ch - tk * 56) * 16));
  }
}
DEV bool flags_ok(v4u rv, u32 tag) { return rv.x == tag && rv.y == ~tag && rv.z == tag && rv.w == ~tag; }
DEV void wait_flags(rsrc_t ws, int off, u32 tag) {
  _Pragma("nounroll") for (int s = 0; s < SPIN; ++s) {
    const v2u rv = ld2c(ws, fresh(off));
    if (wall(rv.x == tag && rv.y == ~tag)) break;
    __builtin_amdgcn_s_sleep(SL1);
  }
}

__global__ void __launch_bounds__(256, 1)
fused(const u16* __restrict__ xin, const u16* __restrict__ wr, const u16* __restrict__ wi, const u16* __restrict__ wg,
      const u16* __restrict__ wu, const u16* __restrict__ wd, const u16* __restrict__ cin, const u16* __restrict__ wm,
      u16* __restrict__ out, char* wsp) {
  __shared__ __attribute__((aligned(16))) char smem[L_SZ];
  char LAS* L = (char LAS*)smem;
  const u32 lbase = (u32)(size_t)L;
  const int tid = threadIdx.x, lane = tid & 63, wave = __builtin_amdgcn_readfirstlane(tid >> 6);
  const int g = lane >> 4, c16 = lane & 15, l16 = lane * 16;
  const int grp = blockIdx.x & 7, j = blockIdx.x >> 3;
  u64 pc;
  asm volatile("s_getpc_b64 %0" : "=s"(pc));
  STAMP(0);
  asm volatile("" :: "s"(xin), "s"(wr), "s"(wi), "s"(wg), "s"(wu), "s"(wd), "s"(cin), "s"(wm), "s"(out), "s"(wsp));
  const rsrc_t ws = mkrs(wsp, WS_END);
  const rsrc_t tab = mkrs(TAB, TAB_N * 4096);
  const rsrc_t code = mkrs((const void*)(pc & ~127ull), CODE_BYTES);
  const bool isR = j >= 14 && j < 30;
  const bool isG = j < 28;
  const int gk = j / 7, ct = j - gk * 7;
  const int tile = 4 * (grp >> 2) + gk, slice = 7 * (grp & 3) + ct;
  const int wdb = WS_WD + (ct * 56 + wave * 14) * 4096;
  const v4u sv = {SENT, SENT, SENT, SENT};
  int lb = L_B + l16;
  pin(lb);
  const char LAS* LB = L + lb - L_B;
  const int pfo = ((j * 4 + wave) * 14 + lane) * 128, pfc = ((j * 4 + wave) * 4 + lane) * 128;
  u32 pfa = ld1(tab, lane < 14 ? pfo : OOB), pfb = ld1(code, lane < 4 ? pfc : OOB);
  v4u wdn[14][4];
  u32 epoch, tag;

  if (isR) {
    const int blk = 16 * grp + (j - 14);
    epoch = ld1c(ws, WS_EPOCH);
    const int t0 = wave == 0 ? 0 : 2 * wave - 1;
    v4u cf[8][4];
#define CF_LOAD(q) { const int chn = TAB_P1 + ((wave == 0 && q >= 4) ? 28 : 4 * t0 + q), vo = (q < 5 || wave != 0) ? l16 : OOB; \
      _Pragma("unroll") for (int s = 0; s < 4; ++s) cf[q][s] = ld4(tab, vo + s * 1024, chn * 4096); sb(); }
#pragma unroll
    for (int q = 0; q < X_AFTER; ++q) CF_LOAD(q)
    v2u xv[7];
    {
      const rsrc_t xr = mkrs(xin, S * D * 2);
      const int off = tok_row(blk, 4 * wave + g) * (D * 2) + c16 * 8;
#pragma unroll
      for (int m = 0; m < 7; ++m) xv[m] = ld2(xr, off + m * 128);
    }
    typedef u32 v3u __attribute__((ext_vector_type(3)));
    v2u fa_[2], fb_[2]; v3u fc_[2], fd_[2];
    {
      const rsrc_t rr = mkrs(wr, S * DF * 2), ri = mkrs(wi, S * DF * 2);
      const int fo_ = 16 * wave + c16 < 57 ? 2 * g * 450 + (16 * wave + c16) * 8 : OOB;
#pragma unroll
      for (int hh = 0; hh < 2; ++hh) {
        const int so = blk * 7200 + hh * 3600;
        fa_[hh] = ld2(rr, fo_, so); fb_[hh] = ld2(ri, fo_, so);
        fc_[hh] = __builtin_amdgcn_raw_buffer_load_b96(rr, fo_ + 448, so, 0); fd_[hh] = __builtin_amdgcn_raw_buffer_load_b96(ri, fo_ + 448, so, 0);
      }
    }
    sb();
#pragma unroll
    for (int q = X_AFTER; q < 8; ++q) CF_LOAD(q)
    STAMP(23);
    {
      v2f f[14], s2 = {0.f, 0.f};
#pragma unroll
      for (int m = 0; m < 7; ++m) {
        f[2 * m] = bun(xv[m].x); f[2 * m + 1] = bun(xv[m].y);
        s2 += f[2 * m] + f[2 * m + 1];
        *(LAS v2u*)(L + L_XS + (4 * wave + g) * XP + c16 * 8 + m * 128) = xv[m];
      }
      STAMP(21);
      sb();
      {
        const int q = 16 * wave + c16, o = WS_WT + q * 16384 + blk * 128 + g * 16;
#pragma unroll
        for (int hh = 0; hh < 2; ++hh) {
          const v2u a = fa_[hh], b = fb_[hh]; const v3u c = fc_[hh], d = fd_[hh];
          st4c(ws, q < 57 ? o + hh * 64 : OOB, v4u{__builtin_amdgcn_perm(b.x, a.x, 0x05040100u), __builtin_amdgcn_perm(b.y, a.y, 0x05040100u),
                                                   __builtin_amdgcn_perm(d.x, c.x, 0x07060302u), __builtin_amdgcn_perm(d.y, c.y, 0x07060302u)});
          st4c(ws, q < 56 ? o + hh * 64 + 57 * 16384 : OOB,
               v4u{__builtin_amdgcn_perm(b.x, a.x, 0x07060302u), __builtin_amdgcn_perm(b.y, a.y, 0x07060302u),
                   __builtin_amdgcn_perm(d.y, c.y, 0x05040100u), __builtin_amdgcn_perm(d.z, c.z, 0x05040100u)});
        }
      }
      sb();
      const float mean = sum16(s2.x + s2.y) * (1.f / D);
      v2f q2 = {0.f, 0.f};
#pragma unroll
      for (int m = 0; m < 14; ++m) { const v2f d = f[m] - mean; q2 = fma2(d, d, q2); }
      const float rstd = __builtin_amdgcn_rsqf(sum16(q2.x + q2.y) * (1.f / D) + 1e-5f);
      if (c16 == 0) *(LAS v2f*)(L + L_ST + (4 * wave + g) * 8) = v2f{mean, rstd};
    }
    pin(pfa); pin(pfb);
    epoch = __builtin_amdgcn_readfirstlane(epoch);
    tag = mktag(epoch);
    STAMP(22);
    bar();
    v4f n[4][2];
    const int d0 = 8 * (4 * wave + g);
    const v2f st = *(const LAS v2f*)(L + L_ST + c16 * 8);
    {
      int xr_ = L_XS + c16 * XP + d0 * 2, xb_ = L_XS + c16 * XP + (216 - d0) * 2, x0_ = d0 == 0 ? xr_ : xb_ + 464;
      pin(xr_); pin(xb_); pin(x0_);
      const v4u a = lds4(L + xr_, 0), e = lds4(L + xr_, 448), c = lds4(L + xb_, 0), b = lds4(L + xb_, 448);
      const u32 c0 = *(const LAS u16*)(L + xb_ + 16), b0 = *(const LAS u16*)(L + x0_);
#pragma unroll
      for (int i = 0; i < 8; ++i) {
        n[0][i >> 2][i & 3] = (i & 1) ? bhi(a[i >> 1]) : blo(a[i >> 1]);
        n[3][i >> 2][i & 3] = (i & 1) ? bhi(e[i >> 1]) : blo(e[i >> 1]);
        n[1][i >> 2][i & 3] = i == 0 ? blo(b0) : (i & 1) ? bhi(b[(8 - i) >> 1]) : blo(b[(8 - i) >> 1]);
        n[2][i >> 2][i & 3] = i == 0 ? blo(c0) : (i & 1) ? bhi(c[(8 - i) >> 1]) : blo(c[(8 - i) >> 1]);
      }
      const float nm = -st.x * st.y;
#pragma unroll
      for (int m = 0; m < 4; ++m) {
#pragma unroll
        for (int h = 0; h < 2; ++h) { n[m][h] = fma4(n[m][h], splat(st.y), splat(nm)); pin(n[m][h]); }
      }
    }
    sb();
    STAMP(1);
    int mb = L_M1 + d0 * 4;
    pin(mb);
    v4u xs[4];
    int to[4];
    to[0] = WS_T1 + blk * T1_BLK + (16 * t0 + c16) * 64 + g * 16;
    to[1] = wave != 0 ? to[0] + 1024 : c16 < 2 ? to[0] + 112 * 64 : OOB;
    to[2] = to[0] + 114 * 64;
    to[3] = wave != 0 ? to[0] + 1024 + 114 * 64 : OOB;
#pragma unroll
    for (int q = 0; q < 4; ++q) pin(to[q]);

    {
      const int kind = tid >= 112, d = 4 * (tid - 112 * kind);
      int wb = L_M1 + (kind ? 0 : 2 * 1792) + d * 4, wrv = L_M1 + (kind ? 0 : 2 * 1792) + 1792 + (445 - d) * 4;
      int wr0 = d == 0 ? wrv + 12 - 1792 : wrv + 12;
      const int mo = tid < 224 ? WS_MOD + grp * MOD_REP + tid * 16 : OOB;
      pin(wb); pin(wrv); pin(wr0);
      const int ro = WS_RDY + grp * 1024 + min(tid >> 1, 112) * 8;
      v4u mv;
      _Pragma("nounroll") for (int s = 0; s < SPIN; ++s) {
        mv = ld4c(ws, fresh(mo));
        const v2u rv = ld2c(ws, fresh(ro));
        if (wall((tid >= 224 || (mv.y == tag && mv.w == tag)) && rv.x == tag && rv.y == ~tag)) break;
        __builtin_amdgcn_s_sleep(SL1);
      }
      STAMP(2);
      if (tid < 224) {
        const v4f v = {blo(mv.x), bhi(mv.x), blo(mv.z), bhi(mv.z)};
        *(LAS v4f*)(L + wb) = v;
        *(LAS float*)(L + wr0) = v.x;
        *(LAS float*)(L + wrv + 8) = v.y;
        *(LAS float*)(L + wrv + 4) = v.z;
        *(LAS float*)(L + wrv) = v.w;
      }
    }
    bar();
    {
      v4u fr[4];
#pragma unroll
      for (int hh = 0; hh < 2; ++hh) {
        v4f h[4];
#pragma unroll
        for (int m = 0; m < 4; ++m) {
          const int o = ((m == 1 || m == 2) ? 1792 : 0) + (m >= 2 ? 896 : 0) + hh * 16;
          h[m] = fma4(n[m][hh], lds4f(L + mb, o), lds4f(L + mb, 2 * 1792 + o));
        }
        const v4f p = h[0] + h[1], q = h[2] + h[3], u = h[0] - h[1], v = h[3] - h[2];
        const v4f t[4] = {p + q, p - q, u + v, u - v};
#pragma unroll
        for (int m = 0; m < 4; ++m) { fr[m][2 * hh] = pk2(lo2(t[m])); fr[m][2 * hh + 1] = pk2(hi2(t[m])); }
      }
#pragma unroll
      for (int m = 0; m < 4; ++m) *(LAS v4u*)(LB + wave * 1024 + L_AF + m * 4096) = fr[m];
    }
    bar();
    {
      v4u a[4][4];
#pragma unroll
      for (int p = 0; p < 2; ++p)
#pragma unroll
        for (int s = 0; s < 4; ++s) { a[p][s] = lds4(LB, L_AF + (p * 4 + s) * 1024); a[2 + p][s] = lds4(LB, L_AF + ((2 + p) * 4 + s) * 1024); }
      v4f re[4], im[4];
#pragma unroll
      for (int q = 0; q < 4; ++q) {
        const int p = q >> 1, t = q & 1;
        mf0(re[q], a[p][0], cf[4 * t + p][0]); mf0(im[q], a[2 + p][0], cf[4 * t + 2 + p][0]);
        if (q > 0) {
          mfence<1>(re[q - 1], im[q - 1]);
          xs[q - 1] = v4u{pk(re[q - 1].x, im[q - 1].x), pk(re[q - 1].y, im[q - 1].y), pk(re[q - 1].z, im[q - 1].z), pk(re[q - 1].w, im[q - 1].w)};
          st4c(ws, to[q - 1], xs[q - 1]);
          sb();
        }
#pragma unroll
        for (int s = 1; s < 4; ++s) { mfa(re[q], a[p][s], cf[4 * t + p][s]); mfa(im[q], a[2 + p][s], cf[4 * t + 2 + p][s]); }
      }
      mfence<7>(re[3], im[3]);
      xs[3] = v4u{pk(re[3].x, im[3].x), pk(re[3].y, im[3].y), pk(re[3].z, im[3].z), pk(re[3].w, im[3].w)};
      st4c(ws, to[3], xs[3]);
    }
    STAMP(3);
    sb();
    sb();
    if (isG) dma_w1(wg, wu, slice, wave, lane, lbase);
#pragma unroll
    for (int q = 0; q < 4; ++q) {
      const int vo = (q < 2 || wave != 0) ? l16 : OOB;
#pragma unroll
      for (int s = 0; s < 4; ++s) cf[2 * q + 1][s] = ld4(tab, vo + s * 1024, (TAB_P3 + 2 * t0 + q) * 4096);
    }
#pragma unroll
    for (int m = 0; m < 4; ++m)
      st4c(ws, tid + 256 * m < 896 ? WS_H2 + blk * 14336 + 4096 * m + tid * 16 : OOB, sv);
    sb();
    STAMP(20);
    v4f xo[2][4];
    int xa = L_XS + c16 * XP + (16 * t0 + 4 * g) * 2, xd = L_XS + c16 * XP + (192 - 16 * t0 - 4 * g) * 2;
    int xs1 = tid < 16 ? L_XS + c16 * XP + 224 : xd + 512, xs3 = tid < 16 ? L_XS + c16 * XP + 672 : xa + 448;
    pin(xa); pin(xd); pin(xs1); pin(xs3);
#define XOFF(t, m, r) ((t) == 0 && (r) == 0 && (m) == 1 ? xs1 : (t) == 0 && (r) == 0 && (m) == 3 ? xs3 : \
    (m) == 0 ? xa + 32 * (t) + 2 * (r) : (m) == 3 ? xa + 448 + 32 * (t) + 2 * (r) : (m) == 1 ? xd + 512 - 32 * (t) - 2 * (r) : xd + 64 - 32 * (t) - 2 * (r))
    {
      v2u w[2][4]; u32 e[2][2], e3;
#pragma unroll
      for (int t = 0; t < 2; ++t) {
        w[t][0] = *(const LAS v2u*)(L + xa + 32 * t); w[t][3] = *(const LAS v2u*)(L + xa + 448 + 32 * t);
        w[t][1] = *(const LAS v2u*)(L + xd + 504 - 32 * t); w[t][2] = *(const LAS v2u*)(L + xd + 56 - 32 * t);
        e[t][0] = *(const LAS u16*)(L + XOFF(t, 1, 0)); e[t][1] = *(const LAS u16*)(L + XOFF(t, 2, 0));
      }
      e3 = *(const LAS u16*)(L + xs3);
      sb();
#pragma unroll
      for (int t = 0; t < 2; ++t) {
        xo[t][0] = v4f{blo(w[t][0].x), bhi(w[t][0].x), blo(w[t][0].y), bhi(w[t][0].y)};
        xo[t][3] = v4f{t == 0 ? blo(e3) : blo(w[t][3].x), bhi(w[t][3].x), blo(w[t][3].y), bhi(w[t][3].y)};
        xo[t][1] = v4f{blo(e[t][0]), bhi(w[t][1].y), blo(w[t][1].y), bhi(w[t][1].x)};
        xo[t][2] = v4f{blo(e[t][1]), bhi(w[t][2].y), blo(w[t][2].y), bhi(w[t][2].x)};
#pragma unroll
        for (int m = 0; m < 4; ++m) { xo[t][m] -= st.x; pin(xo[t][m]); }
      }
    }
    int m0_[2], m1_[2], md_[2];
#pragma unroll
    for (int r = 0; r < 2; ++r) {
      const int q = r == 0 || tid < 80 ? tid + 256 * r : tid;
      const int kind = (q * 586) >> 16, d = 4 * (q - 112 * kind);
      const int reg = (d >= 112) + (d >= 224) + (d >= 336), cc = ((reg + 1) >> 1) * 224, m1 = (0x1320 >> (4 * reg)) & 3;
      const int d1 = abs(d + 1 - cc), d0_ = abs(d - cc), edge = d == 112 * reg && reg > 0;
      m1_[r] = L_M2 + kind * 1792 + (((d1 >> 4) << 6) | (m1 << 4) | (d1 & 15)) * 4;
      m0_[r] = L_M2 + kind * 1792 + (edge ? reg << 4 : ((d0_ >> 4) << 6) | (m1 << 4) | (d0_ & 15)) * 4;
      md_[r] = (reg & 1) ? -4 : 4;
      pin(m0_[r]); pin(m1_[r]); pin(md_[r]);
    }
    {
      v4u mv[2];
      _Pragma("nounroll") for (int s = 0; s < SPIN; ++s) {
        mv[0] = ld4c(ws, fresh(WS_MOD + grp * MOD_REP + (224 + tid) * 16));
        mv[1] = ld4c(ws, fresh(WS_MOD + grp * MOD_REP + (224 + (tid < 80 ? 256 + tid : tid)) * 16));
        const v2u rv = ld2c(ws, fresh(WS_DRDY + grp * 1024 + min(lane, 48) * 8));
        if (wall(mv[0].y == tag && mv[0].w == tag && mv[1].y == tag && mv[1].w == tag && rv.x == tag && rv.y == ~tag)) break;
        __builtin_amdgcn_s_sleep(SL4);
      }
      if (lane == 0) st2c(ws, WS_HRDY + blk * 32 + wave * 8, v2u{tag, ~tag});
      STAMP(12);
#pragma unroll
      for (int f = 0; f < WD_MID; ++f) wdn[f >> 2][f & 3] = ld4c(ws, (isG ? l16 : OOB) + (f & 3) * 1024, wdb + (f >> 2) * 4096);
      sb();
#pragma unroll
      for (int r = 0; r < 2; ++r) {
        const float sc = m0_[r] < L_M2 + 1792 ? 2.f / (float)(S * D) : 1.f;
        *(LAS float*)(L + m0_[r]) = blo(mv[r].x) * sc;
        *(LAS float*)(L + m1_[r]) = bhi(mv[r].x) * sc;
        *(LAS float*)(L + m1_[r] + md_[r]) = blo(mv[r].z) * sc;
        *(LAS float*)(L + m1_[r] + 2 * md_[r]) = bhi(mv[r].z) * sc;
      }
    }
    bar();
    *(LAS v4u*)(L + L_AF + (tid >> 6) * 4096 + 3072 + l16) = v4u{0u, 0u, 0u, 0u};
    STAMP(4);
    int zr = L_AF + lane * 8, ro = L_M2 + (t0 * 64 + 4 * g) * 4, ro1 = wave == 0 ? ro : ro + 256, ex = L_EX + c16 * 8;
    pin(zr); pin(ro); pin(ro1); pin(ex);
    {
      int zl[4];
#pragma unroll
      for (int m = 0; m < 4; ++m) {
        const int kq = (m >> 1) * 128 + (to[m] == OOB ? 127 : wave == 0 && m == 1 ? 112 + c16 : 16 * (t0 + (m & 1)) + c16);
        zl[m] = L_AF + ((kq & ~31) + ((kq & 4) << 2) + ((kq & 24) >> 1) + (kq & 3)) * 32 + g * 8;
        pin(zl[m]);
        if (to[m] == OOB) xs[m] = sv;
      }
      bar();
      if (isG) wait_flags(ws, WS_HRDY + (16 * tile + 4 * wave) * 32 + c16 * 8, tag);
      {
        const int src = (blk * 5 + wave * 13) & 63;
        const int po = __builtin_amdgcn_readlane(to[(0)], 0) + (src & 15) * 64 + (src >> 4) * 16;
        const u32 old = __builtin_amdgcn_ds_bpermute(src * 4, xs[0].x);
        _Pragma("nounroll") for (int s = 0; s < SPIN; ++s) {
          if (wall(ld1c(ws, fresh(po)) != old)) break;
          __builtin_amdgcn_s_sleep(SL2);
        }
      }
      STAMP(13);
      v4u z[4];
      _Pragma("nounroll") for (int s = 0; s < SPIN; ++s) {
        u32 mn = 1;
#pragma unroll
        for (int m = 0; m < 4; ++m) z[m] = ld4c(ws, fresh(to[m]));
#pragma unroll
        for (int m = 0; m < 4; ++m) { const v4u d = z[m] ^ xs[m]; mn = min(min(mn, d.x), min(min(d.y, d.z), d.w)); }
        if (wall(mn != 0)) break;
      }
      STAMP(14);
#pragma unroll
      for (int m = 0; m < 4; ++m) {
        char LAS* p = L + zl[m];
        *(LAS v2u*)(p) = v2u{__builtin_amdgcn_perm(z[m].y, z[m].x, 0x05040100u), __builtin_amdgcn_perm(z[m].w, z[m].z, 0x05040100u)};
        *(LAS v2u*)(p + ZPL) = v2u{__builtin_amdgcn_perm(z[m].y, z[m].x, 0x07060302u), __builtin_amdgcn_perm(z[m].w, z[m].z, 0x07060302u)};
      }
    }
    bar();
    STAMP(5);
    {
      v4u zf[4][4];
#pragma unroll
      for (int ty = 0; ty < 4; ++ty)
#pragma unroll
        for (int s = 0; s < 4; ++s) zf[ty][s] = ldtr2(L + zr, ty * 4096 + s * 1024, ty * 4096 + s * 1024 + 512);
      v4f acc[2][4], x2[2][4], gav[2][4];
      v2f s1 = {0.f, 0.f}, s2 = {0.f, 0.f};
#define P3M(t, ty) { mf0(acc[t][ty], cf[4 * t + ty][0], zf[ty][0]); \
      _Pragma("unroll") for (int s = 1; s < 4; ++s) mfa(acc[t][ty], cf[4 * t + ty][s], zf[ty][s]); }
#define P3X(t, m, yv) { x2[t][m] = fma4(gav[t][m], yv, xo[t][m]); \
      if (t == 0 || wave != 0) { s1 += lo2(x2[t][m]) + hi2(x2[t][m]); \
        s2 = fma2(lo2(x2[t][m]), lo2(x2[t][m]), s2); s2 = fma2(hi2(x2[t][m]), hi2(x2[t][m]), s2); } }
      P3M(1, 0) P3M(1, 1) P3M(1, 2) P3M(1, 3)
#pragma unroll
      for (int m = 0; m < 4; ++m) { gav[0][m] = lds4f(L + ro, m * 64); gav[1][m] = lds4f(L + ro1, m * 64); }
      sb();
      P3M(0, 0)
      mfence<0>(acc[1][0], acc[1][1], acc[1][2], acc[1][3]);
      const v4f A1 = acc[1][0] + acc[1][1], B1 = acc[1][0] - acc[1][1], P1 = acc[1][3] - acc[1][2], Q1 = acc[1][2] + acc[1][3];
      sb();
      P3M(0, 1)
      P3X(1, 0, A1 - P1) P3X(1, 1, A1 + P1)
      sb();
      P3M(0, 2)
      P3X(1, 2, B1 - Q1) P3X(1, 3, B1 + Q1)
      sb();
      P3M(0, 3)
      mfence<7>(acc[0][0], acc[0][1], acc[0][2], acc[0][3]);
      {
        const v4f A = acc[0][0] + acc[0][1], B = acc[0][0] - acc[0][1], P = acc[0][3] - acc[0][2], Q = acc[0][2] + acc[0][3];
        v4f y[4] = {A - P, A + P, B - Q, B + Q};
        const bool sp = tid < 16;
        y[0].x = sp ? A.x : y[0].x; y[2].x = sp ? B.x : y[2].x;
        y[1].x = sp ? acc[1][0].x - acc[0][3].x : y[1].x; y[3].x = sp ? acc[1][0].x + acc[0][3].x : y[3].x;
        P3X(0, 0, y[0]) P3X(0, 1, y[1]) P3X(0, 2, y[2]) P3X(0, 3, y[3])
      }
      float sm = s1.x + s1.y, sq = s2.x + s2.y;
      sumg2(sm, sq);
      if (g == 0) *(LAS v2f*)(L + ex + wave * 128) = v2f{sm, sq};
      v4f sbv[2][4], cbv[2][4];
#pragma unroll
      for (int m = 0; m < 4; ++m) {
        sbv[0][m] = lds4f(L + ro, 1792 + m * 64); cbv[0][m] = lds4f(L + ro, 2 * 1792 + m * 64);
        sbv[1][m] = lds4f(L + ro1, 1792 + m * 64); cbv[1][m] = lds4f(L + ro1, 2 * 1792 + m * 64);
      }
      bar();
      v2f e = *(const LAS v2f*)(L + ex);
#pragma unroll
      for (int w = 1; w < 4; ++w) e += *(const LAS v2f*)(L + ex + w * 128);
      const float mc = e.x * (1.f / D), rstd = __builtin_amdgcn_rsqf(fmaxf(e.y * (1.f / D) - mc * mc, 0.f) + 1e-5f), nb = -mc * rstd;
      const int ho = WS_H2 + blk * 14336 + 2 * t0 * 1024 + l16, ho1 = wave == 0 ? OOB : ho + 2048;
#pragma unroll
      for (int t = 0; t < 2; ++t)
#pragma unroll
        for (int a = 0; a < 2; ++a) {
          v4u o;
#pragma unroll
          for (int hh = 0; hh < 2; ++hh) {
            const int m = 2 * a + hh;
            const v4f hv = fma4(fma4(x2[t][m], splat(rstd), splat(nb)), cbv[t][m], sbv[t][m]);
            o[2 * hh] = pk2(lo2(hv)); o[2 * hh + 1] = pk2(hi2(hv));
          }
          st4c(ws, (t ? ho1 : ho) + a * 1024, o);
        }
      STAMP(6);
      sb();
#pragma unroll
      for (int t = 0; t < 2; ++t)
        if (t == 0 || wave != 0)
#pragma unroll
          for (int m = 0; m < 4; ++m)
#pragma unroll
            for (int r = 0; r < 4; r += 2) {
              const u32 p = pk(x2[t][m][r] + st.x, x2[t][m][r + 1] + st.x);
              *(LAS u16*)(L + XOFF(t, m, r)) = (u16)p;
              *(LAS u16*)(L + XOFF(t, m, r + 1)) = (u16)(p >> 16);
            }
      sb();
    }
    bar();
    if (!isG) {
      x2_flush(L, ws, blk, tid);
      drain();
      if (lane == 0) st2c(ws, WS_XRDY + blk * 64 + wave * 8, v2u{tag, ~tag});
      DUMP(); return;
    }
  } else {
    const int sj = j >= 30 ? j - 30 : j + 2;
    const int idx = sj * 8 + grp;
    const bool isC = j < 14 || idx == 0;
    const int pair = j < 14 ? grp * 14 + j : 112;
    const int lc = grp + 8 * (sj >> 1);
    const int qu = sj < 4 && lc < 14 ? 2 * lc + (sj & 1) : -1;
    const int t1o = WS_T1 + (32 * wave + (lane >> 3)) * T1_BLK + pair * 128 + (lane & 7) * 16;
    ModLoad ml;
    epoch = ld1c(ws, WS_EPOCH);
    if (qu >= 0) mod_load(ml, cin, wm, qu, tid);
#pragma unroll
    for (int q = 0; q < 4; ++q) st4c(ws, isC ? t1o + 8 * q * T1_BLK : OOB, sv);
    u32 pf0, pf1, pf2;
    {
      const rsrc_t mr = mkrs(wm, D * 6 * D * 2), xr = mkrs(xin, S * D * 2), fr_ = mkrs((wave & 1) ? wi : wr, S * DF * 2);
      const int row = (sj * 4 + wave) * 7 + (lane >> 1), l7 = lane / 7;
      pf0 = __builtin_amdgcn_raw_buffer_load_b32(mr, lane < 14 && (grp < 6 || !(lane & 1)) ? row * (6 * D * 2) + (grp + 8 * (lane & 1)) * 128 : OOB, 0, PF_AUX);
      pf1 = __builtin_amdgcn_raw_buffer_load_b32(xr, lane < 28 ? tok_row(16 * grp + sj, 4 * wave + l7) * (D * 2) + (lane - 7 * l7) * 128 : OOB, 0, PF_AUX);
    }
    sb();
    epoch = __builtin_amdgcn_readfirstlane(epoch);
    tag = mktag(epoch);
    if (qu >= 0) mod_part(ml, L, tid);
    asm volatile("s_waitcnt vmcnt(%0)" :: "i"(W_LATE ? 2 : 3) : "memory");
    { const rsrc_t fr_ = mkrs((wave & 1) ? wi : wr, S * DF * 2);
      pf2 = __builtin_amdgcn_raw_buffer_load_b32(fr_, lane < 29 ? (((16 * grp + sj) * 7200) & ~127) + ((wave >> 1) * 29 + lane) * 128 : OOB, 0, PF_AUX);
    }
    bar();
    if (wave == 0 && qu >= 0) mod_publish(L, ws, qu, lane, tag);
    if (wave == 1 && lane < 8) st2c(ws, isC ? WS_RDY + lane * 1024 + pair * 8 : OOB, v2u{tag, ~tag});
    STAMP(1);
    pin(pf0); pin(pf1); pin(pf2); pin(pfa); pin(pfb);
    const int qn = sj >= 4 && sj < 10 && grp < 7 ? 28 + grp * 6 + (sj - 4) : !isC && idx < 15 ? 69 + idx : -1;
    const int tw = sj >= 10 ? (sj - 10) * 8 + grp : sj == 4 && grp == 7 ? 48 : -1;
    v2u dw[2][8];
    if (qn >= 0) mod_load(ml, cin, wm, qn, tid);
    if (tw >= 0) {
      const rsrc_t dr = mkrs(wd, F * D * 2);
      const int u0 = tw * 4 + wave, uc = u0 / 28, kap = 2 * (u0 - uc * 28);
#pragma unroll
      for (int e = 0; e < 16; ++e)
        dw[e >> 3][e & 7] = __builtin_amdgcn_raw_buffer_load_b64(dr, (4 * g * D + 4 * c16) * 2 + (e & 3) * D * 2, ((32 * kap + 16 * (e >> 2)) * D + 64 * uc) * 2, PF_AUX);
    }
    sb();
    {
      const rsrc_t rg = mkrs(wg, D * F * 2), ru = mkrs(wu, D * F * 2);
      const int r7 = lane / 7, po = lane < 56 && j < 14 ? ((j * 4 + wave) * 8 + r7) * (F * 2) + ((grp & 3) * 7 + lane - r7 * 7) * 128 : OOB;
      pf0 = ld1(rg, po); pf1 = ld1(ru, po);
    }
    sb();
    STAMP(20);
    if (tw >= 0) {
#pragma unroll
      for (int tt = 0; tt < 8; ++tt) {
        v4u o;
#pragma unroll
        for (int m = 0; m < 4; ++m)
          o[m] = __builtin_amdgcn_perm(dw[tt >> 2][2 * m + 1][(tt & 3) >> 1], dw[tt >> 2][2 * m][(tt & 3) >> 1], (tt & 1) ? 0x07060302u : 0x05040100u);
        st4c(ws, WS_WD + (tw * 4 + wave) * 8192 + tt * 1024 + l16, o);
      }
    }
    if (qn >= 0) {
      bar();
      mod_part(ml, L, tid);
      bar();
      if (wave == 0) mod_publish(L, ws, qn, lane, tag);
    }
    STAMP(21);
    if (!isC) { DUMP(); return; }
    v4u c64[2][4], c32[2][2][2];
#pragma unroll
    for (int cp = 0; cp < 2; ++cp)
#pragma unroll
      for (int s = 0; s < 4; ++s) c64[cp][s] = ld4(tab, l16 + s * 1024, (TAB_P2 + wave * 2 + cp) * 4096);
#pragma unroll
    for (int t = 0; t < 2; ++t)
#pragma unroll
      for (int f = 0; f < 4; ++f) c32[t][f >> 1][f & 1] = ld4(tab, l16 + f * 1024, (TAB_P2 + 8 + t) * 4096);
    sb();
    if (tw >= 0) {
      asm volatile("s_waitcnt vmcnt(16)" ::: "memory");
      bar();
      if (tid < 8) st2c(ws, WS_DRDY + tid * 1024 + tw * 8, v2u{tag, ~tag});
    }
    v4f t1c[2], t1s[2], t2c[2], t2s[2];
#pragma unroll
    for (int t = 0; t < 2; ++t) {
#pragma unroll
      for (int r = 0; r < 4; ++r) {
        const float a1 = (float)((16 * t + 4 * g + r) * (16 * wave + c16)) * (1.f / S);
        const float a2 = (float)((16 * t + c16) * (16 * wave + 4 * g + r)) * (1.f / S);
        t1c[t][r] = __builtin_amdgcn_cosf(a1); t1s[t][r] = __builtin_amdgcn_sinf(a1);
        t2c[t][r] = __builtin_amdgcn_cosf(a2); t2s[t][r] = -__builtin_amdgcn_sinf(a2);
      }
      pin(t1c[t]); pin(t1s[t]); pin(t2c[t]); pin(t2s[t]);
    }
    STAMP(2);
    int wfo = WS_WT + pair * 16384 + (16 * g + wave) * 128 + c16 * 8;
    pin(wfo);
    v2u wf[2][4];
    {
      int wl = L_CI + ((lane >> 2) & 1) * 8256 + wave * 2048 + ((lane >> 3) * 4 + (lane & 3)) * 16, lo = t1o;
      pin(wl); pin(lo);
      light_poll(ws, WS_T1 + (32 * wave + (pair & 31)) * T1_BLK + pair * 128);
      pin(pf0); pin(pf1);
      STAMP(13);
      v4u z[4];
      _Pragma("nounroll") for (int s = 0; s < SPIN; ++s) {
        lo = fresh(lo);
#pragma unroll
        for (int q = 0; q < 4; ++q) z[q] = ld4c(ws, lo, 8 * q * T1_BLK);
        if (wall(max(max(max4(z[0]), max4(z[1])), max(max4(z[2]), max4(z[3]))) != SENT)) break;
      }
      STAMP(14);
#pragma unroll
      for (int q = 0; q < 4; ++q) *(LAS v4u*)(L + wl + q * 512) = z[q];
#pragma unroll
      for (int t = 0; t < 2; ++t)
#pragma unroll
        for (int r = 0; r < 4; ++r) wf[t][r] = ld2c(ws, wfo + r * 512, t * 8192);
    }
    bar();
    STAMP(4);
    {
      v4u a[2][2][4];
#pragma unroll
      for (int c = 0; c < 2; ++c)
#pragma unroll
        for (int mt = 0; mt < 2; ++mt)
#pragma unroll
          for (int s = 0; s < 4; ++s) a[c][mt][s] = lds4(LB, L_CI + c * 8256 + (s * 2 + mt) * 1024);
      v4u old[2][2];
#pragma unroll
      for (int c = 0; c < 2; ++c)
#pragma unroll
        for (int mt = 0; mt < 2; ++mt) old[c][mt] = lds4(LB + wave * 2048, L_CI + c * 8256 + mt * 1024);
      v4f re[2][2], im[2][2];
      v4u f2[2][2], f3[2][2];
      char LAS* xw = L + lb + wave * 1024 - L_B;
#define S1H(c, mt) { mf0(re[c][mt], a[c][mt][0], c64[0][0]); mf0(im[c][mt], a[c][mt][0], c64[1][0]); }
#define S1T(c, mt) { _Pragma("unroll") for (int s = 1; s < 4; ++s) { mfa(re[c][mt], a[c][mt][s], c64[0][s]); mfa(im[c][mt], a[c][mt][s], c64[1][s]); } }
#define S2(c, t) { mf0(re[c][t], c32[t][0][0], f2[c][0]); mf0(im[c][t], c32[t][1][0], f2[c][0]); \
      mfa(re[c][t], c32[t][0][1], f2[c][1]); mfa(im[c][t], c32[t][1][1], f2[c][1]); }
#define S3(c, t) { mf0(im[c][t], f3[c][0], c32[t][0][0]); mf0(re[c][t], f3[c][0], c32[t][1][0]); \
      mfa(im[c][t], f3[c][1], c32[t][0][1]); mfa(re[c][t], f3[c][1], c32[t][1][1]); }
#define TW1(c, t) { f2[c][t] = cmul_pack<false>(re[c][t], im[c][t], t1c[t], t1s[t]); pin(f2[c][t]); }
#define FLT(c, t) { const v4f fr_ = {blo(wf[t][0][c]), blo(wf[t][1][c]), blo(wf[t][2][c]), blo(wf[t][3][c])}, \
      fi_ = {bhi(wf[t][0][c]), bhi(wf[t][1][c]), bhi(wf[t][2][c]), bhi(wf[t][3][c])}; \
      f3[c][t] = cmul_pack<true>(re[c][t], im[c][t], fr_, -fi_); pin(f3[c][t]); }
#define TW2(c, t) { *(LAS v4u*)(xw + L_CX + (c * 2 + t) * 4096) = cmul_pack<true>(re[c][t], im[c][t], t2c[t], t2s[t]); }
#define F2(c, t) mfence<0>(re[c][t], im[c][t]);
      S1H(0, 0) S1T(0, 0) S1H(0, 1) S1T(0, 1) sb();
      F2(0, 0) S1H(1, 0) S1T(1, 0) TW1(0, 0) sb();
      F2(0, 1) S1H(1, 1) S1T(1, 1) TW1(0, 1) sb();
      F2(1, 0) S2(0, 0) S2(0, 1) TW1(1, 0) sb();
      F2(1, 1) TW1(1, 1) sb();
      F2(0, 0) S2(1, 0) FLT(0, 0) sb();
      F2(0, 1) S2(1, 1) FLT(0, 1) sb();
      F2(1, 0) S3(0, 0) FLT(1, 0) sb();
      F2(1, 1) S3(0, 1) FLT(1, 1) sb();
      F2(0, 0) S3(1, 0) TW2(0, 0) sb();
      F2(0, 1) S3(1, 1) TW2(0, 1) sb();
      F2(1, 0) TW2(1, 0) sb();
      F2(1, 1) TW2(1, 1)
      bar();
#pragma unroll
      for (int c = 0; c < 2; ++c)
#pragma unroll
        for (int nt = 0; nt < 2; ++nt)
#pragma unroll
          for (int s = 0; s < 4; ++s) a[c][nt][s] = lds4(LB, L_CX + ((c * 2 + nt) * 4 + s) * 1024);
      const int o2 = WS_T1 + (32 * wave + (lane >> 2)) * T1_BLK + pair * 128 + (lane & 3) * 16;
#define S4H(c, t) { mf0(im[c][t], c64[0][0], a[c][t][0]); mf0(re[c][t], c64[1][0], a[c][t][0]); }
#define S4T(c, t) { _Pragma("unroll") for (int s = 1; s < 4; ++s) { mfa(im[c][t], c64[0][s], a[c][t][s]); mfa(re[c][t], c64[1][s], a[c][t][s]); } }
#define NEQ(x, y) ((x) == (y) ? (x) ^ 1u : (x))
#define OUT(c, t) { const v4u o = {pk(re[c][t].x, im[c][t].x), pk(re[c][t].y, im[c][t].y), pk(re[c][t].z, im[c][t].z), pk(re[c][t].w, im[c][t].w)}; \
      st4c(ws, o2 + c * 64 + t * 16 * T1_BLK, v4u{NEQ(o.x, old[c][t].x), NEQ(o.y, old[c][t].y), NEQ(o.z, old[c][t].z), NEQ(o.w, old[c][t].w)}); }
      S4H(0, 0) S4T(0, 0) S4H(0, 1) mfence<1>(re[0][0], im[0][0]); OUT(0, 0) sb();
      S4T(0, 1) S4H(1, 0) mfence<1>(re[0][1], im[0][1]); OUT(0, 1) sb();
      S4T(1, 0) S4H(1, 1) mfence<1>(re[1][0], im[1][0]); OUT(1, 0) sb();
      S4T(1, 1) mfence<7>(re[1][1], im[1][1]); OUT(1, 1)
    }
    STAMP(5);
    sb();
    if (!isG) { DUMP(); return; }
    dma_w1(wg, wu, slice, wave, lane, lbase);
    wait_flags(ws, WS_DRDY + grp * 1024 + min(lane, 48) * 8, tag);
#pragma unroll
    for (int f = 0; f < WD_MID; ++f) wdn[f >> 2][f & 3] = ld4c(ws, l16 + (f & 3) * 1024, wdb + (f >> 2) * 4096);
    sb();
    wait_flags(ws, WS_HRDY + (16 * tile + 4 * wave) * 32 + c16 * 8, tag);
    bar();
  }

  STAMP(7);
  const int blk0 = 16 * tile + 4 * (grp & 3);
  const int ab = WS_ACT + blk0 * 57344 + wave * 14 * 1024;
  const int fo = WS_ARDY + (tile * 4 + (grp & 3)) * 512 + (7 * wave + min(lane, 6)) * 8;
  v4u h[14][2];
  {
    const int hb = WS_H2 + (16 * tile + 4 * wave) * 14336;
    const int fw_ = WS_ARDY + (tile * 4 + wave) * 512 + slice * 8;
    int tb[2][4];
#pragma unroll
    for (int u = 0; u < 8; ++u) {
      tb[u >> 2][u & 3] = (((lane >> 2) * 128 + ((lane >> 3) & 3) * 32 + (lane & 3) * 8) ^ ((u & 3) << 5)) + (u >> 2) * 57344;
      pin(tb[u >> 2][u & 3]);
    }
#define W_RD(dst, k, u) dst = ldtr2(L + tb[k >= 7][u & 3], (k % 7) * 8192 + (u >> 2) * 4096, (k % 7) * 8192 + (u >> 2) * 4096 + 2048)
#define H_LD(k, tt, base) h[k][tt] = ld4c(ws, l16 + (k & 3) * 1024, base + (k >> 2) * 4096)
    v4u wfr[2][8];
#pragma unroll
    for (int u = 0; u < 8; ++u) W_RD(wfr[0][u], 0, u);
    light_poll(ws, hb + (lane & 1) * 14336 + 13 * 1024);
    STAMP(15);
#pragma unroll
    for (int e = 0; e < 8; ++e) H_LD((e >> 1), (e & 1), hb + (e & 1) * 14336);
    sb();
    if (isR) x2_flush(L, ws, 16 * grp + j - 14, tid);
    sb();
#define BAD2(x0, x1) wany(max(max(max(max(x0.x, x0.y), x0.z), max(max(x0.w, x1.x), x1.y)), max(x1.z, x1.w)) == SENT)
#define RETRY2(x0, x1, o0, o1) \
      _Pragma("nounroll") for (int s_ = 0; s_ < SPIN; ++s_) { \
        const int lf_ = fresh(l16); SLOWCNT \
        x0 = ld4c(ws, lf_, o0); x1 = ld4c(ws, lf_, o1); \
        if (wall(max(max4(x0), max4(x1)) != SENT)) break; \
      }
    if (__builtin_expect(BAD2(h[0][0], h[0][1]), 0)) { RETRY2(h[0][0], h[0][1], hb, hb + 14336) }
    STAMP(8); CSTAMP(28);
    v4f acc[2][8][2];
    v2u fa;
    const int ao = WS_ACT + (16 * tile + 4 * wave) * 57344 + 2 * slice * 1024 + l16;
#define ACT_OUT(ps, tt, f) { v4u o; \
      _Pragma("unroll") for (int hh = 0; hh < 2; ++hh) { \
        const v4f av = __builtin_elementwise_max(acc[ps][2 * f + hh][tt], splat(0.f)) * acc[ps][4 + 2 * f + hh][tt]; \
        o[2 * hh] = pk2(lo2(av)); o[2 * hh + 1] = pk2(hi2(av)); } \
      st4c(ws, ao + (2 * ps + tt) * 57344 + f * 1024, o); }
#pragma unroll
    for (int ps = 0; ps < 2; ++ps) {
#pragma unroll
      for (int u = 0; u < 8; ++u) { acc[ps][u][0] = v4f{0.f, 0.f, 0.f, 0.f}; acc[ps][u][1] = acc[ps][u][0]; }
#pragma unroll
      for (int ks = 0; ks < 14; ++ks) {
        const int cur = ks & 1, nk = ks == 13 ? 0 : ks + 1;
        const bool more = ps == 0 || ks < 13, epi = ps == 1 && ks < 4;
        __builtin_amdgcn_s_waitcnt(0xC07F);
#pragma unroll
        for (int u = 0; u < 8; ++u) {
          acc[ps][u][0] = mfma(wfr[cur][u], h[ks][0], acc[ps][u][0]);
          if (more) W_RD(wfr[cur ^ 1][u], nk, u);
        }
        if (ps == 0) {
#pragma unroll
          for (int e = 8 + 4 * ks; e < 12 + 4 * ks; ++e)
            if (e < 28) H_LD((e >> 1), (e & 1), hb + (e & 1) * 14336);
#pragma unroll
          for (int e = 0; e < 28; ++e)
            if (e >= RF(ks) && e < RF(ks + 1)) H_LD((e >> 1), (e & 1), hb + (2 + (e & 1)) * 14336);
        } else {
          if (ks == 0) { H_LD(13, 0, hb + 2 * 14336); H_LD(13, 1, hb + 3 * 14336); }
#pragma unroll
          for (int e = 0; e < WD_PER; ++e) {
            const int f = WD_MID + WD_PER * ks + e;
            if (f < 56) wdn[f >> 2][f & 3] = ld4c(ws, l16 + (f & 3) * 1024, wdb + (f >> 2) * 4096);
          }
          if (ks == 12) fa = ld2c(ws, fo);
        }
        const bool bad = more && BAD2(h[nk][0], h[nk][1]);
#pragma unroll
        for (int u = 0; u < 8; ++u) acc[ps][u][1] = mfma(wfr[cur][u], h[ks][1], acc[ps][u][1]);
        if (epi) ACT_OUT(0, (ks >> 1), (ks & 1))
#pragma unroll
        for (int u = 0; u < 16; ++u) {
          __builtin_amdgcn_sched_group_barrier(0x8, 1, 0);
          if (u < 4) __builtin_amdgcn_sched_group_barrier(0x100, 2, 0);
          else if (u < 12) __builtin_amdgcn_sched_group_barrier(0x100, 1, 0);
          if (u >= 4 && u < 12) __builtin_amdgcn_sched_group_barrier(0x20, 1, 0);
          if (epi) __builtin_amdgcn_sched_group_barrier(0x2, 3, 0);
          else if (u >= 10) __builtin_amdgcn_sched_group_barrier(0x2, 1, 0);
        }
        sb();
        if (ps == 1 && ks == 6) {
          static_assert(WD_MID + 7 * WD_PER <= 56, "the count below");
          asm volatile("s_waitcnt vmcnt(%0)" :: "i"(3 * WD_PER) : "memory");
          if (lane < 2) st2c(ws, lane == 0 ? fw_ : isR ? WS_XRDY + (16 * grp + j - 14) * 64 + wave * 8 : OOB, v2u{tag, ~tag});
        }
        if (__builtin_expect(bad, 0)) {
          const int pb = (ks == 13 ? 2 : 2 * ps) * 14336 + nk * 1024;
          RETRY2(h[nk][0], h[nk][1], hb + pb, hb + pb + 14336)
        }
        if (ps == 0 && ks == 13) { STAMP(16); CSTAMP(29); }
        sb();
      }
    }
    STAMP(27);
    if (!wall(fa.x == tag && fa.y == ~tag)) wait_flags(ws, fo, tag);
    STAMP(26);
#pragma unroll
    for (int e = 0; e < A_FIRST; ++e) H_LD((e >> 1), (e & 1), ab + (e & 1) * 57344);
    sb();
    ACT_OUT(1, 0, 0) ACT_OUT(1, 0, 1) ACT_OUT(1, 1, 0) ACT_OUT(1, 1, 1)
    CSTAMP(30);
    sb();
    STAMP(9);
    const int blk = blk0 + (wave >> 1);
    const int xo = WS_X2 + blk * 14336 + c16 * 896 + ct * 128 + g * 32 + (wave & 1) * 16;
    const int go = WS_MOD + grp * MOD_REP + (70 + 2 * ct) * 128 + g * 64 + (wave & 1) * 32;
    const int xf = WS_XRDY + (blk + (lane & 4 ? 2 : 0)) * 64 + (lane & 3) * 8;
    v4u a[14][2];
    v4f ac2[4][4];
#pragma unroll
    for (int t = 0; t < 4; ++t)
#pragma unroll
      for (int n = 0; n < 4; ++n) ac2[t][n] = v4f{0.f, 0.f, 0.f, 0.f};
    v4u xv[2], gv[2];
#pragma unroll
    for (int ks = 0; ks < 14; ++ks) {
#pragma unroll
      for (int tt = 0; tt < 2; ++tt)
#pragma unroll
        for (int n = 0; n < 4; ++n) ac2[tt][n] = mfma(wdn[ks][n], h[ks][tt], ac2[tt][n]);
#pragma unroll
      for (int e = A_FIRST + 3 * ks; e < A_FIRST + 3 * ks + 3; ++e)
        if (e < 28) H_LD((e >> 1), (e & 1), ab + (e & 1) * 57344);
#pragma unroll
      for (int u = 0; u < 4; ++u) {
        __builtin_amdgcn_sched_group_barrier(0x8, 2, 0);
        __builtin_amdgcn_sched_group_barrier(0x20, 1, 0);
      }
      sb();
      if (ks == F_AT) {
        static_assert(A_FIRST + 3 * F_AT + 3 <= 28, "the count below");
        asm volatile("s_waitcnt vmcnt(%0)" :: "i"(3 * F_AT + 3) : "memory");
        if (lane == 0) st2c(ws, fw_ + 256, v2u{tag, ~tag});
      }
      if (ks == 7) {
        fa = ld2c(ws, xf);
        gv[0] = ld4c(ws, go); gv[1] = ld4c(ws, go + 16);
      }
      if (ks == 11) {
        if (!wall(fa.x == tag && fa.y == ~tag)) wait_flags(ws, xf, tag);
        xv[0] = ld4c(ws, xo); xv[1] = ld4c(ws, xo + 2 * 14336);
      }
      sb();
    }
    STAMP(17);
    int rw = lane * 8 + wave * 2048, rr_ = lane * 8 + wave * 8192;
    pin(rw); pin(rr_);
#define RED_WR(ph) _Pragma("unroll") for (int dd = 0; dd < 4; ++dd) if (dd != wave) \
      _Pragma("unroll") for (int n = 0; n < 4; ++n) \
        *(LAS v2f*)(L + rw + ph * 32768 + dd * 8192 + n * 512) = (dd & 1) ? hi2(ac2[2 * ph + (dd >> 1)][n]) : lo2(ac2[2 * ph + (dd >> 1)][n]);
#define RED_FIN(ph) { v2f r[4]; \
      _Pragma("unroll") for (int n = 0; n < 4; ++n) { \
        const v4f own = wave < 2 ? ac2[2 * ph][n] : ac2[2 * ph + 1][n]; r[n] = (wave & 1) ? hi2(own) : lo2(own); } \
      _Pragma("unroll") for (int w = 1; w < 4; ++w) \
        _Pragma("unroll") for (int n = 0; n < 4; ++n) r[n] += *(const LAS v2f*)(L + rr_ + ((wave + w) & 3) * 2048 + ph * 32768 + n * 512); \
      v4u o; \
      _Pragma("unroll") for (int q = 0; q < 2; ++q) { \
        o[2 * q] = pk2(fma2(bun(gv[q].x), v2f{r[0][q], r[1][q]}, bun(xv[ph][2 * q]))); \
        o[2 * q + 1] = pk2(fma2(bun(gv[q].z), v2f{r[2][q], r[3][q]}, bun(xv[ph][2 * q + 1]))); } \
      __builtin_amdgcn_raw_buffer_store_b128(o, mkrs(out, S * D * 2), (tok_row(blk + 2 * ph, c16) * D + ct * 64 + g * 16 + (wave & 1) * 8) * 2, 0, 16); }
    fa = ld2c(ws, fo + 256);
    asm volatile("s_barrier" ::: "memory");
    RED_WR(0)
    _Pragma("nounroll") for (int s = 0; s < SPIN; ++s) {
      if (wall(gv[0].y == tag && gv[0].w == tag && gv[1].y == tag && gv[1].w == tag)) break;
      const int gf = fresh(go);
      gv[0] = ld4c(ws, gf); gv[1] = ld4c(ws, gf + 16);
    }
#define A_LD(e) a[(e) >> 1][(e) & 1] = ld4c(ws, l16 + (((e) >> 1) & 3) * 1024, ab + (2 + ((e) & 1)) * 57344 + ((e) >> 3) * 4096)
    if (!wall(fa.x == tag && fa.y == ~tag)) wait_flags(ws, fo + 256, tag);
    STAMP(18);
#pragma unroll
    for (int e = 0; e < B_FIRST; ++e) A_LD(e);
    sb();
    bar();
    RED_FIN(0)
    sb();
#pragma unroll
    for (int ks = 0; ks < 14; ++ks) {
#pragma unroll
      for (int tt = 0; tt < 2; ++tt)
#pragma unroll
        for (int n = 0; n < 4; ++n) ac2[2 + tt][n] = mfma(wdn[ks][n], a[ks][tt], ac2[2 + tt][n]);
#pragma unroll
      for (int e = B_FIRST + 3 * ks; e < B_FIRST + 3 * ks + 3; ++e)
        if (e < 28) A_LD(e);
#pragma unroll
      for (int u = 0; u < 4; ++u) {
        __builtin_amdgcn_sched_group_barrier(0x8, 2, 0);
        __builtin_amdgcn_sched_group_barrier(0x20, 1, 0);
      }
      sb();
    }
    STAMP(10);
    RED_WR(1)
    bar();
    RED_FIN(1)
    if (blockIdx.x == 0 && wave == 0) {
      _Pragma("nounroll") for (int s = 0; s < SPIN; ++s) {
        const v4u v = ld4c(ws, fresh(WS_MOD + (70 + (lane < 14 ? lane : 0)) * 128));
        if (wall(v.y == tag)) break;
      }
      if (lane == 0) __builtin_amdgcn_raw_buffer_store_b32(epoch + 1u, ws, WS_EPOCH, 0, SC1);
    }
  }
  STAMP(11);
  DUMP();
}

extern "C" void kernel_launch(void* const* d_in, const int* in_sizes, int n_in, void* d_out, int out_size, void* d_ws,
                              size_t ws_size, hipStream_t stream) {
  fused<<<256, 256, 0, stream>>>((const u16*)d_in[0], (const u16*)d_in[1], (const u16*)d_in[2], (const u16*)d_in[4],
                                 (const u16*)d_in[5], (const u16*)d_in[6], (const u16*)d_in[7], (const u16*)d_in[8],
                                 (u16*)d_out, (char*)d_ws);
}
```

```cpp
#include <hip/hip_runtime.h>

typedef unsigned short u16;
typedef unsigned int u32;
typedef unsigned long long u64;
typedef u32 v4u __attribute__((ext_vector_type(4)));
typedef u32 v2u __attribute__((ext_vector_type(2)));
typedef float v4f __attribute__((ext_vector_type(4)));
typedef float v2f __attribute__((ext_vector_type(2)));
typedef short v8s __attribute__((ext_vector_type(8)));
typedef short v4s __attribute__((ext_vector_type(4)));
typedef __bf16 v8b __attribute__((ext_vector_type(8)));
typedef __bf16 v2b __attribute__((ext_vector_type(2)));
#define LAS __attribute__((address_space(3)))
#define GAS __attribute__((address_space(1)))
#define DEV __device__ __forceinline__

#define C_W1_EARLY 0
#define CLR_DELAY 0
#define W_DELAY 0
#define CLR2_DELAY 0
#define PF_POS 2
#define SL1 1
#define SL2 2
#define SL4 4
#define X_AFTER 8
#define PF_AUX 2
#define W_LATE 1
#define W1_PF 1
#define DMA_MOD ""
#define RF(ks) ((ks) <= 5 ? 0 : (3 * ((ks) - 5) < 2 * ((ks) - 1) ? 3 * ((ks) - 5) : 2 * ((ks) - 1)))
#define AF(ks) ((ks) <= A_AT + 1 ? 0 : (6 * ((ks) - A_AT - 1) < 2 * ((ks) - 1) ? 6 * ((ks) - A_AT - 1) : 2 * ((ks) - 1)))
#define WD_PER 3
#define A_FIRST 10
#define B_FIRST 12
#define A_AT 9
#define F_AT 4
#define CODE_BYTES 45056
#define WD_EARLY 8
#define WD_MID 16
#define B_AT 9
constexpr int S = 2048, D = 448, DF = 225, F = 1792;

struct alignas(16) Chain { u16 v[2048]; };
struct Cos448 { u16 v[448]; };
constexpr double c_cos_t(double a) {
  double a2 = a * a, t = 1.0, s = 1.0;
  for (int i = 1; i <= 10; ++i) { t = -t * a2 / ((2 * i - 1) * (2 * i)); s += t; }
  return s;
}
constexpr double c_sin_t(double a) {
  double a2 = a * a, t = a, s = a;
  for (int i = 1; i <= 10; ++i) { t = -t * a2 / ((2 * i) * (2 * i + 1)); s += t; }
  return s;
}
constexpr double c_cos448(int m) {
  if (m > 224) m = 448 - m;
  double sg = 1.0;
  if (m > 112) { m = 224 - m; sg = -1.0; }
  const double w = 6.283185307179586476925286766559 / 448.0;
  return m == 112 ? 0.0 : (m <= 56 ? sg * c_cos_t(w * m) : sg * c_sin_t(w * (112 - m)));
}
constexpr u16 c_bf16(double d) {
  const u32 u = __builtin_bit_cast(u32, (float)d);
  return (u16)((u + 0x7FFFu + ((u >> 16) & 1u)) >> 16);
}
constexpr Cos448 mk_cos() { Cos448 c{}; for (int m = 0; m < 448; ++m) c.v[m] = c_bf16(c_cos448(m)); return c; }
constexpr Cos448 CTAB = mk_cos();
constexpr u16 bcos(int m) { return CTAB.v[m % 448]; }
constexpr u16 bsin(int m) { return CTAB.v[(m % 448 + 336) % 448]; }
constexpr u16 bneg(u16 b) { return b ? (u16)(b ^ 0x8000u) : b; }
constexpr u16 bscale(u16 b, int e) { return b ? (u16)(b + e * 0x80) : b; }

constexpr Chain mk_p1(int c) {
  const int tile = c >> 2, par = c & 1, sn = (c >> 1) & 1;
  Chain ch{};
  for (int s = 0; s < 4; ++s)
    for (int l = 0; l < 64; ++l) {
      const int kp = 16 * tile + (l & 15), k = 2 * kp + par;
      if (k > 224) continue;
      for (int j = 0; j < 8; ++j) {
        const int dl = 32 * s + 8 * (l >> 4) + j;
        if (dl > 112) continue;
        u16 b = sn ? bneg(bsin(k * dl)) : bcos(k * dl);
        if (dl == 0 || dl == 112) b = bscale(b, -1);
        ch.v[(s * 64 + l) * 8 + j] = b;
      }
    }
  return ch;
}
constexpr Chain mk_p3(int c) {
  const int tile = c >> 1, sn = c & 1;
  Chain ch{};
  for (int s = 0; s < 4; ++s)
    for (int l = 0; l < 64; ++l) {
      int dl = 16 * tile + (l & 15);
      if (c == 1 && dl == 0) dl = 112;
      for (int j = 0; j < 8; ++j) {
        const int k = 2 * (32 * s + 8 * (l >> 4) + j) + 1;
        if (k < 224) ch.v[(s * 64 + l) * 8 + j] = sn ? bsin(k * dl) : bcos(k * dl);
      }
    }
  return ch;
}
constexpr Chain mk_p2(int c) {
  Chain ch{};
  for (int f = 0; f < 4; ++f)
    for (int l = 0; l < 64; ++l)
      for (int j = 0; j < 8; ++j) {
        const int comp = c < 8 ? (c & 1) : (f >> 1), s = c < 8 ? f : (f & 1);
        const int o = 16 * (c < 8 ? (c >> 1) : (c - 8)) + (l & 15), i = 16 * s + 4 * (l >> 4) + (j >> 1);
        const int m = (c < 8 ? 7 : 14) * ((i * o) % 64);
        u16 b = 0;
        if (comp == 0) b = (j & 1) ? bsin(m) : bcos(m);
        else b = (j & 1) ? bcos(m) : bneg(bsin(m));
        ch.v[(f * 64 + l) * 8 + j] = b;
      }
  return ch;
}
template <int I> constexpr Chain P1C = mk_p1(I);
template <int I> constexpr Chain P3C = mk_p3(I);
template <int I> constexpr Chain P2C = mk_p2(I);
#define L10(T, b) T<b>, T<b + 1>, T<b + 2>, T<b + 3>, T<b + 4>, T<b + 5>, T<b + 6>, T<b + 7>, T<b + 8>, T<b + 9>
#define L29(T) L10(T, 0), L10(T, 10), T<20>, T<21>, T<22>, T<23>, T<24>, T<25>, T<26>, T<27>, T<28>
constexpr int TAB_P1 = 0, TAB_P3 = 29, TAB_P2 = 43, TAB_N = 53;
static __device__ const Chain TAB[TAB_N] = {L29(P1C), L10(P3C, 0), P3C<10>, P3C<11>, P3C<12>, P3C<13>, L10(P2C, 0)};

constexpr int WS_EPOCH = 0;
constexpr int WS_RDY = 4096;
constexpr int WS_HRDY = 4096 + 8192;
constexpr int WS_DRDY = 4096 + 16384;
constexpr int WS_XRDY = 131072 + 65536;
constexpr int WS_ARDY = 131072;
constexpr int WS_MOD = 32768, MOD_REP = 12288;
constexpr int T1_BLK = 226 * 64;
constexpr int WS_T1 = 1 << 18;
constexpr int WS_WT = WS_T1 + 128 * T1_BLK;
constexpr int WS_H2 = WS_WT + 113 * 16384;
constexpr int WS_X2 = WS_H2 + 128 * 14336;
constexpr int WS_WD = WS_X2 + 128 * 14336;
constexpr int WS_ACT = WS_WD + 7 * 56 * 4096;
constexpr int WS_END = WS_ACT + 128 * 57344;
constexpr int WS_DBG = 32 << 20;

constexpr int L_W1 = 0;
constexpr int L_B = 114688;
constexpr int XP = 912;
constexpr int L_XS = L_B;
constexpr int L_ST = L_XS + 16 * XP;
constexpr int L_M1 = L_ST + 128;
constexpr int L_AF = L_M1 + 4 * 1792;
constexpr int L_M2 = L_AF + 16384;
constexpr int L_EX = L_M2 + 3 * 1792;
constexpr int L_REND = L_EX + 512;
constexpr int ZPL = 8192;
constexpr int L_CI = L_B;
constexpr int L_CX = L_CI + 2 * 8256;
constexpr int L_RED = L_CX + 16384;
constexpr int L_DBG = 163840 - 1024;
constexpr int L_SZ = 163840;
static_assert(L_REND <= L_DBG && L_RED + 2048 <= L_DBG && 2 * ZPL <= 16384, "lds");

constexpr u32 SENT = 0xFFFFFFFFu;
constexpr int SPIN = 1 << 14;
constexpr int SC1 = 16;
constexpr int OOB = 0x7FFF0000;

typedef __amdgpu_buffer_rsrc_t rsrc_t;
DEV rsrc_t mkrs(const void* p, int bytes) { return __builtin_amdgcn_make_buffer_rsrc((void*)p, 0, bytes, 0x00020000); }
DEV v4u ld4(rsrc_t r, int off, int so = 0) { return __builtin_amdgcn_raw_buffer_load_b128(r, off, so, 0); }
DEV v2u ld2(rsrc_t r, int off, int so = 0) { return __builtin_amdgcn_raw_buffer_load_b64(r, off, so, 0); }
DEV v4u ld4c(rsrc_t r, int off, int so = 0) { return __builtin_amdgcn_raw_buffer_load_b128(r, off, so, SC1); }
DEV v2u ld2c(rsrc_t r, int off, int so = 0) { return __builtin_amdgcn_raw_buffer_load_b64(r, off, so, SC1); }
DEV u32 ld1c(rsrc_t r, int off, int so = 0) { return __builtin_amdgcn_raw_buffer_load_b32(r, off, so, SC1); }
DEV void st4c(rsrc_t r, int off, v4u v) { __builtin_amdgcn_raw_buffer_store_b128(v, r, off, 0, SC1); }
DEV void st2c(rsrc_t r, int off, v2u v) { __builtin_amdgcn_raw_buffer_store_b64(v, r, off, 0, SC1); }
DEV float blo(u32 v) { return __builtin_bit_cast(float, v << 16); }
DEV float bhi(u32 v) { return __builtin_bit_cast(float, v & 0xFFFF0000u); }
DEV v2f bun(u32 v) { return v2f{blo(v), bhi(v)}; }
DEV u32 pk(float lo, float hi) { return __builtin_bit_cast(u32, __builtin_convertvector(v2f{lo, hi}, v2b)); }
DEV u32 pk2(v2f v) { return __builtin_bit_cast(u32, __builtin_convertvector(v, v2b)); }
DEV float rb(float f) { return blo(pk(f, 0.f)); }
DEV v2f lo2(v4f v) { return __builtin_shufflevector(v, v, 0, 1); }
DEV v2f hi2(v4f v) { return __builtin_shufflevector(v, v, 2, 3); }
DEV v2f fma2(v2f a, v2f b, v2f c) { return __builtin_elementwise_fma(a, b, c); }
DEV v4f fma4(v4f a, v4f b, v4f c) { return __builtin_elementwise_fma(a, b, c); }
DEV v4f splat(float f) { return v4f{f, f, f, f}; }
DEV v4f mfma(v4u a, v4u b, v4f c) {
  return __builtin_amdgcn_mfma_f32_16x16x32_bf16(__builtin_bit_cast(v8b, a), __builtin_bit_cast(v8b, b), c, 0, 0, 0);
}
DEV void mfa(v4f& c, v4u a, v4u b) { asm volatile("s_nop 1\n\tv_mfma_f32_16x16x32_bf16 %0, %1, %2, %0" : "+v"(c) : "v"(a), "v"(b)); }
DEV void mf0(v4f& c, v4u a, v4u b) { asm volatile("s_nop 1\n\tv_mfma_f32_16x16x32_bf16 %0, %1, %2, 0" : "=&v"(c) : "v"(a), "v"(b)); }
template <int N> DEV void mfence(v4f& a, v4f& b) { asm volatile("s_nop %2" : "+v"(a), "+v"(b) : "i"(N)); }
template <int N> DEV void mfence(v4f& a, v4f& b, v4f& c, v4f& d) { asm volatile("s_nop %4" : "+v"(a), "+v"(b), "+v"(c), "+v"(d) : "i"(N)); }
template <class T> DEV void pin(T& v) { asm volatile("" : "+v"(v)); }
DEV int fresh(int off) { asm volatile("" : "+v"(off) :: "memory"); return off; }
DEV bool wall(bool ok) { return __builtin_amdgcn_ballot_w64(!ok) == 0; }
DEV bool wany(bool b) { return __builtin_amdgcn_ballot_w64(b) != 0; }
DEV void sb() { __builtin_amdgcn_sched_barrier(0); }
DEV u32 max4(v4u v) { return max(max(v.x, v.y), max(v.z, v.w)); }
DEV void drain() { asm volatile("s_waitcnt vmcnt(0)" ::: "memory"); }
DEV void cfence() { asm volatile("" ::: "memory"); }
DEV void bar() { asm volatile("s_waitcnt lgkmcnt(0)\n\ts_barrier" ::: "memory"); }
template <int CTRL> DEV float dpp(float v) {
  return __builtin_bit_cast(float, __builtin_amdgcn_update_dpp(0, __builtin_bit_cast(int, v), CTRL, 0xF, 0xF, true));
}
DEV float sum16(float v) {
  v += dpp<0x128>(v); v += dpp<0x124>(v); v += dpp<0x122>(v); v += dpp<0x121>(v); return v;
}
DEV void sumg2(float& a, float& b) {
  float c = a, d = b;
  asm volatile("s_nop 1\n\tv_permlane16_swap_b32 %0, %1\n\tv_permlane16_swap_b32 %2, %3" : "+v"(a), "+v"(c), "+v"(b), "+v"(d));
  a += c; b += d; c = a; d = b;
  asm volatile("s_nop 1\n\tv_permlane32_swap_b32 %0, %1\n\tv_permlane32_swap_b32 %2, %3" : "+v"(a), "+v"(c), "+v"(b), "+v"(d));
  a += c; b += d;
}
DEV v4u lds4(const char LAS* L, int off) { return *(const LAS v4u*)(L + off); }
DEV v4f lds4f(const char LAS* L, int off) { return *(const LAS v4f*)(L + off); }
DEV v2u ldtr(const char LAS* L, int off) { return __builtin_bit_cast(v2u, __builtin_amdgcn_ds_read_tr16_b64_v4i16((LAS v4s*)(L + off))); }
DEV v4u ldtr2(const char LAS* L, int o0, int o1) { const v2u a = ldtr(L, o0), b = ldtr(L, o1); return v4u{a.x, a.y, b.x, b.y}; }
DEV void dma16(rsrc_t r, int voff, int soff, u32 lds_dst) {
  u32 keep;
  asm volatile("s_mov_b32 %0, m0\n\ts_mov_b32 m0, %1\n\ts_nop 0\n\tbuffer_load_dwordx4 %2, %3, %4 offen" DMA_MOD " lds\n\ts_mov_b32 m0, %0"
               : "=&s"(keep) : "s"(lds_dst), "v"(voff), "s"(r), "s"(soff) : "memory");
}
DEV void light_poll(rsrc_t ws, int off) {
  _Pragma("nounroll") for (int s = 0; s < SPIN; ++s) {
    if (wall(ld1c(ws, fresh(off)) != SENT)) break;
    __builtin_amdgcn_s_sleep(SL2);
  }
}
DEV int tok_row(int blk, int t) {
  const int n1 = 16 * (blk >> 5) + 4 * ((blk >> 2) & 3) + (t & 3), n2 = 16 * ((blk >> 4) & 1) + 4 * (blk & 3) + (t >> 2);
  return n2 + 32 * n1;
}
template <bool SWAP> DEV v4u cmul_pack(v4f a, v4f b, v4f c, v4f s) {
  const v4f re = fma4(b, s, a * c), im = fma4(b, c, -(a * s));
  return SWAP ? v4u{pk(im.x, re.x), pk(im.y, re.y), pk(im.z, re.z), pk(im.w, re.w)}
              : v4u{pk(re.x, im.x), pk(re.y, im.y), pk(re.z, im.z), pk(re.w, im.w)};
}

#define STAMP(i)
#define CSTAMP(i)
#define SLOWCNT
#define DUMP()

DEV void dma_w1(const u16* wg, const u16* wu, int slice, int wave, int lane, u32 lbase) {
  const rsrc_t rg = mkrs(wg, D * F * 2), ru = mkrs(wu, D * F * 2);
  const int r = lane >> 3, ch = ((((lane & 7) >> 1) ^ (lane >> 4)) & 3) * 32 + (lane & 1) * 16 + slice * 128;
  const int vp = r * (F * 2) + ch, vn = (7 - r) * (F * 2) + ch;
#pragma unroll
  for (int n = 0; n < 4; ++n) {
    const int ks = wave + 4 * n;
    if (n == 3 && wave >= 2) break;
    const int t16 = (ks >> 1) * 16, a = ks & 1;
#pragma unroll
    for (int q = 0; q < 4; ++q) {
      const int dl0 = t16 + 8 * (q & 1);
      const bool pos = (q >> 1) == a;
      const int d0 = q < 2 ? (a ? 224 - dl0 - 7 : dl0) : (a ? 224 + dl0 : 448 - dl0 - 7);
      int vo = pos ? vp : vn, so = d0 * (F * 2);
      if (n == 0 && q == 2) {
        vo += so; so = 0;
        if (wave < 2 && lane < 8) vo = (wave ? 336 : 112) * (F * 2) + ch;
      }
      dma16(rg, vo, so, lbase + L_W1 + ks * 8192 + q * 1024);
      dma16(ru, vo, so, lbase + L_W1 + ks * 8192 + 4096 + q * 1024);
    }
  }
}
struct ModLoad { u32 cv[7]; v4u mw[7]; };
DEV void mod_load(ModLoad& m, const u16* cin, const u16* wm, int q, int tid) {
  const rsrc_t cr = mkrs(cin, q >= 0 ? D * 2 : 0), mr = mkrs(wm, q >= 0 ? D * 6 * D * 2 : 0);
#pragma unroll
  for (int i = 0; i < 7; ++i) m.cv[i] = __builtin_amdgcn_raw_buffer_load_b16(cr, (tid >> 2) * 2 + i * 128, 0, 0);
#pragma unroll
  for (int i = 0; i < 7; ++i) m.mw[i] = __builtin_amdgcn_raw_buffer_load_b128(mr, ((tid >> 2) * (6 * D) + q * 32 + (tid & 3) * 8) * 2, i * 64 * 6 * D * 2, PF_AUX);
}
DEV void mod_part(ModLoad& m, char LAS* L, int tid) {
  float sl[7];
#pragma unroll
  for (int i = 0; i < 7; ++i) { const float cc = blo(m.cv[i]); sl[i] = rb(cc * __builtin_amdgcn_rcpf(1.f + __expf(-cc))); pin(sl[i]); }
  v2f a[4];
#pragma unroll
  for (int e = 0; e < 4; ++e) a[e] = v2f{0.f, 0.f};
#pragma unroll
  for (int i = 0; i < 7; ++i)
#pragma unroll
    for (int e = 0; e < 4; ++e) a[e] = fma2(bun(m.mw[i][e]), v2f{sl[i], sl[i]}, a[e]);
#pragma unroll
  for (int e = 0; e < 4; ++e) {
    a[e].x += dpp<0x128>(a[e].x); a[e].y += dpp<0x128>(a[e].y);
    a[e].x += dpp<0x124>(a[e].x); a[e].y += dpp<0x124>(a[e].y);
  }
  if ((tid & 15) < 4) {
    *(LAS v4f*)(L + L_RED + ((tid >> 4) * 32 + (tid & 15) * 8) * 4) = v4f{a[0].x, a[0].y, a[1].x, a[1].y};
    *(LAS v4f*)(L + L_RED + ((tid >> 4) * 32 + (tid & 15) * 8 + 4) * 4) = v4f{a[2].x, a[2].y, a[3].x, a[3].y};
  }
}
DEV void mod_publish(char LAS* L, rsrc_t ws, int q, int lane, u32 tag) {
  v4f s = {0.f, 0.f, 0.f, 0.f};
#pragma unroll
  for (int r = 0; r < 16; ++r) s += lds4f(L, L_RED + (r * 32 + (lane & 7) * 4) * 4);
  const float one = (q / 14 == 1 || q / 14 == 4) ? 1.f : 0.f;
  st4c(ws, WS_MOD + (lane >> 3) * MOD_REP + q * 128 + (lane & 7) * 16,
       v4u{pk(rb(s.x) + one, rb(s.y) + one), tag, pk(rb(s.z) + one, rb(s.w) + one), tag});
}
DEV u32 ld1(rsrc_t r, int off) { return __builtin_amdgcn_raw_buffer_load_b32(r, off, 0, 0); }
DEV u32 mktag(u32 epoch) { return epoch * 0x9E3779B1u + 0x7F4A7C15u; }

DEV void x2_flush(char LAS* L, rsrc_t ws, int blk, int tid) {
#pragma unroll
  for (int m = 0; m < 4; ++m) {
    const int ch = min(tid + 256 * m, 895), tk = ch / 56;
    st4c(ws, tid + 256 * m < 896 ? WS_X2 + blk * 14336 + ch * 16 : OOB, lds4(L, L_XS + tk * XP + (ch - tk * 56) * 16));
  }
}
DEV bool flags_ok(v4u rv, u32 tag) { return rv.x == tag && rv.y == ~tag && rv.z == tag && rv.w == ~tag; }
DEV void wait_flags(rsrc_t ws, int off, u32 tag) {
  _Pragma("nounroll") for (int s = 0; s < SPIN; ++s) {
    const v2u rv = ld2c(ws, fresh(off));
    if (wall(rv.x == tag && rv.y == ~tag)) break;
    __builtin_amdgcn_s_sleep(SL1);
  }
}

__global__ void __launch_bounds__(256, 1)
fused(const u16* __restrict__ xin, const u16* __restrict__ wr, const u16* __restrict__ wi, const u16* __restrict__ wg,
      const u16* __restrict__ wu, const u16* __restrict__ wd, const u16* __restrict__ cin, const u16* __restrict__ wm,
      u16* __restrict__ out, char* wsp) {
  __shared__ __attribute__((aligned(16))) char smem[L_SZ];
  char LAS* L = (char LAS*)smem;
  const u32 lbase = (u32)(size_t)L;
  const int tid = threadIdx.x, lane = tid & 63, wave = __builtin_amdgcn_readfirstlane(tid >> 6);
  const int g = lane >> 4, c16 = lane & 15, l16 = lane * 16;
  const int grp = blockIdx.x & 7, j = blockIdx.x >> 3;
  u64 pc;
  asm volatile("s_getpc_b64 %0" : "=s"(pc));
  STAMP(0);
  asm volatile("" :: "s"(xin), "s"(wr), "s"(wi), "s"(wg), "s"(wu), "s"(wd), "s"(cin), "s"(wm), "s"(out), "s"(wsp));
  const rsrc_t ws = mkrs(wsp, WS_END);
  const rsrc_t tab = mkrs(TAB, TAB_N * 4096);
  const rsrc_t code = mkrs((const void*)(pc & ~127ull), CODE_BYTES);
  const bool isR = j >= 14 && j < 30;
  const bool isG = j < 28;
  const int gk = j / 7, ct = j - gk * 7;
  const int tile = 4 * (grp >> 2) + gk, slice = 7 * (grp & 3) + ct;
  const int wdb = WS_WD + (ct * 56 + wave * 14) * 4096;
  const v4u sv = {SENT, SENT, SENT, SENT};
  int lb = L_B + l16;
  pin(lb);
  const char LAS* LB = L + lb - L_B;
  const int pfo = ((j * 4 + wave) * 14 + lane) * 128, pfc = ((j * 4 + wave) * 4 + lane) * 128;
  u32 pfa = ld1(tab, lane < 14 ? pfo : OOB), pfb = ld1(code, lane < 4 ? pfc : OOB);
  v4u wdn[14][4];
  u32 epoch, tag;

  if (isR) {
    const int blk = 16 * grp + (j - 14);
    epoch = ld1c(ws, WS_EPOCH);
    const int t0 = wave == 0 ? 0 : 2 * wave - 1;
    v4u cf[8][4];
#define CF_LOAD(q) { const int chn = TAB_P1 + ((wave == 0 && q >= 4) ? 28 : 4 * t0 + q), vo = (q < 5 || wave != 0) ? l16 : OOB; \
      _Pragma("unroll") for (int s = 0; s < 4; ++s) cf[q][s] = ld4(tab, vo + s * 1024, chn * 4096); sb(); }
#pragma unroll
    for (int q = 0; q < X_AFTER; ++q) CF_LOAD(q)
    v2u xv[7];
    {
      const rsrc_t xr = mkrs(xin, S * D * 2);
      const int off = tok_row(blk, 4 * wave + g) * (D * 2) + c16 * 8;
#pragma unroll
      for (int m = 0; m < 7; ++m) xv[m] = ld2(xr, off + m * 128);
    }
    typedef u32 v3u __attribute__((ext_vector_type(3)));
    v2u fa_[2], fb_[2]; v3u fc_[2], fd_[2];
    {
      const rsrc_t rr = mkrs(wr, S * DF * 2), ri = mkrs(wi, S * DF * 2);
      const int fo_ = 16 * wave + c16 < 57 ? 2 * g * 450 + (16 * wave + c16) * 8 : OOB;
#pragma unroll
      for (int hh = 0; hh < 2; ++hh) {
        const int so = blk * 7200 + hh * 3600;
        fa_[hh] = ld2(rr, fo_, so); fb_[hh] = ld2(ri, fo_, so);
        fc_[hh] = __builtin_amdgcn_raw_buffer_load_b96(rr, fo_ + 448, so, 0); fd_[hh] = __builtin_amdgcn_raw_buffer_load_b96(ri, fo_ + 448, so, 0);
      }
    }
    sb();
#pragma unroll
    for (int q = X_AFTER; q < 8; ++q) CF_LOAD(q)
    STAMP(23);
    {
      v2f f[14], s2 = {0.f, 0.f};
#pragma unroll
      for (int m = 0; m < 7; ++m) {
        f[2 * m] = bun(xv[m].x); f[2 * m + 1] = bun(xv[m].y);
        s2 += f[2 * m] + f[2 * m + 1];
        *(LAS v2u*)(L + L_XS + (4 * wave + g) * XP + c16 * 8 + m * 128) = xv[m];
      }
      STAMP(21);
      sb();
      {
        const int q = 16 * wave + c16, o = WS_WT + q * 16384 + blk * 128 + g * 16;
#pragma unroll
        for (int hh = 0; hh < 2; ++hh) {
          const v2u a = fa_[hh], b = fb_[hh]; const v3u c = fc_[hh], d = fd_[hh];
          st4c(ws, q < 57 ? o + hh * 64 : OOB, v4u{__builtin_amdgcn_perm(b.x, a.x, 0x05040100u), __builtin_amdgcn_perm(b.y, a.y, 0x05040100u),
                                                   __builtin_amdgcn_perm(d.x, c.x, 0x07060302u), __builtin_amdgcn_perm(d.y, c.y, 0x07060302u)});
          st4c(ws, q < 56 ? o + hh * 64 + 57 * 16384 : OOB,
               v4u{__builtin_amdgcn_perm(b.x, a.x, 0x07060302u), __builtin_amdgcn_perm(b.y, a.y, 0x07060302u),
                   __builtin_amdgcn_perm(d.y, c.y, 0x05040100u), __builtin_amdgcn_perm(d.z, c.z, 0x05040100u)});
        }
      }
      sb();
      const float mean = sum16(s2.x + s2.y) * (1.f / D);
      v2f q2 = {0.f, 0.f};
#pragma unroll
      for (int m = 0; m < 14; ++m) { const v2f d = f[m] - mean; q2 = fma2(d, d, q2); }
      const float rstd = __builtin_amdgcn_rsqf(sum16(q2.x + q2.y) * (1.f / D) + 1e-5f);
      if (c16 == 0) *(LAS v2f*)(L + L_ST + (4 * wave + g) * 8) = v2f{mean, rstd};
    }
    pin(pfa); pin(pfb);
    epoch = __builtin_amdgcn_readfirstlane(epoch);
    tag = mktag(epoch);
    STAMP(22);
    bar();
    v4f n[4][2];
    const int d0 = 8 * (4 * wave + g);
    const v2f st = *(const LAS v2f*)(L + L_ST + c16 * 8);
    {
      int xr_ = L_XS + c16 * XP + d0 * 2, xb_ = L_XS + c16 * XP + (216 - d0) * 2, x0_ = d0 == 0 ? xr_ : xb_ + 464;
      pin(xr_); pin(xb_); pin(x0_);
      const v4u a = lds4(L + xr_, 0), e = lds4(L + xr_, 448), c = lds4(L + xb_, 0), b = lds4(L + xb_, 448);
      const u32 c0 = *(const LAS u16*)(L + xb_ + 16), b0 = *(const LAS u16*)(L + x0_);
#pragma unroll
      for (int i = 0; i < 8; ++i) {
        n[0][i >> 2][i & 3] = (i & 1) ? bhi(a[i >> 1]) : blo(a[i >> 1]);
        n[3][i >> 2][i & 3] = (i & 1) ? bhi(e[i >> 1]) : blo(e[i >> 1]);
        n[1][i >> 2][i & 3] = i == 0 ? blo(b0) : (i & 1) ? bhi(b[(8 - i) >> 1]) : blo(b[(8 - i) >> 1]);
        n[2][i >> 2][i & 3] = i == 0 ? blo(c0) : (i & 1) ? bhi(c[(8 - i) >> 1]) : blo(c[(8 - i) >> 1]);
      }
      const float nm = -st.x * st.y;
#pragma unroll
      for (int m = 0; m < 4; ++m) {
#pragma unroll
        for (int h = 0; h < 2; ++h) { n[m][h] = fma4(n[m][h], splat(st.y), splat(nm)); pin(n[m][h]); }
      }
    }
    sb();
    STAMP(1);
    int mb = L_M1 + d0 * 4;
    pin(mb);
    v4u xs[4];
    int to[4];
    to[0] = WS_T1 + blk * T1_BLK + (16 * t0 + c16) * 64 + g * 16;
    to[1] = wave != 0 ? to[0] + 1024 : c16 < 2 ? to[0] + 112 * 64 : OOB;
    to[2] = to[0] + 114 * 64;
    to[3] = wave != 0 ? to[0] + 1024 + 114 * 64 : OOB;
#pragma unroll
    for (int q = 0; q < 4; ++q) pin(to[q]);

    {
      const int kind = tid >= 112, d = 4 * (tid - 112 * kind);
      int wb = L_M1 + (kind ? 0 : 2 * 1792) + d * 4, wrv = L_M1 + (kind ? 0 : 2 * 1792) + 1792 + (445 - d) * 4;
      int wr0 = d == 0 ? wrv + 12 - 1792 : wrv + 12;
      const int mo = tid < 224 ? WS_MOD + grp * MOD_REP + tid * 16 : OOB;
      pin(wb); pin(wrv); pin(wr0);
      const int ro = WS_RDY + grp * 1024 + min(tid >> 1, 112) * 8;
      v4u mv;
      _Pragma("nounroll") for (int s = 0; s < SPIN; ++s) {
        mv = ld4c(ws, fresh(mo));
        const v2u rv = ld2c(ws, fresh(ro));
        if (wall((tid >= 224 || (mv.y == tag && mv.w == tag)) && rv.x == tag && rv.y == ~tag)) break;
        __builtin_amdgcn_s_sleep(SL1);
      }
      STAMP(2);
      if (tid < 224) {
        const v4f v = {blo(mv.x), bhi(mv.x), blo(mv.z), bhi(mv.z)};
        *(LAS v4f*)(L + wb) = v;
        *(LAS float*)(L + wr0) = v.x;
        *(LAS float*)(L + wrv + 8) = v.y;
        *(LAS float*)(L + wrv + 4) = v.z;
        *(LAS float*)(L + wrv) = v.w;
      }
    }
    bar();
    {
      v4u fr[4];
#pragma unroll
      for (int hh = 0; hh < 2; ++hh) {
        v4f h[4];
#pragma unroll
        for (int m = 0; m < 4; ++m) {
          const int o = ((m == 1 || m == 2) ? 1792 : 0) + (m >= 2 ? 896 : 0) + hh * 16;
          h[m] = fma4(n[m][hh], lds4f(L + mb, o), lds4f(L + mb, 2 * 1792 + o));
        }
        const v4f p = h[0] + h[1], q = h[2] + h[3], u = h[0] - h[1], v = h[3] - h[2];
        const v4f t[4] = {p + q, p - q, u + v, u - v};
#pragma unroll
        for (int m = 0; m < 4; ++m) { fr[m][2 * hh] = pk2(lo2(t[m])); fr[m][2 * hh + 1] = pk2(hi2(t[m])); }
      }
#pragma unroll
      for (int m = 0; m < 4; ++m) *(LAS v4u*)(LB + wave * 1024 + L_AF + m * 4096) = fr[m];
    }
    bar();
    {
      v4u a[4][4];
#pragma unroll
      for (int p = 0; p < 2; ++p)
#pragma unroll
        for (int s = 0; s < 4; ++s) { a[p][s] = lds4(LB, L_AF + (p * 4 + s) * 1024); a[2 + p][s] = lds4(LB, L_AF + ((2 + p) * 4 + s) * 1024); }
      v4f re[4], im[4];
#pragma unroll
      for (int q = 0; q < 4; ++q) {
        const int p = q >> 1, t = q & 1;
        mf0(re[q], a[p][0], cf[4 * t + p][0]); mf0(im[q], a[2 + p][0], cf[4 * t + 2 + p][0]);
        if (q > 0) {
          mfence<1>(re[q - 1], im[q - 1]);
          xs[q - 1] = v4u{pk(re[q - 1].x, im[q - 1].x), pk(re[q - 1].y, im[q - 1].y), pk(re[q - 1].z, im[q - 1].z), pk(re[q - 1].w, im[q - 1].w)};
          st4c(ws, to[q - 1], xs[q - 1]);
          sb();
        }
#pragma unroll
        for (int s = 1; s < 4; ++s) { mfa(re[q], a[p][s], cf[4 * t + p][s]); mfa(im[q], a[2 + p][s], cf[4 * t + 2 + p][s]); }
      }
      mfence<7>(re[3], im[3]);
      xs[3] = v4u{pk(re[3].x, im[3].x), pk(re[3].y, im[3].y), pk(re[3].z, im[3].z), pk(re[3].w, im[3].w)};
      st4c(ws, to[3], xs[3]);
    }
    STAMP(3);
    sb();
    sb();
    if (isG) dma_w1(wg, wu, slice, wave, lane, lbase);
#pragma unroll
    for (int q = 0; q < 4; ++q) {
      const int vo = (q < 2 || wave != 0) ? l16 : OOB;
#pragma unroll
      for (int s = 0; s < 4; ++s) cf[2 * q + 1][s] = ld4(tab, vo + s * 1024, (TAB_P3 + 2 * t0 + q) * 4096);
    }
#pragma unroll
    for (int m = 0; m < 4; ++m)
      st4c(ws, tid + 256 * m < 896 ? WS_H2 + blk * 14336 + 4096 * m + tid * 16 : OOB, sv);
    sb();
    STAMP(20);
    v4f xo[2][4];
    int xa = L_XS + c16 * XP + (16 * t0 + 4 * g) * 2, xd = L_XS + c16 * XP + (192 - 16 * t0 - 4 * g) * 2;
    int xs1 = tid < 16 ? L_XS + c16 * XP + 224 : xd + 512, xs3 = tid < 16 ? L_XS + c16 * XP + 672 : xa + 448;
    pin(xa); pin(xd); pin(xs1); pin(xs3);
#define XOFF(t, m, r) ((t) == 0 && (r) == 0 && (m) == 1 ? xs1 : (t) == 0 && (r) == 0 && (m) == 3 ? xs3 : \
    (m) == 0 ? xa + 32 * (t) + 2 * (r) : (m) == 3 ? xa + 448 + 32 * (t) + 2 * (r) : (m) == 1 ? xd + 512 - 32 * (t) - 2 * (r) : xd + 64 - 32 * (t) - 2 * (r))
    {
      v2u w[2][4]; u32 e[2][2], e3;
#pragma unroll
      for (int t = 0; t < 2; ++t) {
        w[t][0] = *(const LAS v2u*)(L + xa + 32 * t); w[t][3] = *(const LAS v2u*)(L + xa + 448 + 32 * t);
        w[t][1] = *(const LAS v2u*)(L + xd + 504 - 32 * t); w[t][2] = *(const LAS v2u*)(L + xd + 56 - 32 * t);
        e[t][0] = *(const LAS u16*)(L + XOFF(t, 1, 0)); e[t][1] = *(const LAS u16*)(L + XOFF(t, 2, 0));
      }
      e3 = *(const LAS u16*)(L + xs3);
      sb();
#pragma unroll
      for (int t = 0; t < 2; ++t) {
        xo[t][0] = v4f{blo(w[t][0].x), bhi(w[t][0].x), blo(w[t][0].y), bhi(w[t][0].y)};
        xo[t][3] = v4f{t == 0 ? blo(e3) : blo(w[t][3].x), bhi(w[t][3].x), blo(w[t][3].y), bhi(w[t][3].y)};
        xo[t][1] = v4f{blo(e[t][0]), bhi(w[t][1].y), blo(w[t][1].y), bhi(w[t][1].x)};
        xo[t][2] = v4f{blo(e[t][1]), bhi(w[t][2].y), blo(w[t][2].y), bhi(w[t][2].x)};
#pragma unroll
        for (int m = 0; m < 4; ++m) { xo[t][m] -= st.x; pin(xo[t][m]); }
      }
    }
    int m0_[2], m1_[2], md_[2];
#pragma unroll
    for (int r = 0; r < 2; ++r) {
      const int q = r == 0 || tid < 80 ? tid + 256 * r : tid;
      const int kind = (q * 586) >> 16, d = 4 * (q - 112 * kind);
      const int reg = (d >= 112) + (d >= 224) + (d >= 336), cc = ((reg + 1) >> 1) * 224, m1 = (0x1320 >> (4 * reg)) & 3;
      const int d1 = abs(d + 1 - cc), d0_ = abs(d - cc), edge = d == 112 * reg && reg > 0;
      m1_[r] = L_M2 + kind * 1792 + (((d1 >> 4) << 6) | (m1 << 4) | (d1 & 15)) * 4;
      m0_[r] = L_M2 + kind * 1792 + (edge ? reg << 4 : ((d0_ >> 4) << 6) | (m1 << 4) | (d0_ & 15)) * 4;
      md_[r] = (reg & 1) ? -4 : 4;
      pin(m0_[r]); pin(m1_[r]); pin(md_[r]);
    }
    {
      v4u mv[2];
      _Pragma("nounroll") for (int s = 0; s < SPIN; ++s) {
        mv[0] = ld4c(ws, fresh(WS_MOD + grp * MOD_REP + (224 + tid) * 16));
        mv[1] = ld4c(ws, fresh(WS_MOD + grp * MOD_REP + (224 + (tid < 80 ? 256 + tid : tid)) * 16));
        const v2u rv = ld2c(ws, fresh(WS_DRDY + grp * 1024 + min(lane, 48) * 8));
        if (wall(mv[0].y == tag && mv[0].w == tag && mv[1].y == tag && mv[1].w == tag && rv.x == tag && rv.y == ~tag)) break;
        __builtin_amdgcn_s_sleep(SL4);
      }
      if (lane == 0) st2c(ws, WS_HRDY + blk * 32 + wave * 8, v2u{tag, ~tag});
      STAMP(12);
#pragma unroll
      for (int f = 0; f < WD_MID; ++f) wdn[f >> 2][f & 3] = ld4c(ws, (isG ? l16 : OOB) + (f & 3) * 1024, wdb + (f >> 2) * 4096);
      sb();
#pragma unroll
      for (int r = 0; r < 2; ++r) {
        const float sc = m0_[r] < L_M2 + 1792 ? 2.f / (float)(S * D) : 1.f;
        *(LAS float*)(L + m0_[r]) = blo(mv[r].x) * sc;
        *(LAS float*)(L + m1_[r]) = bhi(mv[r].x) * sc;
        *(LAS float*)(L + m1_[r] + md_[r]) = blo(mv[r].z) * sc;
        *(LAS float*)(L + m1_[r] + 2 * md_[r]) = bhi(mv[r].z) * sc;
      }
    }
    bar();
    *(LAS v4u*)(L + L_AF + (tid >> 6) * 4096 + 3072 + l16) = v4u{0u, 0u, 0u, 0u};
    STAMP(4);
    int zr = L_AF + lane * 8, ro = L_M2 + (t0 * 64 + 4 * g) * 4, ro1 = wave == 0 ? ro : ro + 256, ex = L_EX + c16 * 8;
    pin(zr); pin(ro); pin(ro1); pin(ex);
    {
      int zl[4];
#pragma unroll
      for (int m = 0; m < 4; ++m) {
        const int kq = (m >> 1) * 128 + (to[m] == OOB ? 127 : wave == 0 && m == 1 ? 112 + c16 : 16 * (t0 + (m & 1)) + c16);
        zl[m] = L_AF + ((kq & ~31) + ((kq & 4) << 2) + ((kq & 24) >> 1) + (kq & 3)) * 32 + g * 8;
        pin(zl[m]);
        if (to[m] == OOB) xs[m] = sv;
      }
      bar();
      if (isG) wait_flags(ws, WS_HRDY + (16 * tile + 4 * wave) * 32 + c16 * 8, tag);
      {
        const int src = (blk * 5 + wave * 13) & 63;
        const int po = __builtin_amdgcn_readlane(to[(0)], 0) + (src & 15) * 64 + (src >> 4) * 16;
        const u32 old = __builtin_amdgcn_ds_bpermute(src * 4, xs[0].x);
        _Pragma("nounroll") for (int s = 0; s < SPIN; ++s) {
          if (wall(ld1c(ws, fresh(po)) != old)) break;
          __builtin_amdgcn_s_sleep(SL2);
        }
      }
      STAMP(13);
      v4u z[4];
      _Pragma("nounroll") for (int s = 0; s < SPIN; ++s) {
        u32 mn = 1;
#pragma unroll
        for (int m = 0; m < 4; ++m) z[m] = ld4c(ws, fresh(to[m]));
#pragma unroll
        for (int m = 0; m < 4; ++m) { const v4u d = z[m] ^ xs[m]; mn = min(min(mn, d.x), min(min(d.y, d.z), d.w)); }
        if (wall(mn != 0)) break;
      }
      STAMP(14);
#pragma unroll
      for (int m = 0; m < 4; ++m) {
        char LAS* p = L + zl[m];
        *(LAS v2u*)(p) = v2u{__builtin_amdgcn_perm(z[m].y, z[m].x, 0x05040100u), __builtin_amdgcn_perm(z[m].w, z[m].z, 0x05040100u)};
        *(LAS v2u*)(p + ZPL) = v2u{__builtin_amdgcn_perm(z[m].y, z[m].x, 0x07060302u), __builtin_amdgcn_perm(z[m].w, z[m].z, 0x07060302u)};
      }
    }
    bar();
    STAMP(5);
    {
      v4u zf[4][4];
#pragma unroll
      for (int ty = 0; ty < 4; ++ty)
#pragma unroll
        for (int s = 0; s < 4; ++s) zf[ty][s] = ldtr2(L + zr, ty * 4096 + s * 1024, ty * 4096 + s * 1024 + 512);
      v4f acc[2][4], x2[2][4], gav[2][4];
      v2f s1 = {0.f, 0.f}, s2 = {0.f, 0.f};
#define P3M(t, ty) { mf0(acc[t][ty], cf[4 * t + ty][0], zf[ty][0]); \
      _Pragma("unroll") for (int s = 1; s < 4; ++s) mfa(acc[t][ty], cf[4 * t + ty][s], zf[ty][s]); }
#define P3X(t, m, yv) { x2[t][m] = fma4(gav[t][m], yv, xo[t][m]); \
      if (t == 0 || wave != 0) { s1 += lo2(x2[t][m]) + hi2(x2[t][m]); \
        s2 = fma2(lo2(x2[t][m]), lo2(x2[t][m]), s2); s2 = fma2(hi2(x2[t][m]), hi2(x2[t][m]), s2); } }
      P3M(1, 0) P3M(1, 1) P3M(1, 2) P3M(1, 3)
#pragma unroll
      for (int m = 0; m < 4; ++m) { gav[0][m] = lds4f(L + ro, m * 64); gav[1][m] = lds4f(L + ro1, m * 64); }
      sb();
      P3M(0, 0)
      mfence<0>(acc[1][0], acc[1][1], acc[1][2], acc[1][3]);
      const v4f A1 = acc[1][0] + acc[1][1], B1 = acc[1][0] - acc[1][1], P1 = acc[1][3] - acc[1][2], Q1 = acc[1][2] + acc[1][3];
      sb();
      P3M(0, 1)
      P3X(1, 0, A1 - P1) P3X(1, 1, A1 + P1)
      sb();
      P3M(0, 2)
      P3X(1, 2, B1 - Q1) P3X(1, 3, B1 + Q1)
      sb();
      P3M(0, 3)
      mfence<7>(acc[0][0], acc[0][1], acc[0][2], acc[0][3]);
      {
        const v4f A = acc[0][0] + acc[0][1], B = acc[0][0] - acc[0][1], P = acc[0][3] - acc[0][2], Q = acc[0][2] + acc[0][3];
        v4f y[4] = {A - P, A + P, B - Q, B + Q};
        const bool sp = tid < 16;
        y[0].x = sp ? A.x : y[0].x; y[2].x = sp ? B.x : y[2].x;
        y[1].x = sp ? acc[1][0].x - acc[0][3].x : y[1].x; y[3].x = sp ? acc[1][0].x + acc[0][3].x : y[3].x;
        P3X(0, 0, y[0]) P3X(0, 1, y[1]) P3X(0, 2, y[2]) P3X(0, 3, y[3])
      }
      float sm = s1.x + s1.y, sq = s2.x + s2.y;
      sumg2(sm, sq);
      if (g == 0) *(LAS v2f*)(L + ex + wave * 128) = v2f{sm, sq};
      v4f sbv[2][4], cbv[2][4];
#pragma unroll
      for (int m = 0; m < 4; ++m) {
        sbv[0][m] = lds4f(L + ro, 1792 + m * 64); cbv[0][m] = lds4f(L + ro, 2 * 1792 + m * 64);
        sbv[1][m] = lds4f(L + ro1, 1792 + m * 64); cbv[1][m] = lds4f(L + ro1, 2 * 1792 + m * 64);
      }
      bar();
      v2f e = *(const LAS v2f*)(L + ex);
#pragma unroll
      for (int w = 1; w < 4; ++w) e += *(const LAS v2f*)(L + ex + w * 128);
      const float mc = e.x * (1.f / D), rstd = __builtin_amdgcn_rsqf(fmaxf(e.y * (1.f / D) - mc * mc, 0.f) + 1e-5f), nb = -mc * rstd;
      const int ho = WS_H2 + blk * 14336 + 2 * t0 * 1024 + l16, ho1 = wave == 0 ? OOB : ho + 2048;
#pragma unroll
      for (int t = 0; t < 2; ++t)
#pragma unroll
        for (int a = 0; a < 2; ++a) {
          v4u o;
#pragma unroll
          for (int hh = 0; hh < 2; ++hh) {
            const int m = 2 * a + hh;
            const v4f hv = fma4(fma4(x2[t][m], splat(rstd), splat(nb)), cbv[t][m], sbv[t][m]);
            o[2 * hh] = pk2(lo2(hv)); o[2 * hh + 1] = pk2(hi2(hv));
          }
          st4c(ws, (t ? ho1 : ho) + a * 1024, o);
        }
      STAMP(6);
      sb();
#pragma unroll
      for (int t = 0; t < 2; ++t)
        if (t == 0 || wave != 0)
#pragma unroll
          for (int m = 0; m < 4; ++m)
#pragma unroll
            for (int r = 0; r < 4; r += 2) {
              const u32 p = pk(x2[t][m][r] + st.x, x2[t][m][r + 1] + st.x);
              *(LAS u16*)(L + XOFF(t, m, r)) = (u16)p;
              *(LAS u16*)(L + XOFF(t, m, r + 1)) = (u16)(p >> 16);
            }
      sb();
    }
    bar();
    if (!isG) {
      x2_flush(L, ws, blk, tid);
      drain();
      if (lane == 0) st2c(ws, WS_XRDY + blk * 64 + wave * 8, v2u{tag, ~tag});
      DUMP(); return;
    }
  } else {
    const int sj = j >= 30 ? j - 30 : j + 2;
    const int idx = sj * 8 + grp;
    const bool isC = j < 14 || idx == 0;
    const int pair = j < 14 ? grp * 14 + j : 112;
    const int lc = grp + 8 * (sj >> 1);
    const int qu = sj < 4 && lc < 14 ? 2 * lc + (sj & 1) : -1;
    const int t1o = WS_T1 + (32 * wave + (lane >> 3)) * T1_BLK + pair * 128 + (lane & 7) * 16;
    ModLoad ml;
    epoch = ld1c(ws, WS_EPOCH);
    if (qu >= 0) mod_load(ml, cin, wm, qu, tid);
#pragma unroll
    for (int q = 0; q < 4; ++q) st4c(ws, isC ? t1o + 8 * q * T1_BLK : OOB, sv);
    u32 pf0, pf1, pf2;
    {
      const rsrc_t mr = mkrs(wm, D * 6 * D * 2), xr = mkrs(xin, S * D * 2), fr_ = mkrs((wave & 1) ? wi : wr, S * DF * 2);
      const int row = (sj * 4 + wave) * 7 + (lane >> 1), l7 = lane / 7;
      pf0 = __builtin_amdgcn_raw_buffer_load_b32(mr, lane < 14 && (grp < 6 || !(lane & 1)) ? row * (6 * D * 2) + (grp + 8 * (lane & 1)) * 128 : OOB, 0, PF_AUX);
      pf1 = __builtin_amdgcn_raw_buffer_load_b32(xr, lane < 28 ? tok_row(16 * grp + sj, 4 * wave + l7) * (D * 2) + (lane - 7 * l7) * 128 : OOB, 0, PF_AUX);
    }
    sb();
    epoch = __builtin_amdgcn_readfirstlane(epoch);
    tag = mktag(epoch);
    if (qu >= 0) mod_part(ml, L, tid);
    asm volatile("s_waitcnt vmcnt(%0)" :: "i"(W_LATE ? 2 : 3) : "memory");
    { const rsrc_t fr_ = mkrs((wave & 1) ? wi : wr, S * DF * 2);
      pf2 = __builtin_amdgcn_raw_buffer_load_b32(fr_, lane < 29 ? (((16 * grp + sj) * 7200) & ~127) + ((wave >> 1) * 29 + lane) * 128 : OOB, 0, PF_AUX);
    }
    bar();
    if (wave == 0 && qu >= 0) mod_publish(L, ws, qu, lane, tag);
    if (wave == 1 && lane < 8) st2c(ws, isC ? WS_RDY + lane * 1024 + pair * 8 : OOB, v2u{tag, ~tag});
    STAMP(1);
    pin(pf0); pin(pf1); pin(pf2); pin(pfa); pin(pfb);
    const int qn = sj >= 4 && sj < 10 && grp < 7 ? 28 + grp * 6 + (sj - 4) : !isC && idx < 15 ? 69 + idx : -1;
    const int tw = sj >= 10 ? (sj - 10) * 8 + grp : sj == 4 && grp == 7 ? 48 : -1;
    v2u dw[2][8];
    if (qn >= 0) mod_load(ml, cin, wm, qn, tid);
    if (tw >= 0) {
      const rsrc_t dr = mkrs(wd, F * D * 2);
      const int u0 = tw * 4 + wave, uc = u0 / 28, kap = 2 * (u0 - uc * 28);
#pragma unroll
      for (int e = 0; e < 16; ++e)
        dw[e >> 3][e & 7] = __builtin_amdgcn_raw_buffer_load_b64(dr, (4 * g * D + 4 * c16) * 2 + (e & 3) * D * 2, ((32 * kap + 16 * (e >> 2)) * D + 64 * uc) * 2, PF_AUX);
    }
    sb();
    {
      const rsrc_t rg = mkrs(wg, D * F * 2), ru = mkrs(wu, D * F * 2);
      const int r7 = lane / 7, po = lane < 56 && j < 14 ? ((j * 4 + wave) * 8 + r7) * (F * 2) + ((grp & 3) * 7 + lane - r7 * 7) * 128 : OOB;
      pf0 = ld1(rg, po); pf1 = ld1(ru, po);
    }
    sb();
    STAMP(20);
    if (tw >= 0) {
#pragma unroll
      for (int tt = 0; tt < 8; ++tt) {
        v4u o;
#pragma unroll
        for (int m = 0; m < 4; ++m)
          o[m] = __builtin_amdgcn_perm(dw[tt >> 2][2 * m + 1][(tt & 3) >> 1], dw[tt >> 2][2 * m][(tt & 3) >> 1], (tt & 1) ? 0x07060302u : 0x05040100u);
        st4c(ws, WS_WD + (tw * 4 + wave) * 8192 + tt * 1024 + l16, o);
      }
    }
    if (qn >= 0) {
      bar();
      mod_part(ml, L, tid);
      bar();
      if (wave == 0) mod_publish(L, ws, qn, lane, tag);
    }
    STAMP(21);
    if (!isC) { DUMP(); return; }
    v4u c64[2][4], c32[2][2][2];
#pragma unroll
    for (int cp = 0; cp < 2; ++cp)
#pragma unroll
      for (int s = 0; s < 4; ++s) c64[cp][s] = ld4(tab, l16 + s * 1024, (TAB_P2 + wave * 2 + cp) * 4096);
#pragma unroll
    for (int t = 0; t < 2; ++t)
#pragma unroll
      for (int f = 0; f < 4; ++f) c32[t][f >> 1][f & 1] = ld4(tab, l16 + f * 1024, (TAB_P2 + 8 + t) * 4096);
    sb();
    if (tw >= 0) {
      asm volatile("s_waitcnt vmcnt(16)" ::: "memory");
      bar();
      if (tid < 8) st2c(ws, WS_DRDY + tid * 1024 + tw * 8, v2u{tag, ~tag});
    }
    v4f t1c[2], t1s[2], t2c[2], t2s[2];
#pragma unroll
    for (int t = 0; t < 2; ++t) {
#pragma unroll
      for (int r = 0; r < 4; ++r) {
        const float a1 = (float)((16 * t + 4 * g + r) * (16 * wave + c16)) * (1.f / S);
        const float a2 = (float)((16 * t + c16) * (16 * wave + 4 * g + r)) * (1.f / S);
        t1c[t][r] = __builtin_amdgcn_cosf(a1); t1s[t][r] = __builtin_amdgcn_sinf(a1);
        t2c[t][r] = __builtin_amdgcn_cosf(a2); t2s[t][r] = -__builtin_amdgcn_sinf(a2);
      }
      pin(t1c[t]); pin(t1s[t]); pin(t2c[t]); pin(t2s[t]);
    }
    STAMP(2);
    int wfo = WS_WT + pair * 16384 + (16 * g + wave) * 128 + c16 * 8;
    pin(wfo);
    v2u wf[2][4];
    {
      int wl = L_CI + ((lane >> 2) & 1) * 8256 + wave * 2048 + ((lane >> 3) * 4 + (lane & 3)) * 16, lo = t1o;
      pin(wl); pin(lo);
      light_poll(ws, WS_T1 + (32 * wave + (pair & 31)) * T1_BLK + pair * 128);
      pin(pf0); pin(pf1);
      STAMP(13);
      v4u z[4];
      _Pragma("nounroll") for (int s = 0; s < SPIN; ++s) {
        lo = fresh(lo);
#pragma unroll
        for (int q = 0; q < 4; ++q) z[q] = ld4c(ws, lo, 8 * q * T1_BLK);
        if (wall(max(max(max4(z[0]), max4(z[1])), max(max4(z[2]), max4(z[3]))) != SENT)) break;
      }
      STAMP(14);
#pragma unroll
      for (int q = 0; q < 4; ++q) *(LAS v4u*)(L + wl + q * 512) = z[q];
    }
    bar();
#pragma unroll
    for (int t = 0; t < 2; ++t)
#pragma unroll
      for (int r = 0; r < 4; ++r) wf[t][r] = ld2c(ws, wfo + r * 512, t * 8192);
    sb();
    STAMP(4);
    {
      v4u a[2][2][4];
#pragma unroll
      for (int c = 0; c < 2; ++c)
#pragma unroll
        for (int mt = 0; mt < 2; ++mt)
#pragma unroll
          for (int s = 0; s < 4; ++s) a[c][mt][s] = lds4(LB, L_CI + c * 8256 + (s * 2 + mt) * 1024);
      v4u old[2][2];
#pragma unroll
      for (int c = 0; c < 2; ++c)
#pragma unroll
        for (int mt = 0; mt < 2; ++mt) old[c][mt] = lds4(LB + wave * 2048, L_CI + c * 8256 + mt * 1024);
      v4f re[2][2], im[2][2];
      v4u f2[2][2], f3[2][2];
      char LAS* xw = L + lb + wave * 1024 - L_B;
#define S1H(c, mt) { mf0(re[c][mt], a[c][mt][0], c64[0][0]); mf0(im[c][mt], a[c][mt][0], c64[1][0]); }
#define S1T(c, mt) { _Pragma("unroll") for (int s = 1; s < 4; ++s) { mfa(re[c][mt], a[c][mt][s], c64[0][s]); mfa(im[c][mt], a[c][mt][s], c64[1][s]); } }
#define S2(c, t) { mf0(re[c][t], c32[t][0][0], f2[c][0]); mf0(im[c][t], c32[t][1][0], f2[c][0]); \
      mfa(re[c][t], c32[t][0][1], f2[c][1]); mfa(im[c][t], c32[t][1][1], f2[c][1]); }
#define S3(c, t) { mf0(im[c][t], f3[c][0], c32[t][0][0]); mf0(re[c][t], f3[c][0], c32[t][1][0]); \
      mfa(im[c][t], f3[c][1], c32[t][0][1]); mfa(re[c][t], f3[c][1], c32[t][1][1]); }
#define TW1(c, t) { f2[c][t] = cmul_pack<false>(re[c][t], im[c][t], t1c[t], t1s[t]); pin(f2[c][t]); }
#define FLT(c, t) { const v4f fr_ = {blo(wf[t][0][c]), blo(wf[t][1][c]), blo(wf[t][2][c]), blo(wf[t][3][c])}, \
      fi_ = {bhi(wf[t][0][c]), bhi(wf[t][1][c]), bhi(wf[t][2][c]), bhi(wf[t][3][c])}; \
      f3[c][t] = cmul_pack<true>(re[c][t], im[c][t], fr_, -fi_); pin(f3[c][t]); }
#define TW2(c, t) { *(LAS v4u*)(xw + L_CX + (c * 2 + t) * 4096) = cmul_pack<true>(re[c][t], im[c][t], t2c[t], t2s[t]); }
#define F2(c, t) mfence<0>(re[c][t], im[c][t]);
      S1H(0, 0) S1T(0, 0) S1H(0, 1) S1T(0, 1) sb();
      F2(0, 0) S1H(1, 0) S1T(1, 0) TW1(0, 0) sb();
      F2(0, 1) S1H(1, 1) S1T(1, 1) TW1(0, 1) sb();
      F2(1, 0) S2(0, 0) S2(0, 1) TW1(1, 0) sb();
      F2(1, 1) TW1(1, 1) sb();
      F2(0, 0) S2(1, 0) FLT(0, 0) sb();
      F2(0, 1) S2(1, 1) FLT(0, 1) sb();
      F2(1, 0) S3(0, 0) FLT(1, 0) sb();
      F2(1, 1) S3(0, 1) FLT(1, 1) sb();
      F2(0, 0) S3(1, 0) TW2(0, 0) sb();
      F2(0, 1) S3(1, 1) TW2(0, 1) sb();
      F2(1, 0) TW2(1, 0) sb();
      F2(1, 1) TW2(1, 1)
      bar();
#pragma unroll
      for (int c = 0; c < 2; ++c)
#pragma unroll
        for (int nt = 0; nt < 2; ++nt)
#pragma unroll
          for (int s = 0; s < 4; ++s) a[c][nt][s] = lds4(LB, L_CX + ((c * 2 + nt) * 4 + s) * 1024);
      const int o2 = WS_T1 + (32 * wave + (lane >> 2)) * T1_BLK + pair * 128 + (lane & 3) * 16;
#define S4H(c, t) { mf0(im[c][t], c64[0][0], a[c][t][0]); mf0(re[c][t], c64[1][0], a[c][t][0]); }
#define S4T(c, t) { _Pragma("unroll") for (int s = 1; s < 4; ++s) { mfa(im[c][t], c64[0][s], a[c][t][s]); mfa(re[c][t], c64[1][s], a[c][t][s]); } }
#define NEQ(x, y) ((x) == (y) ? (x) ^ 1u : (x))
#define OUT(c, t) { const v4u o = {pk(re[c][t].x, im[c][t].x), pk(re[c][t].y, im[c][t].y), pk(re[c][t].z, im[c][t].z), pk(re[c][t].w, im[c][t].w)}; \
      st4c(ws, o2 + c * 64 + t * 16 * T1_BLK, v4u{NEQ(o.x, old[c][t].x), NEQ(o.y, old[c][t].y), NEQ(o.z, old[c][t].z), NEQ(o.w, old[c][t].w)}); }
      S4H(0, 0) S4T(0, 0) S4H(0, 1) mfence<1>(re[0][0], im[0][0]); OUT(0, 0) sb();
      S4T(0, 1) S4H(1, 0) mfence<1>(re[0][1], im[0][1]); OUT(0, 1) sb();
      S4T(1, 0) S4H(1, 1) mfence<1>(re[1][0], im[1][0]); OUT(1, 0) sb();
      S4T(1, 1) mfence<7>(re[1][1], im[1][1]); OUT(1, 1)
    }
    STAMP(5);
    sb();
    if (!isG) { DUMP(); return; }
    dma_w1(wg, wu, slice, wave, lane, lbase);
    wait_flags(ws, WS_DRDY + grp * 1024 + min(lane, 48) * 8, tag);
#pragma unroll
    for (int f = 0; f < WD_MID; ++f) wdn[f >> 2][f & 3] = ld4c(ws, l16 + (f & 3) * 1024, wdb + (f >> 2) * 4096);
    sb();
    wait_flags(ws, WS_HRDY + (16 * tile + 4 * wave) * 32 + c16 * 8, tag);
    bar();
  }

  STAMP(7);
  const int blk0 = 16 * tile + 4 * (grp & 3);
  const int ab = WS_ACT + blk0 * 57344 + wave * 14 * 1024;
  const int fo = WS_ARDY + (tile * 4 + (grp & 3)) * 512 + (7 * wave + min(lane, 6)) * 8;
  v4u h[14][2];
  {
    const int hb = WS_H2 + (16 * tile + 4 * wave) * 14336;
    const int fw_ = WS_ARDY + (tile * 4 + wave) * 512 + slice * 8;
    int tb[2][4];
#pragma unroll
    for (int u = 0; u < 8; ++u) {
      tb[u >> 2][u & 3] = (((lane >> 2) * 128 + ((lane >> 3) & 3) * 32 + (lane & 3) * 8) ^ ((u & 3) << 5)) + (u >> 2) * 57344;
      pin(tb[u >> 2][u & 3]);
    }
#define W_RD(dst, k, u) dst = ldtr2(L + tb[k >= 7][u & 3], (k % 7) * 8192 + (u >> 2) * 4096, (k % 7) * 8192 + (u >> 2) * 4096 + 2048)
#define H_LD(k, tt, base) h[k][tt] = ld4c(ws, l16 + (k & 3) * 1024, base + (k >> 2) * 4096)
    v4u wfr[2][8];
#pragma unroll
    for (int u = 0; u < 8; ++u) W_RD(wfr[0][u], 0, u);
    light_poll(ws, hb + (lane & 1) * 14336 + 13 * 1024);
    STAMP(15);
#pragma unroll
    for (int e = 0; e < 8; ++e) H_LD((e >> 1), (e & 1), hb + (e & 1) * 14336);
    sb();
    if (isR) x2_flush(L, ws, 16 * grp + j - 14, tid);
    sb();
#define BAD2(x0, x1) wany(max(max(max(max(x0.x, x0.y), x0.z), max(max(x0.w, x1.x), x1.y)), max(x1.z, x1.w)) == SENT)
#define RETRY2(x0, x1, o0, o1) \
      _Pragma("nounroll") for (int s_ = 0; s_ < SPIN; ++s_) { \
        const int lf_ = fresh(l16); SLOWCNT \
        x0 = ld4c(ws, lf_, o0); x1 = ld4c(ws, lf_, o1); \
        if (wall(max(max4(x0), max4(x1)) != SENT)) break; \
      }
    if (__builtin_expect(BAD2(h[0][0], h[0][1]), 0)) { RETRY2(h[0][0], h[0][1], hb, hb + 14336) }
    STAMP(8); CSTAMP(28);
    v4f acc[2][8][2];
    v2u fa;
    const int ao = WS_ACT + (16 * tile + 4 * wave) * 57344 + 2 * slice * 1024 + l16;
#define ACT_OUT(ps, tt, f) { v4u o; \
      _Pragma("unroll") for (int hh = 0; hh < 2; ++hh) { \
        const v4f av = __builtin_elementwise_max(acc[ps][2 * f + hh][tt], splat(0.f)) * acc[ps][4 + 2 * f + hh][tt]; \
        o[2 * hh] = pk2(lo2(av)); o[2 * hh + 1] = pk2(hi2(av)); } \
      st4c(ws, ao + (2 * ps + tt) * 57344 + f * 1024, o); }
#pragma unroll
    for (int ps = 0; ps < 2; ++ps) {
#pragma unroll
      for (int u = 0; u < 8; ++u) { acc[ps][u][0] = v4f{0.f, 0.f, 0.f, 0.f}; acc[ps][u][1] = acc[ps][u][0]; }
#pragma unroll
      for (int ks = 0; ks < 14; ++ks) {
        const int cur = ks & 1, nk = ks == 13 ? 0 : ks + 1;
        const bool more = ps == 0 || ks < 13, epi = ps == 1 && ks < 4;
        __builtin_amdgcn_s_waitcnt(0xC07F);
#pragma unroll
        for (int u = 0; u < 8; ++u) {
          acc[ps][u][0] = mfma(wfr[cur][u], h[ks][0], acc[ps][u][0]);
          if (more) W_RD(wfr[cur ^ 1][u], nk, u);
        }
        if (ps == 0) {
#pragma unroll
          for (int e = 8 + 4 * ks; e < 12 + 4 * ks; ++e)
            if (e < 28) H_LD((e >> 1), (e & 1), hb + (e & 1) * 14336);
#pragma unroll
          for (int e = 0; e < 28; ++e)
            if (e >= RF(ks) && e < RF(ks + 1)) H_LD((e >> 1), (e & 1), hb + (2 + (e & 1)) * 14336);
        } else {
          if (ks == 0) { H_LD(13, 0, hb + 2 * 14336); H_LD(13, 1, hb + 3 * 14336); }
#pragma unroll
          for (int e = 0; e < WD_PER; ++e) {
            const int f = WD_MID + WD_PER * ks + e;
            if (f < 56) wdn[f >> 2][f & 3] = ld4c(ws, l16 + (f & 3) * 1024, wdb + (f >> 2) * 4096);
          }
          if (ks == 10) fa = ld2c(ws, fo);
        }
        const bool bad = more && BAD2(h[nk][0], h[nk][1]);
#pragma unroll
        for (int u = 0; u < 8; ++u) acc[ps][u][1] = mfma(wfr[cur][u], h[ks][1], acc[ps][u][1]);
        if (epi) ACT_OUT(0, (ks >> 1), (ks & 1))
#pragma unroll
        for (int u = 0; u < 16; ++u) {
          __builtin_amdgcn_sched_group_barrier(0x8, 1, 0);
          if (u < 4) __builtin_amdgcn_sched_group_barrier(0x100, 2, 0);
          else if (u < 12) __builtin_amdgcn_sched_group_barrier(0x100, 1, 0);
          if (u >= 4 && u < 12) __builtin_amdgcn_sched_group_barrier(0x20, 1, 0);
          if (epi) __builtin_amdgcn_sched_group_barrier(0x2, 3, 0);
          else if (u >= 10) __builtin_amdgcn_sched_group_barrier(0x2, 1, 0);
        }
        sb();
        if (ps == 1 && ks == 6) {
          static_assert(WD_MID + 7 * WD_PER <= 56, "the count below");
          asm volatile("s_waitcnt vmcnt(%0)" :: "i"(3 * WD_PER) : "memory");
          if (lane < 2) st2c(ws, lane == 0 ? fw_ : isR ? WS_XRDY + (16 * grp + j - 14) * 64 + wave * 8 : OOB, v2u{tag, ~tag});
        }
        if (__builtin_expect(bad, 0)) {
          const int pb = (ks == 13 ? 2 : 2 * ps) * 14336 + nk * 1024;
          RETRY2(h[nk][0], h[nk][1], hb + pb, hb + pb + 14336)
        }
        if (ps == 0 && ks == 13) { STAMP(16); CSTAMP(29); }
        sb();
      }
    }
    STAMP(27);
    if (!wall(fa.x == tag && fa.y == ~tag)) wait_flags(ws, fo, tag);
    STAMP(26);
#pragma unroll
    for (int e = 0; e < A_FIRST; ++e) H_LD((e >> 1), (e & 1), ab + (e & 1) * 57344);
    sb();
    ACT_OUT(1, 0, 0) ACT_OUT(1, 0, 1) ACT_OUT(1, 1, 0) ACT_OUT(1, 1, 1)
    CSTAMP(30);
    sb();
    STAMP(9);
    const int blk = blk0 + (wave >> 1);
    const int xo = WS_X2 + blk * 14336 + c16 * 896 + ct * 128 + g * 32 + (wave & 1) * 16;
    const int go = WS_MOD + grp * MOD_REP + (70 + 2 * ct) * 128 + g * 64 + (wave & 1) * 32;
    const int xf = WS_XRDY + (blk + (lane & 4 ? 2 : 0)) * 64 + (lane & 3) * 8;
    v4u a[14][2];
    v4f ac2[4][4];
#pragma unroll
    for (int t = 0; t < 4; ++t)
#pragma unroll
      for (int n = 0; n < 4; ++n) ac2[t][n] = v4f{0.f, 0.f, 0.f, 0.f};
    v4u xv[2], gv[2];
#pragma unroll
    for (int ks = 0; ks < 14; ++ks) {
#pragma unroll
      for (int tt = 0; tt < 2; ++tt)
#pragma unroll
        for (int n = 0; n < 4; ++n) ac2[tt][n] = mfma(wdn[ks][n], h[ks][tt], ac2[tt][n]);
#pragma unroll
      for (int e = A_FIRST + 3 * ks; e < A_FIRST + 3 * ks + 3; ++e)
        if (e < 28) H_LD((e >> 1), (e & 1), ab + (e & 1) * 57344);
#pragma unroll
      for (int u = 0; u < 4; ++u) {
        __builtin_amdgcn_sched_group_barrier(0x8, 2, 0);
        __builtin_amdgcn_sched_group_barrier(0x20, 1, 0);
      }
      sb();
      if (ks == F_AT) {
        static_assert(A_FIRST + 3 * F_AT + 3 <= 28, "the count below");
        asm volatile("s_waitcnt vmcnt(%0)" :: "i"(3 * F_AT + 3) : "memory");
        if (lane == 0) st2c(ws, fw_ + 256, v2u{tag, ~tag});
      }
      if (ks == 7) {
        fa = ld2c(ws, xf);
        gv[0] = ld4c(ws, go); gv[1] = ld4c(ws, go + 16);
      }
      if (ks == 11) {
        if (!wall(fa.x == tag && fa.y == ~tag)) wait_flags(ws, xf, tag);
        xv[0] = ld4c(ws, xo); xv[1] = ld4c(ws, xo + 2 * 14336);
      }
      sb();
    }
    STAMP(17);
    int rw = lane * 8 + wave * 2048, rr_ = lane * 8 + wave * 8192;
    pin(rw); pin(rr_);
#define RED_WR(ph) _Pragma("unroll") for (int dd = 0; dd < 4; ++dd) if (dd != wave) \
      _Pragma("unroll") for (int n = 0; n < 4; ++n) \
        *(LAS v2f*)(L + rw + ph * 32768 + dd * 8192 + n * 512) = (dd & 1) ? hi2(ac2[2 * ph + (dd >> 1)][n]) : lo2(ac2[2 * ph + (dd >> 1)][n]);
#define RED_FIN(ph) { v2f r[4]; \
      _Pragma("unroll") for (int n = 0; n < 4; ++n) { \
        const v4f own = wave < 2 ? ac2[2 * ph][n] : ac2[2 * ph + 1][n]; r[n] = (wave & 1) ? hi2(own) : lo2(own); } \
      _Pragma("unroll") for (int w = 1; w < 4; ++w) \
        _Pragma("unroll") for (int n = 0; n < 4; ++n) r[n] += *(const LAS v2f*)(L + rr_ + ((wave + w) & 3) * 2048 + ph * 32768 + n * 512); \
      v4u o; \
      _Pragma("unroll") for (int q = 0; q < 2; ++q) { \
        o[2 * q] = pk2(fma2(bun(gv[q].x), v2f{r[0][q], r[1][q]}, bun(xv[ph][2 * q]))); \
        o[2 * q + 1] = pk2(fma2(bun(gv[q].z), v2f{r[2][q], r[3][q]}, bun(xv[ph][2 * q + 1]))); } \
      st4c(mkrs(out, S * D * 2), (tok_row(blk + 2 * ph, c16) * D + ct * 64 + g * 16 + (wave & 1) * 8) * 2, o); }
    fa = ld2c(ws, fo + 256);
    asm volatile("s_barrier" ::: "memory");
    RED_WR(0)
    _Pragma("nounroll") for (int s = 0; s < SPIN; ++s) {
      if (wall(gv[0].y == tag && gv[0].w == tag && gv[1].y == tag && gv[1].w == tag)) break;
      const int gf = fresh(go);
      gv[0] = ld4c(ws, gf); gv[1] = ld4c(ws, gf + 16);
    }
#define A_LD(e) a[(e) >> 1][(e) & 1] = ld4c(ws, l16 + (((e) >> 1) & 3) * 1024, ab + (2 + ((e) & 1)) * 57344 + ((e) >> 3) * 4096)
    if (!wall(fa.x == tag && fa.y == ~tag)) wait_flags(ws, fo + 256, tag);
    STAMP(18);
#pragma unroll
    for (int e = 0; e < B_FIRST; ++e) A_LD(e);
    sb();
    bar();
    RED_FIN(0)
    sb();
#pragma unroll
    for (int ks = 0; ks < 14; ++ks) {
#pragma unroll
      for (int tt = 0; tt < 2; ++tt)
#pragma unroll
        for (int n = 0; n < 4; ++n) ac2[2 + tt][n] = mfma(wdn[ks][n], a[ks][tt], ac2[2 + tt][n]);
#pragma unroll
      for (int e = B_FIRST + 3 * ks; e < B_FIRST + 3 * ks + 3; ++e)
        if (e < 28) A_LD(e);
#pragma unroll
      for (int u = 0; u < 4; ++u) {
        __builtin_amdgcn_sched_group_barrier(0x8, 2, 0);
        __builtin_amdgcn_sched_group_barrier(0x20, 1, 0);
      }
      sb();
    }
    STAMP(10);
    RED_WR(1)
    bar();
    RED_FIN(1)
    if (blockIdx.x == 0 && wave == 0) {
      _Pragma("nounroll") for (int s = 0; s < SPIN; ++s) {
        const v4u v = ld4c(ws, fresh(WS_MOD + (70 + (lane < 14 ? lane : 0)) * 128));
        if (wall(v.y == tag)) break;
      }
      if (lane == 0) __builtin_amdgcn_raw_buffer_store_b32(epoch + 1u, ws, WS_EPOCH, 0, SC1);
    }
  }
  STAMP(11);
  DUMP();
}

__global__ void tiny(int t) { u64 t0 = __builtin_amdgcn_s_memrealtime(); while (__builtin_amdgcn_s_memrealtime() < t0 + t) __builtin_amdgcn_s_sleep(1); }
extern "C" void kernel_launch(void* const* d_in, const int* in_sizes, int n_in, void* d_out, int out_size, void* d_ws,
                              size_t ws_size, hipStream_t stream) {
  tiny<<<256, 64, 0, stream>>>(0);
  fused<<<256, 256, 0, stream>>>((const u16*)d_in[0], (const u16*)d_in[1], (const u16*)d_in[2], (const u16*)d_in[4],
                                 (const u16*)d_in[5], (const u16*)d_in[6], (const u16*)d_in[7], (const u16*)d_in[8],
                                 (u16*)d_out, (char*)d_ws);
}
```

```cpp
#include <hip/hip_runtime.h>

typedef unsigned short u16;
typedef unsigned int u32;
typedef unsigned long long u64;
typedef u32 v4u __attribute__((ext_vector_type(4)));
typedef u32 v2u __attribute__((ext_vector_type(2)));
typedef float v4f __attribute__((ext_vector_type(4)));
typedef float v2f __attribute__((ext_vector_type(2)));
typedef short v8s __attribute__((ext_vector_type(8)));
typedef short v4s __attribute__((ext_vector_type(4)));
typedef __bf16 v8b __attribute__((ext_vector_type(8)));
typedef __bf16 v2b __attribute__((ext_vector_type(2)));
#define LAS __attribute__((address_space(3)))
#define GAS __attribute__((address_space(1)))
#define DEV __device__ __forceinline__

#define C_W1_EARLY 0
#define CLR_DELAY 0
#define W_DELAY 0
#define CLR2_DELAY 0
#define PF_POS 2
#define SL1 1
#define SL2 2
#define SL4 4
#define X_AFTER 8
#define PF_AUX 2
#define W_LATE 1
#define W1_PF 1
#define DMA_MOD ""
#define LEAD 6
#define A_V 22
#define A_PER 4
#define WDV(f) ((f) - WD_MID + 2 < 14 ? (f) - WD_MID + 2 : ((f) - WD_MID - 12) / 2 + 16)
#define B_AT2 12
#define P_AT 7
#define WD_PER 3
#define A_FIRST 10
#define B_FIRST 10
#define A_AT 9
#define F_AT 4
#define CODE_BYTES 45056
#define WD_EARLY 16
#define WD_MID 24
#define B_AT 9
constexpr int S = 2048, D = 448, DF = 225, F = 1792;

struct alignas(16) Chain { u16 v[2048]; };
struct Cos448 { u16 v[448]; };
constexpr double c_cos_t(double a) {
  double a2 = a * a, t = 1.0, s = 1.0;
  for (int i = 1; i <= 10; ++i) { t = -t * a2 / ((2 * i - 1) * (2 * i)); s += t; }
  return s;
}
constexpr double c_sin_t(double a) {
  double a2 = a * a, t = a, s = a;
  for (int i = 1; i <= 10; ++i) { t = -t * a2 / ((2 * i) * (2 * i + 1)); s += t; }
  return s;
}
constexpr double c_cos448(int m) {
  if (m > 224) m = 448 - m;
  double sg = 1.0;
  if (m > 112) { m = 224 - m; sg = -1.0; }
  const double w = 6.283185307179586476925286766559 / 448.0;
  return m == 112 ? 0.0 : (m <= 56 ? sg * c_cos_t(w * m) : sg * c_sin_t(w * (112 - m)));
}
constexpr u16 c_bf16(double d) {
  const u32 u = __builtin_bit_cast(u32, (float)d);
  return (u16)((u + 0x7FFFu + ((u >> 16) & 1u)) >> 16);
}
constexpr Cos448 mk_cos() { Cos448 c{}; for (int m = 0; m < 448; ++m) c.v[m] = c_bf16(c_cos448(m)); return c; }
constexpr Cos448 CTAB = mk_cos();
constexpr u16 bcos(int m) { return CTAB.v[m % 448]; }
constexpr u16 bsin(int m) { return CTAB.v[(m % 448 + 336) % 448]; }
constexpr u16 bneg(u16 b) { return b ? (u16)(b ^ 0x8000u) : b; }
constexpr u16 bscale(u16 b, int e) { return b ? (u16)(b + e * 0x80) : b; }

constexpr Chain mk_p1(int c) {
  const int tile = c >> 2, par = c & 1, sn = (c >> 1) & 1;
  Chain ch{};
  for (int s = 0; s < 4; ++s)
    for (int l = 0; l < 64; ++l) {
      const int kp = 16 * tile + (l & 15), k = 2 * kp + par;
      if (k > 224) continue;
      for (int j = 0; j < 8; ++j) {
        const int dl = 32 * s + 8 * (l >> 4) + j;
        if (dl > 112) continue;
        u16 b = sn ? bneg(bsin(k * dl)) : bcos(k * dl);
        if (dl == 0 || dl == 112) b = bscale(b, -1);
        ch.v[(s * 64 + l) * 8 + j] = b;
      }
    }
  return ch;
}
constexpr Chain mk_p3(int c) {
  const int tile = c >> 1, sn = c & 1;
  Chain ch{};
  for (int s = 0; s < 4; ++s)
    for (int l = 0; l < 64; ++l) {
      int dl = 16 * tile + (l & 15);
      if (c == 1 && dl == 0) dl = 112;
      for (int j = 0; j < 8; ++j) {
        const int k = 2 * (32 * s + 8 * (l >> 4) + j) + 1;
        if (k < 224) ch.v[(s * 64 + l) * 8 + j] = sn ? bsin(k * dl) : bcos(k * dl);
      }
    }
  return ch;
}
constexpr Chain mk_p2(int c) {
  Chain ch{};
  for (int f = 0; f < 4; ++f)
    for (int l = 0; l < 64; ++l)
      for (int j = 0; j < 8; ++j) {
        const int comp = c < 8 ? (c & 1) : (f >> 1), s = c < 8 ? f : (f & 1);
        const int o = 16 * (c < 8 ? (c >> 1) : (c - 8)) + (l & 15), i = 16 * s + 4 * (l >> 4) + (j >> 1);
        const int m = (c < 8 ? 7 : 14) * ((i * o) % 64);
        u16 b = 0;
        if (comp == 0) b = (j & 1) ? bsin(m) : bcos(m);
        else b = (j & 1) ? bcos(m) : bneg(bsin(m));
        ch.v[(f * 64 + l) * 8 + j] = b;
      }
  return ch;
}
template <int I> constexpr Chain P1C = mk_p1(I);
template <int I> constexpr Chain P3C = mk_p3(I);
template <int I> constexpr Chain P2C = mk_p2(I);
#define L10(T, b) T<b>, T<b + 1>, T<b + 2>, T<b + 3>, T<b + 4>, T<b + 5>, T<b + 6>, T<b + 7>, T<b + 8>, T<b + 9>
#define L29(T) L10(T, 0), L10(T, 10), T<20>, T<21>, T<22>, T<23>, T<24>, T<25>, T<26>, T<27>, T<28>
constexpr int TAB_P1 = 0, TAB_P3 = 29, TAB_P2 = 43, TAB_N = 53;
static __device__ const Chain TAB[TAB_N] = {L29(P1C), L10(P3C, 0), P3C<10>, P3C<11>, P3C<12>, P3C<13>, L10(P2C, 0)};

constexpr int WS_EPOCH = 0;
constexpr int WS_RDY = 4096;
constexpr int WS_HRDY = 4096 + 8192;
constexpr int WS_DRDY = 4096 + 16384;
constexpr int WS_XRDY = 131072 + 65536;
constexpr int WS_ARDY = 131072;
constexpr int WS_CRDY = 131072 + 32768;
constexpr int WS_MOD = 32768, MOD_REP = 12288;
constexpr int T1_BLK = 226 * 64;
constexpr int WS_T1 = 1 << 18;
constexpr int WS_WT = WS_T1 + 128 * T1_BLK;
constexpr int WS_H2 = WS_WT + 113 * 16384;
constexpr int WS_X2 = WS_H2 + 128 * 14336;
constexpr int WS_WD = WS_X2 + 128 * 14336;
constexpr int WS_ACT = WS_WD + 7 * 56 * 4096;
constexpr int WS_END = WS_ACT + 128 * 57344;
constexpr int WS_DBG = 32 << 20;

constexpr int L_W1 = 0;
constexpr int L_B = 114688;
constexpr int XP = 912;
constexpr int L_XS = L_B;
constexpr int L_ST = L_XS + 16 * XP;
constexpr int L_M1 = L_ST + 128;
constexpr int L_AF = L_M1 + 4 * 1792;
constexpr int L_M2 = L_AF + 16384;
constexpr int L_EX = L_M2 + 3 * 1792;
constexpr int L_REND = L_EX + 512;
constexpr int ZPL = 8192;
constexpr int L_CI = L_B;
constexpr int L_CX = L_CI + 2 * 8256;
constexpr int L_RED = L_CX + 16384;
constexpr int L_DBG = 163840 - 1024;
constexpr int L_SZ = 163840;
static_assert(L_REND <= L_DBG && L_RED + 2048 <= L_DBG && 2 * ZPL <= 16384, "lds");

constexpr u32 SENT = 0xFFFFFFFFu;
constexpr int SPIN = 1 << 14;
constexpr int SC1 = 16;
constexpr int OOB = 0x7FFF0000;

typedef __amdgpu_buffer_rsrc_t rsrc_t;
DEV rsrc_t mkrs(const void* p, int bytes) { return __builtin_amdgcn_make_buffer_rsrc((void*)p, 0, bytes, 0x00020000); }
DEV v4u ld4(rsrc_t r, int off, int so = 0) { return __builtin_amdgcn_raw_buffer_load_b128(r, off, so, 0); }
DEV v2u ld2(rsrc_t r, int off, int so = 0) { return __builtin_amdgcn_raw_buffer_load_b64(r, off, so, 0); }
DEV v4u ld4c(rsrc_t r, int off, int so = 0) { return __builtin_amdgcn_raw_buffer_load_b128(r, off, so, SC1); }
DEV v2u ld2c(rsrc_t r, int off, int so = 0) { return __builtin_amdgcn_raw_buffer_load_b64(r, off, so, SC1); }
DEV u32 ld1c(rsrc_t r, int off, int so = 0) { return __builtin_amdgcn_raw_buffer_load_b32(r, off, so, SC1); }
DEV void st4c(rsrc_t r, int off, v4u v) { __builtin_amdgcn_raw_buffer_store_b128(v, r, off, 0, SC1); }
DEV void st2c(rsrc_t r, int off, v2u v) { __builtin_amdgcn_raw_buffer_store_b64(v, r, off, 0, SC1); }
DEV float blo(u32 v) { return __builtin_bit_cast(float, v << 16); }
DEV float bhi(u32 v) { return __builtin_bit_cast(float, v & 0xFFFF0000u); }
DEV v2f bun(u32 v) { return v2f{blo(v), bhi(v)}; }
DEV u32 pk(float lo, float hi) { return __builtin_bit_cast(u32, __builtin_convertvector(v2f{lo, hi}, v2b)); }
DEV u32 pk2(v2f v) { return __builtin_bit_cast(u32, __builtin_convertvector(v, v2b)); }
DEV float rb(float f) { return blo(pk(f, 0.f)); }
DEV v2f lo2(v4f v) { return __builtin_shufflevector(v, v, 0, 1); }
DEV v2f hi2(v4f v) { return __builtin_shufflevector(v, v, 2, 3); }
DEV v2f fma2(v2f a, v2f b, v2f c) { return __builtin_elementwise_fma(a, b, c); }
DEV v4f fma4(v4f a, v4f b, v4f c) { return __builtin_elementwise_fma(a, b, c); }
DEV v4f splat(float f) { return v4f{f, f, f, f}; }
DEV v4f mfma(v4u a, v4u b, v4f c) {
  return __builtin_amdgcn_mfma_f32_16x16x32_bf16(__builtin_bit_cast(v8b, a), __builtin_bit_cast(v8b, b), c, 0, 0, 0);
}
DEV void mfa(v4f& c, v4u a, v4u b) { asm volatile("s_nop 1\n\tv_mfma_f32_16x16x32_bf16 %0, %1, %2, %0" : "+v"(c) : "v"(a), "v"(b)); }
DEV void mf0(v4f& c, v4u a, v4u b) { asm volatile("s_nop 1\n\tv_mfma_f32_16x16x32_bf16 %0, %1, %2, 0" : "=&v"(c) : "v"(a), "v"(b)); }
template <int N> DEV void mfence(v4f& a, v4f& b) { asm volatile("s_nop %2" : "+v"(a), "+v"(b) : "i"(N)); }
template <int N> DEV void mfence(v4f& a, v4f& b, v4f& c, v4f& d) { asm volatile("s_nop %4" : "+v"(a), "+v"(b), "+v"(c), "+v"(d) : "i"(N)); }
#define MF "v_mfma_f32_16x16x32_bf16 "
DEV void mf8(v4f& d0, v4f& d1, const v4u (&a0)[4], const v4u (&b0)[4], const v4u (&a1)[4], const v4u (&b1)[4]) {
  asm volatile("s_nop 1\n\t" MF "%0, %2, %6, 0\n\t" MF "%1, %10, %14, 0\n\t" MF "%0, %3, %7, %0\n\t" MF "%1, %11, %15, %1\n\t"
               MF "%0, %4, %8, %0\n\t" MF "%1, %12, %16, %1\n\t" MF "%0, %5, %9, %0\n\t" MF "%1, %13, %17, %1"
               : "=&v"(d0), "=&v"(d1)
               : "v"(a0[0]), "v"(a0[1]), "v"(a0[2]), "v"(a0[3]), "v"(b0[0]), "v"(b0[1]), "v"(b0[2]), "v"(b0[3]),
                 "v"(a1[0]), "v"(a1[1]), "v"(a1[2]), "v"(a1[3]), "v"(b1[0]), "v"(b1[1]), "v"(b1[2]), "v"(b1[3]));
}
#define H4_M(XA, D, X, K, C) MF D ", " XA(X, K) ", " C "\n\t"
#define H4_XA(X, K) X ", " K
#define H4_XB(X, K) K ", " X
#define H4_DEF(NAME, XA, C0, C1, CONS, N0, N1, PA, PB, PC, PD) \
DEV void NAME(v4f& d0, v4f& d1, v4u x0, v4u x1, v4u k00, v4u k01, v4u k10, v4u k11, \
              u32& o0, u32& o1, float p0, float q0, float c0, float s0, float p1, float q1, float c1, float s1) { \
  float t0, t1, t2, t3; \
  asm volatile(H4_M(XA, "%0", "%8", "%10", C0) "v_mul_f32 %4, %14, %16\n\tv_mul_f32 %5, %15, %16\n\t" \
               H4_M(XA, "%1", "%8", "%12", C1) "v_mul_f32 %6, %18, %20\n\tv_mul_f32 %7, %19, %20\n\t" \
               H4_M(XA, "%0", "%9", "%11", "%0") "v_fma_f32 %4, " N0 "%15, %17, %4\n\tv_fma_f32 %5, " N1 "%14, %17, %5\n\t" \
               H4_M(XA, "%1", "%9", "%13", "%1") "v_fma_f32 %6, " N0 "%19, %21, %6\n\tv_fma_f32 %7, " N1 "%18, %21, %7\n\t" \
               "v_cvt_pk_bf16_f32 %2, " PA ", " PB "\n\tv_cvt_pk_bf16_f32 %3, " PC ", " PD \
               : CONS(d0), CONS(d1), "=&v"(o0), "=&v"(o1), "=&v"(t0), "=&v"(t1), "=&v"(t2), "=&v"(t3) \
               : "v"(x0), "v"(x1), "v"(k00), "v"(k01), "v"(k10), "v"(k11), \
                 "v"(p0), "v"(q0), "v"(c0), "v"(s0), "v"(p1), "v"(q1), "v"(c1), "v"(s1)); \
}
H4_DEF(h4_a0m, H4_XA, "0", "0", "=&v", "", "-", "%4", "%5", "%6", "%7")
H4_DEF(h4_a1m, H4_XA, "%0", "%1", "+v", "", "-", "%4", "%5", "%6", "%7")
H4_DEF(h4_b0m, H4_XB, "0", "0", "=&v", "", "-", "%4", "%5", "%6", "%7")
H4_DEF(h4_b0p, H4_XB, "0", "0", "=&v", "-", "", "%5", "%4", "%7", "%6")
H4_DEF(h4_a0p, H4_XA, "0", "0", "=&v", "-", "", "%5", "%4", "%7", "%6")
#define M4_DEF(NAME, XA, C0, C1, CONS) \
DEV void NAME(v4f& d0, v4f& d1, v4u x0, v4u x1, v4u k00, v4u k01, v4u k10, v4u k11) { \
  asm volatile(H4_M(XA, "%0", "%2", "%4", C0) H4_M(XA, "%1", "%2", "%6", C1) H4_M(XA, "%0", "%3", "%5", "%0") MF "%1, " XA("%3", "%7") ", %1" \
               : CONS(d0), CONS(d1) : "v"(x0), "v"(x1), "v"(k00), "v"(k01), "v"(k10), "v"(k11)); \
}
M4_DEF(m4_a0, H4_XA, "0", "0", "=&v") M4_DEF(m4_a1, H4_XA, "%0", "%1", "+v")
M4_DEF(m4_b0, H4_XB, "0", "0", "=&v") M4_DEF(m4_b1, H4_XB, "%0", "%1", "+v")
DEV void nop2() { asm volatile("s_nop 1"); }
template <class T> DEV void pin(T& v) { asm volatile("" : "+v"(v)); }
DEV int fresh(int off) { asm volatile("" : "+v"(off) :: "memory"); return off; }
DEV bool wall(bool ok) { return __builtin_amdgcn_ballot_w64(!ok) == 0; }
DEV bool wany(bool b) { return __builtin_amdgcn_ballot_w64(b) != 0; }
DEV void sb() { __builtin_amdgcn_sched_barrier(0); }
DEV u32 max4(v4u v) { return max(max(v.x, v.y), max(v.z, v.w)); }
DEV void drain() { asm volatile("s_waitcnt vmcnt(0)" ::: "memory"); }
DEV void cfence() { asm volatile("" ::: "memory"); }
DEV void bar() { asm volatile("s_waitcnt lgkmcnt(0)\n\ts_barrier" ::: "memory"); }
template <int CTRL> DEV float dpp(float v) {
  return __builtin_bit_cast(float, __builtin_amdgcn_update_dpp(0, __builtin_bit_cast(int, v), CTRL, 0xF, 0xF, true));
}
DEV float sum16(float v) {
  v += dpp<0x128>(v); v += dpp<0x124>(v); v += dpp<0x122>(v); v += dpp<0x121>(v); return v;
}
DEV void sumg2(float& a, float& b) {
  float c = a, d = b;
  asm volatile("s_nop 1\n\tv_permlane16_swap_b32 %0, %1\n\tv_permlane16_swap_b32 %2, %3" : "+v"(a), "+v"(c), "+v"(b), "+v"(d));
  a += c; b += d; c = a; d = b;
  asm volatile("s_nop 1\n\tv_permlane32_swap_b32 %0, %1\n\tv_permlane32_swap_b32 %2, %3" : "+v"(a), "+v"(c), "+v"(b), "+v"(d));
  a += c; b += d;
}
DEV v4u lds4(const char LAS* L, int off) { return *(const LAS v4u*)(L + off); }
DEV v4f lds4f(const char LAS* L, int off) { return *(const LAS v4f*)(L + off); }
DEV v2u ldtr(const char LAS* L, int off) { return __builtin_bit_cast(v2u, __builtin_amdgcn_ds_read_tr16_b64_v4i16((LAS v4s*)(L + off))); }
DEV v4u ldtr2(const char LAS* L, int o0, int o1) { const v2u a = ldtr(L, o0), b = ldtr(L, o1); return v4u{a.x, a.y, b.x, b.y}; }
DEV void dma16(rsrc_t r, int voff, int soff, u32 lds_dst) {
  u32 keep;
  asm volatile("s_mov_b32 %0, m0\n\ts_mov_b32 m0, %1\n\ts_nop 0\n\tbuffer_load_dwordx4 %2, %3, %4 offen" DMA_MOD " lds\n\ts_mov_b32 m0, %0"
               : "=&s"(keep) : "s"(lds_dst), "v"(voff), "s"(r), "s"(soff) : "memory");
}
DEV void light_poll(rsrc_t ws, int off) {
  _Pragma("nounroll") for (int s = 0; s < SPIN; ++s) {
    if (wall(ld1c(ws, fresh(off)) != SENT)) break;
    __builtin_amdgcn_s_sleep(SL2);
  }
}
DEV int tok_row(int blk, int t) {
  const int n1 = 16 * (blk >> 5) + 4 * ((blk >> 2) & 3) + (t & 3), n2 = 16 * ((blk >> 4) & 1) + 4 * (blk & 3) + (t >> 2);
  return n2 + 32 * n1;
}
template <bool SWAP> DEV v4u cmul_pack(v4f a, v4f b, v4f c, v4f s) {
  const v4f re = fma4(b, s, a * c), im = fma4(b, c, -(a * s));
  return SWAP ? v4u{pk(im.x, re.x), pk(im.y, re.y), pk(im.z, re.z), pk(im.w, re.w)}
              : v4u{pk(re.x, im.x), pk(re.y, im.y), pk(re.z, im.z), pk(re.w, im.w)};
}

template <bool PLUS> DEV void cmul2(u32& o0, u32& o1, v2f p, v2f q, v2f c, v2f s) {
  const v2f re = fma2(PLUS ? -q : q, s, p * c), im = fma2(PLUS ? p : -p, s, q * c);
  o0 = PLUS ? pk(im.x, re.x) : pk(re.x, im.x); o1 = PLUS ? pk(im.y, re.y) : pk(re.y, im.y);
}

#define STAMP(i)
#define CSTAMP(i)
#define SLOWCNT
#define DUMP()

DEV void dma_w1(const u16* wg, const u16* wu, int slice, int wave, int lane, u32 lbase) {
  const rsrc_t rg = mkrs(wg, D * F * 2), ru = mkrs(wu, D * F * 2);
  const int r = lane >> 3, ch = ((((lane & 7) >> 1) ^ (lane >> 4)) & 3) * 32 + (lane & 1) * 16 + slice * 128;
  const int vp = r * (F * 2) + ch, vn = (7 - r) * (F * 2) + ch;
#pragma unroll
  for (int n = 0; n < 4; ++n) {
    const int ks = wave + 4 * n;
    if (n == 3 && wave >= 2) break;
    const int t16 = (ks >> 1) * 16, a = ks & 1;
#pragma unroll
    for (int q = 0; q < 4; ++q) {
      const int dl0 = t16 + 8 * (q & 1);
      const bool pos = (q >> 1) == a;
      const int d0 = q < 2 ? (a ? 224 - dl0 - 7 : dl0) : (a ? 224 + dl0 : 448 - dl0 - 7);
      int vo = pos ? vp : vn, so = d0 * (F * 2);
      if (n == 0 && q == 2) {
        vo += so; so = 0;
        if (wave < 2 && lane < 8) vo = (wave ? 336 : 112) * (F * 2) + ch;
      }
      dma16(rg, vo, so, lbase + L_W1 + ks * 8192 + q * 1024);
      dma16(ru, vo, so, lbase + L_W1 + ks * 8192 + 4096 + q * 1024);
    }
  }
}
struct ModLoad { u32 cv[7]; v4u mw[7]; };
DEV void mod_load(ModLoad& m, const u16* cin, const u16* wm, int q, int tid) {
  const rsrc_t cr = mkrs(cin, q >= 0 ? D * 2 : 0), mr = mkrs(wm, q >= 0 ? D * 6 * D * 2 : 0);
#pragma unroll
  for (int i = 0; i < 7; ++i) m.cv[i] = __builtin_amdgcn_raw_buffer_load_b16(cr, (tid >> 2) * 2 + i * 128, 0, 0);
#pragma unroll
  for (int i = 0; i < 7; ++i) m.mw[i] = __builtin_amdgcn_raw_buffer_load_b128(mr, ((tid >> 2) * (6 * D) + q * 32 + (tid & 3) * 8) * 2, i * 64 * 6 * D * 2, PF_AUX);
}
DEV void mod_part(ModLoad& m, char LAS* L, int tid) {
  float sl[7];
#pragma unroll
  for (int i = 0; i < 7; ++i) { const float cc = blo(m.cv[i]); sl[i] = rb(cc * __builtin_amdgcn_rcpf(1.f + __expf(-cc))); pin(sl[i]); }
  v2f a[4];
#pragma unroll
  for (int e = 0; e < 4; ++e) a[e] = v2f{0.f, 0.f};
#pragma unroll
  for (int i = 0; i < 7; ++i)
#pragma unroll
    for (int e = 0; e < 4; ++e) a[e] = fma2(bun(m.mw[i][e]), v2f{sl[i], sl[i]}, a[e]);
#pragma unroll
  for (int e = 0; e < 4; ++e) {
    a[e].x += dpp<0x128>(a[e].x); a[e].y += dpp<0x128>(a[e].y);
    a[e].x += dpp<0x124>(a[e].x); a[e].y += dpp<0x124>(a[e].y);
  }
  if ((tid & 15) < 4) {
    *(LAS v4f*)(L + L_RED + ((tid >> 4) * 32 + (tid & 15) * 8) * 4) = v4f{a[0].x, a[0].y, a[1].x, a[1].y};
    *(LAS v4f*)(L + L_RED + ((tid >> 4) * 32 + (tid & 15) * 8 + 4) * 4) = v4f{a[2].x, a[2].y, a[3].x, a[3].y};
  }
}
DEV void mod_publish(char LAS* L, rsrc_t ws, int q, int lane, u32 tag) {
  v4f s = {0.f, 0.f, 0.f, 0.f};
#pragma unroll
  for (int r = 0; r < 16; ++r) s += lds4f(L, L_RED + (r * 32 + (lane & 7) * 4) * 4);
  const float one = (q / 14 == 1 || q / 14 == 4) ? 1.f : 0.f;
  st4c(ws, WS_MOD + (lane >> 3) * MOD_REP + q * 128 + (lane & 7) * 16,
       v4u{pk(rb(s.x) + one, rb(s.y) + one), tag, pk(rb(s.z) + one, rb(s.w) + one), tag});
}
DEV u32 ld1(rsrc_t r, int off) { return __builtin_amdgcn_raw_buffer_load_b32(r, off, 0, 0); }
DEV u32 mktag(u32 epoch) { return epoch * 0x9E3779B1u + 0x7F4A7C15u; }

DEV void x2_read(const char LAS* L, v4u (&r)[4], int tid) {
#pragma unroll
  for (int m = 0; m < 4; ++m) { const int ch = min(tid + 256 * m, 895), tk = ch / 56; r[m] = lds4(L, L_XS + tk * XP + (ch - tk * 56) * 16); }
}
DEV void x2_store(rsrc_t ws, const v4u (&r)[4], int blk, int tid) {
#pragma unroll
  for (int m = 0; m < 4; ++m) st4c(ws, tid + 256 * m < 896 ? WS_X2 + blk * 14336 + (tid + 256 * m) * 16 : OOB, r[m]);
}
DEV bool flags_ok(v4u rv, u32 tag) { return rv.x == tag && rv.y == ~tag && rv.z == tag && rv.w == ~tag; }
DEV void wait_flags(rsrc_t ws, int off, u32 tag) {
  _Pragma("nounroll") for (int s = 0; s < SPIN; ++s) {
    const v2u rv = ld2c(ws, fresh(off));
    if (wall(rv.x == tag && rv.y == ~tag)) break;
    __builtin_amdgcn_s_sleep(SL1);
  }
}

__global__ void __launch_bounds__(256, 1)
fused(const u16* __restrict__ xin, const u16* __restrict__ wr, const u16* __restrict__ wi, const u16* __restrict__ wg,
      const u16* __restrict__ wu, const u16* __restrict__ wd, const u16* __restrict__ cin, const u16* __restrict__ wm,
      u16* __restrict__ out, char* wsp) {
  __shared__ __attribute__((aligned(16))) char smem[L_SZ];
  char LAS* L = (char LAS*)smem;
  const u32 lbase = (u32)(size_t)L;
  const int tid = threadIdx.x, lane = tid & 63, wave = __builtin_amdgcn_readfirstlane(tid >> 6);
  const int g = lane >> 4, c16 = lane & 15, l16 = lane * 16;
  const int grp = blockIdx.x & 7, j = blockIdx.x >> 3;
  u64 pc;
  asm volatile("s_getpc_b64 %0" : "=s"(pc));
  STAMP(0); CSTAMP(31);
  asm volatile("" :: "s"(xin), "s"(wr), "s"(wi), "s"(wg), "s"(wu), "s"(wd), "s"(cin), "s"(wm), "s"(out), "s"(wsp));
  const rsrc_t ws = mkrs(wsp, WS_END);
  const rsrc_t tab = mkrs(TAB, TAB_N * 4096);
  const rsrc_t code = mkrs((const void*)(pc & ~127ull), CODE_BYTES);
  const bool isR = j >= 14 && j < 30;
  const bool isG = j < 28;
  const int gk = j / 7, ct = j - gk * 7;
  const int tile = 4 * (grp >> 2) + gk, slice = 7 * (grp & 3) + ct;
  const int wdb = WS_WD + (ct * 56 + wave * 14) * 4096;
  const v4u sv = {SENT, SENT, SENT, SENT};
  int lb = L_B + l16;
  pin(lb);
  const char LAS* LB = L + lb - L_B;
  const int pfo = ((j * 4 + wave) * 14 + lane) * 128, pfc = ((j * 4 + wave) * 4 + lane) * 128;
  u32 pfa = ld1(tab, lane < 14 ? pfo : OOB), pfb = ld1(code, lane < 4 ? pfc : OOB);
  v4u x2r[4];
  v4u wdn[14][4];
  u32 epoch, tag;

  if (isR) {
    const int blk = 16 * grp + (j - 14);
    epoch = ld1c(ws, WS_EPOCH);
    const int t0 = wave == 0 ? 0 : 2 * wave - 1;
    v4u cf[8][4];
#define CF_LOAD(q) { const int chn = TAB_P1 + ((wave == 0 && q >= 4) ? 28 : 4 * t0 + q), vo = (q < 5 || wave != 0) ? l16 : OOB; \
      _Pragma("unroll") for (int s = 0; s < 4; ++s) cf[q][s] = ld4(tab, vo + s * 1024, chn * 4096); sb(); }
#pragma unroll
    for (int q = 0; q < X_AFTER; ++q) CF_LOAD(q)
    v2u xv[7];
    {
      const rsrc_t xr = mkrs(xin, S * D * 2);
      const int off = tok_row(blk, 4 * wave + g) * (D * 2) + c16 * 8;
#pragma unroll
      for (int m = 0; m < 7; ++m) xv[m] = ld2(xr, off + m * 128);
    }
    typedef u32 v3u __attribute__((ext_vector_type(3)));
    v2u fa_[2], fb_[2]; v3u fc_[2], fd_[2];
    {
      const rsrc_t rr = mkrs(wr, S * DF * 2), ri = mkrs(wi, S * DF * 2);
      const int fo_ = 16 * wave + c16 < 57 ? 2 * g * 450 + (16 * wave + c16) * 8 : OOB;
#pragma unroll
      for (int hh = 0; hh < 2; ++hh) {
        const int so = blk * 7200 + hh * 3600;
        fa_[hh] = ld2(rr, fo_, so); fb_[hh] = ld2(ri, fo_, so);
        fc_[hh] = __builtin_amdgcn_raw_buffer_load_b96(rr, fo_ + 448, so, 0); fd_[hh] = __builtin_amdgcn_raw_buffer_load_b96(ri, fo_ + 448, so, 0);
      }
    }
    sb();
#pragma unroll
    for (int q = X_AFTER; q < 8; ++q) CF_LOAD(q)
    STAMP(23);
    {
      v2f f[14], s2 = {0.f, 0.f};
#pragma unroll
      for (int m = 0; m < 7; ++m) {
        f[2 * m] = bun(xv[m].x); f[2 * m + 1] = bun(xv[m].y);
        s2 += f[2 * m] + f[2 * m + 1];
        *(LAS v2u*)(L + L_XS + (4 * wave + g) * XP + c16 * 8 + m * 128) = xv[m];
      }
      STAMP(21);
      sb();
      {
        const int q = 16 * wave + c16, o = WS_WT + q * 16384 + blk * 128 + g * 16;
#pragma unroll
        for (int hh = 0; hh < 2; ++hh) {
          const v2u a = fa_[hh], b = fb_[hh]; const v3u c = fc_[hh], d = fd_[hh];
          st4c(ws, q < 57 ? o + hh * 64 : OOB, v4u{__builtin_amdgcn_perm(b.x, a.x, 0x05040100u), __builtin_amdgcn_perm(b.y, a.y, 0x05040100u),
                                                   __builtin_amdgcn_perm(d.x, c.x, 0x07060302u), __builtin_amdgcn_perm(d.y, c.y, 0x07060302u)});
          st4c(ws, q < 56 ? o + hh * 64 + 57 * 16384 : OOB,
               v4u{__builtin_amdgcn_perm(b.x, a.x, 0x07060302u), __builtin_amdgcn_perm(b.y, a.y, 0x07060302u),
                   __builtin_amdgcn_perm(d.y, c.y, 0x05040100u), __builtin_amdgcn_perm(d.z, c.z, 0x05040100u)});
        }
      }
      sb();
      const float mean = sum16(s2.x + s2.y) * (1.f / D);
      v2f q2 = {0.f, 0.f};
#pragma unroll
      for (int m = 0; m < 14; ++m) { const v2f d = f[m] - mean; q2 = fma2(d, d, q2); }
      const float rstd = __builtin_amdgcn_rsqf(sum16(q2.x + q2.y) * (1.f / D) + 1e-5f);
      if (c16 == 0) *(LAS v2f*)(L + L_ST + (4 * wave + g) * 8) = v2f{mean, rstd};
    }
    pin(pfa); pin(pfb);
    epoch = __builtin_amdgcn_readfirstlane(epoch);
    tag = mktag(epoch);
    STAMP(22);
    bar();
    v4f n[4][2];
    const int d0 = 8 * (4 * wave + g);
    const v2f st = *(const LAS v2f*)(L + L_ST + c16 * 8);
    {
      int xr_ = L_XS + c16 * XP + d0 * 2, xb_ = L_XS + c16 * XP + (216 - d0) * 2, x0_ = d0 == 0 ? xr_ : xb_ + 464;
      pin(xr_); pin(xb_); pin(x0_);
      const v4u a = lds4(L + xr_, 0), e = lds4(L + xr_, 448), c = lds4(L + xb_, 0), b = lds4(L + xb_, 448);
      const u32 c0 = *(const LAS u16*)(L + xb_ + 16), b0 = *(const LAS u16*)(L + x0_);
#pragma unroll
      for (int i = 0; i < 8; ++i) {
        n[0][i >> 2][i & 3] = (i & 1) ? bhi(a[i >> 1]) : blo(a[i >> 1]);
        n[3][i >> 2][i & 3] = (i & 1) ? bhi(e[i >> 1]) : blo(e[i >> 1]);
        n[1][i >> 2][i & 3] = i == 0 ? blo(b0) : (i & 1) ? bhi(b[(8 - i) >> 1]) : blo(b[(8 - i) >> 1]);
        n[2][i >> 2][i & 3] = i == 0 ? blo(c0) : (i & 1) ? bhi(c[(8 - i) >> 1]) : blo(c[(8 - i) >> 1]);
      }
      const float nm = -st.x * st.y;
#pragma unroll
      for (int m = 0; m < 4; ++m) {
#pragma unroll
        for (int h = 0; h < 2; ++h) { n[m][h] = fma4(n[m][h], splat(st.y), splat(nm)); pin(n[m][h]); }
      }
    }
    sb();
    STAMP(1);
    int mb = L_M1 + d0 * 4;
    pin(mb);
    v4u xs[4];
    int to[4];
    to[0] = WS_T1 + blk * T1_BLK + (16 * t0 + c16) * 64 + g * 16;
    to[1] = wave != 0 ? to[0] + 1024 : c16 < 2 ? to[0] + 112 * 64 : OOB;
    to[2] = to[0] + 114 * 64;
    to[3] = wave != 0 ? to[0] + 1024 + 114 * 64 : OOB;
#pragma unroll
    for (int q = 0; q < 4; ++q) pin(to[q]);

    {
      const int kind = tid >= 112, d = 4 * (tid - 112 * kind);
      int wb = L_M1 + (kind ? 0 : 2 * 1792) + d * 4, wrv = L_M1 + (kind ? 0 : 2 * 1792) + 1792 + (445 - d) * 4;
      int wr0 = d == 0 ? wrv + 12 - 1792 : wrv + 12;
      const int mo = tid < 224 ? WS_MOD + grp * MOD_REP + tid * 16 : OOB;
      pin(wb); pin(wrv); pin(wr0);
      const int ro = WS_RDY + grp * 1024 + min(tid >> 1, 112) * 8;
      v4u mv;
      _Pragma("nounroll") for (int s = 0; s < SPIN; ++s) {
        mv = ld4c(ws, fresh(mo));
        const v2u rv = ld2c(ws, fresh(ro));
        if (wall((tid >= 224 || (mv.y == tag && mv.w == tag)) && rv.x == tag && rv.y == ~tag)) break;
        __builtin_amdgcn_s_sleep(SL1);
      }
      STAMP(2);
      if (tid < 224) {
        const v4f v = {blo(mv.x), bhi(mv.x), blo(mv.z), bhi(mv.z)};
        *(LAS v4f*)(L + wb) = v;
        *(LAS float*)(L + wr0) = v.x;
        *(LAS float*)(L + wrv + 8) = v.y;
        *(LAS float*)(L + wrv + 4) = v.z;
        *(LAS float*)(L + wrv) = v.w;
      }
    }
    bar();
    {
      v4u fr[4];
#pragma unroll
      for (int hh = 0; hh < 2; ++hh) {
        v4f h[4];
#pragma unroll
        for (int m = 0; m < 4; ++m) {
          const int o = ((m == 1 || m == 2) ? 1792 : 0) + (m >= 2 ? 896 : 0) + hh * 16;
          h[m] = fma4(n[m][hh], lds4f(L + mb, o), lds4f(L + mb, 2 * 1792 + o));
        }
        const v4f p = h[0] + h[1], q = h[2] + h[3], u = h[0] - h[1], v = h[3] - h[2];
        const v4f t[4] = {p + q, p - q, u + v, u - v};
#pragma unroll
        for (int m = 0; m < 4; ++m) { fr[m][2 * hh] = pk2(lo2(t[m])); fr[m][2 * hh + 1] = pk2(hi2(t[m])); }
      }
#pragma unroll
      for (int m = 0; m < 4; ++m) *(LAS v4u*)(LB + wave * 1024 + L_AF + m * 4096) = fr[m];
    }
    bar();
    {
      v4u a[4][4];
#pragma unroll
      for (int p = 0; p < 2; ++p)
#pragma unroll
        for (int s = 0; s < 4; ++s) { a[p][s] = lds4(LB, L_AF + (p * 4 + s) * 1024); a[2 + p][s] = lds4(LB, L_AF + ((2 + p) * 4 + s) * 1024); }
      v4f re[4], im[4];
#pragma unroll
      for (int q = 0; q < 5; ++q) {
        if (q < 4) mf8(re[q], im[q], a[q >> 1], cf[4 * (q & 1) + (q >> 1)], a[2 + (q >> 1)], cf[4 * (q & 1) + 2 + (q >> 1)]);
        if (q > 0) {
          if (q < 4) mfence<0>(re[q - 1], im[q - 1]); else mfence<7>(re[3], im[3]);
          xs[q - 1] = v4u{pk(re[q - 1].x, im[q - 1].x), pk(re[q - 1].y, im[q - 1].y), pk(re[q - 1].z, im[q - 1].z), pk(re[q - 1].w, im[q - 1].w)};
          st4c(ws, to[q - 1], xs[q - 1]);
          sb();
        }
      }
    }
    STAMP(3);
    sb();
    sb();
    if (isG) dma_w1(wg, wu, slice, wave, lane, lbase);
#pragma unroll
    for (int q = 0; q < 4; ++q) {
      const int vo = (q < 2 || wave != 0) ? l16 : OOB;
#pragma unroll
      for (int s = 0; s < 4; ++s) cf[2 * q + 1][s] = ld4(tab, vo + s * 1024, (TAB_P3 + 2 * t0 + q) * 4096);
    }
#pragma unroll
    for (int m = 0; m < 4; ++m)
      st4c(ws, tid + 256 * m < 896 ? WS_H2 + blk * 14336 + 4096 * m + tid * 16 : OOB, sv);
    sb();
    STAMP(20);
    v4f xo[2][4];
    int xa = L_XS + c16 * XP + (16 * t0 + 4 * g) * 2, xd = L_XS + c16 * XP + (192 - 16 * t0 - 4 * g) * 2;
    int xs1 = tid < 16 ? L_XS + c16 * XP + 224 : xd + 512, xs3 = tid < 16 ? L_XS + c16 * XP + 672 : xa + 448;
    pin(xa); pin(xd); pin(xs1); pin(xs3);
#define XOFF(t, m, r) ((t) == 0 && (r) == 0 && (m) == 1 ? xs1 : (t) == 0 && (r) == 0 && (m) == 3 ? xs3 : \
    (m) == 0 ? xa + 32 * (t) + 2 * (r) : (m) == 3 ? xa + 448 + 32 * (t) + 2 * (r) : (m) == 1 ? xd + 512 - 32 * (t) - 2 * (r) : xd + 64 - 32 * (t) - 2 * (r))
    {
      v2u w[2][4]; u32 e[2][2], e3;
#pragma unroll
      for (int t = 0; t < 2; ++t) {
        w[t][0] = *(const LAS v2u*)(L + xa + 32 * t); w[t][3] = *(const LAS v2u*)(L + xa + 448 + 32 * t);
        w[t][1] = *(const LAS v2u*)(L + xd + 504 - 32 * t); w[t][2] = *(const LAS v2u*)(L + xd + 56 - 32 * t);
        e[t][0] = *(const LAS u16*)(L + XOFF(t, 1, 0)); e[t][1] = *(const LAS u16*)(L + XOFF(t, 2, 0));
      }
      e3 = *(const LAS u16*)(L + xs3);
      sb();
#pragma unroll
      for (int t = 0; t < 2; ++t) {
        xo[t][0] = v4f{blo(w[t][0].x), bhi(w[t][0].x), blo(w[t][0].y), bhi(w[t][0].y)};
        xo[t][3] = v4f{t == 0 ? blo(e3) : blo(w[t][3].x), bhi(w[t][3].x), blo(w[t][3].y), bhi(w[t][3].y)};
        xo[t][1] = v4f{blo(e[t][0]), bhi(w[t][1].y), blo(w[t][1].y), bhi(w[t][1].x)};
        xo[t][2] = v4f{blo(e[t][1]), bhi(w[t][2].y), blo(w[t][2].y), bhi(w[t][2].x)};
#pragma unroll
        for (int m = 0; m < 4; ++m) { xo[t][m] -= st.x; pin(xo[t][m]); }
      }
    }
    int m0_[2], m1_[2], md_[2];
#pragma unroll
    for (int r = 0; r < 2; ++r) {
      const int q = r == 0 || tid < 80 ? tid + 256 * r : tid;
      const int kind = (q * 586) >> 16, d = 4 * (q - 112 * kind);
      const int reg = (d >= 112) + (d >= 224) + (d >= 336), cc = ((reg + 1) >> 1) * 224, m1 = (0x1320 >> (4 * reg)) & 3;
      const int d1 = abs(d + 1 - cc), d0_ = abs(d - cc), edge = d == 112 * reg && reg > 0;
      m1_[r] = L_M2 + kind * 1792 + (((d1 >> 4) << 6) | (m1 << 4) | (d1 & 15)) * 4;
      m0_[r] = L_M2 + kind * 1792 + (edge ? reg << 4 : ((d0_ >> 4) << 6) | (m1 << 4) | (d0_ & 15)) * 4;
      md_[r] = (reg & 1) ? -4 : 4;
      pin(m0_[r]); pin(m1_[r]); pin(md_[r]);
    }
    {
      v4u mv[2];
      _Pragma("nounroll") for (int s = 0; s < SPIN; ++s) {
        mv[0] = ld4c(ws, fresh(WS_MOD + grp * MOD_REP + (224 + tid) * 16));
        mv[1] = ld4c(ws, fresh(WS_MOD + grp * MOD_REP + (224 + (tid < 80 ? 256 + tid : tid)) * 16));
        const v2u rv = ld2c(ws, fresh(WS_DRDY + grp * 1024 + min(lane, 48) * 8));
        if (wall(mv[0].y == tag && mv[0].w == tag && mv[1].y == tag && mv[1].w == tag && rv.x == tag && rv.y == ~tag)) break;
        __builtin_amdgcn_s_sleep(SL4);
      }
      if (lane == 0) st2c(ws, WS_HRDY + blk * 32 + wave * 8, v2u{tag, ~tag});
      STAMP(12);
#pragma unroll
      for (int f = 0; f < WD_EARLY; ++f) wdn[f >> 2][f & 3] = ld4c(ws, (isG ? l16 : OOB) + (f & 3) * 1024, wdb + (f >> 2) * 4096);
      sb();
#pragma unroll
      for (int r = 0; r < 2; ++r) {
        const float sc = m0_[r] < L_M2 + 1792 ? 2.f / (float)(S * D) : 1.f;
        *(LAS float*)(L + m0_[r]) = blo(mv[r].x) * sc;
        *(LAS float*)(L + m1_[r]) = bhi(mv[r].x) * sc;
        *(LAS float*)(L + m1_[r] + md_[r]) = blo(mv[r].z) * sc;
        *(LAS float*)(L + m1_[r] + 2 * md_[r]) = bhi(mv[r].z) * sc;
      }
    }
    bar();
    *(LAS v4u*)(L + L_AF + (tid >> 6) * 4096 + 3072 + l16) = v4u{0u, 0u, 0u, 0u};
    STAMP(4);
    int zr = L_AF + lane * 8, ro = L_M2 + (t0 * 64 + 4 * g) * 4, ro1 = wave == 0 ? ro : ro + 256, ex = L_EX + c16 * 8;
    pin(zr); pin(ro); pin(ro1); pin(ex);
    {
      int zl[4];
#pragma unroll
      for (int m = 0; m < 4; ++m) {
        const int kq = (m >> 1) * 128 + (to[m] == OOB ? 127 : wave == 0 && m == 1 ? 112 + c16 : 16 * (t0 + (m & 1)) + c16);
        zl[m] = L_AF + ((kq & ~31) + ((kq & 4) << 2) + ((kq & 24) >> 1) + (kq & 3)) * 32 + g * 8;
        pin(zl[m]);
        if (to[m] == OOB) xs[m] = sv;
      }
      bar();
      if (isG) wait_flags(ws, WS_HRDY + (16 * tile + 4 * wave) * 32 + c16 * 8, tag);
      {
        const int src = (blk * 5 + wave * 13) & 63;
        const int po = __builtin_amdgcn_readlane(to[(0)], 0) + (src & 15) * 64 + (src >> 4) * 16;
        const u32 old = __builtin_amdgcn_ds_bpermute(src * 4, xs[0].x);
        _Pragma("nounroll") for (int s = 0; s < SPIN; ++s) {
          if (wall(ld1c(ws, fresh(po)) != old)) break;
          __builtin_amdgcn_s_sleep(SL2);
        }
      }
      STAMP(13);
      v4u z[4];
      _Pragma("nounroll") for (int s = 0; s < SPIN; ++s) {
        u32 mn = 1;
#pragma unroll
        for (int m = 0; m < 4; ++m) z[m] = ld4c(ws, fresh(to[m]));
#pragma unroll
        for (int m = 0; m < 4; ++m) { const v4u d = z[m] ^ xs[m]; mn = min(min(mn, d.x), min(min(d.y, d.z), d.w)); }
        if (wall(mn != 0)) break;
      }
      STAMP(14);
#pragma unroll
      for (int m = 0; m < 4; ++m) {
        char LAS* p = L + zl[m];
        *(LAS v2u*)(p) = v2u{__builtin_amdgcn_perm(z[m].y, z[m].x, 0x05040100u), __builtin_amdgcn_perm(z[m].w, z[m].z, 0x05040100u)};
        *(LAS v2u*)(p + ZPL) = v2u{__builtin_amdgcn_perm(z[m].y, z[m].x, 0x07060302u), __builtin_amdgcn_perm(z[m].w, z[m].z, 0x07060302u)};
      }
    }
    bar();
    STAMP(5);
    {
      v4u zf[4][4];
#pragma unroll
      for (int ty = 0; ty < 4; ++ty)
#pragma unroll
        for (int s = 0; s < 4; ++s) zf[ty][s] = ldtr2(L + zr, ty * 4096 + s * 1024, ty * 4096 + s * 1024 + 512);
      v4f acc[2][4], x2[2][4], gav[2][4];
      v2f s1 = {0.f, 0.f}, s2 = {0.f, 0.f};
#define P3M(t, ty) mf8(acc[t][ty], acc[t][ty + 1], cf[4 * t + ty], zf[ty], cf[4 * t + ty + 1], zf[ty + 1]);
#define P3X(t, m, yv) { x2[t][m] = fma4(gav[t][m], yv, xo[t][m]); \
      if (t == 0 || wave != 0) { s1 += lo2(x2[t][m]) + hi2(x2[t][m]); \
        s2 = fma2(lo2(x2[t][m]), lo2(x2[t][m]), s2); s2 = fma2(hi2(x2[t][m]), hi2(x2[t][m]), s2); } }
      P3M(1, 0) P3M(1, 2)
#pragma unroll
      for (int m = 0; m < 4; ++m) { gav[0][m] = lds4f(L + ro, m * 64); gav[1][m] = lds4f(L + ro1, m * 64); }
      sb();
      P3M(0, 0) P3M(0, 2)
      mfence<0>(acc[1][0], acc[1][1], acc[1][2], acc[1][3]);
      const v4f A1 = acc[1][0] + acc[1][1], B1 = acc[1][0] - acc[1][1], P1 = acc[1][3] - acc[1][2], Q1 = acc[1][2] + acc[1][3];
      P3X(1, 0, A1 - P1) P3X(1, 1, A1 + P1) P3X(1, 2, B1 - Q1) P3X(1, 3, B1 + Q1)
      sb();
      mfence<0>(acc[0][0], acc[0][1], acc[0][2], acc[0][3]);
      {
        const v4f A = acc[0][0] + acc[0][1], B = acc[0][0] - acc[0][1], P = acc[0][3] - acc[0][2], Q = acc[0][2] + acc[0][3];
        v4f y[4] = {A - P, A + P, B - Q, B + Q};
        const bool sp = tid < 16;
        y[0].x = sp ? A.x : y[0].x; y[2].x = sp ? B.x : y[2].x;
        y[1].x = sp ? acc[1][0].x - acc[0][3].x : y[1].x; y[3].x = sp ? acc[1][0].x + acc[0][3].x : y[3].x;
        P3X(0, 0, y[0]) P3X(0, 1, y[1]) P3X(0, 2, y[2]) P3X(0, 3, y[3])
      }
      float sm = s1.x + s1.y, sq = s2.x + s2.y;
      sumg2(sm, sq);
      if (g == 0) *(LAS v2f*)(L + ex + wave * 128) = v2f{sm, sq};
      v4f sbv[2][4], cbv[2][4];
#pragma unroll
      for (int m = 0; m < 4; ++m) {
        sbv[0][m] = lds4f(L + ro, 1792 + m * 64); cbv[0][m] = lds4f(L + ro, 2 * 1792 + m * 64);
        sbv[1][m] = lds4f(L + ro1, 1792 + m * 64); cbv[1][m] = lds4f(L + ro1, 2 * 1792 + m * 64);
      }
      bar();
      v2f e = *(const LAS v2f*)(L + ex);
#pragma unroll
      for (int w = 1; w < 4; ++w) e += *(const LAS v2f*)(L + ex + w * 128);
      const float mc = e.x * (1.f / D), rstd = __builtin_amdgcn_rsqf(fmaxf(e.y * (1.f / D) - mc * mc, 0.f) + 1e-5f), nb = -mc * rstd;
      const int ho = WS_H2 + blk * 14336 + 2 * t0 * 1024 + l16, ho1 = wave == 0 ? OOB : ho + 2048;
#pragma unroll
      for (int t = 0; t < 2; ++t)
#pragma unroll
        for (int a = 0; a < 2; ++a) {
          v4u o;
#pragma unroll
          for (int hh = 0; hh < 2; ++hh) {
            const int m = 2 * a + hh;
            const v4f hv = fma4(fma4(x2[t][m], splat(rstd), splat(nb)), cbv[t][m], sbv[t][m]);
            o[2 * hh] = pk2(lo2(hv)); o[2 * hh + 1] = pk2(hi2(hv));
          }
          st4c(ws, (t ? ho1 : ho) + a * 1024, o);
        }
      STAMP(6);
      sb();
#pragma unroll
      for (int f = WD_EARLY; f < WD_MID; ++f) wdn[f >> 2][f & 3] = ld4c(ws, (isG ? l16 : OOB) + (f & 3) * 1024, wdb + (f >> 2) * 4096);
      sb();
#pragma unroll
      for (int t = 0; t < 2; ++t)
        if (t == 0 || wave != 0)
#pragma unroll
          for (int m = 0; m < 4; ++m)
#pragma unroll
            for (int r = 0; r < 4; r += 2) {
              const u32 p = pk(x2[t][m][r] + st.x, x2[t][m][r + 1] + st.x);
              *(LAS u16*)(L + XOFF(t, m, r)) = (u16)p;
              *(LAS u16*)(L + XOFF(t, m, r + 1)) = (u16)(p >> 16);
            }
      sb();
    }
    bar();
    x2_read(L, x2r, tid);
    bar();
    if (!isG) {
      x2_store(ws, x2r, blk, tid);
      drain();
      if (lane == 0) st2c(ws, WS_XRDY + blk * 64 + wave * 8, v2u{tag, ~tag});
      DUMP(); return;
    }
  } else {
    const int sj = j >= 30 ? j - 30 : j + 2;
    const int idx = sj * 8 + grp;
    const bool isC = j < 14 || idx == 0;
    const int pair = j < 14 ? grp * 14 + j : 112;
    const int lc = grp + 8 * (sj >> 1);
    const int qu = sj < 4 && lc < 14 ? 2 * lc + (sj & 1) : -1;
    const int t1o = WS_T1 + (32 * wave + (lane >> 3)) * T1_BLK + pair * 128 + (lane & 7) * 16;
    ModLoad ml;
    epoch = ld1c(ws, WS_EPOCH);
    if (qu >= 0) mod_load(ml, cin, wm, qu, tid);
#pragma unroll
    for (int q = 0; q < 4; ++q) st4c(ws, isC ? t1o + 8 * q * T1_BLK : OOB, sv);
    u32 pf0, pf1, pf2;
    {
      const rsrc_t mr = mkrs(wm, D * 6 * D * 2), xr = mkrs(xin, S * D * 2), fr_ = mkrs((wave & 1) ? wi : wr, S * DF * 2);
      const int row = (sj * 4 + wave) * 7 + (lane >> 1), l7 = lane / 7;
      pf0 = __builtin_amdgcn_raw_buffer_load_b32(mr, lane < 14 && (grp < 6 || !(lane & 1)) ? row * (6 * D * 2) + (grp + 8 * (lane & 1)) * 128 : OOB, 0, PF_AUX);
      pf1 = __builtin_amdgcn_raw_buffer_load_b32(xr, lane < 28 ? tok_row(16 * grp + sj, 4 * wave + l7) * (D * 2) + (lane - 7 * l7) * 128 : OOB, 0, PF_AUX);
    }
    sb();
    epoch = __builtin_amdgcn_readfirstlane(epoch);
    tag = mktag(epoch);
    if (qu >= 0) mod_part(ml, L, tid);
    asm volatile("s_waitcnt vmcnt(%0)" :: "i"(W_LATE ? 2 : 3) : "memory");
    { const rsrc_t fr_ = mkrs((wave & 1) ? wi : wr, S * DF * 2);
      pf2 = __builtin_amdgcn_raw_buffer_load_b32(fr_, lane < 29 ? (((16 * grp + sj) * 7200) & ~127) + ((wave >> 1) * 29 + lane) * 128 : OOB, 0, PF_AUX);
    }
    bar();
    if (wave == 0 && qu >= 0) mod_publish(L, ws, qu, lane, tag);
    if (wave == 1 && lane < 8) st2c(ws, isC ? WS_RDY + lane * 1024 + pair * 8 : OOB, v2u{tag, ~tag});
    STAMP(1);
    pin(pf0); pin(pf1); pin(pf2); pin(pfa); pin(pfb);
    const int qn = sj >= 4 && sj < 10 && grp < 7 ? 28 + grp * 6 + (sj - 4) : !isC && idx < 15 ? 69 + idx : -1;
    const int tw = sj >= 10 ? (sj - 10) * 8 + grp : sj == 4 && grp == 7 ? 48 : -1;
    v2u dw[2][8];
    if (qn >= 0) mod_load(ml, cin, wm, qn, tid);
    if (tw >= 0) {
      const rsrc_t dr = mkrs(wd, F * D * 2);
      const int u0 = tw * 4 + wave, uc = u0 / 28, kap = 2 * (u0 - uc * 28);
#pragma unroll
      for (int e = 0; e < 16; ++e)
        dw[e >> 3][e & 7] = __builtin_amdgcn_raw_buffer_load_b64(dr, (4 * g * D + 4 * c16) * 2 + (e & 3) * D * 2, ((32 * kap + 16 * (e >> 2)) * D + 64 * uc) * 2, PF_AUX);
    }
    sb();
    u32 pw[4];
    {
      const rsrc_t rg = mkrs(wg, D * F * 2), ru = mkrs(wu, D * F * 2);
      const int r7 = lane / 7, r0 = sj < 4 ? (sj * 4 + wave) * 16 : 256 + ((sj - 4) * 4 + wave) * 4;
      const int po = (r0 + r7) * (F * 2) + ((grp & 3) * 7 + lane - r7 * 7) * 128;
      const int o0 = lane < (sj < 4 ? 56 : 28) ? po : OOB, o1 = lane < 56 && sj < 4 ? po + 8 * (F * 2) : OOB;
      pw[0] = ld1(rg, o0); pw[1] = ld1(ru, o0); pw[2] = ld1(rg, o1); pw[3] = ld1(ru, o1);
    }
    sb();
    STAMP(20);
    v4f t1c[2], t1s[2], t2c[2], t2s[2];
#pragma unroll
    for (int t = 0; t < 2; ++t) {
#pragma unroll
      for (int r = 0; r < 4; ++r) {
        const float a1 = (float)((16 * t + 4 * g + r) * (16 * wave + c16)) * (1.f / S);
        const float a2 = (float)((16 * t + c16) * (16 * wave + 4 * g + r)) * (1.f / S);
        t1c[t][r] = __builtin_amdgcn_cosf(a1); t1s[t][r] = __builtin_amdgcn_sinf(a1);
        t2c[t][r] = __builtin_amdgcn_cosf(a2); t2s[t][r] = __builtin_amdgcn_sinf(a2);
      }
      pin(t1c[t]); pin(t1s[t]); pin(t2c[t]); pin(t2s[t]);
    }
    if (tw >= 0) {
#pragma unroll
      for (int tt = 0; tt < 8; ++tt) {
        v4u o;
#pragma unroll
        for (int m = 0; m < 4; ++m)
          o[m] = __builtin_amdgcn_perm(dw[tt >> 2][2 * m + 1][(tt & 3) >> 1], dw[tt >> 2][2 * m][(tt & 3) >> 1], (tt & 1) ? 0x07060302u : 0x05040100u);
        st4c(ws, WS_WD + (tw * 4 + wave) * 8192 + tt * 1024 + l16, o);
      }
    }
    if (qn >= 0) {
      bar();
      mod_part(ml, L, tid);
      bar();
      if (wave == 0) mod_publish(L, ws, qn, lane, tag);
    }
    pin(pw[0]); pin(pw[1]); pin(pw[2]); pin(pw[3]);
    STAMP(21);
    if (!isC) { DUMP(); return; }
    v4u c64[2][4], c32[2][2][2];
#pragma unroll
    for (int cp = 0; cp < 2; ++cp)
#pragma unroll
      for (int s = 0; s < 4; ++s) c64[cp][s] = ld4(tab, l16 + s * 1024, (TAB_P2 + wave * 2 + cp) * 4096);
#pragma unroll
    for (int t = 0; t < 2; ++t)
#pragma unroll
      for (int f = 0; f < 4; ++f) c32[t][f >> 1][f & 1] = ld4(tab, l16 + f * 1024, (TAB_P2 + 8 + t) * 4096);
    sb();
    if (tw >= 0) {
      asm volatile("s_waitcnt vmcnt(16)" ::: "memory");
      bar();
      if (tid < 8) st2c(ws, WS_DRDY + tid * 1024 + tw * 8, v2u{tag, ~tag});
    }
    STAMP(2);
    int wfo = WS_WT + pair * 16384 + (16 * g + wave) * 128 + c16 * 8;
    pin(wfo);
    v2u wf[2][4];
    {
      int wl = L_CI + ((lane >> 2) & 1) * 8256 + wave * 2048 + ((lane >> 3) * 4 + (lane & 3)) * 16, lo = t1o;
      pin(wl); pin(lo);
      light_poll(ws, WS_T1 + (32 * wave + (pair & 31)) * T1_BLK + pair * 128);
      STAMP(13);
      v4u z[4];
      _Pragma("nounroll") for (int s = 0; s < SPIN; ++s) {
        lo = fresh(lo);
#pragma unroll
        for (int q = 0; q < 4; ++q) z[q] = ld4c(ws, lo, 8 * q * T1_BLK);
        if (wall(max(max(max4(z[0]), max4(z[1])), max(max4(z[2]), max4(z[3]))) != SENT)) break;
      }
      STAMP(14);
#pragma unroll
      for (int q = 0; q < 4; ++q) *(LAS v4u*)(L + wl + q * 512) = z[q];
    }
    bar();
#pragma unroll
    for (int t = 0; t < 2; ++t)
#pragma unroll
      for (int r = 0; r < 4; ++r) wf[t][r] = ld2c(ws, wfo + r * 512, t * 8192);
    v2u fd = ld2c(ws, WS_DRDY + grp * 1024 + min(lane, 48) * 8);
    sb();
    STAMP(4);
    {
      v4u a[2][2][4];
#pragma unroll
      for (int c = 0; c < 2; ++c)
#pragma unroll
        for (int mt = 0; mt < 2; ++mt)
#pragma unroll
          for (int s = 0; s < 4; ++s) a[c][mt][s] = lds4(LB, L_CI + c * 8256 + (s * 2 + mt) * 1024);
      v4u old[2][2];
#pragma unroll
      for (int c = 0; c < 2; ++c)
#pragma unroll
        for (int mt = 0; mt < 2; ++mt) old[c][mt] = lds4(LB + wave * 2048, L_CI + c * 8256 + mt * 1024);
      v4f re[2][2], im[2][2], ur[2][2], ui[2][2], fr_[2][2], fi_[2][2];
      u32 f2[2][2][4], f3[2][2][4], f4[4];
      char LAS* xw = L + lb + wave * 1024 - L_B;
#define V4(x) v4u{x[0], x[1], x[2], x[3]}
#define S1K(h) c64[0][2 * h], c64[0][2 * h + 1], c64[1][2 * h], c64[1][2 * h + 1]
#define S1X(c, t, h) re[c][t], im[c][t], a[c][t][2 * h], a[c][t][2 * h + 1], S1K(h)
#define S2X(c, t) ur[c][t], ui[c][t], V4(f2[c][0]), V4(f2[c][1]), c32[t][0][0], c32[t][0][1], c32[t][1][0], c32[t][1][1]
#define S3X(c, t) im[c][t], re[c][t], V4(f3[c][0]), V4(f3[c][1]), c32[t][0][0], c32[t][0][1], c32[t][1][0], c32[t][1][1]
#define CM(o, P, Q, C, S_, h) o[2 * h], o[2 * h + 1], P[2 * h], Q[2 * h], C[2 * h], S_[2 * h], P[2 * h + 1], Q[2 * h + 1], C[2 * h + 1], S_[2 * h + 1]
#define TW1(c, t, h) CM(f2[c][t], re[c][t], im[c][t], t1c[t], t1s[t], h)
#define FLT(c, t, h) CM(f3[c][t], ur[c][t], ui[c][t], fr_[c][t], fi_[c][t], h)
#define TW2(c, t, h) CM(f4, re[c][t], im[c][t], t2c[t], t2s[t], h)
#define HV(x, h) (h ? hi2(x) : lo2(x))
#define CP(PL, o, P, Q, C, S_, h) cmul2<PL>(o[2 * h], o[2 * h + 1], HV(P, h), HV(Q, h), HV(C, h), HV(S_, h));
#define XW(c, t) *(LAS v4u*)(xw + L_CX + (c * 2 + t) * 4096) = V4(f4);
#define UNP(c, t) { _Pragma("unroll") for (int r = 0; r < 4; ++r) { fr_[c][t][r] = blo(wf[t][r][c]); fi_[c][t][r] = bhi(wf[t][r][c]); } \
      pin(fr_[c][t]); pin(fi_[c][t]); }
      nop2();
      m4_a0(S1X(0, 0, 0)); m4_a1(S1X(0, 0, 1)); m4_a0(S1X(0, 1, 0)); mfence<3>(re[0][0], im[0][0]);
      h4_a1m(S1X(0, 1, 1), TW1(0, 0, 0)); h4_a0m(S1X(1, 0, 0), TW1(0, 0, 1));
      h4_a1m(S1X(1, 0, 1), TW1(0, 1, 0)); h4_a0m(S1X(1, 1, 0), TW1(0, 1, 1));
      h4_a1m(S1X(1, 1, 1), TW1(1, 0, 0)); h4_b0m(S2X(0, 0), TW1(1, 0, 1));
      h4_b0m(S2X(0, 1), TW1(1, 1, 0));
      sb();
      CP(false, f2[1][1], re[1][1], im[1][1], t1c[1], t1s[1], 1)
      UNP(0, 0) UNP(0, 1)
      sb();
      h4_b0p(S2X(1, 0), FLT(0, 0, 0)); h4_b0p(S2X(1, 1), FLT(0, 0, 1));
      sb();
      CP(true, f3[0][1], ur[0][1], ui[0][1], fr_[0][1], fi_[0][1], 0) CP(true, f3[0][1], ur[0][1], ui[0][1], fr_[0][1], fi_[0][1], 1)
      UNP(1, 0) UNP(1, 1)
      sb();
      h4_a0p(S3X(0, 0), FLT(1, 0, 0)); h4_a0p(S3X(0, 1), FLT(1, 0, 1));
      sb();
      CP(true, f3[1][1], ur[1][1], ui[1][1], fr_[1][1], fi_[1][1], 0) CP(true, f3[1][1], ur[1][1], ui[1][1], fr_[1][1], fi_[1][1], 1)
#pragma unroll
      for (int e = 0; e < 4; ++e) pin(f3[1][1][e]);
      nop2();
      h4_a0p(S3X(1, 0), TW2(0, 0, 0)); h4_a0p(S3X(1, 1), TW2(0, 0, 1));
      XW(0, 0)
      sb();
      CP(true, f4, re[0][1], im[0][1], t2c[1], t2s[1], 0) CP(true, f4, re[0][1], im[0][1], t2c[1], t2s[1], 1) XW(0, 1)
      sb();
      mfence<0>(re[1][0], im[1][0], re[1][1], im[1][1]);
      CP(true, f4, re[1][0], im[1][0], t2c[0], t2s[0], 0) CP(true, f4, re[1][0], im[1][0], t2c[0], t2s[0], 1) XW(1, 0)
      CP(true, f4, re[1][1], im[1][1], t2c[1], t2s[1], 0) CP(true, f4, re[1][1], im[1][1], t2c[1], t2s[1], 1) XW(1, 1)
      bar();
#pragma unroll
      for (int c = 0; c < 2; ++c)
#pragma unroll
        for (int nt = 0; nt < 2; ++nt)
#pragma unroll
          for (int s = 0; s < 4; ++s) a[c][nt][s] = lds4(LB, L_CX + ((c * 2 + nt) * 4 + s) * 1024);
      const int o2 = WS_T1 + (32 * wave + (lane >> 2)) * T1_BLK + pair * 128 + (lane & 3) * 16;
#define S4(c, t) mf8(im[c][t], re[c][t], c64[0], a[c][t], c64[1], a[c][t]);
#define NEQ(x, y) ((x) == (y) ? (x) ^ 1u : (x))
#define OUT(c, t) { const v4u o = {pk(re[c][t].x, im[c][t].x), pk(re[c][t].y, im[c][t].y), pk(re[c][t].z, im[c][t].z), pk(re[c][t].w, im[c][t].w)}; \
      st4c(ws, o2 + c * 64 + t * 16 * T1_BLK, v4u{NEQ(o.x, old[c][t].x), NEQ(o.y, old[c][t].y), NEQ(o.z, old[c][t].z), NEQ(o.w, old[c][t].w)}); sb(); }
      S4(0, 0) S4(0, 1) mfence<0>(re[0][0], im[0][0]); OUT(0, 0)
      S4(1, 0) mfence<0>(re[0][1], im[0][1]); OUT(0, 1)
      S4(1, 1) mfence<0>(re[1][0], im[1][0]); OUT(1, 0)
      mfence<0>(re[1][1], im[1][1]); OUT(1, 1)
    }
    STAMP(5);
    sb();
    if (!isG) { DUMP(); return; }
    if (!wall(fd.x == tag && fd.y == ~tag)) wait_flags(ws, WS_DRDY + grp * 1024 + min(lane, 48) * 8, tag);
    dma_w1(wg, wu, slice, wave, lane, lbase);
#pragma unroll
    for (int f = 0; f < WD_MID; ++f) wdn[f >> 2][f & 3] = ld4c(ws, l16 + (f & 3) * 1024, wdb + (f >> 2) * 4096);
    fd = ld2c(ws, WS_HRDY + (16 * tile + 4 * wave) * 32 + c16 * 8);
    sb();
#pragma unroll
    for (int m = 0; m < 16; ++m)
      st4c(ws, WS_ACT + (16 * (tile + 2 * (m >> 3)) + 4 * (grp & 3)) * 57344 + ct * 32768 + (m & 7) * 4096 + tid * 16, sv);
    sb();
    if (!wall(fd.x == tag && fd.y == ~tag)) wait_flags(ws, WS_HRDY + (16 * tile + 4 * wave) * 32 + c16 * 8, tag);
    bar();
  }

  STAMP(7);
  const int blk0 = 16 * tile + 4 * (grp & 3);
  const int ab = WS_ACT + blk0 * 57344 + wave * 14 * 1024;
  v4u h[14][2];
  {
    const int hb = WS_H2 + (16 * tile + 4 * wave) * 14336;
    int tb[2][4];
#pragma unroll
    for (int u = 0; u < 8; ++u) {
      tb[u >> 2][u & 3] = (((lane >> 2) * 128 + ((lane >> 3) & 3) * 32 + (lane & 3) * 8) ^ ((u & 3) << 5)) + (u >> 2) * 57344;
      pin(tb[u >> 2][u & 3]);
    }
#define W_RD(dst, k, u) dst = ldtr2(L + tb[k >= 7][u & 3], (k % 7) * 8192 + (u >> 2) * 4096, (k % 7) * 8192 + (u >> 2) * 4096 + 2048)
#define H_LD(v, tt) hh_[v][tt] = ld4c(ws, l16 + (((v) % 14) & 3) * 1024, hb + (2 * ((v) / 14) + (tt)) * 14336 + (((v) % 14) >> 2) * 4096)
#define G_LD(e) h[(e) >> 1][(e) & 1] = ld4c(ws, l16 + (((e) >> 1) & 3) * 1024, ab + ((e) & 1) * 57344 + ((e) >> 3) * 4096)
    v4u wfr[2][8], hh_[28][2];
#pragma unroll
    for (int u = 0; u < 8; ++u) W_RD(wfr[0][u], 0, u);
    light_poll(ws, hb + (lane & 1) * 14336 + 13 * 1024);
    STAMP(15);
#pragma unroll
    for (int v = 0; v < LEAD; ++v) { H_LD(v, 0); H_LD(v, 1); }
    sb();
    if (!isR && lane < 2)
      st2c(ws, WS_CRDY + ((tile + 2 * lane) * 4 + (grp & 3)) * 256 + (ct * 4 + wave) * 8, v2u{tag, ~tag});
    sb();
#define BAD2(x0, x1) wany(max(max(max(max(x0.x, x0.y), x0.z), max(max(x0.w, x1.x), x1.y)), max(x1.z, x1.w)) == SENT)
#define RETRY2(x0, x1, o0, o1) \
      _Pragma("nounroll") for (int s_ = 0; s_ < SPIN; ++s_) { \
        const int lf_ = fresh(l16); SLOWCNT \
        x0 = ld4c(ws, lf_, o0); x1 = ld4c(ws, lf_, o1); \
        if (wall(max(max4(x0), max4(x1)) != SENT)) break; \
      }
    if (__builtin_expect(BAD2(hh_[0][0], hh_[0][1]), 0)) { RETRY2(hh_[0][0], hh_[0][1], hb, hb + 14336) }
    STAMP(8); CSTAMP(28);
    v4f acc[2][8][2];
    v2u fa, fb;
    const int xf = WS_XRDY + (blk0 + (wave >> 1) + (lane & 4 ? 2 : 0)) * 64 + (lane & 3) * 8;
    const int fx = lane < 36 ? WS_CRDY + (tile * 4 + wave) * 256 + max(lane - 8, 0) * 8 : WS_CRDY + (tile * 4 + (grp & 3)) * 256 + (lane - 36) * 8;
    const int ao = WS_ACT + (16 * tile + 4 * wave) * 57344 + 2 * slice * 1024 + l16;
#define ACT_OUT(ps, tt, f) { v4u o; \
      _Pragma("unroll") for (int hh = 0; hh < 2; ++hh) { \
        const v4f av = __builtin_elementwise_max(acc[ps][2 * f + hh][tt], splat(0.f)) * acc[ps][4 + 2 * f + hh][tt]; \
        o[2 * hh] = pk2(lo2(av)); o[2 * hh + 1] = pk2(hi2(av)); } \
      st4c(ws, ao + (2 * ps + tt) * 57344 + f * 1024, o); }
#pragma unroll
    for (int u = 0; u < 32; ++u) acc[u >> 4][(u >> 1) & 7][u & 1] = v4f{0.f, 0.f, 0.f, 0.f};
#pragma unroll
    for (int ps = 0; ps < 2; ++ps)
#pragma unroll
    for (int ks = 0; ks < 14; ++ks) {
      const int v = ps * 14 + ks, cur = v & 1, nk = (v + 1) % 14;
      const bool more = v < 27, epi = v == 14 || v == 15;
      __builtin_amdgcn_s_waitcnt(0xC07F);
#pragma unroll
      for (int u = 0; u < 8; ++u) {
        acc[ps][u][0] = mfma(wfr[cur][u], hh_[v][0], acc[ps][u][0]);
        if (more) W_RD(wfr[cur ^ 1][u], nk, u);
      }
      if (v + LEAD < 28) { H_LD(v + LEAD, 0); H_LD(v + LEAD, 1); }
#pragma unroll
      for (int e = 0; e < WD_PER; ++e) {
        const int f = WD_MID + (v < 14 ? v - 2 : 12 + WD_PER * (v - 16) + e);
        if (f < 56 && ((v >= 2 && v < 14 && e == 0) || v >= 16)) wdn[f >> 2][f & 3] = ld4c(ws, l16 + (f & 3) * 1024, wdb + (f >> 2) * 4096);
      }
      if (v == 8) fa = ld2c(ws, fx);
      if (v == 25) fb = ld2c(ws, xf);
      if (v > A_V) {
#pragma unroll
        for (int e = A_PER * (v - A_V - 1); e < A_PER * (v - A_V); ++e) G_LD(e);
      }
      const bool bad = more && BAD2(hh_[more ? v + 1 : v][0], hh_[more ? v + 1 : v][1]);
#pragma unroll
      for (int u = 0; u < 8; ++u) acc[ps][u][1] = mfma(wfr[cur][u], hh_[v][1], acc[ps][u][1]);
      if (epi) { ACT_OUT(0, ks, 0) ACT_OUT(0, ks, 1) }
#pragma unroll
      for (int u = 0; u < 16; ++u) {
        __builtin_amdgcn_sched_group_barrier(0x8, 1, 0);
        if (u < 4) __builtin_amdgcn_sched_group_barrier(0x100, 2, 0);
        else if (u < 12) __builtin_amdgcn_sched_group_barrier(0x100, 1, 0);
        if (u >= 5 && (u & 1)) __builtin_amdgcn_sched_group_barrier(0x20, 1, 0);
        if (epi) __builtin_amdgcn_sched_group_barrier(0x2, 5, 0);
        else if (u >= 10) __builtin_amdgcn_sched_group_barrier(0x2, 1, 0);
      }
      sb();
      if (v == 15 && isR) x2_store(ws, x2r, 16 * grp + j - 14, tid);
      if (v == 18 && isR) {
        static_assert(18 + LEAD < 28, "the count below");
        asm volatile("s_waitcnt vmcnt(6)" ::: "memory");
        if (lane == 0) st2c(ws, WS_XRDY + (16 * grp + j - 14) * 64 + wave * 8, v2u{tag, ~tag});
      }
      if (v == 12 && !wall(fa.x == tag && fa.y == ~tag)) wait_flags(ws, fx, tag);
      if (__builtin_expect(bad, 0)) {
        const int pb = 2 * ((v + 1) / 14) * 14336 + nk * 1024;
        RETRY2(hh_[more ? v + 1 : v][0], hh_[more ? v + 1 : v][1], hb + pb, hb + pb + 14336)
      }
      if (v == 13) { STAMP(16); CSTAMP(29); }
      sb();
    }
    STAMP(27);
    const int blk = blk0 + (wave >> 1);
    const int xo = WS_X2 + blk * 14336 + c16 * 896 + ct * 128 + g * 32 + (wave & 1) * 16;
    const int go = WS_MOD + grp * MOD_REP + (70 + 2 * ct) * 128 + g * 64 + (wave & 1) * 32;
    v4u xv[2], gv[2];
    gv[0] = ld4c(ws, go); gv[1] = ld4c(ws, go + 16);
    if (!wall(fb.x == tag && fb.y == ~tag)) wait_flags(ws, xf, tag);
    xv[0] = ld4c(ws, xo); xv[1] = ld4c(ws, xo + 2 * 14336);
    sb();
    ACT_OUT(1, 0, 0) ACT_OUT(1, 0, 1) ACT_OUT(1, 1, 0) ACT_OUT(1, 1, 1)
    CSTAMP(30);
    sb();
#pragma unroll
    for (int e = A_PER * (27 - A_V); e < 28; ++e) G_LD(e);
    sb();
    STAMP(9);
    v4u a[14][2];
    v4f ac2[4][4];
#pragma unroll
    for (int t = 0; t < 4; ++t)
#pragma unroll
      for (int n = 0; n < 4; ++n) ac2[t][n] = v4f{0.f, 0.f, 0.f, 0.f};
#define A_LD(e) a[(e) >> 1][(e) & 1] = ld4c(ws, l16 + (((e) >> 1) & 3) * 1024, ab + (2 + ((e) & 1)) * 57344 + ((e) >> 3) * 4096)
    const int po = ab + (2 + (lane & 1)) * 57344 + min(lane >> 1, 13) * 1024;
    u32 probe;
#pragma unroll
    for (int ks = 0; ks < 14; ++ks) {
      if (__builtin_expect(BAD2(h[ks][0], h[ks][1]), 0)) { RETRY2(h[ks][0], h[ks][1], ab + ks * 1024, ab + 57344 + ks * 1024) }
#pragma unroll
      for (int tt = 0; tt < 2; ++tt)
#pragma unroll
        for (int n = 0; n < 4; ++n) ac2[tt][n] = mfma(wdn[ks][n], h[ks][tt], ac2[tt][n]);
      if (ks == P_AT) probe = ld1c(ws, po);
      sb();
      if (ks == B_AT2) {
        STAMP(25);
        if (wany(probe == SENT)) light_poll(ws, po);
        STAMP(26);
      }
      if (ks >= B_AT2) {
#pragma unroll
        for (int e = 4 * (ks - B_AT2); e < 4 * (ks - B_AT2 + 1); ++e) A_LD(e);
      }
      sb();
    }
    STAMP(17);
    int rr_ = L_B + lane * 8 + wave * 6144;
    pin(rr_);
#define RED_WR(ph) _Pragma("unroll") for (int dd = 0; dd < 4; ++dd) if (dd != wave) \
      _Pragma("unroll") for (int n = 0; n < 4; ++n) \
        *(LAS v2f*)(L + L_B + lane * 8 + ((wave - dd - 1) & 3) * 2048 + ph * 24576 + dd * 6144 + n * 512) = \
            (dd & 1) ? hi2(ac2[2 * ph + (dd >> 1)][n]) : lo2(ac2[2 * ph + (dd >> 1)][n]);
#define RED_FIN(ph) { v2f r[4]; \
      _Pragma("unroll") for (int n = 0; n < 4; ++n) { \
        const v4f own = wave < 2 ? ac2[2 * ph][n] : ac2[2 * ph + 1][n]; r[n] = (wave & 1) ? hi2(own) : lo2(own); } \
      _Pragma("unroll") for (int w = 0; w < 3; ++w) \
        _Pragma("unroll") for (int n = 0; n < 4; ++n) r[n] += *(const LAS v2f*)(L + rr_ + w * 2048 + ph * 24576 + n * 512); \
      v4u o; \
      _Pragma("unroll") for (int q = 0; q < 2; ++q) { \
        o[2 * q] = pk2(fma2(bun(gv[q].x), v2f{r[0][q], r[1][q]}, bun(xv[ph][2 * q]))); \
        o[2 * q + 1] = pk2(fma2(bun(gv[q].z), v2f{r[2][q], r[3][q]}, bun(xv[ph][2 * q + 1]))); } \
      st4c(mkrs(out, S * D * 2), oo[ph], o); }
#pragma unroll
    for (int e = 4 * (14 - B_AT2); e < 4 * (14 - B_AT2) + 4; ++e) A_LD(e);
    sb();
    RED_WR(0)
    int oo[2];
#pragma unroll
    for (int ph = 0; ph < 2; ++ph) { oo[ph] = (tok_row(blk + 2 * ph, c16) * D + ct * 64 + g * 16 + (wave & 1) * 8) * 2; pin(oo[ph]); }
    _Pragma("nounroll") for (int s = 0; s < SPIN; ++s) {
      if (wall(gv[0].y == tag && gv[0].w == tag && gv[1].y == tag && gv[1].w == tag)) break;
      const int gf = fresh(go);
      gv[0] = ld4c(ws, gf); gv[1] = ld4c(ws, gf + 16);
    }
    STAMP(18);
    sb();
#pragma unroll
    for (int ks = 0; ks < 14; ++ks) {
      if (__builtin_expect(BAD2(a[ks][0], a[ks][1]), 0)) { RETRY2(a[ks][0], a[ks][1], ab + 2 * 57344 + ks * 1024, ab + 3 * 57344 + ks * 1024) }
#pragma unroll
      for (int tt = 0; tt < 2; ++tt)
#pragma unroll
        for (int n = 0; n < 4; ++n) ac2[2 + tt][n] = mfma(wdn[ks][n], a[ks][tt], ac2[2 + tt][n]);
#pragma unroll
      for (int e = 4 * (15 - B_AT2) + 3 * ks; e < 4 * (15 - B_AT2) + 3 * ks + 3; ++e)
        if (e < 28) A_LD(e);
#pragma unroll
      for (int u = 0; u < 4; ++u) {
        __builtin_amdgcn_sched_group_barrier(0x8, 2, 0);
        __builtin_amdgcn_sched_group_barrier(0x20, 1, 0);
      }
      sb();
    }
    STAMP(10);
    RED_WR(1)
    bar();
    RED_FIN(0)
    RED_FIN(1)
    if (blockIdx.x == 0 && wave == 0) {
      _Pragma("nounroll") for (int s = 0; s < SPIN; ++s) {
        const v4u v = ld4c(ws, fresh(WS_MOD + (70 + (lane < 14 ? lane : 0)) * 128));
        if (wall(v.y == tag)) break;
      }
      if (lane == 0) __builtin_amdgcn_raw_buffer_store_b32(epoch + 1u, ws, WS_EPOCH, 0, SC1);
    }
  }
  STAMP(11);
  DUMP();
}

extern "C" void kernel_launch(void* const* d_in, const int* in_sizes, int n_in, void* d_out, int out_size, void* d_ws,
                              size_t ws_size, hipStream_t stream) {
  fused<<<256, 256, 0, stream>>>((const u16*)d_in[0], (const u16*)d_in[1], (const u16*)d_in[2], (const u16*)d_in[4],
                                 (const u16*)d_in[5], (const u16*)d_in[6], (const u16*)d_in[7], (const u16*)d_in[8],
                                 (u16*)d_out, (char*)d_ws);
}
```

```cpp
#define PADI 6
#define PADB 130
#define PADT 2000
#include <hip/hip_runtime.h>

typedef unsigned short u16;
typedef unsigned int u32;
typedef unsigned long long u64;
typedef u32 v4u __attribute__((ext_vector_type(4)));
typedef u32 v2u __attribute__((ext_vector_type(2)));
typedef float v4f __attribute__((ext_vector_type(4)));
typedef float v2f __attribute__((ext_vector_type(2)));
typedef short v8s __attribute__((ext_vector_type(8)));
typedef short v4s __attribute__((ext_vector_type(4)));
typedef __bf16 v8b __attribute__((ext_vector_type(8)));
typedef __bf16 v2b __attribute__((ext_vector_type(2)));
#define LAS __attribute__((address_space(3)))
#define GAS __attribute__((address_space(1)))
#define DEV __device__ __forceinline__

#define C_W1_EARLY 0
#define CLR_DELAY 0
#define W_DELAY 0
#define CLR2_DELAY 0
#define PF_POS 2
#define SL1 1
#define SL2 2
#define SL4 4
#define X_AFTER 8
#define PF_AUX 2
#define W_LATE 1
#define W1_PF 1
#define DMA_MOD ""
#define LEAD 6
#define A_V 22
#define A_PER 4
#define WDV(f) ((f) - WD_MID + 2 < 14 ? (f) - WD_MID + 2 : ((f) - WD_MID - 12) / 2 + 16)
#define B_AT2 12
#define P_AT 7
#define WD_PER 3
#define A_FIRST 10
#define B_FIRST 10
#define A_AT 9
#define F_AT 4
#define CODE_BYTES 45056
#define WD_EARLY 16
#define WD_MID 24
#define B_AT 9
constexpr int S = 2048, D = 448, DF = 225, F = 1792;

struct alignas(16) Chain { u16 v[2048]; };
struct Cos448 { u16 v[448]; };
constexpr double c_cos_t(double a) {
  double a2 = a * a, t = 1.0, s = 1.0;
  for (int i = 1; i <= 10; ++i) { t = -t * a2 / ((2 * i - 1) * (2 * i)); s += t; }
  return s;
}
constexpr double c_sin_t(double a) {
  double a2 = a * a, t = a, s = a;
  for (int i = 1; i <= 10; ++i) { t = -t * a2 / ((2 * i) * (2 * i + 1)); s += t; }
  return s;
}
constexpr double c_cos448(int m) {
  if (m > 224) m = 448 - m;
  double sg = 1.0;
  if (m > 112) { m = 224 - m; sg = -1.0; }
  const double w = 6.283185307179586476925286766559 / 448.0;
  return m == 112 ? 0.0 : (m <= 56 ? sg * c_cos_t(w * m) : sg * c_sin_t(w * (112 - m)));
}
constexpr u16 c_bf16(double d) {
  const u32 u = __builtin_bit_cast(u32, (float)d);
  return (u16)((u + 0x7FFFu + ((u >> 16) & 1u)) >> 16);
}
constexpr Cos448 mk_cos() { Cos448 c{}; for (int m = 0; m < 448; ++m) c.v[m] = c_bf16(c_cos448(m)); return c; }
constexpr Cos448 CTAB = mk_cos();
constexpr u16 bcos(int m) { return CTAB.v[m % 448]; }
constexpr u16 bsin(int m) { return CTAB.v[(m % 448 + 336) % 448]; }
constexpr u16 bneg(u16 b) { return b ? (u16)(b ^ 0x8000u) : b; }
constexpr u16 bscale(u16 b, int e) { return b ? (u16)(b + e * 0x80) : b; }

constexpr Chain mk_p1(int c) {
  const int tile = c >> 2, par = c & 1, sn = (c >> 1) & 1;
  Chain ch{};
  for (int s = 0; s < 4; ++s)
    for (int l = 0; l < 64; ++l) {
      const int kp = 16 * tile + (l & 15), k = 2 * kp + par;
      if (k > 224) continue;
      for (int j = 0; j < 8; ++j) {
        const int dl = 32 * s + 8 * (l >> 4) + j;
        if (dl > 112) continue;
        u16 b = sn ? bneg(bsin(k * dl)) : bcos(k * dl);
        if (dl == 0 || dl == 112) b = bscale(b, -1);
        ch.v[(s * 64 + l) * 8 + j] = b;
      }
    }
  return ch;
}
constexpr Chain mk_p3(int c) {
  const int tile = c >> 1, sn = c & 1;
  Chain ch{};
  for (int s = 0; s < 4; ++s)
    for (int l = 0; l < 64; ++l) {
      int dl = 16 * tile + (l & 15);
      if (c == 1 && dl == 0) dl = 112;
      for (int j = 0; j < 8; ++j) {
        const int k = 2 * (32 * s + 8 * (l >> 4) + j) + 1;
        if (k < 224) ch.v[(s * 64 + l) * 8 + j] = sn ? bsin(k * dl) : bcos(k * dl);
      }
    }
  return ch;
}
constexpr Chain mk_p2(int c) {
  Chain ch{};
  for (int f = 0; f < 4; ++f)
    for (int l = 0; l < 64; ++l)
      for (int j = 0; j < 8; ++j) {
        const int comp = c < 8 ? (c & 1) : (f >> 1), s = c < 8 ? f : (f & 1);
        const int o = 16 * (c < 8 ? (c >> 1) : (c - 8)) + (l & 15), i = 16 * s + 4 * (l >> 4) + (j >> 1);
        const int m = (c < 8 ? 7 : 14) * ((i * o) % 64);
        u16 b = 0;
        if (comp == 0) b = (j & 1) ? bsin(m) : bcos(m);
        else b = (j & 1) ? bcos(m) : bneg(bsin(m));
        ch.v[(f * 64 + l) * 8 + j] = b;
      }
  return ch;
}
template <int I> constexpr Chain P1C = mk_p1(I);
template <int I> constexpr Chain P3C = mk_p3(I);
template <int I> constexpr Chain P2C = mk_p2(I);
#define L10(T, b) T<b>, T<b + 1>, T<b + 2>, T<b + 3>, T<b + 4>, T<b + 5>, T<b + 6>, T<b + 7>, T<b + 8>, T<b + 9>
#define L29(T) L10(T, 0), L10(T, 10), T<20>, T<21>, T<22>, T<23>, T<24>, T<25>, T<26>, T<27>, T<28>
constexpr int TAB_P1 = 0, TAB_P3 = 29, TAB_P2 = 43, TAB_N = 53;
static __device__ const Chain TAB[TAB_N] = {L29(P1C), L10(P3C, 0), P3C<10>, P3C<11>, P3C<12>, P3C<13>, L10(P2C, 0)};

constexpr int WS_EPOCH = 0;
constexpr int WS_RDY = 4096;
constexpr int WS_HRDY = 4096 + 8192;
constexpr int WS_DRDY = 4096 + 16384;
constexpr int WS_XRDY = 131072 + 65536;
constexpr int WS_ARDY = 131072;
constexpr int WS_CRDY = 131072 + 32768;
constexpr int WS_MOD = 32768, MOD_REP = 12288;
constexpr int T1_BLK = 226 * 64;
constexpr int WS_T1 = 1 << 18;
constexpr int WS_WT = WS_T1 + 128 * T1_BLK;
constexpr int WS_H2 = WS_WT + 113 * 16384;
constexpr int WS_X2 = WS_H2 + 128 * 14336;
constexpr int WS_WD = WS_X2 + 128 * 14336;
constexpr int WS_ACT = WS_WD + 7 * 56 * 4096;
constexpr int WS_END = WS_ACT + 128 * 57344;
constexpr int WS_DBG = 32 << 20;

constexpr int L_W1 = 0;
constexpr int L_B = 114688;
constexpr int XP = 912;
constexpr int L_XS = L_B;
constexpr int L_ST = L_XS + 16 * XP;
constexpr int L_M1 = L_ST + 128;
constexpr int L_AF = L_M1 + 4 * 1792;
constexpr int L_M2 = L_AF + 16384;
constexpr int L_EX = L_M2 + 3 * 1792;
constexpr int L_REND = L_EX + 512;
constexpr int ZPL = 8192;
constexpr int L_CI = L_B;
constexpr int L_CX = L_CI + 2 * 8256;
constexpr int L_RED = L_CX + 16384;
constexpr int L_DBG = 163840 - 1024;
constexpr int L_SZ = 163840;
static_assert(L_REND <= L_DBG && L_RED + 2048 <= L_DBG && 2 * ZPL <= 16384, "lds");

constexpr u32 SENT = 0xFFFFFFFFu;
constexpr int SPIN = 1 << 14;
constexpr int SC1 = 16;
constexpr int OOB = 0x7FFF0000;

typedef __amdgpu_buffer_rsrc_t rsrc_t;
DEV rsrc_t mkrs(const void* p, int bytes) { return __builtin_amdgcn_make_buffer_rsrc((void*)p, 0, bytes, 0x00020000); }
DEV v4u ld4(rsrc_t r, int off, int so = 0) { return __builtin_amdgcn_raw_buffer_load_b128(r, off, so, 0); }
DEV v2u ld2(rsrc_t r, int off, int so = 0) { return __builtin_amdgcn_raw_buffer_load_b64(r, off, so, 0); }
DEV v4u ld4c(rsrc_t r, int off, int so = 0) { return __builtin_amdgcn_raw_buffer_load_b128(r, off, so, SC1); }
DEV v2u ld2c(rsrc_t r, int off, int so = 0) { return __builtin_amdgcn_raw_buffer_load_b64(r, off, so, SC1); }
DEV u32 ld1c(rsrc_t r, int off, int so = 0) { return __builtin_amdgcn_raw_buffer_load_b32(r, off, so, SC1); }
DEV void st4c(rsrc_t r, int off, v4u v) { __builtin_amdgcn_raw_buffer_store_b128(v, r, off, 0, SC1); }
DEV void st2c(rsrc_t r, int off, v2u v) { __builtin_amdgcn_raw_buffer_store_b64(v, r, off, 0, SC1); }
DEV float blo(u32 v) { return __builtin_bit_cast(float, v << 16); }
DEV float bhi(u32 v) { return __builtin_bit_cast(float, v & 0xFFFF0000u); }
DEV v2f bun(u32 v) { return v2f{blo(v), bhi(v)}; }
DEV u32 pk(float lo, float hi) { return __builtin_bit_cast(u32, __builtin_convertvector(v2f{lo, hi}, v2b)); }
DEV u32 pk2(v2f v) { return __builtin_bit_cast(u32, __builtin_convertvector(v, v2b)); }
DEV float rb(float f) { return blo(pk(f, 0.f)); }
DEV v2f lo2(v4f v) { return __builtin_shufflevector(v, v, 0, 1); }
DEV v2f hi2(v4f v) { return __builtin_shufflevector(v, v, 2, 3); }
DEV v2f fma2(v2f a, v2f b, v2f c) { return __builtin_elementwise_fma(a, b, c); }
DEV v4f fma4(v4f a, v4f b, v4f c) { return __builtin_elementwise_fma(a, b, c); }
DEV v4f splat(float f) { return v4f{f, f, f, f}; }
DEV v4f mfma(v4u a, v4u b, v4f c) {
  return __builtin_amdgcn_mfma_f32_16x16x32_bf16(__builtin_bit_cast(v8b, a), __builtin_bit_cast(v8b, b), c, 0, 0, 0);
}
DEV void mfa(v4f& c, v4u a, v4u b) { asm volatile("s_nop 1\n\tv_mfma_f32_16x16x32_bf16 %0, %1, %2, %0" : "+v"(c) : "v"(a), "v"(b)); }
DEV void mf0(v4f& c, v4u a, v4u b) { asm volatile("s_nop 1\n\tv_mfma_f32_16x16x32_bf16 %0, %1, %2, 0" : "=&v"(c) : "v"(a), "v"(b)); }
template <int N> DEV void mfence(v4f& a, v4f& b) { asm volatile("s_nop %2" : "+v"(a), "+v"(b) : "i"(N)); }
template <int N> DEV void mfence(v4f& a, v4f& b, v4f& c, v4f& d) { asm volatile("s_nop %4" : "+v"(a), "+v"(b), "+v"(c), "+v"(d) : "i"(N)); }
#define MF "v_mfma_f32_16x16x32_bf16 "
DEV void mf8(v4f& d0, v4f& d1, const v4u (&a0)[4], const v4u (&b0)[4], const v4u (&a1)[4], const v4u (&b1)[4]) {
  asm volatile("s_nop 1\n\t" MF "%0, %2, %6, 0\n\t" MF "%1, %10, %14, 0\n\t" MF "%0, %3, %7, %0\n\t" MF "%1, %11, %15, %1\n\t"
               MF "%0, %4, %8, %0\n\t" MF "%1, %12, %16, %1\n\t" MF "%0, %5, %9, %0\n\t" MF "%1, %13, %17, %1"
               : "=&v"(d0), "=&v"(d1)
               : "v"(a0[0]), "v"(a0[1]), "v"(a0[2]), "v"(a0[3]), "v"(b0[0]), "v"(b0[1]), "v"(b0[2]), "v"(b0[3]),
                 "v"(a1[0]), "v"(a1[1]), "v"(a1[2]), "v"(a1[3]), "v"(b1[0]), "v"(b1[1]), "v"(b1[2]), "v"(b1[3]));
}
#define H4_M(XA, D, X, K, C) MF D ", " XA(X, K) ", " C "\n\t"
#define H4_XA(X, K) X ", " K
#define H4_XB(X, K) K ", " X
#define H4_DEF(NAME, XA, C0, C1, CONS, N0, N1, PA, PB, PC, PD) \
DEV void NAME(v4f& d0, v4f& d1, v4u x0, v4u x1, v4u k00, v4u k01, v4u k10, v4u k11, \
              u32& o0, u32& o1, float p0, float q0, float c0, float s0, float p1, float q1, float c1, float s1) { \
  float t0, t1, t2, t3; \
  asm volatile(H4_M(XA, "%0", "%8", "%10", C0) "v_mul_f32 %4, %14, %16\n\tv_mul_f32 %5, %15, %16\n\t" \
               H4_M(XA, "%1", "%8", "%12", C1) "v_mul_f32 %6, %18, %20\n\tv_mul_f32 %7, %19, %20\n\t" \
               H4_M(XA, "%0", "%9", "%11", "%0") "v_fma_f32 %4, " N0 "%15, %17, %4\n\tv_fma_f32 %5, " N1 "%14, %17, %5\n\t" \
               H4_M(XA, "%1", "%9", "%13", "%1") "v_fma_f32 %6, " N0 "%19, %21, %6\n\tv_fma_f32 %7, " N1 "%18, %21, %7\n\t" \
               "v_cvt_pk_bf16_f32 %2, " PA ", " PB "\n\tv_cvt_pk_bf16_f32 %3, " PC ", " PD \
               : CONS(d0), CONS(d1), "=&v"(o0), "=&v"(o1), "=&v"(t0), "=&v"(t1), "=&v"(t2), "=&v"(t3) \
               : "v"(x0), "v"(x1), "v"(k00), "v"(k01), "v"(k10), "v"(k11), \
                 "v"(p0), "v"(q0), "v"(c0), "v"(s0), "v"(p1), "v"(q1), "v"(c1), "v"(s1)); \
}
H4_DEF(h4_a0m, H4_XA, "0", "0", "=&v", "", "-", "%4", "%5", "%6", "%7")
H4_DEF(h4_a1m, H4_XA, "%0", "%1", "+v", "", "-", "%4", "%5", "%6", "%7")
H4_DEF(h4_b0m, H4_XB, "0", "0", "=&v", "", "-", "%4", "%5", "%6", "%7")
H4_DEF(h4_b0p, H4_XB, "0", "0", "=&v", "-", "", "%5", "%4", "%7", "%6")
H4_DEF(h4_a0p, H4_XA, "0", "0", "=&v", "-", "", "%5", "%4", "%7", "%6")
#define M4_DEF(NAME, XA, C0, C1, CONS) \
DEV void NAME(v4f& d0, v4f& d1, v4u x0, v4u x1, v4u k00, v4u k01, v4u k10, v4u k11) { \
  asm volatile(H4_M(XA, "%0", "%2", "%4", C0) H4_M(XA, "%1", "%2", "%6", C1) H4_M(XA, "%0", "%3", "%5", "%0") MF "%1, " XA("%3", "%7") ", %1" \
               : CONS(d0), CONS(d1) : "v"(x0), "v"(x1), "v"(k00), "v"(k01), "v"(k10), "v"(k11)); \
}
M4_DEF(m4_a0, H4_XA, "0", "0", "=&v") M4_DEF(m4_a1, H4_XA, "%0", "%1", "+v")
M4_DEF(m4_b0, H4_XB, "0", "0", "=&v") M4_DEF(m4_b1, H4_XB, "%0", "%1", "+v")
DEV void nop2() { asm volatile("s_nop 1"); }
template <class T> DEV void pin(T& v) { asm volatile("" : "+v"(v)); }
DEV int fresh(int off) { asm volatile("" : "+v"(off) :: "memory"); return off; }
DEV bool wall(bool ok) { return __builtin_amdgcn_ballot_w64(!ok) == 0; }
DEV bool wany(bool b) { return __builtin_amdgcn_ballot_w64(b) != 0; }
DEV void sb() { __builtin_amdgcn_sched_barrier(0); }
DEV u32 max4(v4u v) { return max(max(v.x, v.y), max(v.z, v.w)); }
DEV void drain() { asm volatile("s_waitcnt vmcnt(0)" ::: "memory"); }
DEV void cfence() { asm volatile("" ::: "memory"); }
DEV void bar() { asm volatile("s_waitcnt lgkmcnt(0)\n\ts_barrier" ::: "memory"); }
template <int CTRL> DEV float dpp(float v) {
  return __builtin_bit_cast(float, __builtin_amdgcn_update_dpp(0, __builtin_bit_cast(int, v), CTRL, 0xF, 0xF, true));
}
DEV float sum16(float v) {
  v += dpp<0x128>(v); v += dpp<0x124>(v); v += dpp<0x122>(v); v += dpp<0x121>(v); return v;
}
DEV void sumg2(float& a, float& b) {
  float c = a, d = b;
  asm volatile("s_nop 1\n\tv_permlane16_swap_b32 %0, %1\n\tv_permlane16_swap_b32 %2, %3" : "+v"(a), "+v"(c), "+v"(b), "+v"(d));
  a += c; b += d; c = a; d = b;
  asm volatile("s_nop 1\n\tv_permlane32_swap_b32 %0, %1\n\tv_permlane32_swap_b32 %2, %3" : "+v"(a), "+v"(c), "+v"(b), "+v"(d));
  a += c; b += d;
}
DEV v4u lds4(const char LAS* L, int off) { return *(const LAS v4u*)(L + off); }
DEV v4f lds4f(const char LAS* L, int off) { return *(const LAS v4f*)(L + off); }
DEV v2u ldtr(const char LAS* L, int off) { return __builtin_bit_cast(v2u, __builtin_amdgcn_ds_read_tr16_b64_v4i16((LAS v4s*)(L + off))); }
DEV v4u ldtr2(const char LAS* L, int o0, int o1) { const v2u a = ldtr(L, o0), b = ldtr(L, o1); return v4u{a.x, a.y, b.x, b.y}; }
DEV void dma16(rsrc_t r, int voff, int soff, u32 lds_dst) {
  u32 keep;
  asm volatile("s_mov_b32 %0, m0\n\ts_mov_b32 m0, %1\n\ts_nop 0\n\tbuffer_load_dwordx4 %2, %3, %4 offen" DMA_MOD " lds\n\ts_mov_b32 m0, %0"
               : "=&s"(keep) : "s"(lds_dst), "v"(voff), "s"(r), "s"(soff) : "memory");
}
DEV void light_poll(rsrc_t ws, int off) {
  _Pragma("nounroll") for (int s = 0; s < SPIN; ++s) {
    if (wall(ld1c(ws, fresh(off)) != SENT)) break;
    __builtin_amdgcn_s_sleep(SL2);
  }
}
DEV int tok_row(int blk, int t) {
  const int n1 = 16 * (blk >> 5) + 4 * ((blk >> 2) & 3) + (t & 3), n2 = 16 * ((blk >> 4) & 1) + 4 * (blk & 3) + (t >> 2);
  return n2 + 32 * n1;
}
template <bool SWAP> DEV v4u cmul_pack(v4f a, v4f b, v4f c, v4f s) {
  const v4f re = fma4(b, s, a * c), im = fma4(b, c, -(a * s));
  return SWAP ? v4u{pk(im.x, re.x), pk(im.y, re.y), pk(im.z, re.z), pk(im.w, re.w)}
              : v4u{pk(re.x, im.x), pk(re.y, im.y), pk(re.z, im.z), pk(re.w, im.w)};
}

template <bool PLUS> DEV void cmul2(u32& o0, u32& o1, v2f p, v2f q, v2f c, v2f s) {
  const v2f re = fma2(PLUS ? -q : q, s, p * c), im = fma2(PLUS ? p : -p, s, q * c);
  o0 = PLUS ? pk(im.x, re.x) : pk(re.x, im.x); o1 = PLUS ? pk(im.y, re.y) : pk(re.y, im.y);
}

#define STAMP(i) if ((i) == PADI) padt = __builtin_amdgcn_s_memrealtime();
#define CSTAMP(i)
#define SLOWCNT
#define DUMP()

DEV void dma_w1(const u16* wg, const u16* wu, int slice, int wave, int lane, u32 lbase) {
  const rsrc_t rg = mkrs(wg, D * F * 2), ru = mkrs(wu, D * F * 2);
  const int r = lane >> 3, ch = ((((lane & 7) >> 1) ^ (lane >> 4)) & 3) * 32 + (lane & 1) * 16 + slice * 128;
  const int vp = r * (F * 2) + ch, vn = (7 - r) * (F * 2) + ch;
#pragma unroll
  for (int n = 0; n < 4; ++n) {
    const int ks = wave + 4 * n;
    if (n == 3 && wave >= 2) break;
    const int t16 = (ks >> 1) * 16, a = ks & 1;
#pragma unroll
    for (int q = 0; q < 4; ++q) {
      const int dl0 = t16 + 8 * (q & 1);
      const bool pos = (q >> 1) == a;
      const int d0 = q < 2 ? (a ? 224 - dl0 - 7 : dl0) : (a ? 224 + dl0 : 448 - dl0 - 7);
      int vo = pos ? vp : vn, so = d0 * (F * 2);
      if (n == 0 && q == 2) {
        vo += so; so = 0;
        if (wave < 2 && lane < 8) vo = (wave ? 336 : 112) * (F * 2) + ch;
      }
      dma16(rg, vo, so, lbase + L_W1 + ks * 8192 + q * 1024);
      dma16(ru, vo, so, lbase + L_W1 + ks * 8192 + 4096 + q * 1024);
    }
  }
}
struct ModLoad { u32 cv[7]; v4u mw[7]; };
DEV void mod_load(ModLoad& m, const u16* cin, const u16* wm, int q, int tid) {
  const rsrc_t cr = mkrs(cin, q >= 0 ? D * 2 : 0), mr = mkrs(wm, q >= 0 ? D * 6 * D * 2 : 0);
#pragma unroll
  for (int i = 0; i < 7; ++i) m.cv[i] = __builtin_amdgcn_raw_buffer_load_b16(cr, (tid >> 2) * 2 + i * 128, 0, 0);
#pragma unroll
  for (int i = 0; i < 7; ++i) m.mw[i] = __builtin_amdgcn_raw_buffer_load_b128(mr, ((tid >> 2) * (6 * D) + q * 32 + (tid & 3) * 8) * 2, i * 64 * 6 * D * 2, PF_AUX);
}
DEV void mod_part(ModLoad& m, char LAS* L, int tid) {
  float sl[7];
#pragma unroll
  for (int i = 0; i < 7; ++i) { const float cc = blo(m.cv[i]); sl[i] = rb(cc * __builtin_amdgcn_rcpf(1.f + __expf(-cc))); pin(sl[i]); }
  v2f a[4];
#pragma unroll
  for (int e = 0; e < 4; ++e) a[e] = v2f{0.f, 0.f};
#pragma unroll
  for (int i = 0; i < 7; ++i)
#pragma unroll
    for (int e = 0; e < 4; ++e) a[e] = fma2(bun(m.mw[i][e]), v2f{sl[i], sl[i]}, a[e]);
#pragma unroll
  for (int e = 0; e < 4; ++e) {
    a[e].x += dpp<0x128>(a[e].x); a[e].y += dpp<0x128>(a[e].y);
    a[e].x += dpp<0x124>(a[e].x); a[e].y += dpp<0x124>(a[e].y);
  }
  if ((tid & 15) < 4) {
    *(LAS v4f*)(L + L_RED + ((tid >> 4) * 32 + (tid & 15) * 8) * 4) = v4f{a[0].x, a[0].y, a[1].x, a[1].y};
    *(LAS v4f*)(L + L_RED + ((tid >> 4) * 32 + (tid & 15) * 8 + 4) * 4) = v4f{a[2].x, a[2].y, a[3].x, a[3].y};
  }
}
DEV void mod_publish(char LAS* L, rsrc_t ws, int q, int lane, u32 tag) {
  v4f s = {0.f, 0.f, 0.f, 0.f};
#pragma unroll
  for (int r = 0; r < 16; ++r) s += lds4f(L, L_RED + (r * 32 + (lane & 7) * 4) * 4);
  const float one = (q / 14 == 1 || q / 14 == 4) ? 1.f : 0.f;
  st4c(ws, WS_MOD + (lane >> 3) * MOD_REP + q * 128 + (lane & 7) * 16,
       v4u{pk(rb(s.x) + one, rb(s.y) + one), tag, pk(rb(s.z) + one, rb(s.w) + one), tag});
}
DEV u32 ld1(rsrc_t r, int off) { return __builtin_amdgcn_raw_buffer_load_b32(r, off, 0, 0); }
DEV u32 mktag(u32 epoch) { return epoch * 0x9E3779B1u + 0x7F4A7C15u; }

DEV void x2_read(const char LAS* L, v4u (&r)[4], int tid) {
#pragma unroll
  for (int m = 0; m < 4; ++m) { const int ch = min(tid + 256 * m, 895), tk = ch / 56; r[m] = lds4(L, L_XS + tk * XP + (ch - tk * 56) * 16); }
}
DEV void x2_store(rsrc_t ws, const v4u (&r)[4], int blk, int tid) {
#pragma unroll
  for (int m = 0; m < 4; ++m) st4c(ws, tid + 256 * m < 896 ? WS_X2 + blk * 14336 + (tid + 256 * m) * 16 : OOB, r[m]);
}
DEV bool flags_ok(v4u rv, u32 tag) { return rv.x == tag && rv.y == ~tag && rv.z == tag && rv.w == ~tag; }
DEV void wait_flags(rsrc_t ws, int off, u32 tag) {
  _Pragma("nounroll") for (int s = 0; s < SPIN; ++s) {
    const v2u rv = ld2c(ws, fresh(off));
    if (wall(rv.x == tag && rv.y == ~tag)) break;
    __builtin_amdgcn_s_sleep(SL1);
  }
}

__global__ void __launch_bounds__(256, 1)
fused(const u16* __restrict__ xin, const u16* __restrict__ wr, const u16* __restrict__ wi, const u16* __restrict__ wg,
      const u16* __restrict__ wu, const u16* __restrict__ wd, const u16* __restrict__ cin, const u16* __restrict__ wm,
      u16* __restrict__ out, char* wsp) {
  __shared__ __attribute__((aligned(16))) char smem[L_SZ];
  char LAS* L = (char LAS*)smem;
  const u32 lbase = (u32)(size_t)L;
  const int tid = threadIdx.x, lane = tid & 63, wave = __builtin_amdgcn_readfirstlane(tid >> 6);
  const int g = lane >> 4, c16 = lane & 15, l16 = lane * 16;
  const int grp = blockIdx.x & 7, j = blockIdx.x >> 3;
  u64 pc, padt = 0;
  asm volatile("s_getpc_b64 %0" : "=s"(pc));
  STAMP(0); CSTAMP(31);
  asm volatile("" :: "s"(xin), "s"(wr), "s"(wi), "s"(wg), "s"(wu), "s"(wd), "s"(cin), "s"(wm), "s"(out), "s"(wsp));
  const rsrc_t ws = mkrs(wsp, WS_END);
  const rsrc_t tab = mkrs(TAB, TAB_N * 4096);
  const rsrc_t code = mkrs((const void*)(pc & ~127ull), CODE_BYTES);
  const bool isR = j >= 14 && j < 30;
  const bool isG = j < 28;
  const int gk = j / 7, ct = j - gk * 7;
  const int tile = 4 * (grp >> 2) + gk, slice = 7 * (grp & 3) + ct;
  const int wdb = WS_WD + (ct * 56 + wave * 14) * 4096;
  const v4u sv = {SENT, SENT, SENT, SENT};
  int lb = L_B + l16;
  pin(lb);
  const char LAS* LB = L + lb - L_B;
  const int pfo = ((j * 4 + wave) * 14 + lane) * 128, pfc = ((j * 4 + wave) * 4 + lane) * 128;
  u32 pfa = ld1(tab, lane < 14 ? pfo : OOB), pfb = ld1(code, lane < 4 ? pfc : OOB);
  v4u x2r[4];
  v4u wdn[14][4];
  u32 epoch, tag;

  if (isR) {
    const int blk = 16 * grp + (j - 14);
    epoch = ld1c(ws, WS_EPOCH);
    const int t0 = wave == 0 ? 0 : 2 * wave - 1;
    v4u cf[8][4];
#define CF_LOAD(q) { const int chn = TAB_P1 + ((wave == 0 && q >= 4) ? 28 : 4 * t0 + q), vo = (q < 5 || wave != 0) ? l16 : OOB; \
      _Pragma("unroll") for (int s = 0; s < 4; ++s) cf[q][s] = ld4(tab, vo + s * 1024, chn * 4096); sb(); }
#pragma unroll
    for (int q = 0; q < X_AFTER; ++q) CF_LOAD(q)
    v2u xv[7];
    {
      const rsrc_t xr = mkrs(xin, S * D * 2);
      const int off = tok_row(blk, 4 * wave + g) * (D * 2) + c16 * 8;
#pragma unroll
      for (int m = 0; m < 7; ++m) xv[m] = ld2(xr, off + m * 128);
    }
    typedef u32 v3u __attribute__((ext_vector_type(3)));
    v2u fa_[2], fb_[2]; v3u fc_[2], fd_[2];
    {
      const rsrc_t rr = mkrs(wr, S * DF * 2), ri = mkrs(wi, S * DF * 2);
      const int fo_ = 16 * wave + c16 < 57 ? 2 * g * 450 + (16 * wave + c16) * 8 : OOB;
#pragma unroll
      for (int hh = 0; hh < 2; ++hh) {
        const int so = blk * 7200 + hh * 3600;
        fa_[hh] = ld2(rr, fo_, so); fb_[hh] = ld2(ri, fo_, so);
        fc_[hh] = __builtin_amdgcn_raw_buffer_load_b96(rr, fo_ + 448, so, 0); fd_[hh] = __builtin_amdgcn_raw_buffer_load_b96(ri, fo_ + 448, so, 0);
      }
    }
    sb();
#pragma unroll
    for (int q = X_AFTER; q < 8; ++q) CF_LOAD(q)
    STAMP(23);
    {
      v2f f[14], s2 = {0.f, 0.f};
#pragma unroll
      for (int m = 0; m < 7; ++m) {
        f[2 * m] = bun(xv[m].x); f[2 * m + 1] = bun(xv[m].y);
        s2 += f[2 * m] + f[2 * m + 1];
        *(LAS v2u*)(L + L_XS + (4 * wave + g) * XP + c16 * 8 + m * 128) = xv[m];
      }
      STAMP(21);
      sb();
      {
        const int q = 16 * wave + c16, o = WS_WT + q * 16384 + blk * 128 + g * 16;
#pragma unroll
        for (int hh = 0; hh < 2; ++hh) {
          const v2u a = fa_[hh], b = fb_[hh]; const v3u c = fc_[hh], d = fd_[hh];
          st4c(ws, q < 57 ? o + hh * 64 : OOB, v4u{__builtin_amdgcn_perm(b.x, a.x, 0x05040100u), __builtin_amdgcn_perm(b.y, a.y, 0x05040100u),
                                                   __builtin_amdgcn_perm(d.x, c.x, 0x07060302u), __builtin_amdgcn_perm(d.y, c.y, 0x07060302u)});
          st4c(ws, q < 56 ? o + hh * 64 + 57 * 16384 : OOB,
               v4u{__builtin_amdgcn_perm(b.x, a.x, 0x07060302u), __builtin_amdgcn_perm(b.y, a.y, 0x07060302u),
                   __builtin_amdgcn_perm(d.y, c.y, 0x05040100u), __builtin_amdgcn_perm(d.z, c.z, 0x05040100u)});
        }
      }
      sb();
      const float mean = sum16(s2.x + s2.y) * (1.f / D);
      v2f q2 = {0.f, 0.f};
#pragma unroll
      for (int m = 0; m < 14; ++m) { const v2f d = f[m] - mean; q2 = fma2(d, d, q2); }
      const float rstd = __builtin_amdgcn_rsqf(sum16(q2.x + q2.y) * (1.f / D) + 1e-5f);
      if (c16 == 0) *(LAS v2f*)(L + L_ST + (4 * wave + g) * 8) = v2f{mean, rstd};
    }
    pin(pfa); pin(pfb);
    epoch = __builtin_amdgcn_readfirstlane(epoch);
    tag = mktag(epoch);
    STAMP(22);
    bar();
    v4f n[4][2];
    const int d0 = 8 * (4 * wave + g);
    const v2f st = *(const LAS v2f*)(L + L_ST + c16 * 8);
    {
      int xr_ = L_XS + c16 * XP + d0 * 2, xb_ = L_XS + c16 * XP + (216 - d0) * 2, x0_ = d0 == 0 ? xr_ : xb_ + 464;
      pin(xr_); pin(xb_); pin(x0_);
      const v4u a = lds4(L + xr_, 0), e = lds4(L + xr_, 448), c = lds4(L + xb_, 0), b = lds4(L + xb_, 448);
      const u32 c0 = *(const LAS u16*)(L + xb_ + 16), b0 = *(const LAS u16*)(L + x0_);
#pragma unroll
      for (int i = 0; i < 8; ++i) {
        n[0][i >> 2][i & 3] = (i & 1) ? bhi(a[i >> 1]) : blo(a[i >> 1]);
        n[3][i >> 2][i & 3] = (i & 1) ? bhi(e[i >> 1]) : blo(e[i >> 1]);
        n[1][i >> 2][i & 3] = i == 0 ? blo(b0) : (i & 1) ? bhi(b[(8 - i) >> 1]) : blo(b[(8 - i) >> 1]);
        n[2][i >> 2][i & 3] = i == 0 ? blo(c0) : (i & 1) ? bhi(c[(8 - i) >> 1]) : blo(c[(8 - i) >> 1]);
      }
      const float nm = -st.x * st.y;
#pragma unroll
      for (int m = 0; m < 4; ++m) {
#pragma unroll
        for (int h = 0; h < 2; ++h) { n[m][h] = fma4(n[m][h], splat(st.y), splat(nm)); pin(n[m][h]); }
      }
    }
    sb();
    STAMP(1);
    int mb = L_M1 + d0 * 4;
    pin(mb);
    v4u xs[4];
    int to[4];
    to[0] = WS_T1 + blk * T1_BLK + (16 * t0 + c16) * 64 + g * 16;
    to[1] = wave != 0 ? to[0] + 1024 : c16 < 2 ? to[0] + 112 * 64 : OOB;
    to[2] = to[0] + 114 * 64;
    to[3] = wave != 0 ? to[0] + 1024 + 114 * 64 : OOB;
#pragma unroll
    for (int q = 0; q < 4; ++q) pin(to[q]);

    {
      const int kind = tid >= 112, d = 4 * (tid - 112 * kind);
      int wb = L_M1 + (kind ? 0 : 2 * 1792) + d * 4, wrv = L_M1 + (kind ? 0 : 2 * 1792) + 1792 + (445 - d) * 4;
      int wr0 = d == 0 ? wrv + 12 - 1792 : wrv + 12;
      const int mo = tid < 224 ? WS_MOD + grp * MOD_REP + tid * 16 : OOB;
      pin(wb); pin(wrv); pin(wr0);
      const int ro = WS_RDY + grp * 1024 + min(tid >> 1, 112) * 8;
      v4u mv;
      _Pragma("nounroll") for (int s = 0; s < SPIN; ++s) {
        mv = ld4c(ws, fresh(mo));
        const v2u rv = ld2c(ws, fresh(ro));
        if (wall((tid >= 224 || (mv.y == tag && mv.w == tag)) && rv.x == tag && rv.y == ~tag)) break;
        __builtin_amdgcn_s_sleep(SL1);
      }
      STAMP(2);
      if (tid < 224) {
        const v4f v = {blo(mv.x), bhi(mv.x), blo(mv.z), bhi(mv.z)};
        *(LAS v4f*)(L + wb) = v;
        *(LAS float*)(L + wr0) = v.x;
        *(LAS float*)(L + wrv + 8) = v.y;
        *(LAS float*)(L + wrv + 4) = v.z;
        *(LAS float*)(L + wrv) = v.w;
      }
    }
    bar();
    {
      v4u fr[4];
#pragma unroll
      for (int hh = 0; hh < 2; ++hh) {
        v4f h[4];
#pragma unroll
        for (int m = 0; m < 4; ++m) {
          const int o = ((m == 1 || m == 2) ? 1792 : 0) + (m >= 2 ? 896 : 0) + hh * 16;
          h[m] = fma4(n[m][hh], lds4f(L + mb, o), lds4f(L + mb, 2 * 1792 + o));
        }
        const v4f p = h[0] + h[1], q = h[2] + h[3], u = h[0] - h[1], v = h[3] - h[2];
        const v4f t[4] = {p + q, p - q, u + v, u - v};
#pragma unroll
        for (int m = 0; m < 4; ++m) { fr[m][2 * hh] = pk2(lo2(t[m])); fr[m][2 * hh + 1] = pk2(hi2(t[m])); }
      }
#pragma unroll
      for (int m = 0; m < 4; ++m) *(LAS v4u*)(LB + wave * 1024 + L_AF + m * 4096) = fr[m];
    }
    bar();
    {
      v4u a[4][4];
#pragma unroll
      for (int p = 0; p < 2; ++p)
#pragma unroll
        for (int s = 0; s < 4; ++s) { a[p][s] = lds4(LB, L_AF + (p * 4 + s) * 1024); a[2 + p][s] = lds4(LB, L_AF + ((2 + p) * 4 + s) * 1024); }
      v4f re[4], im[4];
#pragma unroll
      for (int q = 0; q < 5; ++q) {
        if (q < 4) mf8(re[q], im[q], a[q >> 1], cf[4 * (q & 1) + (q >> 1)], a[2 + (q >> 1)], cf[4 * (q & 1) + 2 + (q >> 1)]);
        if (q > 0) {
          if (q < 4) mfence<0>(re[q - 1], im[q - 1]); else mfence<7>(re[3], im[3]);
          xs[q - 1] = v4u{pk(re[q - 1].x, im[q - 1].x), pk(re[q - 1].y, im[q - 1].y), pk(re[q - 1].z, im[q - 1].z), pk(re[q - 1].w, im[q - 1].w)};
          st4c(ws, to[q - 1], xs[q - 1]);
          sb();
        }
      }
    }
    STAMP(3);
    sb();
    sb();
    if (isG) dma_w1(wg, wu, slice, wave, lane, lbase);
#pragma unroll
    for (int q = 0; q < 4; ++q) {
      const int vo = (q < 2 || wave != 0) ? l16 : OOB;
#pragma unroll
      for (int s = 0; s < 4; ++s) cf[2 * q + 1][s] = ld4(tab, vo + s * 1024, (TAB_P3 + 2 * t0 + q) * 4096);
    }
#pragma unroll
    for (int m = 0; m < 4; ++m)
      st4c(ws, tid + 256 * m < 896 ? WS_H2 + blk * 14336 + 4096 * m + tid * 16 : OOB, sv);
    sb();
    STAMP(20);
    v4f xo[2][4];
    int xa = L_XS + c16 * XP + (16 * t0 + 4 * g) * 2, xd = L_XS + c16 * XP + (192 - 16 * t0 - 4 * g) * 2;
    int xs1 = tid < 16 ? L_XS + c16 * XP + 224 : xd + 512, xs3 = tid < 16 ? L_XS + c16 * XP + 672 : xa + 448;
    pin(xa); pin(xd); pin(xs1); pin(xs3);
#define XOFF(t, m, r) ((t) == 0 && (r) == 0 && (m) == 1 ? xs1 : (t) == 0 && (r) == 0 && (m) == 3 ? xs3 : \
    (m) == 0 ? xa + 32 * (t) + 2 * (r) : (m) == 3 ? xa + 448 + 32 * (t) + 2 * (r) : (m) == 1 ? xd + 512 - 32 * (t) - 2 * (r) : xd + 64 - 32 * (t) - 2 * (r))
    {
      v2u w[2][4]; u32 e[2][2], e3;
#pragma unroll
      for (int t = 0; t < 2; ++t) {
        w[t][0] = *(const LAS v2u*)(L + xa + 32 * t); w[t][3] = *(const LAS v2u*)(L + xa + 448 + 32 * t);
        w[t][1] = *(const LAS v2u*)(L + xd + 504 - 32 * t); w[t][2] = *(const LAS v2u*)(L + xd + 56 - 32 * t);
        e[t][0] = *(const LAS u16*)(L + XOFF(t, 1, 0)); e[t][1] = *(const LAS u16*)(L + XOFF(t, 2, 0));
      }
      e3 = *(const LAS u16*)(L + xs3);
      sb();
#pragma unroll
      for (int t = 0; t < 2; ++t) {
        xo[t][0] = v4f{blo(w[t][0].x), bhi(w[t][0].x), blo(w[t][0].y), bhi(w[t][0].y)};
        xo[t][3] = v4f{t == 0 ? blo(e3) : blo(w[t][3].x), bhi(w[t][3].x), blo(w[t][3].y), bhi(w[t][3].y)};
        xo[t][1] = v4f{blo(e[t][0]), bhi(w[t][1].y), blo(w[t][1].y), bhi(w[t][1].x)};
        xo[t][2] = v4f{blo(e[t][1]), bhi(w[t][2].y), blo(w[t][2].y), bhi(w[t][2].x)};
#pragma unroll
        for (int m = 0; m < 4; ++m) { xo[t][m] -= st.x; pin(xo[t][m]); }
      }
    }
    int m0_[2], m1_[2], md_[2];
#pragma unroll
    for (int r = 0; r < 2; ++r) {
      const int q = r == 0 || tid < 80 ? tid + 256 * r : tid;
      const int kind = (q * 586) >> 16, d = 4 * (q - 112 * kind);
      const int reg = (d >= 112) + (d >= 224) + (d >= 336), cc = ((reg + 1) >> 1) * 224, m1 = (0x1320 >> (4 * reg)) & 3;
      const int d1 = abs(d + 1 - cc), d0_ = abs(d - cc), edge = d == 112 * reg && reg > 0;
      m1_[r] = L_M2 + kind * 1792 + (((d1 >> 4) << 6) | (m1 << 4) | (d1 & 15)) * 4;
      m0_[r] = L_M2 + kind * 1792 + (edge ? reg << 4 : ((d0_ >> 4) << 6) | (m1 << 4) | (d0_ & 15)) * 4;
      md_[r] = (reg & 1) ? -4 : 4;
      pin(m0_[r]); pin(m1_[r]); pin(md_[r]);
    }
    {
      v4u mv[2];
      _Pragma("nounroll") for (int s = 0; s < SPIN; ++s) {
        mv[0] = ld4c(ws, fresh(WS_MOD + grp * MOD_REP + (224 + tid) * 16));
        mv[1] = ld4c(ws, fresh(WS_MOD + grp * MOD_REP + (224 + (tid < 80 ? 256 + tid : tid)) * 16));
        const v2u rv = ld2c(ws, fresh(WS_DRDY + grp * 1024 + min(lane, 48) * 8));
        if (wall(mv[0].y == tag && mv[0].w == tag && mv[1].y == tag && mv[1].w == tag && rv.x == tag && rv.y == ~tag)) break;
        __builtin_amdgcn_s_sleep(SL4);
      }
      if (lane == 0) st2c(ws, WS_HRDY + blk * 32 + wave * 8, v2u{tag, ~tag});
      STAMP(12);
#pragma unroll
      for (int f = 0; f < WD_EARLY; ++f) wdn[f >> 2][f & 3] = ld4c(ws, (isG ? l16 : OOB) + (f & 3) * 1024, wdb + (f >> 2) * 4096);
      sb();
#pragma unroll
      for (int r = 0; r < 2; ++r) {
        const float sc = m0_[r] < L_M2 + 1792 ? 2.f / (float)(S * D) : 1.f;
        *(LAS float*)(L + m0_[r]) = blo(mv[r].x) * sc;
        *(LAS float*)(L + m1_[r]) = bhi(mv[r].x) * sc;
        *(LAS float*)(L + m1_[r] + md_[r]) = blo(mv[r].z) * sc;
        *(LAS float*)(L + m1_[r] + 2 * md_[r]) = bhi(mv[r].z) * sc;
      }
    }
    bar();
    *(LAS v4u*)(L + L_AF + (tid >> 6) * 4096 + 3072 + l16) = v4u{0u, 0u, 0u, 0u};
    STAMP(4);
    int zr = L_AF + lane * 8, ro = L_M2 + (t0 * 64 + 4 * g) * 4, ro1 = wave == 0 ? ro : ro + 256, ex = L_EX + c16 * 8;
    pin(zr); pin(ro); pin(ro1); pin(ex);
    {
      int zl[4];
#pragma unroll
      for (int m = 0; m < 4; ++m) {
        const int kq = (m >> 1) * 128 + (to[m] == OOB ? 127 : wave == 0 && m == 1 ? 112 + c16 : 16 * (t0 + (m & 1)) + c16);
        zl[m] = L_AF + ((kq & ~31) + ((kq & 4) << 2) + ((kq & 24) >> 1) + (kq & 3)) * 32 + g * 8;
        pin(zl[m]);
        if (to[m] == OOB) xs[m] = sv;
      }
      bar();
      if (isG) wait_flags(ws, WS_HRDY + (16 * tile + 4 * wave) * 32 + c16 * 8, tag);
      {
        const int src = (blk * 5 + wave * 13) & 63;
        const int po = __builtin_amdgcn_readlane(to[(0)], 0) + (src & 15) * 64 + (src >> 4) * 16;
        const u32 old = __builtin_amdgcn_ds_bpermute(src * 4, xs[0].x);
        _Pragma("nounroll") for (int s = 0; s < SPIN; ++s) {
          if (wall(ld1c(ws, fresh(po)) != old)) break;
          __builtin_amdgcn_s_sleep(SL2);
        }
      }
      STAMP(13);
      v4u z[4];
      _Pragma("nounroll") for (int s = 0; s < SPIN; ++s) {
        u32 mn = 1;
#pragma unroll
        for (int m = 0; m < 4; ++m) z[m] = ld4c(ws, fresh(to[m]));
#pragma unroll
        for (int m = 0; m < 4; ++m) { const v4u d = z[m] ^ xs[m]; mn = min(min(mn, d.x), min(min(d.y, d.z), d.w)); }
        if (wall(mn != 0)) break;
      }
      STAMP(14);
#pragma unroll
      for (int m = 0; m < 4; ++m) {
        char LAS* p = L + zl[m];
        *(LAS v2u*)(p) = v2u{__builtin_amdgcn_perm(z[m].y, z[m].x, 0x05040100u), __builtin_amdgcn_perm(z[m].w, z[m].z, 0x05040100u)};
        *(LAS v2u*)(p + ZPL) = v2u{__builtin_amdgcn_perm(z[m].y, z[m].x, 0x07060302u), __builtin_amdgcn_perm(z[m].w, z[m].z, 0x07060302u)};
      }
    }
    bar();
    STAMP(5);
    {
      v4u zf[4][4];
#pragma unroll
      for (int ty = 0; ty < 4; ++ty)
#pragma unroll
        for (int s = 0; s < 4; ++s) zf[ty][s] = ldtr2(L + zr, ty * 4096 + s * 1024, ty * 4096 + s * 1024 + 512);
      v4f acc[2][4], x2[2][4], gav[2][4];
      v2f s1 = {0.f, 0.f}, s2 = {0.f, 0.f};
#define P3M(t, ty) mf8(acc[t][ty], acc[t][ty + 1], cf[4 * t + ty], zf[ty], cf[4 * t + ty + 1], zf[ty + 1]);
#define P3X(t, m, yv) { x2[t][m] = fma4(gav[t][m], yv, xo[t][m]); \
      if (t == 0 || wave != 0) { s1 += lo2(x2[t][m]) + hi2(x2[t][m]); \
        s2 = fma2(lo2(x2[t][m]), lo2(x2[t][m]), s2); s2 = fma2(hi2(x2[t][m]), hi2(x2[t][m]), s2); } }
      P3M(1, 0) P3M(1, 2)
#pragma unroll
      for (int m = 0; m < 4; ++m) { gav[0][m] = lds4f(L + ro, m * 64); gav[1][m] = lds4f(L + ro1, m * 64); }
      sb();
      P3M(0, 0) P3M(0, 2)
      mfence<0>(acc[1][0], acc[1][1], acc[1][2], acc[1][3]);
      const v4f A1 = acc[1][0] + acc[1][1], B1 = acc[1][0] - acc[1][1], P1 = acc[1][3] - acc[1][2], Q1 = acc[1][2] + acc[1][3];
      P3X(1, 0, A1 - P1) P3X(1, 1, A1 + P1) P3X(1, 2, B1 - Q1) P3X(1, 3, B1 + Q1)
      sb();
      mfence<0>(acc[0][0], acc[0][1], acc[0][2], acc[0][3]);
      {
        const v4f A = acc[0][0] + acc[0][1], B = acc[0][0] - acc[0][1], P = acc[0][3] - acc[0][2], Q = acc[0][2] + acc[0][3];
        v4f y[4] = {A - P, A + P, B - Q, B + Q};
        const bool sp = tid < 16;
        y[0].x = sp ? A.x : y[0].x; y[2].x = sp ? B.x : y[2].x;
        y[1].x = sp ? acc[1][0].x - acc[0][3].x : y[1].x; y[3].x = sp ? acc[1][0].x + acc[0][3].x : y[3].x;
        P3X(0, 0, y[0]) P3X(0, 1, y[1]) P3X(0, 2, y[2]) P3X(0, 3, y[3])
      }
      float sm = s1.x + s1.y, sq = s2.x + s2.y;
      sumg2(sm, sq);
      if (g == 0) *(LAS v2f*)(L + ex + wave * 128) = v2f{sm, sq};
      v4f sbv[2][4], cbv[2][4];
#pragma unroll
      for (int m = 0; m < 4; ++m) {
        sbv[0][m] = lds4f(L + ro, 1792 + m * 64); cbv[0][m] = lds4f(L + ro, 2 * 1792 + m * 64);
        sbv[1][m] = lds4f(L + ro1, 1792 + m * 64); cbv[1][m] = lds4f(L + ro1, 2 * 1792 + m * 64);
      }
      bar();
      v2f e = *(const LAS v2f*)(L + ex);
#pragma unroll
      for (int w = 1; w < 4; ++w) e += *(const LAS v2f*)(L + ex + w * 128);
      const float mc = e.x * (1.f / D), rstd = __builtin_amdgcn_rsqf(fmaxf(e.y * (1.f / D) - mc * mc, 0.f) + 1e-5f), nb = -mc * rstd;
      const int ho = WS_H2 + blk * 14336 + 2 * t0 * 1024 + l16, ho1 = wave == 0 ? OOB : ho + 2048;
#pragma unroll
      for (int t = 0; t < 2; ++t)
#pragma unroll
        for (int a = 0; a < 2; ++a) {
          v4u o;
#pragma unroll
          for (int hh = 0; hh < 2; ++hh) {
            const int m = 2 * a + hh;
            const v4f hv = fma4(fma4(x2[t][m], splat(rstd), splat(nb)), cbv[t][m], sbv[t][m]);
            o[2 * hh] = pk2(lo2(hv)); o[2 * hh + 1] = pk2(hi2(hv));
          }
          st4c(ws, (t ? ho1 : ho) + a * 1024, o);
        }
      STAMP(6);
      sb();
#pragma unroll
      for (int f = WD_EARLY; f < WD_MID; ++f) wdn[f >> 2][f & 3] = ld4c(ws, (isG ? l16 : OOB) + (f & 3) * 1024, wdb + (f >> 2) * 4096);
      sb();
#pragma unroll
      for (int t = 0; t < 2; ++t)
        if (t == 0 || wave != 0)
#pragma unroll
          for (int m = 0; m < 4; ++m)
#pragma unroll
            for (int r = 0; r < 4; r += 2) {
              const u32 p = pk(x2[t][m][r] + st.x, x2[t][m][r + 1] + st.x);
              *(LAS u16*)(L + XOFF(t, m, r)) = (u16)p;
              *(LAS u16*)(L + XOFF(t, m, r + 1)) = (u16)(p >> 16);
            }
      sb();
    }
    bar();
    x2_read(L, x2r, tid);
    bar();
    if (!isG) {
      x2_store(ws, x2r, blk, tid);
      drain();
      if (lane == 0) st2c(ws, WS_XRDY + blk * 64 + wave * 8, v2u{tag, ~tag});
      DUMP(); return;
    }
  } else {
    const int sj = j >= 30 ? j - 30 : j + 2;
    const int idx = sj * 8 + grp;
    const bool isC = j < 14 || idx == 0;
    const int pair = j < 14 ? grp * 14 + j : 112;
    const int lc = grp + 8 * (sj >> 1);
    const int qu = sj < 4 && lc < 14 ? 2 * lc + (sj & 1) : -1;
    const int t1o = WS_T1 + (32 * wave + (lane >> 3)) * T1_BLK + pair * 128 + (lane & 7) * 16;
    ModLoad ml;
    epoch = ld1c(ws, WS_EPOCH);
    if (qu >= 0) mod_load(ml, cin, wm, qu, tid);
#pragma unroll
    for (int q = 0; q < 4; ++q) st4c(ws, isC ? t1o + 8 * q * T1_BLK : OOB, sv);
    u32 pf0, pf1, pf2;
    {
      const rsrc_t mr = mkrs(wm, D * 6 * D * 2), xr = mkrs(xin, S * D * 2), fr_ = mkrs((wave & 1) ? wi : wr, S * DF * 2);
      const int row = (sj * 4 + wave) * 7 + (lane >> 1), l7 = lane / 7;
      pf0 = __builtin_amdgcn_raw_buffer_load_b32(mr, lane < 14 && (grp < 6 || !(lane & 1)) ? row * (6 * D * 2) + (grp + 8 * (lane & 1)) * 128 : OOB, 0, PF_AUX);
      pf1 = __builtin_amdgcn_raw_buffer_load_b32(xr, lane < 28 ? tok_row(16 * grp + sj, 4 * wave + l7) * (D * 2) + (lane - 7 * l7) * 128 : OOB, 0, PF_AUX);
    }
    sb();
    epoch = __builtin_amdgcn_readfirstlane(epoch);
    tag = mktag(epoch);
    if (qu >= 0) mod_part(ml, L, tid);
    asm volatile("s_waitcnt vmcnt(%0)" :: "i"(W_LATE ? 2 : 3) : "memory");
    { const rsrc_t fr_ = mkrs((wave & 1) ? wi : wr, S * DF * 2);
      pf2 = __builtin_amdgcn_raw_buffer_load_b32(fr_, lane < 29 ? (((16 * grp + sj) * 7200) & ~127) + ((wave >> 1) * 29 + lane) * 128 : OOB, 0, PF_AUX);
    }
    bar();
    if (wave == 0 && qu >= 0) mod_publish(L, ws, qu, lane, tag);
    if (wave == 1 && lane < 8) st2c(ws, isC ? WS_RDY + lane * 1024 + pair * 8 : OOB, v2u{tag, ~tag});
    STAMP(1);
    pin(pf0); pin(pf1); pin(pf2); pin(pfa); pin(pfb);
    const int qn = sj >= 4 && sj < 10 && grp < 7 ? 28 + grp * 6 + (sj - 4) : !isC && idx < 15 ? 69 + idx : -1;
    const int tw = sj >= 10 ? (sj - 10) * 8 + grp : sj == 4 && grp == 7 ? 48 : -1;
    v2u dw[2][8];
    if (qn >= 0) mod_load(ml, cin, wm, qn, tid);
    if (tw >= 0) {
      const rsrc_t dr = mkrs(wd, F * D * 2);
      const int u0 = tw * 4 + wave, uc = u0 / 28, kap = 2 * (u0 - uc * 28);
#pragma unroll
      for (int e = 0; e < 16; ++e)
        dw[e >> 3][e & 7] = __builtin_amdgcn_raw_buffer_load_b64(dr, (4 * g * D + 4 * c16) * 2 + (e & 3) * D * 2, ((32 * kap + 16 * (e >> 2)) * D + 64 * uc) * 2, PF_AUX);
    }
    sb();
    u32 pw[4];
    {
      const rsrc_t rg = mkrs(wg, D * F * 2), ru = mkrs(wu, D * F * 2);
      const int r7 = lane / 7, r0 = sj < 4 ? (sj * 4 + wave) * 16 : 256 + ((sj - 4) * 4 + wave) * 4;
      const int po = (r0 + r7) * (F * 2) + ((grp & 3) * 7 + lane - r7 * 7) * 128;
      const int o0 = lane < (sj < 4 ? 56 : 28) ? po : OOB, o1 = lane < 56 && sj < 4 ? po + 8 * (F * 2) : OOB;
      pw[0] = ld1(rg, o0); pw[1] = ld1(ru, o0); pw[2] = ld1(rg, o1); pw[3] = ld1(ru, o1);
    }
    sb();
    STAMP(20);
    v4f t1c[2], t1s[2], t2c[2], t2s[2];
#pragma unroll
    for (int t = 0; t < 2; ++t) {
#pragma unroll
      for (int r = 0; r < 4; ++r) {
        const float a1 = (float)((16 * t + 4 * g + r) * (16 * wave + c16)) * (1.f / S);
        const float a2 = (float)((16 * t + c16) * (16 * wave + 4 * g + r)) * (1.f / S);
        t1c[t][r] = __builtin_amdgcn_cosf(a1); t1s[t][r] = __builtin_amdgcn_sinf(a1);
        t2c[t][r] = __builtin_amdgcn_cosf(a2); t2s[t][r] = __builtin_amdgcn_sinf(a2);
      }
      pin(t1c[t]); pin(t1s[t]); pin(t2c[t]); pin(t2s[t]);
    }
    if (tw >= 0) {
#pragma unroll
      for (int tt = 0; tt < 8; ++tt) {
        v4u o;
#pragma unroll
        for (int m = 0; m < 4; ++m)
          o[m] = __builtin_amdgcn_perm(dw[tt >> 2][2 * m + 1][(tt & 3) >> 1], dw[tt >> 2][2 * m][(tt & 3) >> 1], (tt & 1) ? 0x07060302u : 0x05040100u);
        st4c(ws, WS_WD + (tw * 4 + wave) * 8192 + tt * 1024 + l16, o);
      }
    }
    if (qn >= 0) {
      bar();
      mod_part(ml, L, tid);
      bar();
      if (wave == 0) mod_publish(L, ws, qn, lane, tag);
    }
    pin(pw[0]); pin(pw[1]); pin(pw[2]); pin(pw[3]);
    STAMP(21);
    if (!isC) { DUMP(); return; }
    v4u c64[2][4], c32[2][2][2];
#pragma unroll
    for (int cp = 0; cp < 2; ++cp)
#pragma unroll
      for (int s = 0; s < 4; ++s) c64[cp][s] = ld4(tab, l16 + s * 1024, (TAB_P2 + wave * 2 + cp) * 4096);
#pragma unroll
    for (int t = 0; t < 2; ++t)
#pragma unroll
      for (int f = 0; f < 4; ++f) c32[t][f >> 1][f & 1] = ld4(tab, l16 + f * 1024, (TAB_P2 + 8 + t) * 4096);
    sb();
    if (tw >= 0) {
      asm volatile("s_waitcnt vmcnt(16)" ::: "memory");
      bar();
      if (tid < 8) st2c(ws, WS_DRDY + tid * 1024 + tw * 8, v2u{tag, ~tag});
    }
    STAMP(2);
    int wfo = WS_WT + pair * 16384 + (16 * g + wave) * 128 + c16 * 8;
    pin(wfo);
    v2u wf[2][4];
    {
      int wl = L_CI + ((lane >> 2) & 1) * 8256 + wave * 2048 + ((lane >> 3) * 4 + (lane & 3)) * 16, lo = t1o;
      pin(wl); pin(lo);
      light_poll(ws, WS_T1 + (32 * wave + (pair & 31)) * T1_BLK + pair * 128);
      STAMP(13);
      v4u z[4];
      _Pragma("nounroll") for (int s = 0; s < SPIN; ++s) {
        lo = fresh(lo);
#pragma unroll
        for (int q = 0; q < 4; ++q) z[q] = ld4c(ws, lo, 8 * q * T1_BLK);
        if (wall(max(max(max4(z[0]), max4(z[1])), max(max4(z[2]), max4(z[3]))) != SENT)) break;
      }
      STAMP(14);
#pragma unroll
      for (int q = 0; q < 4; ++q) *(LAS v4u*)(L + wl + q * 512) = z[q];
    }
    bar();
#pragma unroll
    for (int t = 0; t < 2; ++t)
#pragma unroll
      for (int r = 0; r < 4; ++r) wf[t][r] = ld2c(ws, wfo + r * 512, t * 8192);
    v2u fd = ld2c(ws, WS_DRDY + grp * 1024 + min(lane, 48) * 8);
    sb();
    STAMP(4);
    {
      v4u a[2][2][4];
#pragma unroll
      for (int c = 0; c < 2; ++c)
#pragma unroll
        for (int mt = 0; mt < 2; ++mt)
#pragma unroll
          for (int s = 0; s < 4; ++s) a[c][mt][s] = lds4(LB, L_CI + c * 8256 + (s * 2 + mt) * 1024);
      v4u old[2][2];
#pragma unroll
      for (int c = 0; c < 2; ++c)
#pragma unroll
        for (int mt = 0; mt < 2; ++mt) old[c][mt] = lds4(LB + wave * 2048, L_CI + c * 8256 + mt * 1024);
      v4f re[2][2], im[2][2], ur[2][2], ui[2][2], fr_[2][2], fi_[2][2];
      u32 f2[2][2][4], f3[2][2][4], f4[4];
      char LAS* xw = L + lb + wave * 1024 - L_B;
#define V4(x) v4u{x[0], x[1], x[2], x[3]}
#define S1K(h) c64[0][2 * h], c64[0][2 * h + 1], c64[1][2 * h], c64[1][2 * h + 1]
#define S1X(c, t, h) re[c][t], im[c][t], a[c][t][2 * h], a[c][t][2 * h + 1], S1K(h)
#define S2X(c, t) ur[c][t], ui[c][t], V4(f2[c][0]), V4(f2[c][1]), c32[t][0][0], c32[t][0][1], c32[t][1][0], c32[t][1][1]
#define S3X(c, t) im[c][t], re[c][t], V4(f3[c][0]), V4(f3[c][1]), c32[t][0][0], c32[t][0][1], c32[t][1][0], c32[t][1][1]
#define CM(o, P, Q, C, S_, h) o[2 * h], o[2 * h + 1], P[2 * h], Q[2 * h], C[2 * h], S_[2 * h], P[2 * h + 1], Q[2 * h + 1], C[2 * h + 1], S_[2 * h + 1]
#define TW1(c, t, h) CM(f2[c][t], re[c][t], im[c][t], t1c[t], t1s[t], h)
#define FLT(c, t, h) CM(f3[c][t], ur[c][t], ui[c][t], fr_[c][t], fi_[c][t], h)
#define TW2(c, t, h) CM(f4, re[c][t], im[c][t], t2c[t], t2s[t], h)
#define HV(x, h) (h ? hi2(x) : lo2(x))
#define CP(PL, o, P, Q, C, S_, h) cmul2<PL>(o[2 * h], o[2 * h + 1], HV(P, h), HV(Q, h), HV(C, h), HV(S_, h));
#define XW(c, t) *(LAS v4u*)(xw + L_CX + (c * 2 + t) * 4096) = V4(f4);
#define UNP(c, t) { _Pragma("unroll") for (int r = 0; r < 4; ++r) { fr_[c][t][r] = blo(wf[t][r][c]); fi_[c][t][r] = bhi(wf[t][r][c]); } \
      pin(fr_[c][t]); pin(fi_[c][t]); }
      nop2();
      m4_a0(S1X(0, 0, 0)); m4_a1(S1X(0, 0, 1)); m4_a0(S1X(0, 1, 0)); mfence<3>(re[0][0], im[0][0]);
      h4_a1m(S1X(0, 1, 1), TW1(0, 0, 0)); h4_a0m(S1X(1, 0, 0), TW1(0, 0, 1));
      h4_a1m(S1X(1, 0, 1), TW1(0, 1, 0)); h4_a0m(S1X(1, 1, 0), TW1(0, 1, 1));
      h4_a1m(S1X(1, 1, 1), TW1(1, 0, 0)); h4_b0m(S2X(0, 0), TW1(1, 0, 1));
      h4_b0m(S2X(0, 1), TW1(1, 1, 0));
      sb();
      CP(false, f2[1][1], re[1][1], im[1][1], t1c[1], t1s[1], 1)
      UNP(0, 0) UNP(0, 1)
      sb();
      h4_b0p(S2X(1, 0), FLT(0, 0, 0)); h4_b0p(S2X(1, 1), FLT(0, 0, 1));
      sb();
      CP(true, f3[0][1], ur[0][1], ui[0][1], fr_[0][1], fi_[0][1], 0) CP(true, f3[0][1], ur[0][1], ui[0][1], fr_[0][1], fi_[0][1], 1)
      UNP(1, 0) UNP(1, 1)
      sb();
      h4_a0p(S3X(0, 0), FLT(1, 0, 0)); h4_a0p(S3X(0, 1), FLT(1, 0, 1));
      sb();
      CP(true, f3[1][1], ur[1][1], ui[1][1], fr_[1][1], fi_[1][1], 0) CP(true, f3[1][1], ur[1][1], ui[1][1], fr_[1][1], fi_[1][1], 1)
#pragma unroll
      for (int e = 0; e < 4; ++e) pin(f3[1][1][e]);
      nop2();
      h4_a0p(S3X(1, 0), TW2(0, 0, 0)); h4_a0p(S3X(1, 1), TW2(0, 0, 1));
      XW(0, 0)
      sb();
      CP(true, f4, re[0][1], im[0][1], t2c[1], t2s[1], 0) CP(true, f4, re[0][1], im[0][1], t2c[1], t2s[1], 1) XW(0, 1)
      sb();
      mfence<0>(re[1][0], im[1][0], re[1][1], im[1][1]);
      CP(true, f4, re[1][0], im[1][0], t2c[0], t2s[0], 0) CP(true, f4, re[1][0], im[1][0], t2c[0], t2s[0], 1) XW(1, 0)
      CP(true, f4, re[1][1], im[1][1], t2c[1], t2s[1], 0) CP(true, f4, re[1][1], im[1][1], t2c[1], t2s[1], 1) XW(1, 1)
      bar();
#pragma unroll
      for (int c = 0; c < 2; ++c)
#pragma unroll
        for (int nt = 0; nt < 2; ++nt)
#pragma unroll
          for (int s = 0; s < 4; ++s) a[c][nt][s] = lds4(LB, L_CX + ((c * 2 + nt) * 4 + s) * 1024);
      const int o2 = WS_T1 + (32 * wave + (lane >> 2)) * T1_BLK + pair * 128 + (lane & 3) * 16;
#define S4(c, t) mf8(im[c][t], re[c][t], c64[0], a[c][t], c64[1], a[c][t]);
#define NEQ(x, y) ((x) == (y) ? (x) ^ 1u : (x))
#define OUT(c, t) { const v4u o = {pk(re[c][t].x, im[c][t].x), pk(re[c][t].y, im[c][t].y), pk(re[c][t].z, im[c][t].z), pk(re[c][t].w, im[c][t].w)}; \
      st4c(ws, o2 + c * 64 + t * 16 * T1_BLK, v4u{NEQ(o.x, old[c][t].x), NEQ(o.y, old[c][t].y), NEQ(o.z, old[c][t].z), NEQ(o.w, old[c][t].w)}); sb(); }
      S4(0, 0) S4(0, 1) mfence<0>(re[0][0], im[0][0]); OUT(0, 0)
      S4(1, 0) mfence<0>(re[0][1], im[0][1]); OUT(0, 1)
      S4(1, 1) mfence<0>(re[1][0], im[1][0]); OUT(1, 0)
      mfence<0>(re[1][1], im[1][1]); OUT(1, 1)
    }
    STAMP(5);
    sb();
    if (!isG) { DUMP(); return; }
    if (!wall(fd.x == tag && fd.y == ~tag)) wait_flags(ws, WS_DRDY + grp * 1024 + min(lane, 48) * 8, tag);
    dma_w1(wg, wu, slice, wave, lane, lbase);
#pragma unroll
    for (int f = 0; f < WD_MID; ++f) wdn[f >> 2][f & 3] = ld4c(ws, l16 + (f & 3) * 1024, wdb + (f >> 2) * 4096);
    fd = ld2c(ws, WS_HRDY + (16 * tile + 4 * wave) * 32 + c16 * 8);
    sb();
#pragma unroll
    for (int m = 0; m < 16; ++m)
      st4c(ws, WS_ACT + (16 * (tile + 2 * (m >> 3)) + 4 * (grp & 3)) * 57344 + ct * 32768 + (m & 7) * 4096 + tid * 16, sv);
    sb();
    if (!wall(fd.x == tag && fd.y == ~tag)) wait_flags(ws, WS_HRDY + (16 * tile + 4 * wave) * 32 + c16 * 8, tag);
    bar();
  }

  STAMP(7);
  const int blk0 = 16 * tile + 4 * (grp & 3);
  const int ab = WS_ACT + blk0 * 57344 + wave * 14 * 1024;
  v4u h[14][2];
  {
    const int hb = WS_H2 + (16 * tile + 4 * wave) * 14336;
    int tb[2][4];
#pragma unroll
    for (int u = 0; u < 8; ++u) {
      tb[u >> 2][u & 3] = (((lane >> 2) * 128 + ((lane >> 3) & 3) * 32 + (lane & 3) * 8) ^ ((u & 3) << 5)) + (u >> 2) * 57344;
      pin(tb[u >> 2][u & 3]);
    }
#define W_RD(dst, k, u) dst = ldtr2(L + tb[k >= 7][u & 3], (k % 7) * 8192 + (u >> 2) * 4096, (k % 7) * 8192 + (u >> 2) * 4096 + 2048)
#define H_LD(v, tt) hh_[v][tt] = ld4c(ws, l16 + (((v) % 14) & 3) * 1024, hb + (2 * ((v) / 14) + (tt)) * 14336 + (((v) % 14) >> 2) * 4096)
#define G_LD(e) h[(e) >> 1][(e) & 1] = ld4c(ws, l16 + (((e) >> 1) & 3) * 1024, ab + ((e) & 1) * 57344 + ((e) >> 3) * 4096)
    v4u wfr[2][8], hh_[28][2];
#pragma unroll
    for (int u = 0; u < 8; ++u) W_RD(wfr[0][u], 0, u);
    light_poll(ws, hb + (lane & 1) * 14336 + 13 * 1024);
    STAMP(15);
#pragma unroll
    for (int v = 0; v < LEAD; ++v) { H_LD(v, 0); H_LD(v, 1); }
    sb();
    if (!isR && lane < 2)
      st2c(ws, WS_CRDY + ((tile + 2 * lane) * 4 + (grp & 3)) * 256 + (ct * 4 + wave) * 8, v2u{tag, ~tag});
    sb();
#define BAD2(x0, x1) wany(max(max(max(max(x0.x, x0.y), x0.z), max(max(x0.w, x1.x), x1.y)), max(x1.z, x1.w)) == SENT)
#define RETRY2(x0, x1, o0, o1) \
      _Pragma("nounroll") for (int s_ = 0; s_ < SPIN; ++s_) { \
        const int lf_ = fresh(l16); SLOWCNT \
        x0 = ld4c(ws, lf_, o0); x1 = ld4c(ws, lf_, o1); \
        if (wall(max(max4(x0), max4(x1)) != SENT)) break; \
      }
    if (__builtin_expect(BAD2(hh_[0][0], hh_[0][1]), 0)) { RETRY2(hh_[0][0], hh_[0][1], hb, hb + 14336) }
    STAMP(8); CSTAMP(28);
    v4f acc[2][8][2];
    v2u fa, fb;
    const int xf = WS_XRDY + (blk0 + (wave >> 1) + (lane & 4 ? 2 : 0)) * 64 + (lane & 3) * 8;
    const int fx = lane < 36 ? WS_CRDY + (tile * 4 + wave) * 256 + max(lane - 8, 0) * 8 : WS_CRDY + (tile * 4 + (grp & 3)) * 256 + (lane - 36) * 8;
    const int ao = WS_ACT + (16 * tile + 4 * wave) * 57344 + 2 * slice * 1024 + l16;
#define ACT_OUT(ps, tt, f) { v4u o; \
      _Pragma("unroll") for (int hh = 0; hh < 2; ++hh) { \
        const v4f av = __builtin_elementwise_max(acc[ps][2 * f + hh][tt], splat(0.f)) * acc[ps][4 + 2 * f + hh][tt]; \
        o[2 * hh] = pk2(lo2(av)); o[2 * hh + 1] = pk2(hi2(av)); } \
      st4c(ws, ao + (2 * ps + tt) * 57344 + f * 1024, o); }
#pragma unroll
    for (int u = 0; u < 32; ++u) acc[u >> 4][(u >> 1) & 7][u & 1] = v4f{0.f, 0.f, 0.f, 0.f};
#pragma unroll
    for (int ps = 0; ps < 2; ++ps)
#pragma unroll
    for (int ks = 0; ks < 14; ++ks) {
      const int v = ps * 14 + ks, cur = v & 1, nk = (v + 1) % 14;
      const bool more = v < 27, epi = v == 14 || v == 15;
      __builtin_amdgcn_s_waitcnt(0xC07F);
#pragma unroll
      for (int u = 0; u < 8; ++u) {
        acc[ps][u][0] = mfma(wfr[cur][u], hh_[v][0], acc[ps][u][0]);
        if (more) W_RD(wfr[cur ^ 1][u], nk, u);
      }
      if (v + LEAD < 28) { H_LD(v + LEAD, 0); H_LD(v + LEAD, 1); }
#pragma unroll
      for (int e = 0; e < WD_PER; ++e) {
        const int f = WD_MID + (v < 14 ? v - 2 : 12 + WD_PER * (v - 16) + e);
        if (f < 56 && ((v >= 2 && v < 14 && e == 0) || v >= 16)) wdn[f >> 2][f & 3] = ld4c(ws, l16 + (f & 3) * 1024, wdb + (f >> 2) * 4096);
      }
      if (v == 8) fa = ld2c(ws, fx);
      if (v == 25) fb = ld2c(ws, xf);
      if (v > A_V) {
#pragma unroll
        for (int e = A_PER * (v - A_V - 1); e < A_PER * (v - A_V); ++e) G_LD(e);
      }
      const bool bad = more && BAD2(hh_[more ? v + 1 : v][0], hh_[more ? v + 1 : v][1]);
#pragma unroll
      for (int u = 0; u < 8; ++u) acc[ps][u][1] = mfma(wfr[cur][u], hh_[v][1], acc[ps][u][1]);
      if (epi) { ACT_OUT(0, ks, 0) ACT_OUT(0, ks, 1) }
#pragma unroll
      for (int u = 0; u < 16; ++u) {
        __builtin_amdgcn_sched_group_barrier(0x8, 1, 0);
        if (u < 4) __builtin_amdgcn_sched_group_barrier(0x100, 2, 0);
        else if (u < 12) __builtin_amdgcn_sched_group_barrier(0x100, 1, 0);
        if (u >= 5 && (u & 1)) __builtin_amdgcn_sched_group_barrier(0x20, 1, 0);
        if (epi) __builtin_amdgcn_sched_group_barrier(0x2, 5, 0);
        else if (u >= 10) __builtin_amdgcn_sched_group_barrier(0x2, 1, 0);
      }
      sb();
      if (v == 15 && isR) x2_store(ws, x2r, 16 * grp + j - 14, tid);
      if (v == 18 && isR) {
        static_assert(18 + LEAD < 28, "the count below");
        asm volatile("s_waitcnt vmcnt(6)" ::: "memory");
        if (lane == 0) st2c(ws, WS_XRDY + (16 * grp + j - 14) * 64 + wave * 8, v2u{tag, ~tag});
      }
      if (v == 12 && !wall(fa.x == tag && fa.y == ~tag)) wait_flags(ws, fx, tag);
      if (__builtin_expect(bad, 0)) {
        const int pb = 2 * ((v + 1) / 14) * 14336 + nk * 1024;
        RETRY2(hh_[more ? v + 1 : v][0], hh_[more ? v + 1 : v][1], hb + pb, hb + pb + 14336)
      }
      if (v == 13) { STAMP(16); CSTAMP(29); }
      sb();
    }
    STAMP(27);
    const int blk = blk0 + (wave >> 1);
    const int xo = WS_X2 + blk * 14336 + c16 * 896 + ct * 128 + g * 32 + (wave & 1) * 16;
    const int go = WS_MOD + grp * MOD_REP + (70 + 2 * ct) * 128 + g * 64 + (wave & 1) * 32;
    v4u xv[2], gv[2];
    gv[0] = ld4c(ws, go); gv[1] = ld4c(ws, go + 16);
    if (!wall(fb.x == tag && fb.y == ~tag)) wait_flags(ws, xf, tag);
    xv[0] = ld4c(ws, xo); xv[1] = ld4c(ws, xo + 2 * 14336);
    sb();
    ACT_OUT(1, 0, 0) ACT_OUT(1, 0, 1) ACT_OUT(1, 1, 0) ACT_OUT(1, 1, 1)
    CSTAMP(30);
    sb();
#pragma unroll
    for (int e = A_PER * (27 - A_V); e < 28; ++e) G_LD(e);
    sb();
    STAMP(9);
    v4u a[14][2];
    v4f ac2[4][4];
#pragma unroll
    for (int t = 0; t < 4; ++t)
#pragma unroll
      for (int n = 0; n < 4; ++n) ac2[t][n] = v4f{0.f, 0.f, 0.f, 0.f};
#define A_LD(e) a[(e) >> 1][(e) & 1] = ld4c(ws, l16 + (((e) >> 1) & 3) * 1024, ab + (2 + ((e) & 1)) * 57344 + ((e) >> 3) * 4096)
    const int po = ab + (2 + (lane & 1)) * 57344 + min(lane >> 1, 13) * 1024;
    u32 probe;
#pragma unroll
    for (int ks = 0; ks < 14; ++ks) {
      if (__builtin_expect(BAD2(h[ks][0], h[ks][1]), 0)) { RETRY2(h[ks][0], h[ks][1], ab + ks * 1024, ab + 57344 + ks * 1024) }
#pragma unroll
      for (int tt = 0; tt < 2; ++tt)
#pragma unroll
        for (int n = 0; n < 4; ++n) ac2[tt][n] = mfma(wdn[ks][n], h[ks][tt], ac2[tt][n]);
      if (ks == P_AT) probe = ld1c(ws, po);
      sb();
      if (ks == B_AT2) {
        STAMP(25);
        if (wany(probe == SENT)) light_poll(ws, po);
        STAMP(26);
      }
      if (ks >= B_AT2) {
#pragma unroll
        for (int e = 4 * (ks - B_AT2); e < 4 * (ks - B_AT2 + 1); ++e) A_LD(e);
      }
      sb();
    }
    STAMP(17);
    int rr_ = L_B + lane * 8 + wave * 6144;
    pin(rr_);
#define RED_WR(ph) _Pragma("unroll") for (int dd = 0; dd < 4; ++dd) if (dd != wave) \
      _Pragma("unroll") for (int n = 0; n < 4; ++n) \
        *(LAS v2f*)(L + L_B + lane * 8 + ((wave - dd - 1) & 3) * 2048 + ph * 24576 + dd * 6144 + n * 512) = \
            (dd & 1) ? hi2(ac2[2 * ph + (dd >> 1)][n]) : lo2(ac2[2 * ph + (dd >> 1)][n]);
#define RED_FIN(ph) { v2f r[4]; \
      _Pragma("unroll") for (int n = 0; n < 4; ++n) { \
        const v4f own = wave < 2 ? ac2[2 * ph][n] : ac2[2 * ph + 1][n]; r[n] = (wave & 1) ? hi2(own) : lo2(own); } \
      _Pragma("unroll") for (int w = 0; w < 3; ++w) \
        _Pragma("unroll") for (int n = 0; n < 4; ++n) r[n] += *(const LAS v2f*)(L + rr_ + w * 2048 + ph * 24576 + n * 512); \
      v4u o; \
      _Pragma("unroll") for (int q = 0; q < 2; ++q) { \
        o[2 * q] = pk2(fma2(bun(gv[q].x), v2f{r[0][q], r[1][q]}, bun(xv[ph][2 * q]))); \
        o[2 * q + 1] = pk2(fma2(bun(gv[q].z), v2f{r[2][q], r[3][q]}, bun(xv[ph][2 * q + 1]))); } \
      st4c(mkrs(out, S * D * 2), oo[ph], o); }
#pragma unroll
    for (int e = 4 * (14 - B_AT2); e < 4 * (14 - B_AT2) + 4; ++e) A_LD(e);
    sb();
    RED_WR(0)
    int oo[2];
#pragma unroll
    for (int ph = 0; ph < 2; ++ph) { oo[ph] = (tok_row(blk + 2 * ph, c16) * D + ct * 64 + g * 16 + (wave & 1) * 8) * 2; pin(oo[ph]); }
    _Pragma("nounroll") for (int s = 0; s < SPIN; ++s) {
      if (wall(gv[0].y == tag && gv[0].w == tag && gv[1].y == tag && gv[1].w == tag)) break;
      const int gf = fresh(go);
      gv[0] = ld4c(ws, gf); gv[1] = ld4c(ws, gf + 16);
    }
    STAMP(18);
    sb();
#pragma unroll
    for (int ks = 0; ks < 14; ++ks) {
      if (__builtin_expect(BAD2(a[ks][0], a[ks][1]), 0)) { RETRY2(a[ks][0], a[ks][1], ab + 2 * 57344 + ks * 1024, ab + 3 * 57344 + ks * 1024) }
#pragma unroll
      for (int tt = 0; tt < 2; ++tt)
#pragma unroll
        for (int n = 0; n < 4; ++n) ac2[2 + tt][n] = mfma(wdn[ks][n], a[ks][tt], ac2[2 + tt][n]);
#pragma unroll
      for (int e = 4 * (15 - B_AT2) + 3 * ks; e < 4 * (15 - B_AT2) + 3 * ks + 3; ++e)
        if (e < 28) A_LD(e);
#pragma unroll
      for (int u = 0; u < 4; ++u) {
        __builtin_amdgcn_sched_group_barrier(0x8, 2, 0);
        __builtin_amdgcn_sched_group_barrier(0x20, 1, 0);
      }
      sb();
    }
    STAMP(10);
    RED_WR(1)
    bar();
    RED_FIN(0)
    RED_FIN(1)
    if (blockIdx.x == 0 && wave == 0) {
      _Pragma("nounroll") for (int s = 0; s < SPIN; ++s) {
        const v4u v = ld4c(ws, fresh(WS_MOD + (70 + (lane < 14 ? lane : 0)) * 128));
        if (wall(v.y == tag)) break;
      }
      if (lane == 0) __builtin_amdgcn_raw_buffer_store_b32(epoch + 1u, ws, WS_EPOCH, 0, SC1);
    }
  }
  if (blockIdx.x == PADB) { while (__builtin_amdgcn_s_memrealtime() < padt + PADT) __builtin_amdgcn_s_sleep(1); }
  DUMP();
}

extern "C" void kernel_launch(void* const* d_in, const int* in_sizes, int n_in, void* d_out, int out_size, void* d_ws,
                              size_t ws_size, hipStream_t stream) {
  fused<<<256, 256, 0, stream>>>((const u16*)d_in[0], (const u16*)d_in[1], (const u16*)d_in[2], (const u16*)d_in[4],
                                 (const u16*)d_in[5], (const u16*)d_in[6], (const u16*)d_in[7], (const u16*)d_in[8],
                                 (u16*)d_out, (char*)d_ws);
}
```

```cpp
#include <hip/hip_runtime.h>

typedef unsigned short u16;
typedef unsigned int u32;
typedef unsigned long long u64;
typedef u32 v4u __attribute__((ext_vector_type(4)));
typedef u32 v2u __attribute__((ext_vector_type(2)));
typedef float v4f __attribute__((ext_vector_type(4)));
typedef float v2f __attribute__((ext_vector_type(2)));
typedef short v8s __attribute__((ext_vector_type(8)));
typedef short v4s __attribute__((ext_vector_type(4)));
typedef __bf16 v8b __attribute__((ext_vector_type(8)));
typedef __bf16 v2b __attribute__((ext_vector_type(2)));
#define LAS __attribute__((address_space(3)))
#define GAS __attribute__((address_space(1)))
#define DEV __device__ __forceinline__

#define C_W1_EARLY 0
#define CLR_DELAY 0
#define W_DELAY 0
#define CLR2_DELAY 0
#define PF_POS 2
#define SL1 1
#define SL2 2
#define SL4 4
#define X_AFTER 8
#define PF_AUX 2
#define W_LATE 1
#define W1_PF 1
#define DMA_MOD ""
#define LEAD 6
#define A_V 22
#define A_PER 4
#define WDV(f) ((f) - WD_MID + 2 < 14 ? (f) - WD_MID + 2 : ((f) - WD_MID - 12) / 2 + 16)
#define B_AT2 12
#define RE_PF 3
#define P_AT 7
#define WD_PER 3
#define A_FIRST 10
#define B_FIRST 10
#define A_AT 9
#define F_AT 4
#define CODE_BYTES 45056
#define WD_EARLY 16
#define WD_MID 24
#define B_AT 9
constexpr int S = 2048, D = 448, DF = 225, F = 1792;

struct alignas(16) Chain { u16 v[2048]; };
struct Cos448 { u16 v[448]; };
constexpr double c_cos_t(double a) {
  double a2 = a * a, t = 1.0, s = 1.0;
  for (int i = 1; i <= 10; ++i) { t = -t * a2 / ((2 * i - 1) * (2 * i)); s += t; }
  return s;
}
constexpr double c_sin_t(double a) {
  double a2 = a * a, t = a, s = a;
  for (int i = 1; i <= 10; ++i) { t = -t * a2 / ((2 * i) * (2 * i + 1)); s += t; }
  return s;
}
constexpr double c_cos448(int m) {
  if (m > 224) m = 448 - m;
  double sg = 1.0;
  if (m > 112) { m = 224 - m; sg = -1.0; }
  const double w = 6.283185307179586476925286766559 / 448.0;
  return m == 112 ? 0.0 : (m <= 56 ? sg * c_cos_t(w * m) : sg * c_sin_t(w * (112 - m)));
}
constexpr u16 c_bf16(double d) {
  const u32 u = __builtin_bit_cast(u32, (float)d);
  return (u16)((u + 0x7FFFu + ((u >> 16) & 1u)) >> 16);
}
constexpr Cos448 mk_cos() { Cos448 c{}; for (int m = 0; m < 448; ++m) c.v[m] = c_bf16(c_cos448(m)); return c; }
constexpr Cos448 CTAB = mk_cos();
constexpr u16 bcos(int m) { return CTAB.v[m % 448]; }
constexpr u16 bsin(int m) { return CTAB.v[(m % 448 + 336) % 448]; }
constexpr u16 bneg(u16 b) { return b ? (u16)(b ^ 0x8000u) : b; }
constexpr u16 bscale(u16 b, int e) { return b ? (u16)(b + e * 0x80) : b; }

constexpr Chain mk_p1(int c) {
  const int tile = c >> 2, par = c & 1, sn = (c >> 1) & 1;
  Chain ch{};
  for (int s = 0; s < 4; ++s)
    for (int l = 0; l < 64; ++l) {
      const int kp = 16 * tile + (l & 15), k = 2 * kp + par;
      if (k > 224) continue;
      for (int j = 0; j < 8; ++j) {
        const int dl = 32 * s + 8 * (l >> 4) + j;
        if (dl > 112) continue;
        u16 b = sn ? bneg(bsin(k * dl)) : bcos(k * dl);
        if (dl == 0 || dl == 112) b = bscale(b, -1);
        ch.v[(s * 64 + l) * 8 + j] = b;
      }
    }
  return ch;
}
constexpr Chain mk_p3(int c) {
  const int tile = c >> 1, sn = c & 1;
  Chain ch{};
  for (int s = 0; s < 4; ++s)
    for (int l = 0; l < 64; ++l) {
      int dl = 16 * tile + (l & 15);
      if (c == 1 && dl == 0) dl = 112;
      for (int j = 0; j < 8; ++j) {
        const int k = 2 * (32 * s + 8 * (l >> 4) + j) + 1;
        if (k < 224) ch.v[(s * 64 + l) * 8 + j] = sn ? bsin(k * dl) : bcos(k * dl);
      }
    }
  return ch;
}
constexpr Chain mk_p2(int c) {
  Chain ch{};
  for (int f = 0; f < 4; ++f)
    for (int l = 0; l < 64; ++l)
      for (int j = 0; j < 8; ++j) {
        const int comp = c < 8 ? (c & 1) : (f >> 1), s = c < 8 ? f : (f & 1);
        const int o = 16 * (c < 8 ? (c >> 1) : (c - 8)) + (l & 15), i = 16 * s + 4 * (l >> 4) + (j >> 1);
        const int m = (c < 8 ? 7 : 14) * ((i * o) % 64);
        u16 b = 0;
        if (comp == 0) b = (j & 1) ? bsin(m) : bcos(m);
        else b = (j & 1) ? bcos(m) : bneg(bsin(m));
        ch.v[(f * 64 + l) * 8 + j] = b;
      }
  return ch;
}
template <int I> constexpr Chain P1C = mk_p1(I);
template <int I> constexpr Chain P3C = mk_p3(I);
template <int I> constexpr Chain P2C = mk_p2(I);
#define L10(T, b) T<b>, T<b + 1>, T<b + 2>, T<b + 3>, T<b + 4>, T<b + 5>, T<b + 6>, T<b + 7>, T<b + 8>, T<b + 9>
#define L29(T) L10(T, 0), L10(T, 10), T<20>, T<21>, T<22>, T<23>, T<24>, T<25>, T<26>, T<27>, T<28>
constexpr int TAB_P1 = 0, TAB_P3 = 29, TAB_P2 = 43, TAB_N = 53;
static __device__ const Chain TAB[TAB_N] = {L29(P1C), L10(P3C, 0), P3C<10>, P3C<11>, P3C<12>, P3C<13>, L10(P2C, 0)};

constexpr int WS_EPOCH = 0;
constexpr int WS_RDY = 4096;
constexpr int WS_HRDY = 4096 + 8192;
constexpr int WS_DRDY = 4096 + 16384;
constexpr int WS_XRDY = 131072 + 65536;
constexpr int WS_ARDY = 131072;
constexpr int WS_CRDY = 131072 + 32768;
constexpr int WS_MOD = 32768, MOD_REP = 12288;
constexpr int T1_BLK = 226 * 64;
constexpr int WS_T1 = 1 << 18;
constexpr int WS_WT = WS_T1 + 128 * T1_BLK;
constexpr int WS_H2 = WS_WT + 113 * 16384;
constexpr int WS_X2 = WS_H2 + 128 * 14336;
constexpr int WS_WD = WS_X2 + 128 * 14336;
constexpr int WS_ACT = WS_WD + 7 * 56 * 4096;
constexpr int WS_END = WS_ACT + 128 * 57344;
constexpr int WS_DBG = 32 << 20;

constexpr int L_W1 = 0;
constexpr int L_B = 114688;
constexpr int XP = 912;
constexpr int L_XS = L_B;
constexpr int L_ST = L_XS + 16 * XP;
constexpr int L_M1 = L_ST + 128;
constexpr int L_AF = L_M1 + 4 * 1792;
constexpr int L_M2 = L_AF + 16384;
constexpr int L_EX = L_M2 + 3 * 1792;
constexpr int L_REND = L_EX + 512;
constexpr int ZPL = 8192;
constexpr int L_CI = L_B;
constexpr int L_CX = L_CI + 2 * 8256;
constexpr int L_RED = L_CX + 16384;
constexpr int L_DBG = 163840 - 1024;
constexpr int L_SZ = 163840;
static_assert(L_REND <= L_DBG && L_RED + 2048 <= L_DBG && 2 * ZPL <= 16384, "lds");

constexpr u32 SENT = 0xFFFFFFFFu;
constexpr int SPIN = 1 << 14;
constexpr int SC1 = 16;
constexpr int OOB = 0x7FFF0000;

typedef __amdgpu_buffer_rsrc_t rsrc_t;
DEV rsrc_t mkrs(const void* p, int bytes) { return __builtin_amdgcn_make_buffer_rsrc((void*)p, 0, bytes, 0x00020000); }
DEV v4u ld4(rsrc_t r, int off, int so = 0) { return __builtin_amdgcn_raw_buffer_load_b128(r, off, so, 0); }
DEV v2u ld2(rsrc_t r, int off, int so = 0) { return __builtin_amdgcn_raw_buffer_load_b64(r, off, so, 0); }
DEV v4u ld4c(rsrc_t r, int off, int so = 0) { return __builtin_amdgcn_raw_buffer_load_b128(r, off, so, SC1); }
DEV v2u ld2c(rsrc_t r, int off, int so = 0) { return __builtin_amdgcn_raw_buffer_load_b64(r, off, so, SC1); }
DEV u32 ld1c(rsrc_t r, int off, int so = 0) { return __builtin_amdgcn_raw_buffer_load_b32(r, off, so, SC1); }
DEV void st4c(rsrc_t r, int off, v4u v) { __builtin_amdgcn_raw_buffer_store_b128(v, r, off, 0, SC1); }
DEV void st2c(rsrc_t r, int off, v2u v) { __builtin_amdgcn_raw_buffer_store_b64(v, r, off, 0, SC1); }
DEV float blo(u32 v) { return __builtin_bit_cast(float, v << 16); }
DEV float bhi(u32 v) { return __builtin_bit_cast(float, v & 0xFFFF0000u); }
DEV v2f bun(u32 v) { return v2f{blo(v), bhi(v)}; }
DEV u32 pk(float lo, float hi) { return __builtin_bit_cast(u32, __builtin_convertvector(v2f{lo, hi}, v2b)); }
DEV u32 pk2(v2f v) { return __builtin_bit_cast(u32, __builtin_convertvector(v, v2b)); }
DEV float rb(float f) { return blo(pk(f, 0.f)); }
DEV v2f lo2(v4f v) { return __builtin_shufflevector(v, v, 0, 1); }
DEV v2f hi2(v4f v) { return __builtin_shufflevector(v, v, 2, 3); }
DEV v2f fma2(v2f a, v2f b, v2f c) { return __builtin_elementwise_fma(a, b, c); }
DEV v4f fma4(v4f a, v4f b, v4f c) { return __builtin_elementwise_fma(a, b, c); }
typedef int v4i __attribute__((ext_vector_type(4)));
DEV v4f relu4(v4f v) { return __builtin_bit_cast(v4f, __builtin_elementwise_max(__builtin_bit_cast(v4i, v), v4i{0, 0, 0, 0})); }
DEV v4f splat(float f) { return v4f{f, f, f, f}; }
DEV v4f mfma(v4u a, v4u b, v4f c) {
  return __builtin_amdgcn_mfma_f32_16x16x32_bf16(__builtin_bit_cast(v8b, a), __builtin_bit_cast(v8b, b), c, 0, 0, 0);
}
DEV void mfa(v4f& c, v4u a, v4u b) { asm volatile("s_nop 1\n\tv_mfma_f32_16x16x32_bf16 %0, %1, %2, %0" : "+v"(c) : "v"(a), "v"(b)); }
DEV void mf0(v4f& c, v4u a, v4u b) { asm volatile("s_nop 1\n\tv_mfma_f32_16x16x32_bf16 %0, %1, %2, 0" : "=&v"(c) : "v"(a), "v"(b)); }
template <int N> DEV void mfence(v4f& a, v4f& b) { asm volatile("s_nop %2" : "+v"(a), "+v"(b) : "i"(N)); }
template <int N> DEV void mfence(v4f& a, v4f& b, v4f& c, v4f& d) { asm volatile("s_nop %4" : "+v"(a), "+v"(b), "+v"(c), "+v"(d) : "i"(N)); }
#define MF "v_mfma_f32_16x16x32_bf16 "
DEV void mf8(v4f& d0, v4f& d1, const v4u (&a0)[4], const v4u (&b0)[4], const v4u (&a1)[4], const v4u (&b1)[4]) {
  asm volatile("s_nop 1\n\t" MF "%0, %2, %6, 0\n\t" MF "%1, %10, %14, 0\n\t" MF "%0, %3, %7, %0\n\t" MF "%1, %11, %15, %1\n\t"
               MF "%0, %4, %8, %0\n\t" MF "%1, %12, %16, %1\n\t" MF "%0, %5, %9, %0\n\t" MF "%1, %13, %17, %1"
               : "=&v"(d0), "=&v"(d1)
               : "v"(a0[0]), "v"(a0[1]), "v"(a0[2]), "v"(a0[3]), "v"(b0[0]), "v"(b0[1]), "v"(b0[2]), "v"(b0[3]),
                 "v"(a1[0]), "v"(a1[1]), "v"(a1[2]), "v"(a1[3]), "v"(b1[0]), "v"(b1[1]), "v"(b1[2]), "v"(b1[3]));
}
#define H4_M(XA, D, X, K, C) MF D ", " XA(X, K) ", " C "\n\t"
#define H4_XA(X, K) X ", " K
#define H4_XB(X, K) K ", " X
#define H4_DEF(NAME, XA, C0, C1, CONS, N0, N1, PA, PB, PC, PD) \
DEV void NAME(v4f& d0, v4f& d1, v4u x0, v4u x1, v4u k00, v4u k01, v4u k10, v4u k11, \
              u32& o0, u32& o1, float p0, float q0, float c0, float s0, float p1, float q1, float c1, float s1) { \
  float t0, t1, t2, t3; \
  asm volatile(H4_M(XA, "%0", "%8", "%10", C0) "v_mul_f32 %4, %14, %16\n\tv_mul_f32 %5, %15, %16\n\t" \
               H4_M(XA, "%1", "%8", "%12", C1) "v_mul_f32 %6, %18, %20\n\tv_mul_f32 %7, %19, %20\n\t" \
               H4_M(XA, "%0", "%9", "%11", "%0") "v_fma_f32 %4, " N0 "%15, %17, %4\n\tv_fma_f32 %5, " N1 "%14, %17, %5\n\t" \
               H4_M(XA, "%1", "%9", "%13", "%1") "v_fma_f32 %6, " N0 "%19, %21, %6\n\tv_fma_f32 %7, " N1 "%18, %21, %7\n\t" \
               "v_cvt_pk_bf16_f32 %2, " PA ", " PB "\n\tv_cvt_pk_bf16_f32 %3, " PC ", " PD \
               : CONS(d0), CONS(d1), "=&v"(o0), "=&v"(o1), "=&v"(t0), "=&v"(t1), "=&v"(t2), "=&v"(t3) \
               : "v"(x0), "v"(x1), "v"(k00), "v"(k01), "v"(k10), "v"(k11), \
                 "v"(p0), "v"(q0), "v"(c0), "v"(s0), "v"(p1), "v"(q1), "v"(c1), "v"(s1)); \
}
H4_DEF(h4_a0m, H4_XA, "0", "0", "=&v", "", "-", "%4", "%5", "%6", "%7")
H4_DEF(h4_a1m, H4_XA, "%0", "%1", "+v", "", "-", "%4", "%5", "%6", "%7")
H4_DEF(h4_b0m, H4_XB, "0", "0", "=&v", "", "-", "%4", "%5", "%6", "%7")
H4_DEF(h4_b0p, H4_XB, "0", "0", "=&v", "-", "", "%5", "%4", "%7", "%6")
H4_DEF(h4_a0p, H4_XA, "0", "0", "=&v", "-", "", "%5", "%4", "%7", "%6")
#define M4_DEF(NAME, XA, C0, C1, CONS) \
DEV void NAME(v4f& d0, v4f& d1, v4u x0, v4u x1, v4u k00, v4u k01, v4u k10, v4u k11) { \
  asm volatile(H4_M(XA, "%0", "%2", "%4", C0) H4_M(XA, "%1", "%2", "%6", C1) H4_M(XA, "%0", "%3", "%5", "%0") MF "%1, " XA("%3", "%7") ", %1" \
               : CONS(d0), CONS(d1) : "v"(x0), "v"(x1), "v"(k00), "v"(k01), "v"(k10), "v"(k11)); \
}
M4_DEF(m4_a0, H4_XA, "0", "0", "=&v") M4_DEF(m4_a1, H4_XA, "%0", "%1", "+v")
M4_DEF(m4_b0, H4_XB, "0", "0", "=&v") M4_DEF(m4_b1, H4_XB, "%0", "%1", "+v")
DEV void nop2() { asm volatile("s_nop 1"); }
template <class T> DEV void pin(T& v) { asm volatile("" : "+v"(v)); }
DEV int fresh(int off) { asm volatile("" : "+v"(off) :: "memory"); return off; }
DEV bool wall(bool ok) { return __builtin_amdgcn_ballot_w64(!ok) == 0; }
DEV bool wany(bool b) { return __builtin_amdgcn_ballot_w64(b) != 0; }
DEV void sb() { __builtin_amdgcn_sched_barrier(0); }
DEV u32 max4(v4u v) { return max(max(v.x, v.y), max(v.z, v.w)); }
DEV void drain() { asm volatile("s_waitcnt vmcnt(0)" ::: "memory"); }
DEV void cfence() { asm volatile("" ::: "memory"); }
DEV void bar() { asm volatile("s_waitcnt lgkmcnt(0)\n\ts_barrier" ::: "memory"); }
template <int CTRL> DEV float dpp(float v) {
  return __builtin_bit_cast(float, __builtin_amdgcn_update_dpp(0, __builtin_bit_cast(int, v), CTRL, 0xF, 0xF, true));
}
DEV float sum16(float v) {
  v += dpp<0x128>(v); v += dpp<0x124>(v); v += dpp<0x122>(v); v += dpp<0x121>(v); return v;
}
DEV void sumg2(float& a, float& b) {
  float c = a, d = b;
  asm volatile("s_nop 1\n\tv_permlane16_swap_b32 %0, %1\n\tv_permlane16_swap_b32 %2, %3" : "+v"(a), "+v"(c), "+v"(b), "+v"(d));
  a += c; b += d; c = a; d = b;
  asm volatile("s_nop 1\n\tv_permlane32_swap_b32 %0, %1\n\tv_permlane32_swap_b32 %2, %3" : "+v"(a), "+v"(c), "+v"(b), "+v"(d));
  a += c; b += d;
}
DEV v4u lds4(const char LAS* L, int off) { return *(const LAS v4u*)(L + off); }
DEV v4f lds4f(const char LAS* L, int off) { return *(const LAS v4f*)(L + off); }
DEV v2u ldtr(const char LAS* L, int off) { return __builtin_bit_cast(v2u, __builtin_amdgcn_ds_read_tr16_b64_v4i16((LAS v4s*)(L + off))); }
DEV v4u ldtr2(const char LAS* L, int o0, int o1) { const v2u a = ldtr(L, o0), b = ldtr(L, o1); return v4u{a.x, a.y, b.x, b.y}; }
DEV void dma16(rsrc_t r, int voff, int soff, u32 lds_dst) {
  u32 keep;
  asm volatile("s_mov_b32 %0, m0\n\ts_mov_b32 m0, %1\n\ts_nop 0\n\tbuffer_load_dwordx4 %2, %3, %4 offen" DMA_MOD " lds\n\ts_mov_b32 m0, %0"
               : "=&s"(keep) : "s"(lds_dst), "v"(voff), "s"(r), "s"(soff) : "memory");
}
DEV void light_poll(rsrc_t ws, int off) {
  _Pragma("nounroll") for (int s = 0; s < SPIN; ++s) {
    if (wall(ld1c(ws, fresh(off)) != SENT)) break;
    __builtin_amdgcn_s_sleep(SL2);
  }
}
DEV int tok_row(int blk, int t) {
  const int n1 = 16 * (blk >> 5) + 4 * ((blk >> 2) & 3) + (t & 3), n2 = 16 * ((blk >> 4) & 1) + 4 * (blk & 3) + (t >> 2);
  return n2 + 32 * n1;
}
template <bool SWAP> DEV v4u cmul_pack(v4f a, v4f b, v4f c, v4f s) {
  const v4f re = fma4(b, s, a * c), im = fma4(b, c, -(a * s));
  return SWAP ? v4u{pk(im.x, re.x), pk(im.y, re.y), pk(im.z, re.z), pk(im.w, re.w)}
              : v4u{pk(re.x, im.x), pk(re.y, im.y), pk(re.z, im.z), pk(re.w, im.w)};
}

template <bool PLUS> DEV void cmul2(u32& o0, u32& o1, v2f p, v2f q, v2f c, v2f s) {
  const v2f re = fma2(PLUS ? -q : q, s, p * c), im = fma2(PLUS ? p : -p, s, q * c);
  o0 = PLUS ? pk(im.x, re.x) : pk(re.x, im.x); o1 = PLUS ? pk(im.y, re.y) : pk(re.y, im.y);
}

#define STAMP(i)
#define CSTAMP(i)
#define SLOWCNT
#define DUMP()

DEV void dma_w1(const u16* wg, const u16* wu, int slice, int wave, int lane, u32 lbase) {
  const rsrc_t rg = mkrs(wg, D * F * 2), ru = mkrs(wu, D * F * 2);
  const int r = lane >> 3, ch = ((((lane & 7) >> 1) ^ (lane >> 4)) & 3) * 32 + (lane & 1) * 16 + slice * 128;
  const int vp = r * (F * 2) + ch, vn = (7 - r) * (F * 2) + ch;
#pragma unroll
  for (int n = 0; n < 4; ++n) {
    const int ks = wave + 4 * n;
    if (n == 3 && wave >= 2) break;
    const int t16 = (ks >> 1) * 16, a = ks & 1;
#pragma unroll
    for (int q = 0; q < 4; ++q) {
      const int dl0 = t16 + 8 * (q & 1);
      const bool pos = (q >> 1) == a;
      const int d0 = q < 2 ? (a ? 224 - dl0 - 7 : dl0) : (a ? 224 + dl0 : 448 - dl0 - 7);
      int vo = pos ? vp : vn, so = d0 * (F * 2);
      if (n == 0 && q == 2) {
        vo += so; so = 0;
        if (wave < 2 && lane < 8) vo = (wave ? 336 : 112) * (F * 2) + ch;
      }
      dma16(rg, vo, so, lbase + L_W1 + ks * 8192 + q * 1024);
      dma16(ru, vo, so, lbase + L_W1 + ks * 8192 + 4096 + q * 1024);
    }
  }
}
struct ModLoad { u32 cv[7]; v4u mw[7]; };
DEV void mod_load(ModLoad& m, const u16* cin, const u16* wm, int q, int tid) {
  const rsrc_t cr = mkrs(cin, q >= 0 ? D * 2 : 0), mr = mkrs(wm, q >= 0 ? D * 6 * D * 2 : 0);
#pragma unroll
  for (int i = 0; i < 7; ++i) m.cv[i] = __builtin_amdgcn_raw_buffer_load_b16(cr, (tid >> 2) * 2 + i * 128, 0, 0);
#pragma unroll
  for (int i = 0; i < 7; ++i) m.mw[i] = __builtin_amdgcn_raw_buffer_load_b128(mr, ((tid >> 2) * (6 * D) + q * 32 + (tid & 3) * 8) * 2, i * 64 * 6 * D * 2, PF_AUX);
}
DEV void mod_part(ModLoad& m, char LAS* L, int tid) {
  float sl[7];
#pragma unroll
  for (int i = 0; i < 7; ++i) { const float cc = blo(m.cv[i]); sl[i] = rb(cc * __builtin_amdgcn_rcpf(1.f + __expf(-cc))); pin(sl[i]); }
  v2f a[4];
#pragma unroll
  for (int e = 0; e < 4; ++e) a[e] = v2f{0.f, 0.f};
#pragma unroll
  for (int i = 0; i < 7; ++i)
#pragma unroll
    for (int e = 0; e < 4; ++e) a[e] = fma2(bun(m.mw[i][e]), v2f{sl[i], sl[i]}, a[e]);
#pragma unroll
  for (int e = 0; e < 4; ++e) {
    a[e].x += dpp<0x128>(a[e].x); a[e].y += dpp<0x128>(a[e].y);
    a[e].x += dpp<0x124>(a[e].x); a[e].y += dpp<0x124>(a[e].y);
  }
  if ((tid & 15) < 4) {
    *(LAS v4f*)(L + L_RED + ((tid >> 4) * 32 + (tid & 15) * 8) * 4) = v4f{a[0].x, a[0].y, a[1].x, a[1].y};
    *(LAS v4f*)(L + L_RED + ((tid >> 4) * 32 + (tid & 15) * 8 + 4) * 4) = v4f{a[2].x, a[2].y, a[3].x, a[3].y};
  }
}
DEV void mod_publish(char LAS* L, rsrc_t ws, int q, int lane, u32 tag) {
  v4f s = {0.f, 0.f, 0.f, 0.f};
#pragma unroll
  for (int r = 0; r < 16; ++r) s += lds4f(L, L_RED + (r * 32 + (lane & 7) * 4) * 4);
  const float one = (q / 14 == 1 || q / 14 == 4) ? 1.f : 0.f;
  st4c(ws, WS_MOD + (lane >> 3) * MOD_REP + q * 128 + (lane & 7) * 16,
       v4u{pk(rb(s.x) + one, rb(s.y) + one), tag, pk(rb(s.z) + one, rb(s.w) + one), tag});
}
DEV u32 ld1(rsrc_t r, int off) { return __builtin_amdgcn_raw_buffer_load_b32(r, off, 0, 0); }
DEV u32 mktag(u32 epoch) { return epoch * 0x9E3779B1u + 0x7F4A7C15u; }

DEV void x2_read(const char LAS* L, v4u (&r)[4], int tid) {
#pragma unroll
  for (int m = 0; m < 4; ++m) { const int ch = min(tid + 256 * m, 895), tk = ch / 56; r[m] = lds4(L, L_XS + tk * XP + (ch - tk * 56) * 16); }
}
DEV void x2_store(rsrc_t ws, const v4u (&r)[4], int blk, int tid) {
#pragma unroll
  for (int m = 0; m < 4; ++m) st4c(ws, tid + 256 * m < 896 ? WS_X2 + blk * 14336 + (tid + 256 * m) * 16 : OOB, r[m]);
}
DEV bool flags_ok(v4u rv, u32 tag) { return rv.x == tag && rv.y == ~tag && rv.z == tag && rv.w == ~tag; }
DEV void wait_flags(rsrc_t ws, int off, u32 tag) {
  _Pragma("nounroll") for (int s = 0; s < SPIN; ++s) {
    const v2u rv = ld2c(ws, fresh(off));
    if (wall(rv.x == tag && rv.y == ~tag)) break;
    __builtin_amdgcn_s_sleep(SL1);
  }
}

__global__ void __launch_bounds__(256, 1)
fused(const u16* __restrict__ xin, const u16* __restrict__ wr, const u16* __restrict__ wi, const u16* __restrict__ wg,
      const u16* __restrict__ wu, const u16* __restrict__ wd, const u16* __restrict__ cin, const u16* __restrict__ wm,
      u16* __restrict__ out, char* wsp) {
  __shared__ __attribute__((aligned(16))) char smem[L_SZ];
  char LAS* L = (char LAS*)smem;
  const u32 lbase = (u32)(size_t)L;
  const int tid = threadIdx.x, lane = tid & 63, wave = __builtin_amdgcn_readfirstlane(tid >> 6);
  const int g = lane >> 4, c16 = lane & 15, l16 = lane * 16;
  const int grp = blockIdx.x & 7, j = blockIdx.x >> 3;
  u64 pc;
  asm volatile("s_getpc_b64 %0" : "=s"(pc));
  STAMP(0); CSTAMP(31);
  asm volatile("" :: "s"(xin), "s"(wr), "s"(wi), "s"(wg), "s"(wu), "s"(wd), "s"(cin), "s"(wm), "s"(out), "s"(wsp));
  const rsrc_t ws = mkrs(wsp, WS_END);
  const rsrc_t tab = mkrs(TAB, TAB_N * 4096);
  const rsrc_t code = mkrs((const void*)(pc & ~127ull), CODE_BYTES);
  const bool isR = j >= 14 && j < 30;
  const bool isG = j < 28;
  const int gk = j / 7, ct = j - gk * 7;
  const int tile = 4 * (grp >> 2) + gk, slice = 7 * (grp & 3) + ct;
  const int wdb = WS_WD + (ct * 56 + wave * 14) * 4096;
  const v4u sv = {SENT, SENT, SENT, SENT};
  int lb = L_B + l16;
  pin(lb);
  const char LAS* LB = L + lb - L_B;
  const int pfo = ((j * 4 + wave) * 14 + lane) * 128, pfc = ((j * 4 + wave) * 4 + lane) * 128;
  u32 pfa = ld1(tab, lane < 14 ? pfo : OOB), pfb = ld1(code, lane < 4 ? pfc : OOB);
  v4u x2r[4];
  v4u wdn[14][4];
  u32 epoch, tag;

  if (isR) {
    const int blk = 16 * grp + (j - 14);
    epoch = ld1c(ws, WS_EPOCH);
    const int t0 = wave == 0 ? 0 : 2 * wave - 1;
    v4u cf[8][4];
#define CF_LOAD(q) { const int chn = TAB_P1 + ((wave == 0 && q >= 4) ? 28 : 4 * t0 + q), vo = (q < 5 || wave != 0) ? l16 : OOB; \
      _Pragma("unroll") for (int s = 0; s < 4; ++s) cf[q][s] = ld4(tab, vo + s * 1024, chn * 4096); sb(); }
#pragma unroll
    for (int q = 0; q < X_AFTER; ++q) CF_LOAD(q)
    v2u xv[7];
    {
      const rsrc_t xr = mkrs(xin, S * D * 2);
      const int off = tok_row(blk, 4 * wave + g) * (D * 2) + c16 * 8;
#pragma unroll
      for (int m = 0; m < 7; ++m) xv[m] = ld2(xr, off + m * 128);
    }
    typedef u32 v3u __attribute__((ext_vector_type(3)));
    v2u fa_[2], fb_[2]; v3u fc_[2], fd_[2];
    {
      const rsrc_t rr = mkrs(wr, S * DF * 2), ri = mkrs(wi, S * DF * 2);
      const int fo_ = 16 * wave + c16 < 57 ? 2 * g * 450 + (16 * wave + c16) * 8 : OOB;
#pragma unroll
      for (int hh = 0; hh < 2; ++hh) {
        const int so = blk * 7200 + hh * 3600;
        fa_[hh] = ld2(rr, fo_, so); fb_[hh] = ld2(ri, fo_, so);
        fc_[hh] = __builtin_amdgcn_raw_buffer_load_b96(rr, fo_ + 448, so, 0); fd_[hh] = __builtin_amdgcn_raw_buffer_load_b96(ri, fo_ + 448, so, 0);
      }
    }
    sb();
#pragma unroll
    for (int q = X_AFTER; q < 8; ++q) CF_LOAD(q)
    STAMP(23);
    {
      v2f f[14], s2 = {0.f, 0.f};
#pragma unroll
      for (int m = 0; m < 7; ++m) {
        f[2 * m] = bun(xv[m].x); f[2 * m + 1] = bun(xv[m].y);
        s2 += f[2 * m] + f[2 * m + 1];
        *(LAS v2u*)(L + L_XS + (4 * wave + g) * XP + c16 * 8 + m * 128) = xv[m];
      }
      STAMP(21);
      sb();
      {
        const int q = 16 * wave + c16, o = WS_WT + q * 16384 + blk * 128 + g * 16;
#pragma unroll
        for (int hh = 0; hh < 2; ++hh) {
          const v2u a = fa_[hh], b = fb_[hh]; const v3u c = fc_[hh], d = fd_[hh];
          st4c(ws, q < 57 ? o + hh * 64 : OOB, v4u{__builtin_amdgcn_perm(b.x, a.x, 0x05040100u), __builtin_amdgcn_perm(b.y, a.y, 0x05040100u),
                                                   __builtin_amdgcn_perm(d.x, c.x, 0x07060302u), __builtin_amdgcn_perm(d.y, c.y, 0x07060302u)});
          st4c(ws, q < 56 ? o + hh * 64 + 57 * 16384 : OOB,
               v4u{__builtin_amdgcn_perm(b.x, a.x, 0x07060302u), __builtin_amdgcn_perm(b.y, a.y, 0x07060302u),
                   __builtin_amdgcn_perm(d.y, c.y, 0x05040100u), __builtin_amdgcn_perm(d.z, c.z, 0x05040100u)});
        }
      }
      sb();
      const float mean = sum16(s2.x + s2.y) * (1.f / D);
      v2f q2 = {0.f, 0.f};
#pragma unroll
      for (int m = 0; m < 14; ++m) { const v2f d = f[m] - mean; q2 = fma2(d, d, q2); }
      const float rstd = __builtin_amdgcn_rsqf(sum16(q2.x + q2.y) * (1.f / D) + 1e-5f);
      if (c16 == 0) *(LAS v2f*)(L + L_ST + (4 * wave + g) * 8) = v2f{mean, rstd};
    }
    pin(pfa); pin(pfb);
    epoch = __builtin_amdgcn_readfirstlane(epoch);
    tag = mktag(epoch);
    STAMP(22);
    bar();
    v4f n[4][2];
    const int d0 = 8 * (4 * wave + g);
    const v2f st = *(const LAS v2f*)(L + L_ST + c16 * 8);
    {
      int xr_ = L_XS + c16 * XP + d0 * 2, xb_ = L_XS + c16 * XP + (216 - d0) * 2, x0_ = d0 == 0 ? xr_ : xb_ + 464;
      pin(xr_); pin(xb_); pin(x0_);
      const v4u a = lds4(L + xr_, 0), e = lds4(L + xr_, 448), c = lds4(L + xb_, 0), b = lds4(L + xb_, 448);
      const u32 c0 = *(const LAS u16*)(L + xb_ + 16), b0 = *(const LAS u16*)(L + x0_);
#pragma unroll
      for (int i = 0; i < 8; ++i) {
        n[0][i >> 2][i & 3] = (i & 1) ? bhi(a[i >> 1]) : blo(a[i >> 1]);
        n[3][i >> 2][i & 3] = (i & 1) ? bhi(e[i >> 1]) : blo(e[i >> 1]);
        n[1][i >> 2][i & 3] = i == 0 ? blo(b0) : (i & 1) ? bhi(b[(8 - i) >> 1]) : blo(b[(8 - i) >> 1]);
        n[2][i >> 2][i & 3] = i == 0 ? blo(c0) : (i & 1) ? bhi(c[(8 - i) >> 1]) : blo(c[(8 - i) >> 1]);
      }
      const float nm = -st.x * st.y;
#pragma unroll
      for (int m = 0; m < 4; ++m) {
#pragma unroll
        for (int h = 0; h < 2; ++h) { n[m][h] = fma4(n[m][h], splat(st.y), splat(nm)); pin(n[m][h]); }
      }
    }
    sb();
    STAMP(1);
    int mb = L_M1 + d0 * 4;
    pin(mb);
    v4u xs[4];
    int to[4];
    to[0] = WS_T1 + blk * T1_BLK + (16 * t0 + c16) * 64 + g * 16;
    to[1] = wave != 0 ? to[0] + 1024 : c16 < 2 ? to[0] + 112 * 64 : OOB;
    to[2] = to[0] + 114 * 64;
    to[3] = wave != 0 ? to[0] + 1024 + 114 * 64 : OOB;
#pragma unroll
    for (int q = 0; q < 4; ++q) pin(to[q]);

    {
      const int kind = tid >= 112, d = 4 * (tid - 112 * kind);
      int wb = L_M1 + (kind ? 0 : 2 * 1792) + d * 4, wrv = L_M1 + (kind ? 0 : 2 * 1792) + 1792 + (445 - d) * 4;
      int wr0 = d == 0 ? wrv + 12 - 1792 : wrv + 12;
      const int mo = tid < 224 ? WS_MOD + grp * MOD_REP + tid * 16 : OOB;
      pin(wb); pin(wrv); pin(wr0);
      const int ro = WS_RDY + grp * 1024 + min(tid >> 1, 112) * 8;
      v4u mv;
      _Pragma("nounroll") for (int s = 0; s < SPIN; ++s) {
        mv = ld4c(ws, fresh(mo));
        const v2u rv = ld2c(ws, fresh(ro));
        if (wall((tid >= 224 || (mv.y == tag && mv.w == tag)) && rv.x == tag && rv.y == ~tag)) break;
        __builtin_amdgcn_s_sleep(SL1);
      }
      STAMP(2);
      if (tid < 224) {
        const v4f v = {blo(mv.x), bhi(mv.x), blo(mv.z), bhi(mv.z)};
        *(LAS v4f*)(L + wb) = v;
        *(LAS float*)(L + wr0) = v.x;
        *(LAS float*)(L + wrv + 8) = v.y;
        *(LAS float*)(L + wrv + 4) = v.z;
        *(LAS float*)(L + wrv) = v.w;
      }
    }
    bar();
    {
      v4u fr[4];
#pragma unroll
      for (int hh = 0; hh < 2; ++hh) {
        v4f h[4];
#pragma unroll
        for (int m = 0; m < 4; ++m) {
          const int o = ((m == 1 || m == 2) ? 1792 : 0) + (m >= 2 ? 896 : 0) + hh * 16;
          h[m] = fma4(n[m][hh], lds4f(L + mb, o), lds4f(L + mb, 2 * 1792 + o));
        }
        const v4f p = h[0] + h[1], q = h[2] + h[3], u = h[0] - h[1], v = h[3] - h[2];
        const v4f t[4] = {p + q, p - q, u + v, u - v};
#pragma unroll
        for (int m = 0; m < 4; ++m) { fr[m][2 * hh] = pk2(lo2(t[m])); fr[m][2 * hh + 1] = pk2(hi2(t[m])); }
      }
#pragma unroll
      for (int m = 0; m < 4; ++m) *(LAS v4u*)(LB + wave * 1024 + L_AF + m * 4096) = fr[m];
    }
    bar();
    {
      v4u a[4][4];
#pragma unroll
      for (int p = 0; p < 2; ++p)
#pragma unroll
        for (int s = 0; s < 4; ++s) { a[p][s] = lds4(LB, L_AF + (p * 4 + s) * 1024); a[2 + p][s] = lds4(LB, L_AF + ((2 + p) * 4 + s) * 1024); }
      v4f re[4], im[4];
#pragma unroll
      for (int q = 0; q < 5; ++q) {
        if (q < 4) mf8(re[q], im[q], a[q >> 1], cf[4 * (q & 1) + (q >> 1)], a[2 + (q >> 1)], cf[4 * (q & 1) + 2 + (q >> 1)]);
        if (q > 0) {
          if (q < 4) mfence<0>(re[q - 1], im[q - 1]); else mfence<7>(re[3], im[3]);
          xs[q - 1] = v4u{pk(re[q - 1].x, im[q - 1].x), pk(re[q - 1].y, im[q - 1].y), pk(re[q - 1].z, im[q - 1].z), pk(re[q - 1].w, im[q - 1].w)};
          st4c(ws, to[q - 1], xs[q - 1]);
          sb();
        }
      }
    }
    STAMP(3);
    sb();
    sb();
    if (isG) dma_w1(wg, wu, slice, wave, lane, lbase);
#pragma unroll
    for (int q = 0; q < 4; ++q) {
      const int vo = (q < 2 || wave != 0) ? l16 : OOB;
#pragma unroll
      for (int s = 0; s < 4; ++s) cf[2 * q + 1][s] = ld4(tab, vo + s * 1024, (TAB_P3 + 2 * t0 + q) * 4096);
    }
#pragma unroll
    for (int m = 0; m < 4; ++m)
      st4c(ws, tid + 256 * m < 896 ? WS_H2 + blk * 14336 + 4096 * m + tid * 16 : OOB, sv);
    sb();
    STAMP(20);
    v4f xo[2][4];
    int xa = L_XS + c16 * XP + (16 * t0 + 4 * g) * 2, xd = L_XS + c16 * XP + (192 - 16 * t0 - 4 * g) * 2;
    int xs1 = tid < 16 ? L_XS + c16 * XP + 224 : xd + 512, xs3 = tid < 16 ? L_XS + c16 * XP + 672 : xa + 448;
    pin(xa); pin(xd); pin(xs1); pin(xs3);
#define XOFF(t, m, r) ((t) == 0 && (r) == 0 && (m) == 1 ? xs1 : (t) == 0 && (r) == 0 && (m) == 3 ? xs3 : \
    (m) == 0 ? xa + 32 * (t) + 2 * (r) : (m) == 3 ? xa + 448 + 32 * (t) + 2 * (r) : (m) == 1 ? xd + 512 - 32 * (t) - 2 * (r) : xd + 64 - 32 * (t) - 2 * (r))
    {
      v2u w[2][4]; u32 e[2][2], e3;
#pragma unroll
      for (int t = 0; t < 2; ++t) {
        w[t][0] = *(const LAS v2u*)(L + xa + 32 * t); w[t][3] = *(const LAS v2u*)(L + xa + 448 + 32 * t);
        w[t][1] = *(const LAS v2u*)(L + xd + 504 - 32 * t); w[t][2] = *(const LAS v2u*)(L + xd + 56 - 32 * t);
        e[t][0] = *(const LAS u16*)(L + XOFF(t, 1, 0)); e[t][1] = *(const LAS u16*)(L + XOFF(t, 2, 0));
      }
      e3 = *(const LAS u16*)(L + xs3);
      sb();
#pragma unroll
      for (int t = 0; t < 2; ++t) {
        xo[t][0] = v4f{blo(w[t][0].x), bhi(w[t][0].x), blo(w[t][0].y), bhi(w[t][0].y)};
        xo[t][3] = v4f{t == 0 ? blo(e3) : blo(w[t][3].x), bhi(w[t][3].x), blo(w[t][3].y), bhi(w[t][3].y)};
        xo[t][1] = v4f{blo(e[t][0]), bhi(w[t][1].y), blo(w[t][1].y), bhi(w[t][1].x)};
        xo[t][2] = v4f{blo(e[t][1]), bhi(w[t][2].y), blo(w[t][2].y), bhi(w[t][2].x)};
#pragma unroll
        for (int m = 0; m < 4; ++m) { xo[t][m] -= st.x; pin(xo[t][m]); }
      }
    }
    int m0_[2], m1_[2], md_[2];
#pragma unroll
    for (int r = 0; r < 2; ++r) {
      const int q = r == 0 || tid < 80 ? tid + 256 * r : tid;
      const int kind = (q * 586) >> 16, d = 4 * (q - 112 * kind);
      const int reg = (d >= 112) + (d >= 224) + (d >= 336), cc = ((reg + 1) >> 1) * 224, m1 = (0x1320 >> (4 * reg)) & 3;
      const int d1 = abs(d + 1 - cc), d0_ = abs(d - cc), edge = d == 112 * reg && reg > 0;
      m1_[r] = L_M2 + kind * 1792 + (((d1 >> 4) << 6) | (m1 << 4) | (d1 & 15)) * 4;
      m0_[r] = L_M2 + kind * 1792 + (edge ? reg << 4 : ((d0_ >> 4) << 6) | (m1 << 4) | (d0_ & 15)) * 4;
      md_[r] = (reg & 1) ? -4 : 4;
      pin(m0_[r]); pin(m1_[r]); pin(md_[r]);
    }
    {
      v4u mv[2];
      _Pragma("nounroll") for (int s = 0; s < SPIN; ++s) {
        mv[0] = ld4c(ws, fresh(WS_MOD + grp * MOD_REP + (224 + tid) * 16));
        mv[1] = ld4c(ws, fresh(WS_MOD + grp * MOD_REP + (224 + (tid < 80 ? 256 + tid : tid)) * 16));
        const v2u rv = ld2c(ws, fresh(WS_DRDY + grp * 1024 + min(lane, 48) * 8));
        if (wall(mv[0].y == tag && mv[0].w == tag && mv[1].y == tag && mv[1].w == tag && rv.x == tag && rv.y == ~tag)) break;
        __builtin_amdgcn_s_sleep(SL4);
      }
      if (lane == 0) st2c(ws, WS_HRDY + blk * 32 + wave * 8, v2u{tag, ~tag});
      STAMP(12);
#pragma unroll
      for (int f = 0; f < WD_EARLY; ++f) wdn[f >> 2][f & 3] = ld4c(ws, (isG ? l16 : OOB) + (f & 3) * 1024, wdb + (f >> 2) * 4096);
      sb();
#pragma unroll
      for (int r = 0; r < 2; ++r) {
        const float sc = m0_[r] < L_M2 + 1792 ? 2.f / (float)(S * D) : 1.f;
        *(LAS float*)(L + m0_[r]) = blo(mv[r].x) * sc;
        *(LAS float*)(L + m1_[r]) = bhi(mv[r].x) * sc;
        *(LAS float*)(L + m1_[r] + md_[r]) = blo(mv[r].z) * sc;
        *(LAS float*)(L + m1_[r] + 2 * md_[r]) = bhi(mv[r].z) * sc;
      }
    }
    bar();
    *(LAS v4u*)(L + L_AF + (tid >> 6) * 4096 + 3072 + l16) = v4u{0u, 0u, 0u, 0u};
    STAMP(4);
    int zr = L_AF + lane * 8, ro = L_M2 + (t0 * 64 + 4 * g) * 4, ro1 = wave == 0 ? ro : ro + 256, ex = L_EX + c16 * 8;
    pin(zr); pin(ro); pin(ro1); pin(ex);
    {
      int zl[4];
#pragma unroll
      for (int m = 0; m < 4; ++m) {
        const int kq = (m >> 1) * 128 + (to[m] == OOB ? 127 : wave == 0 && m == 1 ? 112 + c16 : 16 * (t0 + (m & 1)) + c16);
        zl[m] = L_AF + ((kq & ~31) + ((kq & 4) << 2) + ((kq & 24) >> 1) + (kq & 3)) * 32 + g * 8;
        pin(zl[m]);
        if (to[m] == OOB) xs[m] = sv;
      }
      bar();
      if (isG) wait_flags(ws, WS_HRDY + (16 * tile + 4 * wave) * 32 + c16 * 8, tag);
      {
        const int src = (blk * 5 + wave * 13) & 63;
        const int po = __builtin_amdgcn_readlane(to[(0)], 0) + (src & 15) * 64 + (src >> 4) * 16;
        const u32 old = __builtin_amdgcn_ds_bpermute(src * 4, xs[0].x);
        _Pragma("nounroll") for (int s = 0; s < SPIN; ++s) {
          if (wall(ld1c(ws, fresh(po)) != old)) break;
          __builtin_amdgcn_s_sleep(SL2);
        }
      }
      STAMP(13);
      v4u z[4];
      _Pragma("nounroll") for (int s = 0; s < SPIN; ++s) {
        u32 mn = 1;
#pragma unroll
        for (int m = 0; m < 4; ++m) z[m] = ld4c(ws, fresh(to[m]));
#pragma unroll
        for (int m = 0; m < 4; ++m) { const v4u d = z[m] ^ xs[m]; mn = min(min(mn, d.x), min(min(d.y, d.z), d.w)); }
        if (wall(mn != 0)) break;
      }
      STAMP(14);
#pragma unroll
      for (int m = 0; m < 4; ++m) {
        char LAS* p = L + zl[m];
        *(LAS v2u*)(p) = v2u{__builtin_amdgcn_perm(z[m].y, z[m].x, 0x05040100u), __builtin_amdgcn_perm(z[m].w, z[m].z, 0x05040100u)};
        *(LAS v2u*)(p + ZPL) = v2u{__builtin_amdgcn_perm(z[m].y, z[m].x, 0x07060302u), __builtin_amdgcn_perm(z[m].w, z[m].z, 0x07060302u)};
      }
    }
    bar();
    STAMP(5);
    {
      v4u zf[4][4];
#pragma unroll
      for (int ty = 0; ty < 4; ++ty)
#pragma unroll
        for (int s = 0; s < 4; ++s) zf[ty][s] = ldtr2(L + zr, ty * 4096 + s * 1024, ty * 4096 + s * 1024 + 512);
      v4f acc[2][4], x2[2][4], gav[2][4];
      v2f s1 = {0.f, 0.f}, s2 = {0.f, 0.f};
#define P3M(t, ty) mf8(acc[t][ty], acc[t][ty + 1], cf[4 * t + ty], zf[ty], cf[4 * t + ty + 1], zf[ty + 1]);
#define P3X(t, m, yv) { x2[t][m] = fma4(gav[t][m], yv, xo[t][m]); \
      if (t == 0 || wave != 0) { s1 += lo2(x2[t][m]) + hi2(x2[t][m]); \
        s2 = fma2(lo2(x2[t][m]), lo2(x2[t][m]), s2); s2 = fma2(hi2(x2[t][m]), hi2(x2[t][m]), s2); } }
      P3M(1, 0) P3M(1, 2)
#pragma unroll
      for (int m = 0; m < 4; ++m) { gav[0][m] = lds4f(L + ro, m * 64); gav[1][m] = lds4f(L + ro1, m * 64); }
      sb();
      P3M(0, 0) P3M(0, 2)
      mfence<0>(acc[1][0], acc[1][1], acc[1][2], acc[1][3]);
      const v4f A1 = acc[1][0] + acc[1][1], B1 = acc[1][0] - acc[1][1], P1 = acc[1][3] - acc[1][2], Q1 = acc[1][2] + acc[1][3];
      P3X(1, 0, A1 - P1) P3X(1, 1, A1 + P1) P3X(1, 2, B1 - Q1) P3X(1, 3, B1 + Q1)
      sb();
      mfence<0>(acc[0][0], acc[0][1], acc[0][2], acc[0][3]);
      {
        const v4f A = acc[0][0] + acc[0][1], B = acc[0][0] - acc[0][1], P = acc[0][3] - acc[0][2], Q = acc[0][2] + acc[0][3];
        v4f y[4] = {A - P, A + P, B - Q, B + Q};
        const bool sp = tid < 16;
        y[0].x = sp ? A.x : y[0].x; y[2].x = sp ? B.x : y[2].x;
        y[1].x = sp ? acc[1][0].x - acc[0][3].x : y[1].x; y[3].x = sp ? acc[1][0].x + acc[0][3].x : y[3].x;
        P3X(0, 0, y[0]) P3X(0, 1, y[1]) P3X(0, 2, y[2]) P3X(0, 3, y[3])
      }
      float sm = s1.x + s1.y, sq = s2.x + s2.y;
      sumg2(sm, sq);
      if (g == 0) *(LAS v2f*)(L + ex + wave * 128) = v2f{sm, sq};
      v4f sbv[2][4], cbv[2][4];
#pragma unroll
      for (int m = 0; m < 4; ++m) {
        sbv[0][m] = lds4f(L + ro, 1792 + m * 64); cbv[0][m] = lds4f(L + ro, 2 * 1792 + m * 64);
        sbv[1][m] = lds4f(L + ro1, 1792 + m * 64); cbv[1][m] = lds4f(L + ro1, 2 * 1792 + m * 64);
      }
      bar();
      v2f e = *(const LAS v2f*)(L + ex);
#pragma unroll
      for (int w = 1; w < 4; ++w) e += *(const LAS v2f*)(L + ex + w * 128);
      const float mc = e.x * (1.f / D), rstd = __builtin_amdgcn_rsqf(fmaxf(e.y * (1.f / D) - mc * mc, 0.f) + 1e-5f), nb = -mc * rstd;
      const int ho = WS_H2 + blk * 14336 + 2 * t0 * 1024 + l16, ho1 = wave == 0 ? OOB : ho + 2048;
#pragma unroll
      for (int t = 0; t < 2; ++t)
#pragma unroll
        for (int a = 0; a < 2; ++a) {
          v4u o;
#pragma unroll
          for (int hh = 0; hh < 2; ++hh) {
            const int m = 2 * a + hh;
            const v4f hv = fma4(fma4(x2[t][m], splat(rstd), splat(nb)), cbv[t][m], sbv[t][m]);
            o[2 * hh] = pk2(lo2(hv)); o[2 * hh + 1] = pk2(hi2(hv));
          }
          st4c(ws, (t ? ho1 : ho) + a * 1024, o);
        }
      STAMP(6);
      sb();
#pragma unroll
      for (int f = WD_EARLY; f < WD_MID; ++f) wdn[f >> 2][f & 3] = ld4c(ws, (isG ? l16 : OOB) + (f & 3) * 1024, wdb + (f >> 2) * 4096);
      sb();
#pragma unroll
      for (int t = 0; t < 2; ++t)
        if (t == 0 || wave != 0)
#pragma unroll
          for (int m = 0; m < 4; ++m)
#pragma unroll
            for (int r = 0; r < 4; r += 2) {
              const u32 p = pk(x2[t][m][r] + st.x, x2[t][m][r + 1] + st.x);
              *(LAS u16*)(L + XOFF(t, m, r)) = (u16)p;
              *(LAS u16*)(L + XOFF(t, m, r + 1)) = (u16)(p >> 16);
            }
      sb();
    }
    bar();
    x2_read(L, x2r, tid);
    bar();
    if (!isG) {
      x2_store(ws, x2r, blk, tid);
      drain();
      if (lane == 0) st2c(ws, WS_XRDY + blk * 64 + wave * 8, v2u{tag, ~tag});
      DUMP(); return;
    }
  } else {
    const int sj = j >= 30 ? j - 30 : j + 2;
    const int idx = sj * 8 + grp;
    const bool isC = j < 14 || idx == 0;
    const int pair = j < 14 ? grp * 14 + j : 112;
    const int lc = grp + 8 * (sj >> 1);
    const int qu = sj < 4 && lc < 14 ? 2 * lc + (sj & 1) : -1;
    const int t1o = WS_T1 + (32 * wave + (lane >> 3)) * T1_BLK + pair * 128 + (lane & 7) * 16;
    ModLoad ml;
    epoch = ld1c(ws, WS_EPOCH);
    if (qu >= 0) mod_load(ml, cin, wm, qu, tid);
#pragma unroll
    for (int q = 0; q < 4; ++q) st4c(ws, isC ? t1o + 8 * q * T1_BLK : OOB, sv);
    u32 pf0, pf1, pf2;
    {
      const rsrc_t mr = mkrs(wm, D * 6 * D * 2), xr = mkrs(xin, S * D * 2), fr_ = mkrs((wave & 1) ? wi : wr, S * DF * 2);
      const int row = (sj * 4 + wave) * 7 + (lane >> 1), l7 = lane / 7;
      pf0 = __builtin_amdgcn_raw_buffer_load_b32(mr, lane < 14 && (grp < 6 || !(lane & 1)) ? row * (6 * D * 2) + (grp + 8 * (lane & 1)) * 128 : OOB, 0, PF_AUX);
      pf1 = __builtin_amdgcn_raw_buffer_load_b32(xr, lane < 28 ? tok_row(16 * grp + sj, 4 * wave + l7) * (D * 2) + (lane - 7 * l7) * 128 : OOB, 0, PF_AUX);
    }
    sb();
    epoch = __builtin_amdgcn_readfirstlane(epoch);
    tag = mktag(epoch);
    if (qu >= 0) mod_part(ml, L, tid);
    asm volatile("s_waitcnt vmcnt(%0)" :: "i"(W_LATE ? 2 : 3) : "memory");
    { const rsrc_t fr_ = mkrs((wave & 1) ? wi : wr, S * DF * 2);
      pf2 = __builtin_amdgcn_raw_buffer_load_b32(fr_, lane < 29 ? (((16 * grp + sj) * 7200) & ~127) + ((wave >> 1) * 29 + lane) * 128 : OOB, 0, PF_AUX);
    }
    bar();
    if (wave == 0 && qu >= 0) mod_publish(L, ws, qu, lane, tag);
    if (wave == 1 && lane < 8) st2c(ws, isC ? WS_RDY + lane * 1024 + pair * 8 : OOB, v2u{tag, ~tag});
    STAMP(1);
    pin(pf0); pin(pf1); pin(pf2); pin(pfa); pin(pfb);
    const int qn = sj >= 4 && sj < 10 && grp < 7 ? 28 + grp * 6 + (sj - 4) : !isC && idx < 15 ? 69 + idx : -1;
    const int tw = sj >= 10 ? (sj - 10) * 8 + grp : sj == 4 && grp == 7 ? 48 : -1;
    v2u dw[2][8];
    if (qn >= 0) mod_load(ml, cin, wm, qn, tid);
    if (tw >= 0) {
      const rsrc_t dr = mkrs(wd, F * D * 2);
      const int u0 = tw * 4 + wave, uc = u0 / 28, kap = 2 * (u0 - uc * 28);
#pragma unroll
      for (int e = 0; e < 16; ++e)
        dw[e >> 3][e & 7] = __builtin_amdgcn_raw_buffer_load_b64(dr, (4 * g * D + 4 * c16) * 2 + (e & 3) * D * 2, ((32 * kap + 16 * (e >> 2)) * D + 64 * uc) * 2, PF_AUX);
    }
    sb();
    u32 pw[4];
    {
      const rsrc_t rg = mkrs(wg, D * F * 2), ru = mkrs(wu, D * F * 2);
      const int r7 = lane / 7, r0 = sj < 4 ? (sj * 4 + wave) * 16 : 256 + ((sj - 4) * 4 + wave) * 4;
      const int po = (r0 + r7) * (F * 2) + ((grp & 3) * 7 + lane - r7 * 7) * 128;
      const int o0 = lane < (sj < 4 ? 56 : 28) ? po : OOB, o1 = lane < 56 && sj < 4 ? po + 8 * (F * 2) : OOB;
      pw[0] = ld1(rg, o0); pw[1] = ld1(ru, o0); pw[2] = ld1(rg, o1); pw[3] = ld1(ru, o1);
    }
    sb();
    STAMP(20);
    v4f t1c[2], t1s[2], t2c[2], t2s[2];
#pragma unroll
    for (int t = 0; t < 2; ++t) {
#pragma unroll
      for (int r = 0; r < 4; ++r) {
        const float a1 = (float)((16 * t + 4 * g + r) * (16 * wave + c16)) * (1.f / S);
        const float a2 = (float)((16 * t + c16) * (16 * wave + 4 * g + r)) * (1.f / S);
        t1c[t][r] = __builtin_amdgcn_cosf(a1); t1s[t][r] = __builtin_amdgcn_sinf(a1);
        t2c[t][r] = __builtin_amdgcn_cosf(a2); t2s[t][r] = __builtin_amdgcn_sinf(a2);
      }
      pin(t1c[t]); pin(t1s[t]); pin(t2c[t]); pin(t2s[t]);
    }
    if (tw >= 0) {
#pragma unroll
      for (int tt = 0; tt < 8; ++tt) {
        v4u o;
#pragma unroll
        for (int m = 0; m < 4; ++m)
          o[m] = __builtin_amdgcn_perm(dw[tt >> 2][2 * m + 1][(tt & 3) >> 1], dw[tt >> 2][2 * m][(tt & 3) >> 1], (tt & 1) ? 0x07060302u : 0x05040100u);
        st4c(ws, WS_WD + (tw * 4 + wave) * 8192 + tt * 1024 + l16, o);
      }
    }
    if (qn >= 0) {
      bar();
      mod_part(ml, L, tid);
      bar();
      if (wave == 0) mod_publish(L, ws, qn, lane, tag);
    }
    pin(pw[0]); pin(pw[1]); pin(pw[2]); pin(pw[3]);
    STAMP(21);
    if (!isC) { DUMP(); return; }
    v4u c64[2][4], c32[2][2][2];
#pragma unroll
    for (int cp = 0; cp < 2; ++cp)
#pragma unroll
      for (int s = 0; s < 4; ++s) c64[cp][s] = ld4(tab, l16 + s * 1024, (TAB_P2 + wave * 2 + cp) * 4096);
#pragma unroll
    for (int t = 0; t < 2; ++t)
#pragma unroll
      for (int f = 0; f < 4; ++f) c32[t][f >> 1][f & 1] = ld4(tab, l16 + f * 1024, (TAB_P2 + 8 + t) * 4096);
    sb();
    if (tw >= 0) {
      asm volatile("s_waitcnt vmcnt(16)" ::: "memory");
      bar();
      if (tid < 8) st2c(ws, WS_DRDY + tid * 1024 + tw * 8, v2u{tag, ~tag});
    }
    STAMP(2);
    int wfo = WS_WT + pair * 16384 + (16 * g + wave) * 128 + c16 * 8;
    pin(wfo);
    v2u wf[2][4];
    {
      int wl = L_CI + ((lane >> 2) & 1) * 8256 + wave * 2048 + ((lane >> 3) * 4 + (lane & 3)) * 16, lo = t1o;
      pin(wl); pin(lo);
      light_poll(ws, WS_T1 + (32 * wave + (pair & 31)) * T1_BLK + pair * 128);
      STAMP(13);
      v4u z[4];
      _Pragma("nounroll") for (int s = 0; s < SPIN; ++s) {
        lo = fresh(lo);
#pragma unroll
        for (int q = 0; q < 4; ++q) z[q] = ld4c(ws, lo, 8 * q * T1_BLK);
        if (wall(max(max(max4(z[0]), max4(z[1])), max(max4(z[2]), max4(z[3]))) != SENT)) break;
      }
      STAMP(14);
#pragma unroll
      for (int q = 0; q < 4; ++q) *(LAS v4u*)(L + wl + q * 512) = z[q];
    }
    bar();
#pragma unroll
    for (int t = 0; t < 2; ++t)
#pragma unroll
      for (int r = 0; r < 4; ++r) wf[t][r] = ld2c(ws, wfo + r * 512, t * 8192);
    v2u fd = ld2c(ws, WS_DRDY + grp * 1024 + min(lane, 48) * 8);
    pfa = ld1(code, lane < 7 ? ((j * 4 + wave) * 7 + lane) * 128 : OOB);
    sb();
    STAMP(4);
    {
      v4u a[2][2][4];
#pragma unroll
      for (int c = 0; c < 2; ++c)
#pragma unroll
        for (int mt = 0; mt < 2; ++mt)
#pragma unroll
          for (int s = 0; s < 4; ++s) a[c][mt][s] = lds4(LB, L_CI + c * 8256 + (s * 2 + mt) * 1024);
      v4u old[2][2];
#pragma unroll
      for (int c = 0; c < 2; ++c)
#pragma unroll
        for (int mt = 0; mt < 2; ++mt) old[c][mt] = lds4(LB + wave * 2048, L_CI + c * 8256 + mt * 1024);
      v4f re[2][2], im[2][2], ur[2][2], ui[2][2], fr_[2][2], fi_[2][2];
      u32 f2[2][2][4], f3[2][2][4], f4[4];
      char LAS* xw = L + lb + wave * 1024 - L_B;
#define V4(x) v4u{x[0], x[1], x[2], x[3]}
#define S1K(h) c64[0][2 * h], c64[0][2 * h + 1], c64[1][2 * h], c64[1][2 * h + 1]
#define S1X(c, t, h) re[c][t], im[c][t], a[c][t][2 * h], a[c][t][2 * h + 1], S1K(h)
#define S2X(c, t) ur[c][t], ui[c][t], V4(f2[c][0]), V4(f2[c][1]), c32[t][0][0], c32[t][0][1], c32[t][1][0], c32[t][1][1]
#define S3X(c, t) im[c][t], re[c][t], V4(f3[c][0]), V4(f3[c][1]), c32[t][0][0], c32[t][0][1], c32[t][1][0], c32[t][1][1]
#define CM(o, P, Q, C, S_, h) o[2 * h], o[2 * h + 1], P[2 * h], Q[2 * h], C[2 * h], S_[2 * h], P[2 * h + 1], Q[2 * h + 1], C[2 * h + 1], S_[2 * h + 1]
#define TW1(c, t, h) CM(f2[c][t], re[c][t], im[c][t], t1c[t], t1s[t], h)
#define FLT(c, t, h) CM(f3[c][t], ur[c][t], ui[c][t], fr_[c][t], fi_[c][t], h)
#define TW2(c, t, h) CM(f4, re[c][t], im[c][t], t2c[t], t2s[t], h)
#define HV(x, h) (h ? hi2(x) : lo2(x))
#define CP(PL, o, P, Q, C, S_, h) cmul2<PL>(o[2 * h], o[2 * h + 1], HV(P, h), HV(Q, h), HV(C, h), HV(S_, h));
#define XW(c, t) *(LAS v4u*)(xw + L_CX + (c * 2 + t) * 4096) = V4(f4);
#define UNP(c, t) { _Pragma("unroll") for (int r = 0; r < 4; ++r) { fr_[c][t][r] = blo(wf[t][r][c]); fi_[c][t][r] = bhi(wf[t][r][c]); } \
      pin(fr_[c][t]); pin(fi_[c][t]); }
      nop2();
      m4_a0(S1X(0, 0, 0)); m4_a1(S1X(0, 0, 1)); m4_a0(S1X(0, 1, 0)); mfence<3>(re[0][0], im[0][0]);
      h4_a1m(S1X(0, 1, 1), TW1(0, 0, 0)); h4_a0m(S1X(1, 0, 0), TW1(0, 0, 1));
      h4_a1m(S1X(1, 0, 1), TW1(0, 1, 0)); h4_a0m(S1X(1, 1, 0), TW1(0, 1, 1));
      h4_a1m(S1X(1, 1, 1), TW1(1, 0, 0)); h4_b0m(S2X(0, 0), TW1(1, 0, 1));
      h4_b0m(S2X(0, 1), TW1(1, 1, 0));
      sb();
      CP(false, f2[1][1], re[1][1], im[1][1], t1c[1], t1s[1], 1)
      UNP(0, 0) UNP(0, 1)
      sb();
      h4_b0p(S2X(1, 0), FLT(0, 0, 0)); h4_b0p(S2X(1, 1), FLT(0, 0, 1));
      sb();
      CP(true, f3[0][1], ur[0][1], ui[0][1], fr_[0][1], fi_[0][1], 0) CP(true, f3[0][1], ur[0][1], ui[0][1], fr_[0][1], fi_[0][1], 1)
      UNP(1, 0) UNP(1, 1)
      sb();
      h4_a0p(S3X(0, 0), FLT(1, 0, 0)); h4_a0p(S3X(0, 1), FLT(1, 0, 1));
      sb();
      CP(true, f3[1][1], ur[1][1], ui[1][1], fr_[1][1], fi_[1][1], 0) CP(true, f3[1][1], ur[1][1], ui[1][1], fr_[1][1], fi_[1][1], 1)
#pragma unroll
      for (int e = 0; e < 4; ++e) pin(f3[1][1][e]);
      nop2();
      h4_a0p(S3X(1, 0), TW2(0, 0, 0)); h4_a0p(S3X(1, 1), TW2(0, 0, 1));
      XW(0, 0)
      sb();
      CP(true, f4, re[0][1], im[0][1], t2c[1], t2s[1], 0) CP(true, f4, re[0][1], im[0][1], t2c[1], t2s[1], 1) XW(0, 1)
      sb();
      mfence<0>(re[1][0], im[1][0], re[1][1], im[1][1]);
      CP(true, f4, re[1][0], im[1][0], t2c[0], t2s[0], 0) CP(true, f4, re[1][0], im[1][0], t2c[0], t2s[0], 1) XW(1, 0)
      CP(true, f4, re[1][1], im[1][1], t2c[1], t2s[1], 0) CP(true, f4, re[1][1], im[1][1], t2c[1], t2s[1], 1) XW(1, 1)
      bar();
#pragma unroll
      for (int c = 0; c < 2; ++c)
#pragma unroll
        for (int nt = 0; nt < 2; ++nt)
#pragma unroll
          for (int s = 0; s < 4; ++s) a[c][nt][s] = lds4(LB, L_CX + ((c * 2 + nt) * 4 + s) * 1024);
      const int o2 = WS_T1 + (32 * wave + (lane >> 2)) * T1_BLK + pair * 128 + (lane & 3) * 16;
#define S4(c, t) mf8(im[c][t], re[c][t], c64[0], a[c][t], c64[1], a[c][t]);
#define NEQ(x, y) ((x) == (y) ? (x) ^ 1u : (x))
#define OUT(c, t) { const v4u o = {pk(re[c][t].x, im[c][t].x), pk(re[c][t].y, im[c][t].y), pk(re[c][t].z, im[c][t].z), pk(re[c][t].w, im[c][t].w)}; \
      st4c(ws, o2 + c * 64 + t * 16 * T1_BLK, v4u{NEQ(o.x, old[c][t].x), NEQ(o.y, old[c][t].y), NEQ(o.z, old[c][t].z), NEQ(o.w, old[c][t].w)}); sb(); }
      S4(0, 0) S4(0, 1) mfence<0>(re[0][0], im[0][0]); OUT(0, 0)
      S4(1, 0) mfence<0>(re[0][1], im[0][1]); OUT(0, 1)
      S4(1, 1) mfence<0>(re[1][0], im[1][0]); OUT(1, 0)
      mfence<0>(re[1][1], im[1][1]); OUT(1, 1)
    }
    STAMP(5);
    sb();
    if (!isG) { DUMP(); return; }
    if (!wall(fd.x == tag && fd.y == ~tag)) wait_flags(ws, WS_DRDY + grp * 1024 + min(lane, 48) * 8, tag);
    pin(pfa);
    dma_w1(wg, wu, slice, wave, lane, lbase);
#pragma unroll
    for (int f = 0; f < WD_MID; ++f) wdn[f >> 2][f & 3] = ld4c(ws, l16 + (f & 3) * 1024, wdb + (f >> 2) * 4096);
    pfa = ld1(code, lane < 7 ? ((j * 4 + wave) * 7 + lane) * 128 : OOB);
    fd = ld2c(ws, WS_HRDY + (16 * tile + 4 * wave) * 32 + c16 * 8);
    sb();
#pragma unroll
    for (int m = 0; m < 16; ++m)
      st4c(ws, WS_ACT + (16 * (tile + 2 * (m >> 3)) + 4 * (grp & 3)) * 57344 + ct * 32768 + (m & 7) * 4096 + tid * 16, sv);
    sb();
    if (!wall(fd.x == tag && fd.y == ~tag)) wait_flags(ws, WS_HRDY + (16 * tile + 4 * wave) * 32 + c16 * 8, tag);
    pin(pfa);
    bar();
  }

  STAMP(7);
  const int blk0 = 16 * tile + 4 * (grp & 3);
  const int ab = WS_ACT + blk0 * 57344 + wave * 14 * 1024;
  v4u h[14][2];
  {
    const int hb = WS_H2 + (16 * tile + 4 * wave) * 14336;
    int tb[2][4];
#pragma unroll
    for (int u = 0; u < 8; ++u) {
      tb[u >> 2][u & 3] = (((lane >> 2) * 128 + ((lane >> 3) & 3) * 32 + (lane & 3) * 8) ^ ((u & 3) << 5)) + (u >> 2) * 57344;
      pin(tb[u >> 2][u & 3]);
    }
#define W_RD(dst, k, u) dst = ldtr2(L + tb[k >= 7][u & 3], (k % 7) * 8192 + (u >> 2) * 4096, (k % 7) * 8192 + (u >> 2) * 4096 + 2048)
#define H_LD(v, tt) hh_[v][tt] = ld4c(ws, l16 + (((v) % 14) & 3) * 1024, hb + (2 * ((v) / 14) + (tt)) * 14336 + (((v) % 14) >> 2) * 4096)
#define G_LD(e) h[(e) >> 1][(e) & 1] = ld4c(ws, l16 + (((e) >> 1) & 3) * 1024, ab + ((e) & 1) * 57344 + ((e) >> 3) * 4096)
    v4u wfr[2][8], hh_[28][2];
#pragma unroll
    for (int u = 0; u < 8; ++u) W_RD(wfr[0][u], 0, u);
    light_poll(ws, hb + (lane & 1) * 14336 + 13 * 1024);
    STAMP(15);
#pragma unroll
    for (int v = 0; v < LEAD; ++v) { H_LD(v, 0); H_LD(v, 1); }
    sb();
    if (!isR && lane < 2)
      st2c(ws, WS_CRDY + ((tile + 2 * lane) * 4 + (grp & 3)) * 256 + (ct * 4 + wave) * 8, v2u{tag, ~tag});
    sb();
#define BAD2(x0, x1) wany(max(max(max(max(x0.x, x0.y), x0.z), max(max(x0.w, x1.x), x1.y)), max(x1.z, x1.w)) == SENT)
#define RETRY2(x0, x1, o0, o1) \
      _Pragma("nounroll") for (int s_ = 0; s_ < SPIN; ++s_) { \
        const int lf_ = fresh(l16); SLOWCNT \
        x0 = ld4c(ws, lf_, o0); x1 = ld4c(ws, lf_, o1); \
        if (wall(max(max4(x0), max4(x1)) != SENT)) break; \
      }
    if (__builtin_expect(BAD2(hh_[0][0], hh_[0][1]), 0)) { RETRY2(hh_[0][0], hh_[0][1], hb, hb + 14336) }
    STAMP(8); CSTAMP(28);
    v4f acc[2][8][2];
    v2u fa, fb;
    const int xf = WS_XRDY + (blk0 + (wave >> 1) + (lane & 4 ? 2 : 0)) * 64 + (lane & 3) * 8;
    const int fx = lane < 36 ? WS_CRDY + (tile * 4 + wave) * 256 + max(lane - 8, 0) * 8 : WS_CRDY + (tile * 4 + (grp & 3)) * 256 + (lane - 36) * 8;
    const int ao = WS_ACT + (16 * tile + 4 * wave) * 57344 + 2 * slice * 1024 + l16;
#define ACT_OUT(ps, tt, f) { v4u o; \
      _Pragma("unroll") for (int hh = 0; hh < 2; ++hh) { \
        const v4f av = relu4(acc[ps][2 * f + hh][tt]) * acc[ps][4 + 2 * f + hh][tt]; \
        o[2 * hh] = pk2(lo2(av)); o[2 * hh + 1] = pk2(hi2(av)); } \
      st4c(ws, ao + (2 * ps + tt) * 57344 + f * 1024, o); }
#pragma unroll
    for (int u = 0; u < 32; ++u) acc[u >> 4][(u >> 1) & 7][u & 1] = v4f{0.f, 0.f, 0.f, 0.f};
#pragma unroll
    for (int ps = 0; ps < 2; ++ps)
#pragma unroll
    for (int ks = 0; ks < 14; ++ks) {
      const int v = ps * 14 + ks, cur = v & 1, nk = (v + 1) % 14;
      const bool more = v < 27, epi = v == 14 || v == 15;
      __builtin_amdgcn_s_waitcnt(0xC07F);
#pragma unroll
      for (int u = 0; u < 8; ++u) {
        acc[ps][u][0] = mfma(wfr[cur][u], hh_[v][0], acc[ps][u][0]);
        if (more) W_RD(wfr[cur ^ 1][u], nk, u);
      }
      if (v + LEAD < 28) { H_LD(v + LEAD, 0); H_LD(v + LEAD, 1); }
#pragma unroll
      for (int e = 0; e < WD_PER; ++e) {
        const int f = WD_MID + (v < 14 ? v - 2 : 12 + WD_PER * (v - 16) + e);
        if (f < 56 && ((v >= 2 && v < 14 && e == 0) || v >= 16)) wdn[f >> 2][f & 3] = ld4c(ws, l16 + (f & 3) * 1024, wdb + (f >> 2) * 4096);
      }
      if (v == 8) fa = ld2c(ws, fx);
      if (v == 25) fb = ld2c(ws, xf);
      if (v > A_V) {
#pragma unroll
        for (int e = A_PER * (v - A_V - 1); e < A_PER * (v - A_V); ++e) G_LD(e);
      }
      const bool bad = more && BAD2(hh_[more ? v + 1 : v][0], hh_[more ? v + 1 : v][1]);
#pragma unroll
      for (int u = 0; u < 8; ++u) acc[ps][u][1] = mfma(wfr[cur][u], hh_[v][1], acc[ps][u][1]);
      if (epi) { ACT_OUT(0, ks, 0) ACT_OUT(0, ks, 1) }
#pragma unroll
      for (int u = 0; u < 16; ++u) {
        __builtin_amdgcn_sched_group_barrier(0x8, 1, 0);
        if (u < 4) __builtin_amdgcn_sched_group_barrier(0x100, 2, 0);
        else if (u < 12) __builtin_amdgcn_sched_group_barrier(0x100, 1, 0);
        if (u >= 5 && (u & 1)) __builtin_amdgcn_sched_group_barrier(0x20, 1, 0);
        if (epi) __builtin_amdgcn_sched_group_barrier(0x2, 5, 0);
        else if (u >= 10) __builtin_amdgcn_sched_group_barrier(0x2, 1, 0);
      }
      sb();
      if (v == 15 && isR) x2_store(ws, x2r, 16 * grp + j - 14, tid);
      if (v == 18 && isR) {
        static_assert(18 + LEAD < 28, "the count below");
        asm volatile("s_waitcnt vmcnt(6)" ::: "memory");
        if (lane == 0) st2c(ws, WS_XRDY + (16 * grp + j - 14) * 64 + wave * 8, v2u{tag, ~tag});
      }
      if (v == 12 && !wall(fa.x == tag && fa.y == ~tag)) wait_flags(ws, fx, tag);
      if (__builtin_expect(bad, 0)) {
        const int pb = 2 * ((v + 1) / 14) * 14336 + nk * 1024;
        RETRY2(hh_[more ? v + 1 : v][0], hh_[more ? v + 1 : v][1], hb + pb, hb + pb + 14336)
      }
      if (v == 13) { STAMP(16); CSTAMP(29); }
      sb();
    }
    STAMP(27);
    const int blk = blk0 + (wave >> 1);
    const int xo = WS_X2 + blk * 14336 + c16 * 896 + ct * 128 + g * 32 + (wave & 1) * 16;
    const int go = WS_MOD + grp * MOD_REP + (70 + 2 * ct) * 128 + g * 64 + (wave & 1) * 32;
    v4u xv[2], gv[2];
    gv[0] = ld4c(ws, go); gv[1] = ld4c(ws, go + 16);
    if (!wall(fb.x == tag && fb.y == ~tag)) wait_flags(ws, xf, tag);
    xv[0] = ld4c(ws, xo); xv[1] = ld4c(ws, xo + 2 * 14336);
    sb();
    ACT_OUT(1, 0, 0) ACT_OUT(1, 0, 1) ACT_OUT(1, 1, 0) ACT_OUT(1, 1, 1)
    CSTAMP(30);
    sb();
#pragma unroll
    for (int e = A_PER * (27 - A_V); e < 28; ++e) G_LD(e);
    sb();
    STAMP(9);
    v4u a[14][2];
    v4f ac2[4][4];
#pragma unroll
    for (int t = 0; t < 4; ++t)
#pragma unroll
      for (int n = 0; n < 4; ++n) ac2[t][n] = v4f{0.f, 0.f, 0.f, 0.f};
#define A_LD(e) a[(e) >> 1][(e) & 1] = ld4c(ws, l16 + (((e) >> 1) & 3) * 1024, ab + (2 + ((e) & 1)) * 57344 + ((e) >> 3) * 4096)
    const int po = ab + (2 + (lane & 1)) * 57344 + min(lane >> 1, 13) * 1024;
    u32 probe;
#pragma unroll
    for (int ks = 0; ks < 14; ++ks) {
      if (__builtin_expect(BAD2(h[ks][0], h[ks][1]), 0)) { RETRY2(h[ks][0], h[ks][1], ab + ks * 1024, ab + 57344 + ks * 1024) }
#pragma unroll
      for (int tt = 0; tt < 2; ++tt)
#pragma unroll
        for (int n = 0; n < 4; ++n) ac2[tt][n] = mfma(wdn[ks][n], h[ks][tt], ac2[tt][n]);
      if (ks == P_AT) probe = ld1c(ws, po);
      sb();
      if (ks == B_AT2) {
        STAMP(25);
        if (wany(probe == SENT)) light_poll(ws, po);
        STAMP(26);
      }
      if (ks >= B_AT2) {
#pragma unroll
        for (int e = 4 * (ks - B_AT2); e < 4 * (ks - B_AT2 + 1); ++e) A_LD(e);
      }
      sb();
    }
    STAMP(17);
    int rr_ = L_B + lane * 8 + wave * 6144;
    pin(rr_);
#define RED_WR(ph) _Pragma("unroll") for (int dd = 0; dd < 4; ++dd) if (dd != wave) \
      _Pragma("unroll") for (int n = 0; n < 4; ++n) \
        *(LAS v2f*)(L + L_B + lane * 8 + ((wave - dd - 1) & 3) * 2048 + ph * 24576 + dd * 6144 + n * 512) = \
            (dd & 1) ? hi2(ac2[2 * ph + (dd >> 1)][n]) : lo2(ac2[2 * ph + (dd >> 1)][n]);
#define RED_FIN(ph) { v2f r[4]; \
      _Pragma("unroll") for (int n = 0; n < 4; ++n) { \
        const v4f own = wave < 2 ? ac2[2 * ph][n] : ac2[2 * ph + 1][n]; r[n] = (wave & 1) ? hi2(own) : lo2(own); } \
      _Pragma("unroll") for (int w = 0; w < 3; ++w) \
        _Pragma("unroll") for (int n = 0; n < 4; ++n) r[n] += *(const LAS v2f*)(L + rr_ + w * 2048 + ph * 24576 + n * 512); \
      v4u o; \
      _Pragma("unroll") for (int q = 0; q < 2; ++q) { \
        o[2 * q] = pk2(fma2(bun(gv[q].x), v2f{r[0][q], r[1][q]}, bun(xv[ph][2 * q]))); \
        o[2 * q + 1] = pk2(fma2(bun(gv[q].z), v2f{r[2][q], r[3][q]}, bun(xv[ph][2 * q + 1]))); } \
      st4c(mkrs(out, S * D * 2), oo[ph], o); }
#pragma unroll
    for (int e = 4 * (14 - B_AT2); e < 4 * (14 - B_AT2) + 4; ++e) A_LD(e);
    sb();
    RED_WR(0)
    int oo[2];
#pragma unroll
    for (int ph = 0; ph < 2; ++ph) { oo[ph] = (tok_row(blk + 2 * ph, c16) * D + ct * 64 + g * 16 + (wave & 1) * 8) * 2; pin(oo[ph]); }
    _Pragma("nounroll") for (int s = 0; s < SPIN; ++s) {
      if (wall(gv[0].y == tag && gv[0].w == tag && gv[1].y == tag && gv[1].w == tag)) break;
      const int gf = fresh(go);
      gv[0] = ld4c(ws, gf); gv[1] = ld4c(ws, gf + 16);
    }
    STAMP(18);
    sb();
#pragma unroll
    for (int ks = 0; ks < 14; ++ks) {
      if (__builtin_expect(BAD2(a[ks][0], a[ks][1]), 0)) { RETRY2(a[ks][0], a[ks][1], ab + 2 * 57344 + ks * 1024, ab + 3 * 57344 + ks * 1024) }
#pragma unroll
      for (int tt = 0; tt < 2; ++tt)
#pragma unroll
        for (int n = 0; n < 4; ++n) ac2[2 + tt][n] = mfma(wdn[ks][n], a[ks][tt], ac2[2 + tt][n]);
#pragma unroll
      for (int e = 4 * (15 - B_AT2) + 3 * ks; e < 4 * (15 - B_AT2) + 3 * ks + 3; ++e)
        if (e < 28) A_LD(e);
#pragma unroll
      for (int u = 0; u < 4; ++u) {
        __builtin_amdgcn_sched_group_barrier(0x8, 2, 0);
        __builtin_amdgcn_sched_group_barrier(0x20, 1, 0);
      }
      sb();
    }
    STAMP(10);
    RED_WR(1)
    bar();
    RED_FIN(0)
    RED_FIN(1)
    if (blockIdx.x == 0 && wave == 0) {
      _Pragma("nounroll") for (int s = 0; s < SPIN; ++s) {
        const v4u v = ld4c(ws, fresh(WS_MOD + (70 + (lane < 14 ? lane : 0)) * 128));
        if (wall(v.y == tag)) break;
      }
      if (lane == 0) __builtin_amdgcn_raw_buffer_store_b32(epoch + 1u, ws, WS_EPOCH, 0, SC1);
    }
  }
  STAMP(11);
  DUMP();
}

extern "C" void kernel_launch(void* const* d_in, const int* in_sizes, int n_in, void* d_out, int out_size, void* d_ws,
                              size_t ws_size, hipStream_t stream) {
  fused<<<256, 256, 0, stream>>>((const u16*)d_in[0], (const u16*)d_in[1], (const u16*)d_in[2], (const u16*)d_in[4],
                                 (const u16*)d_in[5], (const u16*)d_in[6], (const u16*)d_in[7], (const u16*)d_in[8],
                                 (u16*)d_out, (char*)d_ws);
}
```

```cpp
#define XBAL 1
#include <hip/hip_runtime.h>

typedef unsigned short u16;
typedef unsigned int u32;
typedef unsigned long long u64;
typedef u32 v4u __attribute__((ext_vector_type(4)));
typedef u32 v2u __attribute__((ext_vector_type(2)));
typedef float v4f __attribute__((ext_vector_type(4)));
typedef float v2f __attribute__((ext_vector_type(2)));
typedef short v8s __attribute__((ext_vector_type(8)));
typedef short v4s __attribute__((ext_vector_type(4)));
typedef __bf16 v8b __attribute__((ext_vector_type(8)));
typedef __bf16 v2b __attribute__((ext_vector_type(2)));
#define LAS __attribute__((address_space(3)))
#define GAS __attribute__((address_space(1)))
#define DEV __device__ __forceinline__

#define C_W1_EARLY 0
#define CLR_DELAY 0
#define W_DELAY 0
#define CLR2_DELAY 0
#define PF_POS 2
#define SL1 1
#define SL2 2
#define SL4 4
#define X_AFTER 8
#define PF_AUX 2
#define W_LATE 1
#define W1_PF 1
#define DMA_MOD ""
#define LEAD 6
#define A_V 22
#define A_PER 4
#define WDV(f) ((f) - WD_MID + 2 < 14 ? (f) - WD_MID + 2 : ((f) - WD_MID - 12) / 2 + 16)
#define B_AT2 12
#define RE_PF 0
#define PF2_DELAY 0
#define C_DMA_EARLY 0
#define EVEN_PF 0
#define W1EVEN 0
#define P_AT 7
#define WD_PER 3
#define A_FIRST 10
#define B_FIRST 10
#define A_AT 9
#define F_AT 4
#define CODE_BYTES 45056
#define WD_EARLY 16
#define WD_MID 24
#define B_AT 9
constexpr int S = 2048, D = 448, DF = 225, F = 1792;

struct alignas(16) Chain { u16 v[2048]; };
struct Cos448 { u16 v[448]; };
constexpr double c_cos_t(double a) {
  double a2 = a * a, t = 1.0, s = 1.0;
  for (int i = 1; i <= 10; ++i) { t = -t * a2 / ((2 * i - 1) * (2 * i)); s += t; }
  return s;
}
constexpr double c_sin_t(double a) {
  double a2 = a * a, t = a, s = a;
  for (int i = 1; i <= 10; ++i) { t = -t * a2 / ((2 * i) * (2 * i + 1)); s += t; }
  return s;
}
constexpr double c_cos448(int m) {
  if (m > 224) m = 448 - m;
  double sg = 1.0;
  if (m > 112) { m = 224 - m; sg = -1.0; }
  const double w = 6.283185307179586476925286766559 / 448.0;
  return m == 112 ? 0.0 : (m <= 56 ? sg * c_cos_t(w * m) : sg * c_sin_t(w * (112 - m)));
}
constexpr u16 c_bf16(double d) {
  const u32 u = __builtin_bit_cast(u32, (float)d);
  return (u16)((u + 0x7FFFu + ((u >> 16) & 1u)) >> 16);
}
constexpr Cos448 mk_cos() { Cos448 c{}; for (int m = 0; m < 448; ++m) c.v[m] = c_bf16(c_cos448(m)); return c; }
constexpr Cos448 CTAB = mk_cos();
constexpr u16 bcos(int m) { return CTAB.v[m % 448]; }
constexpr u16 bsin(int m) { return CTAB.v[(m % 448 + 336) % 448]; }
constexpr u16 bneg(u16 b) { return b ? (u16)(b ^ 0x8000u) : b; }
constexpr u16 bscale(u16 b, int e) { return b ? (u16)(b + e * 0x80) : b; }

constexpr Chain mk_p1(int c) {
  const int tile = c >> 2, par = c & 1, sn = (c >> 1) & 1;
  Chain ch{};
  for (int s = 0; s < 4; ++s)
    for (int l = 0; l < 64; ++l) {
      const int kp = 16 * tile + (l & 15), k = 2 * kp + par;
      if (k > 224) continue;
      for (int j = 0; j < 8; ++j) {
        const int dl = 32 * s + 8 * (l >> 4) + j;
        if (dl > 112) continue;
        u16 b = sn ? bneg(bsin(k * dl)) : bcos(k * dl);
        if (dl == 0 || dl == 112) b = bscale(b, -1);
        ch.v[(s * 64 + l) * 8 + j] = b;
      }
    }
  return ch;
}
constexpr Chain mk_p3(int c) {
  const int tile = c >> 1, sn = c & 1;
  Chain ch{};
  for (int s = 0; s < 4; ++s)
    for (int l = 0; l < 64; ++l) {
      int dl = 16 * tile + (l & 15);
      if (c == 1 && dl == 0) dl = 112;
      for (int j = 0; j < 8; ++j) {
        const int k = 2 * (32 * s + 8 * (l >> 4) + j) + 1;
        if (k < 224) ch.v[(s * 64 + l) * 8 + j] = sn ? bsin(k * dl) : bcos(k * dl);
      }
    }
  return ch;
}
constexpr Chain mk_p2(int c) {
  Chain ch{};
  for (int f = 0; f < 4; ++f)
    for (int l = 0; l < 64; ++l)
      for (int j = 0; j < 8; ++j) {
        const int comp = c < 8 ? (c & 1) : (f >> 1), s = c < 8 ? f : (f & 1);
        const int o = 16 * (c < 8 ? (c >> 1) : (c - 8)) + (l & 15), i = 16 * s + 4 * (l >> 4) + (j >> 1);
        const int m = (c < 8 ? 7 : 14) * ((i * o) % 64);
        u16 b = 0;
        if (comp == 0) b = (j & 1) ? bsin(m) : bcos(m);
        else b = (j & 1) ? bcos(m) : bneg(bsin(m));
        ch.v[(f * 64 + l) * 8 + j] = b;
      }
  return ch;
}
template <int I> constexpr Chain P1C = mk_p1(I);
template <int I> constexpr Chain P3C = mk_p3(I);
template <int I> constexpr Chain P2C = mk_p2(I);
#define L10(T, b) T<b>, T<b + 1>, T<b + 2>, T<b + 3>, T<b + 4>, T<b + 5>, T<b + 6>, T<b + 7>, T<b + 8>, T<b + 9>
#define L29(T) L10(T, 0), L10(T, 10), T<20>, T<21>, T<22>, T<23>, T<24>, T<25>, T<26>, T<27>, T<28>
constexpr int TAB_P1 = 0, TAB_P3 = 29, TAB_P2 = 43, TAB_N = 53;
static __device__ const Chain TAB[TAB_N] = {L29(P1C), L10(P3C, 0), P3C<10>, P3C<11>, P3C<12>, P3C<13>, L10(P2C, 0)};

constexpr int WS_EPOCH = 0;
constexpr int WS_RDY = 4096;
constexpr int WS_HRDY = 4096 + 8192;
constexpr int WS_DRDY = 4096 + 16384;
constexpr int WS_XRDY = 131072 + 65536;
constexpr int WS_ARDY = 131072;
constexpr int WS_CRDY = 131072 + 32768;
constexpr int WS_MOD = 32768, MOD_REP = 12288;
constexpr int T1_BLK = 226 * 64;
constexpr int WS_T1 = 1 << 18;
constexpr int WS_WT = WS_T1 + 128 * T1_BLK;
constexpr int WS_H2 = WS_WT + 113 * 16384;
constexpr int WS_X2 = WS_H2 + 128 * 14336;
constexpr int WS_WD = WS_X2 + 128 * 14336;
constexpr int WS_ACT = WS_WD + 7 * 56 * 4096;
constexpr int WS_END = WS_ACT + 128 * 57344;
constexpr int WS_DBG = 32 << 20;

constexpr int L_W1 = 0;
constexpr int L_B = 114688;
constexpr int XP = 912;
constexpr int L_XS = L_B;
constexpr int L_ST = L_XS + 16 * XP;
constexpr int L_M1 = L_ST + 128;
constexpr int L_AF = L_M1 + 4 * 1792;
constexpr int L_M2 = L_AF + 16384;
constexpr int L_EX = L_M2 + 3 * 1792;
constexpr int L_REND = L_EX + 512;
constexpr int ZPL = 8192;
constexpr int L_CI = L_B;
constexpr int L_CX = L_CI + 2 * 8256;
constexpr int L_RED = L_CX + 16384;
constexpr int L_DBG = 163840 - 1024;
constexpr int L_SZ = 163840;
static_assert(L_REND <= L_DBG && L_RED + 2048 <= L_DBG && 2 * ZPL <= 16384, "lds");

constexpr u32 SENT = 0xFFFFFFFFu;
constexpr int SPIN = 1 << 14;
constexpr int SC1 = 16;
constexpr int OOB = 0x7FFF0000;

typedef __amdgpu_buffer_rsrc_t rsrc_t;
DEV rsrc_t mkrs(const void* p, int bytes) { return __builtin_amdgcn_make_buffer_rsrc((void*)p, 0, bytes, 0x00020000); }
DEV v4u ld4(rsrc_t r, int off, int so = 0) { return __builtin_amdgcn_raw_buffer_load_b128(r, off, so, 0); }
DEV v2u ld2(rsrc_t r, int off, int so = 0) { return __builtin_amdgcn_raw_buffer_load_b64(r, off, so, 0); }
DEV v4u ld4c(rsrc_t r, int off, int so = 0) { return __builtin_amdgcn_raw_buffer_load_b128(r, off, so, SC1); }
DEV v2u ld2c(rsrc_t r, int off, int so = 0) { return __builtin_amdgcn_raw_buffer_load_b64(r, off, so, SC1); }
DEV u32 ld1c(rsrc_t r, int off, int so = 0) { return __builtin_amdgcn_raw_buffer_load_b32(r, off, so, SC1); }
DEV void st4c(rsrc_t r, int off, v4u v) { __builtin_amdgcn_raw_buffer_store_b128(v, r, off, 0, SC1); }
DEV void st2c(rsrc_t r, int off, v2u v) { __builtin_amdgcn_raw_buffer_store_b64(v, r, off, 0, SC1); }
DEV float blo(u32 v) { return __builtin_bit_cast(float, v << 16); }
DEV float bhi(u32 v) { return __builtin_bit_cast(float, v & 0xFFFF0000u); }
DEV v2f bun(u32 v) { return v2f{blo(v), bhi(v)}; }
DEV u32 pk(float lo, float hi) { return __builtin_bit_cast(u32, __builtin_convertvector(v2f{lo, hi}, v2b)); }
DEV u32 pk2(v2f v) { return __builtin_bit_cast(u32, __builtin_convertvector(v, v2b)); }
DEV float rb(float f) { return blo(pk(f, 0.f)); }
DEV v2f lo2(v4f v) { return __builtin_shufflevector(v, v, 0, 1); }
DEV v2f hi2(v4f v) { return __builtin_shufflevector(v, v, 2, 3); }
DEV v2f fma2(v2f a, v2f b, v2f c) { return __builtin_elementwise_fma(a, b, c); }
DEV v4f fma4(v4f a, v4f b, v4f c) { return __builtin_elementwise_fma(a, b, c); }
typedef int v4i __attribute__((ext_vector_type(4)));
DEV v4f relu4(v4f v) { return __builtin_bit_cast(v4f, __builtin_elementwise_max(__builtin_bit_cast(v4i, v), v4i{0, 0, 0, 0})); }
DEV v4f splat(float f) { return v4f{f, f, f, f}; }
DEV v4f mfma(v4u a, v4u b, v4f c) {
  return __builtin_amdgcn_mfma_f32_16x16x32_bf16(__builtin_bit_cast(v8b, a), __builtin_bit_cast(v8b, b), c, 0, 0, 0);
}
DEV void mfa(v4f& c, v4u a, v4u b) { asm volatile("s_nop 1\n\tv_mfma_f32_16x16x32_bf16 %0, %1, %2, %0" : "+v"(c) : "v"(a), "v"(b)); }
DEV void mf0(v4f& c, v4u a, v4u b) { asm volatile("s_nop 1\n\tv_mfma_f32_16x16x32_bf16 %0, %1, %2, 0" : "=&v"(c) : "v"(a), "v"(b)); }
template <int N> DEV void mfence(v4f& a, v4f& b) { asm volatile("s_nop %2" : "+v"(a), "+v"(b) : "i"(N)); }
template <int N> DEV void mfence(v4f& a, v4f& b, v4f& c, v4f& d) { asm volatile("s_nop %4" : "+v"(a), "+v"(b), "+v"(c), "+v"(d) : "i"(N)); }
#define MF "v_mfma_f32_16x16x32_bf16 "
DEV void mf8(v4f& d0, v4f& d1, const v4u (&a0)[4], const v4u (&b0)[4], const v4u (&a1)[4], const v4u (&b1)[4]) {
  asm volatile("s_nop 1\n\t" MF "%0, %2, %6, 0\n\t" MF "%1, %10, %14, 0\n\t" MF "%0, %3, %7, %0\n\t" MF "%1, %11, %15, %1\n\t"
               MF "%0, %4, %8, %0\n\t" MF "%1, %12, %16, %1\n\t" MF "%0, %5, %9, %0\n\t" MF "%1, %13, %17, %1"
               : "=&v"(d0), "=&v"(d1)
               : "v"(a0[0]), "v"(a0[1]), "v"(a0[2]), "v"(a0[3]), "v"(b0[0]), "v"(b0[1]), "v"(b0[2]), "v"(b0[3]),
                 "v"(a1[0]), "v"(a1[1]), "v"(a1[2]), "v"(a1[3]), "v"(b1[0]), "v"(b1[1]), "v"(b1[2]), "v"(b1[3]));
}
#define H4_M(XA, D, X, K, C) MF D ", " XA(X, K) ", " C "\n\t"
#define H4_XA(X, K) X ", " K
#define H4_XB(X, K) K ", " X
#define H4_DEF(NAME, XA, C0, C1, CONS, N0, N1, PA, PB, PC, PD) \
DEV void NAME(v4f& d0, v4f& d1, v4u x0, v4u x1, v4u k00, v4u k01, v4u k10, v4u k11, \
              u32& o0, u32& o1, float p0, float q0, float c0, float s0, float p1, float q1, float c1, float s1) { \
  float t0, t1, t2, t3; \
  asm volatile(H4_M(XA, "%0", "%8", "%10", C0) "v_mul_f32 %4, %14, %16\n\tv_mul_f32 %5, %15, %16\n\t" \
               H4_M(XA, "%1", "%8", "%12", C1) "v_mul_f32 %6, %18, %20\n\tv_mul_f32 %7, %19, %20\n\t" \
               H4_M(XA, "%0", "%9", "%11", "%0") "v_fma_f32 %4, " N0 "%15, %17, %4\n\tv_fma_f32 %5, " N1 "%14, %17, %5\n\t" \
               H4_M(XA, "%1", "%9", "%13", "%1") "v_fma_f32 %6, " N0 "%19, %21, %6\n\tv_fma_f32 %7, " N1 "%18, %21, %7\n\t" \
               "v_cvt_pk_bf16_f32 %2, " PA ", " PB "\n\tv_cvt_pk_bf16_f32 %3, " PC ", " PD \
               : CONS(d0), CONS(d1), "=&v"(o0), "=&v"(o1), "=&v"(t0), "=&v"(t1), "=&v"(t2), "=&v"(t3) \
               : "v"(x0), "v"(x1), "v"(k00), "v"(k01), "v"(k10), "v"(k11), \
                 "v"(p0), "v"(q0), "v"(c0), "v"(s0), "v"(p1), "v"(q1), "v"(c1), "v"(s1)); \
}
H4_DEF(h4_a0m, H4_XA, "0", "0", "=&v", "", "-", "%4", "%5", "%6", "%7")
H4_DEF(h4_a1m, H4_XA, "%0", "%1", "+v", "", "-", "%4", "%5", "%6", "%7")
H4_DEF(h4_b0m, H4_XB, "0", "0", "=&v", "", "-", "%4", "%5", "%6", "%7")
H4_DEF(h4_b0p, H4_XB, "0", "0", "=&v", "-", "", "%5", "%4", "%7", "%6")
H4_DEF(h4_a0p, H4_XA, "0", "0", "=&v", "-", "", "%5", "%4", "%7", "%6")
#define M4_DEF(NAME, XA, C0, C1, CONS) \
DEV void NAME(v4f& d0, v4f& d1, v4u x0, v4u x1, v4u k00, v4u k01, v4u k10, v4u k11) { \
  asm volatile(H4_M(XA, "%0", "%2", "%4", C0) H4_M(XA, "%1", "%2", "%6", C1) H4_M(XA, "%0", "%3", "%5", "%0") MF "%1, " XA("%3", "%7") ", %1" \
               : CONS(d0), CONS(d1) : "v"(x0), "v"(x1), "v"(k00), "v"(k01), "v"(k10), "v"(k11)); \
}
M4_DEF(m4_a0, H4_XA, "0", "0", "=&v") M4_DEF(m4_a1, H4_XA, "%0", "%1", "+v")
M4_DEF(m4_b0, H4_XB, "0", "0", "=&v") M4_DEF(m4_b1, H4_XB, "%0", "%1", "+v")
DEV void mv(v4f& c, v4u a, v4u b) { asm volatile(MF "%0, %1, %2, %0" : "+v"(c) : "v"(a), "v"(b)); }
DEV void mv0(v4f& c, v4u a, v4u b) { asm volatile(MF "%0, %1, %2, 0" : "=&v"(c) : "v"(a), "v"(b)); }
DEV void nop2() { asm volatile("s_nop 1"); }
template <class T> DEV void pin(T& v) { asm volatile("" : "+v"(v)); }
DEV int fresh(int off) { asm volatile("" : "+v"(off) :: "memory"); return off; }
DEV bool wall(bool ok) { return __builtin_amdgcn_ballot_w64(!ok) == 0; }
DEV bool wany(bool b) { return __builtin_amdgcn_ballot_w64(b) != 0; }
DEV void sb() { __builtin_amdgcn_sched_barrier(0); }
DEV u32 max4(v4u v) { return max(max(v.x, v.y), max(v.z, v.w)); }
DEV void drain() { asm volatile("s_waitcnt vmcnt(0)" ::: "memory"); }
DEV void cfence() { asm volatile("" ::: "memory"); }
DEV void bar() { asm volatile("s_waitcnt lgkmcnt(0)\n\ts_barrier" ::: "memory"); }
template <int CTRL> DEV float dpp(float v) {
  return __builtin_bit_cast(float, __builtin_amdgcn_update_dpp(0, __builtin_bit_cast(int, v), CTRL, 0xF, 0xF, true));
}
DEV float sum16(float v) {
  v += dpp<0x128>(v); v += dpp<0x124>(v); v += dpp<0x122>(v); v += dpp<0x121>(v); return v;
}
DEV void sumg2(float& a, float& b) {
  float c = a, d = b;
  asm volatile("s_nop 1\n\tv_permlane16_swap_b32 %0, %1\n\tv_permlane16_swap_b32 %2, %3" : "+v"(a), "+v"(c), "+v"(b), "+v"(d));
  a += c; b += d; c = a; d = b;
  asm volatile("s_nop 1\n\tv_permlane32_swap_b32 %0, %1\n\tv_permlane32_swap_b32 %2, %3" : "+v"(a), "+v"(c), "+v"(b), "+v"(d));
  a += c; b += d;
}
DEV v4u lds4(const char LAS* L, int off) { return *(const LAS v4u*)(L + off); }
DEV v4f lds4f(const char LAS* L, int off) { return *(const LAS v4f*)(L + off); }
DEV v2u ldtr(const char LAS* L, int off) { return __builtin_bit_cast(v2u, __builtin_amdgcn_ds_read_tr16_b64_v4i16((LAS v4s*)(L + off))); }
DEV v4u ldtr2(const char LAS* L, int o0, int o1) { const v2u a = ldtr(L, o0), b = ldtr(L, o1); return v4u{a.x, a.y, b.x, b.y}; }
DEV void dma16(rsrc_t r, int voff, int soff, u32 lds_dst) {
  u32 keep;
  asm volatile("s_mov_b32 %0, m0\n\ts_mov_b32 m0, %1\n\ts_nop 0\n\tbuffer_load_dwordx4 %2, %3, %4 offen" DMA_MOD " lds\n\ts_mov_b32 m0, %0"
               : "=&s"(keep) : "s"(lds_dst), "v"(voff), "s"(r), "s"(soff) : "memory");
}
DEV void light_poll(rsrc_t ws, int off) {
  _Pragma("nounroll") for (int s = 0; s < SPIN; ++s) {
    if (wall(ld1c(ws, fresh(off)) != SENT)) break;
    __builtin_amdgcn_s_sleep(SL2);
  }
}
DEV int tok_row(int blk, int t) {
  const int n1 = 16 * (blk >> 5) + 4 * ((blk >> 2) & 3) + (t & 3), n2 = 16 * ((blk >> 4) & 1) + 4 * (blk & 3) + (t >> 2);
  return n2 + 32 * n1;
}
template <bool SWAP> DEV v4u cmul_pack(v4f a, v4f b, v4f c, v4f s) {
  const v4f re = fma4(b, s, a * c), im = fma4(b, c, -(a * s));
  return SWAP ? v4u{pk(im.x, re.x), pk(im.y, re.y), pk(im.z, re.z), pk(im.w, re.w)}
              : v4u{pk(re.x, im.x), pk(re.y, im.y), pk(re.z, im.z), pk(re.w, im.w)};
}

template <bool PLUS> DEV void cmul2(u32& o0, u32& o1, v2f p, v2f q, v2f c, v2f s) {
  const v2f re = fma2(PLUS ? -q : q, s, p * c), im = fma2(PLUS ? p : -p, s, q * c);
  o0 = PLUS ? pk(im.x, re.x) : pk(re.x, im.x); o1 = PLUS ? pk(im.y, re.y) : pk(re.y, im.y);
}

#define STAMP(i)
#define CSTAMP(i)
#define SLOWCNT
#define DUMP()

DEV void dma_w1(const u16* wg, const u16* wu, int slice, int wave, int lane, u32 lbase) {
  const rsrc_t rg = mkrs(wg, D * F * 2), ru = mkrs(wu, D * F * 2);
  const int r = lane >> 3, ch = ((((lane & 7) >> 1) ^ (lane >> 4)) & 3) * 32 + (lane & 1) * 16 + slice * 128;
  const int vp = r * (F * 2) + ch, vn = (7 - r) * (F * 2) + ch;
#pragma unroll
  for (int n = 0; n < 4; ++n) {
    const int ks = wave + 4 * n;
    if (n == 3 && wave >= 2) break;
    const int t16 = (ks >> 1) * 16, a = ks & 1;
#pragma unroll
    for (int q = 0; q < 4; ++q) {
      const int dl0 = t16 + 8 * (q & 1);
      const bool pos = (q >> 1) == a;
      const int d0 = q < 2 ? (a ? 224 - dl0 - 7 : dl0) : (a ? 224 + dl0 : 448 - dl0 - 7);
      int vo = pos ? vp : vn, so = d0 * (F * 2);
      if (n == 0 && q == 2) {
        vo += so; so = 0;
        if (wave < 2 && lane < 8) vo = (wave ? 336 : 112) * (F * 2) + ch;
      }
      dma16(rg, vo, so, lbase + L_W1 + ks * 8192 + q * 1024);
      dma16(ru, vo, so, lbase + L_W1 + ks * 8192 + 4096 + q * 1024);
    }
  }
}
struct ModLoad { u32 cv[7]; v4u mw[7]; };
DEV void mod_load(ModLoad& m, const u16* cin, const u16* wm, int q, int tid) {
  const rsrc_t cr = mkrs(cin, q >= 0 ? D * 2 : 0), mr = mkrs(wm, q >= 0 ? D * 6 * D * 2 : 0);
#pragma unroll
  for (int i = 0; i < 7; ++i) m.cv[i] = __builtin_amdgcn_raw_buffer_load_b16(cr, (tid >> 2) * 2 + i * 128, 0, 0);
#pragma unroll
  for (int i = 0; i < 7; ++i) m.mw[i] = __builtin_amdgcn_raw_buffer_load_b128(mr, ((tid >> 2) * (6 * D) + q * 32 + (tid & 3) * 8) * 2, i * 64 * 6 * D * 2, PF_AUX);
}
DEV void mod_part(ModLoad& m, char LAS* L, int tid) {
  float sl[7];
#pragma unroll
  for (int i = 0; i < 7; ++i) { const float cc = blo(m.cv[i]); sl[i] = rb(cc * __builtin_amdgcn_rcpf(1.f + __expf(-cc))); pin(sl[i]); }
  v2f a[4];
#pragma unroll
  for (int e = 0; e < 4; ++e) a[e] = v2f{0.f, 0.f};
#pragma unroll
  for (int i = 0; i < 7; ++i)
#pragma unroll
    for (int e = 0; e < 4; ++e) a[e] = fma2(bun(m.mw[i][e]), v2f{sl[i], sl[i]}, a[e]);
#pragma unroll
  for (int e = 0; e < 4; ++e) {
    a[e].x += dpp<0x128>(a[e].x); a[e].y += dpp<0x128>(a[e].y);
    a[e].x += dpp<0x124>(a[e].x); a[e].y += dpp<0x124>(a[e].y);
  }
  if ((tid & 15) < 4) {
    *(LAS v4f*)(L + L_RED + ((tid >> 4) * 32 + (tid & 15) * 8) * 4) = v4f{a[0].x, a[0].y, a[1].x, a[1].y};
    *(LAS v4f*)(L + L_RED + ((tid >> 4) * 32 + (tid & 15) * 8 + 4) * 4) = v4f{a[2].x, a[2].y, a[3].x, a[3].y};
  }
}
DEV void mod_publish(char LAS* L, rsrc_t ws, int q, int lane, u32 tag) {
  v4f s = {0.f, 0.f, 0.f, 0.f};
#pragma unroll
  for (int r = 0; r < 16; ++r) s += lds4f(L, L_RED + (r * 32 + (lane & 7) * 4) * 4);
  const float one = (q / 14 == 1 || q / 14 == 4) ? 1.f : 0.f;
  st4c(ws, WS_MOD + (lane >> 3) * MOD_REP + q * 128 + (lane & 7) * 16,
       v4u{pk(rb(s.x) + one, rb(s.y) + one), tag, pk(rb(s.z) + one, rb(s.w) + one), tag});
}
DEV u32 ld1(rsrc_t r, int off) { return __builtin_amdgcn_raw_buffer_load_b32(r, off, 0, 0); }
DEV u32 ld1n(rsrc_t r, int off) { return __builtin_amdgcn_raw_buffer_load_b32(r, off, 0, PF_AUX); }
DEV u32 mktag(u32 epoch) { return epoch * 0x9E3779B1u + 0x7F4A7C15u; }

DEV void x2_read(const char LAS* L, v4u (&r)[4], int tid) {
#pragma unroll
  for (int m = 0; m < 4; ++m) { const int ch = min(tid + 256 * m, 895), tk = ch / 56; r[m] = lds4(L, L_XS + tk * XP + (ch - tk * 56) * 16); }
}
DEV void x2_store(rsrc_t ws, const v4u (&r)[4], int blk, int tid) {
#pragma unroll
  for (int m = 0; m < 4; ++m) st4c(ws, tid + 256 * m < 896 ? WS_X2 + blk * 14336 + (tid + 256 * m) * 16 : OOB, r[m]);
}
DEV bool flags_ok(v4u rv, u32 tag) { return rv.x == tag && rv.y == ~tag && rv.z == tag && rv.w == ~tag; }
DEV void wait_flags(rsrc_t ws, int off, u32 tag) {
  _Pragma("nounroll") for (int s = 0; s < SPIN; ++s) {
    const v2u rv = ld2c(ws, fresh(off));
    if (wall(rv.x == tag && rv.y == ~tag)) break;
    __builtin_amdgcn_s_sleep(SL1);
  }
}

__global__ void __launch_bounds__(256, 1)
fused(const u16* __restrict__ xin, const u16* __restrict__ wr, const u16* __restrict__ wi, const u16* __restrict__ wg,
      const u16* __restrict__ wu, const u16* __restrict__ wd, const u16* __restrict__ cin, const u16* __restrict__ wm,
      u16* __restrict__ out, char* wsp) {
  __shared__ __attribute__((aligned(16))) char smem[L_SZ];
  char LAS* L = (char LAS*)smem;
  const u32 lbase = (u32)(size_t)L;
  const int tid = threadIdx.x, lane = tid & 63, wave = __builtin_amdgcn_readfirstlane(tid >> 6);
  const int g = lane >> 4, c16 = lane & 15, l16 = lane * 16;
  const int grp = blockIdx.x & 7, j = blockIdx.x >> 3;
  u64 pc;
  asm volatile("s_getpc_b64 %0" : "=s"(pc));
  STAMP(0); CSTAMP(31);
  asm volatile("" :: "s"(xin), "s"(wr), "s"(wi), "s"(wg), "s"(wu), "s"(wd), "s"(cin), "s"(wm), "s"(out), "s"(wsp));
  const rsrc_t ws = mkrs(wsp, WS_END);
  const rsrc_t tab = mkrs(TAB, TAB_N * 4096);
  const rsrc_t code = mkrs((const void*)(pc & ~127ull), CODE_BYTES);
  const bool isR = j >= 14 && j < 30;
  const bool isG = j < 28;
  const int gk = j / 7, ct = j - gk * 7;
  const int tile = 4 * (grp >> 2) + gk, slice = 7 * (grp & 3) + ct;
  const int wdb = WS_WD + (ct * 56 + wave * 14) * 4096;
  const v4u sv = {SENT, SENT, SENT, SENT};
  int lb = L_B + l16;
  pin(lb);
  const char LAS* LB = L + lb - L_B;
  const int pfo = ((j * 4 + wave) * 14 + lane) * 128, pfc = ((j * 4 + wave) * 4 + lane) * 128;
  u32 pfa = ld1(tab, lane < 14 ? pfo : OOB), pfb = ld1(code, lane < 4 ? pfc : OOB);
  const int hro = WS_HRDY + (16 * tile + 4 * (lane < 32 ? wave : grp & 3)) * 32 + c16 * 8;
  v4u x2r[4];
  const int wl16 = (lane ^ ((wave & 1) << 1)) * 16;
  v4u wdn[14][4];
  u32 epoch, tag;

  if (isR) {
    const int blk = 16 * grp + (j - 14);
    epoch = ld1c(ws, WS_EPOCH);
    const int t0 = wave == 0 ? 0 : 2 * wave - 1;
    v4u cf[8][4];
#define CF_LOAD(q) { const int chn = TAB_P1 + ((wave == 0 && q >= 4) ? 28 : 4 * t0 + q), vo = (q < 5 || wave != 0) ? l16 : OOB; \
      _Pragma("unroll") for (int s = 0; s < 4; ++s) cf[q][s] = ld4(tab, vo + s * 1024, chn * 4096); sb(); }
#pragma unroll
    for (int q = 0; q < X_AFTER; ++q) CF_LOAD(q)
    v2u xv[7];
    {
      const rsrc_t xr = mkrs(xin, S * D * 2);
      const int off = tok_row(blk, 4 * wave + g) * (D * 2) + c16 * 8;
#pragma unroll
      for (int m = 0; m < 7; ++m) xv[m] = ld2(xr, off + m * 128);
    }
    typedef u32 v3u __attribute__((ext_vector_type(3)));
    v2u fa_[2], fb_[2]; v3u fc_[2], fd_[2];
    {
      const rsrc_t rr = mkrs(wr, S * DF * 2), ri = mkrs(wi, S * DF * 2);
      const int fo_ = 16 * wave + c16 < 57 ? 2 * g * 450 + (16 * wave + c16) * 8 : OOB;
#pragma unroll
      for (int hh = 0; hh < 2; ++hh) {
        const int so = blk * 7200 + hh * 3600;
        fa_[hh] = ld2(rr, fo_, so); fb_[hh] = ld2(ri, fo_, so);
        fc_[hh] = __builtin_amdgcn_raw_buffer_load_b96(rr, fo_ + 448, so, 0); fd_[hh] = __builtin_amdgcn_raw_buffer_load_b96(ri, fo_ + 448, so, 0);
      }
    }
    sb();
#pragma unroll
    for (int q = X_AFTER; q < 8; ++q) CF_LOAD(q)
    STAMP(23);
    {
      v2f f[14], s2 = {0.f, 0.f};
#pragma unroll
      for (int m = 0; m < 7; ++m) {
        f[2 * m] = bun(xv[m].x); f[2 * m + 1] = bun(xv[m].y);
        s2 += f[2 * m] + f[2 * m + 1];
        *(LAS v2u*)(L + L_XS + (4 * wave + g) * XP + c16 * 8 + m * 128) = xv[m];
      }
      STAMP(21);
      sb();
      {
        const int q = 16 * wave + c16, o = WS_WT + q * 16384 + blk * 128 + g * 16;
#pragma unroll
        for (int hh = 0; hh < 2; ++hh) {
          const v2u a = fa_[hh], b = fb_[hh]; const v3u c = fc_[hh], d = fd_[hh];
          st4c(ws, q < 57 ? o + hh * 64 : OOB, v4u{__builtin_amdgcn_perm(b.x, a.x, 0x05040100u), __builtin_amdgcn_perm(b.y, a.y, 0x05040100u),
                                                   __builtin_amdgcn_perm(d.x, c.x, 0x07060302u), __builtin_amdgcn_perm(d.y, c.y, 0x07060302u)});
          st4c(ws, q < 56 ? o + hh * 64 + 57 * 16384 : OOB,
               v4u{__builtin_amdgcn_perm(b.x, a.x, 0x07060302u), __builtin_amdgcn_perm(b.y, a.y, 0x07060302u),
                   __builtin_amdgcn_perm(d.y, c.y, 0x05040100u), __builtin_amdgcn_perm(d.z, c.z, 0x05040100u)});
        }
      }
      sb();
      const float mean = sum16(s2.x + s2.y) * (1.f / D);
      v2f q2 = {0.f, 0.f};
#pragma unroll
      for (int m = 0; m < 14; ++m) { const v2f d = f[m] - mean; q2 = fma2(d, d, q2); }
      const float rstd = __builtin_amdgcn_rsqf(sum16(q2.x + q2.y) * (1.f / D) + 1e-5f);
      if (c16 == 0) *(LAS v2f*)(L + L_ST + (4 * wave + g) * 8) = v2f{mean, rstd};
    }
    pin(pfa); pin(pfb);
    epoch = __builtin_amdgcn_readfirstlane(epoch);
    tag = mktag(epoch);
    STAMP(22);
    bar();
    v4f n[4][2];
    const int d0 = 8 * (4 * wave + g);
    const v2f st = *(const LAS v2f*)(L + L_ST + c16 * 8);
    {
      int xr_ = L_XS + c16 * XP + d0 * 2, xb_ = L_XS + c16 * XP + (216 - d0) * 2, x0_ = d0 == 0 ? xr_ : xb_ + 464;
      pin(xr_); pin(xb_); pin(x0_);
      const v4u a = lds4(L + xr_, 0), e = lds4(L + xr_, 448), c = lds4(L + xb_, 0), b = lds4(L + xb_, 448);
      const u32 c0 = *(const LAS u16*)(L + xb_ + 16), b0 = *(const LAS u16*)(L + x0_);
#pragma unroll
      for (int i = 0; i < 8; ++i) {
        n[0][i >> 2][i & 3] = (i & 1) ? bhi(a[i >> 1]) : blo(a[i >> 1]);
        n[3][i >> 2][i & 3] = (i & 1) ? bhi(e[i >> 1]) : blo(e[i >> 1]);
        n[1][i >> 2][i & 3] = i == 0 ? blo(b0) : (i & 1) ? bhi(b[(8 - i) >> 1]) : blo(b[(8 - i) >> 1]);
        n[2][i >> 2][i & 3] = i == 0 ? blo(c0) : (i & 1) ? bhi(c[(8 - i) >> 1]) : blo(c[(8 - i) >> 1]);
      }
      const float nm = -st.x * st.y;
#pragma unroll
      for (int m = 0; m < 4; ++m) {
#pragma unroll
        for (int h = 0; h < 2; ++h) { n[m][h] = fma4(n[m][h], splat(st.y), splat(nm)); pin(n[m][h]); }
      }
    }
    sb();
    STAMP(1);
    int mb = L_M1 + d0 * 4;
    pin(mb);
    v4u xs[4];
    int to[4];
    to[0] = WS_T1 + blk * T1_BLK + (16 * t0 + c16) * 64 + g * 16;
    to[1] = wave != 0 ? to[0] + 1024 : c16 < 2 ? to[0] + 112 * 64 : OOB;
    to[2] = to[0] + 114 * 64;
    to[3] = wave != 0 ? to[0] + 1024 + 114 * 64 : OOB;
#pragma unroll
    for (int q = 0; q < 4; ++q) pin(to[q]);

    {
      const int kind = tid >= 112, d = 4 * (tid - 112 * kind);
      int wb = L_M1 + (kind ? 0 : 2 * 1792) + d * 4, wrv = L_M1 + (kind ? 0 : 2 * 1792) + 1792 + (445 - d) * 4;
      int wr0 = d == 0 ? wrv + 12 - 1792 : wrv + 12;
      const int mo = tid < 224 ? WS_MOD + grp * MOD_REP + tid * 16 : OOB;
      pin(wb); pin(wrv); pin(wr0);
      const int ro = WS_RDY + grp * 1024 + min(tid >> 1, 112) * 8;
      v4u mv;
      _Pragma("nounroll") for (int s = 0; s < SPIN; ++s) {
        mv = ld4c(ws, fresh(mo));
        const v2u rv = ld2c(ws, fresh(ro));
        if (wall((tid >= 224 || (mv.y == tag && mv.w == tag)) && rv.x == tag && rv.y == ~tag)) break;
        __builtin_amdgcn_s_sleep(SL1);
      }
      STAMP(2);
      if (tid < 224) {
        const v4f v = {blo(mv.x), bhi(mv.x), blo(mv.z), bhi(mv.z)};
        *(LAS v4f*)(L + wb) = v;
        *(LAS float*)(L + wr0) = v.x;
        *(LAS float*)(L + wrv + 8) = v.y;
        *(LAS float*)(L + wrv + 4) = v.z;
        *(LAS float*)(L + wrv) = v.w;
      }
    }
    bar();
    {
      v4u fr[4];
#pragma unroll
      for (int hh = 0; hh < 2; ++hh) {
        v4f h[4];
#pragma unroll
        for (int m = 0; m < 4; ++m) {
          const int o = ((m == 1 || m == 2) ? 1792 : 0) + (m >= 2 ? 896 : 0) + hh * 16;
          h[m] = fma4(n[m][hh], lds4f(L + mb, o), lds4f(L + mb, 2 * 1792 + o));
        }
        const v4f p = h[0] + h[1], q = h[2] + h[3], u = h[0] - h[1], v = h[3] - h[2];
        const v4f t[4] = {p + q, p - q, u + v, u - v};
#pragma unroll
        for (int m = 0; m < 4; ++m) { fr[m][2 * hh] = pk2(lo2(t[m])); fr[m][2 * hh + 1] = pk2(hi2(t[m])); }
      }
#pragma unroll
      for (int m = 0; m < 4; ++m) *(LAS v4u*)(LB + wave * 1024 + L_AF + m * 4096) = fr[m];
    }
    bar();
    {
      v4u a[4][4];
#pragma unroll
      for (int p = 0; p < 2; ++p)
#pragma unroll
        for (int s = 0; s < 4; ++s) { a[p][s] = lds4(LB, L_AF + (p * 4 + s) * 1024); a[2 + p][s] = lds4(LB, L_AF + ((2 + p) * 4 + s) * 1024); }
      v4f re[4], im[4];
#pragma unroll
      for (int q = 0; q < 5; ++q) {
        if (q < 4) mf8(re[q], im[q], a[q >> 1], cf[4 * (q & 1) + (q >> 1)], a[2 + (q >> 1)], cf[4 * (q & 1) + 2 + (q >> 1)]);
        if (q > 0) {
          if (q < 4) mfence<0>(re[q - 1], im[q - 1]); else mfence<7>(re[3], im[3]);
          xs[q - 1] = v4u{pk(re[q - 1].x, im[q - 1].x), pk(re[q - 1].y, im[q - 1].y), pk(re[q - 1].z, im[q - 1].z), pk(re[q - 1].w, im[q - 1].w)};
          st4c(ws, to[q - 1], xs[q - 1]);
          sb();
        }
      }
    }
    STAMP(3);
    sb();
    sb();
    if (isG) dma_w1(wg, wu, slice, wave, lane, lbase);
#pragma unroll
    for (int q = 0; q < 4; ++q) {
      const int vo = (q < 2 || wave != 0) ? l16 : OOB;
#pragma unroll
      for (int s = 0; s < 4; ++s) cf[2 * q + 1][s] = ld4(tab, vo + s * 1024, (TAB_P3 + 2 * t0 + q) * 4096);
    }
#pragma unroll
    for (int m = 0; m < 4; ++m) {
      st4c(ws, tid + 256 * m < 896 ? WS_H2 + blk * 14336 + 4096 * m + tid * 16 : OOB, sv);
      st4c(ws, tid + 256 * m < 896 ? WS_X2 + blk * 14336 + 4096 * m + tid * 16 : OOB, sv);
    }
    sb();
    STAMP(20);
    v4f xo[2][4];
    int xa = L_XS + c16 * XP + (16 * t0 + 4 * g) * 2, xd = L_XS + c16 * XP + (192 - 16 * t0 - 4 * g) * 2;
    int xs1 = tid < 16 ? L_XS + c16 * XP + 224 : xd + 512, xs3 = tid < 16 ? L_XS + c16 * XP + 672 : xa + 448;
    pin(xa); pin(xd); pin(xs1); pin(xs3);
#define XOFF(t, m, r) ((t) == 0 && (r) == 0 && (m) == 1 ? xs1 : (t) == 0 && (r) == 0 && (m) == 3 ? xs3 : \
    (m) == 0 ? xa + 32 * (t) + 2 * (r) : (m) == 3 ? xa + 448 + 32 * (t) + 2 * (r) : (m) == 1 ? xd + 512 - 32 * (t) - 2 * (r) : xd + 64 - 32 * (t) - 2 * (r))
    {
      v2u w[2][4]; u32 e[2][2], e3;
#pragma unroll
      for (int t = 0; t < 2; ++t) {
        w[t][0] = *(const LAS v2u*)(L + xa + 32 * t); w[t][3] = *(const LAS v2u*)(L + xa + 448 + 32 * t);
        w[t][1] = *(const LAS v2u*)(L + xd + 504 - 32 * t); w[t][2] = *(const LAS v2u*)(L + xd + 56 - 32 * t);
        e[t][0] = *(const LAS u16*)(L + XOFF(t, 1, 0)); e[t][1] = *(const LAS u16*)(L + XOFF(t, 2, 0));
      }
      e3 = *(const LAS u16*)(L + xs3);
      sb();
#pragma unroll
      for (int t = 0; t < 2; ++t) {
        xo[t][0] = v4f{blo(w[t][0].x), bhi(w[t][0].x), blo(w[t][0].y), bhi(w[t][0].y)};
        xo[t][3] = v4f{t == 0 ? blo(e3) : blo(w[t][3].x), bhi(w[t][3].x), blo(w[t][3].y), bhi(w[t][3].y)};
        xo[t][1] = v4f{blo(e[t][0]), bhi(w[t][1].y), blo(w[t][1].y), bhi(w[t][1].x)};
        xo[t][2] = v4f{blo(e[t][1]), bhi(w[t][2].y), blo(w[t][2].y), bhi(w[t][2].x)};
#pragma unroll
        for (int m = 0; m < 4; ++m) { xo[t][m] -= st.x; pin(xo[t][m]); }
      }
    }
    int m0_[2], m1_[2], md_[2];
#pragma unroll
    for (int r = 0; r < 2; ++r) {
      const int q = r == 0 || tid < 80 ? tid + 256 * r : tid;
      const int kind = (q * 586) >> 16, d = 4 * (q - 112 * kind);
      const int reg = (d >= 112) + (d >= 224) + (d >= 336), cc = ((reg + 1) >> 1) * 224, m1 = (0x1320 >> (4 * reg)) & 3;
      const int d1 = abs(d + 1 - cc), d0_ = abs(d - cc), edge = d == 112 * reg && reg > 0;
      m1_[r] = L_M2 + kind * 1792 + (((d1 >> 4) << 6) | (m1 << 4) | (d1 & 15)) * 4;
      m0_[r] = L_M2 + kind * 1792 + (edge ? reg << 4 : ((d0_ >> 4) << 6) | (m1 << 4) | (d0_ & 15)) * 4;
      md_[r] = (reg & 1) ? -4 : 4;
      pin(m0_[r]); pin(m1_[r]); pin(md_[r]);
    }
    {
      v4u mv[2];
      _Pragma("nounroll") for (int s = 0; s < SPIN; ++s) {
        mv[0] = ld4c(ws, fresh(WS_MOD + grp * MOD_REP + (224 + tid) * 16));
        mv[1] = ld4c(ws, fresh(WS_MOD + grp * MOD_REP + (224 + (tid < 80 ? 256 + tid : tid)) * 16));
        const v2u rv = ld2c(ws, fresh(WS_DRDY + grp * 1024 + min(lane, 48) * 8));
        if (wall(mv[0].y == tag && mv[0].w == tag && mv[1].y == tag && mv[1].w == tag && rv.x == tag && rv.y == ~tag)) break;
        __builtin_amdgcn_s_sleep(SL4);
      }
      if (lane == 0) st2c(ws, WS_HRDY + blk * 32 + wave * 8, v2u{tag, ~tag});
      STAMP(12);
#pragma unroll
      for (int f = 0; f < WD_EARLY; ++f) wdn[f >> 2][f & 3] = ld4c(ws, (isG ? wl16 : OOB) + (f & 3) * 1024, wdb + (f >> 2) * 4096);
      sb();
#pragma unroll
      for (int r = 0; r < 2; ++r) {
        const float sc = m0_[r] < L_M2 + 1792 ? 2.f / (float)(S * D) : 1.f;
        *(LAS float*)(L + m0_[r]) = blo(mv[r].x) * sc;
        *(LAS float*)(L + m1_[r]) = bhi(mv[r].x) * sc;
        *(LAS float*)(L + m1_[r] + md_[r]) = blo(mv[r].z) * sc;
        *(LAS float*)(L + m1_[r] + 2 * md_[r]) = bhi(mv[r].z) * sc;
      }
    }
    bar();
    *(LAS v4u*)(L + L_AF + (tid >> 6) * 4096 + 3072 + l16) = v4u{0u, 0u, 0u, 0u};
    STAMP(4);
    int zr = L_AF + lane * 8, ro = L_M2 + (t0 * 64 + 4 * g) * 4, ro1 = wave == 0 ? ro : ro + 256, ex = L_EX + c16 * 8;
    pin(zr); pin(ro); pin(ro1); pin(ex);
    {
      int zl[4];
#pragma unroll
      for (int m = 0; m < 4; ++m) {
        const int kq = (m >> 1) * 128 + (to[m] == OOB ? 127 : wave == 0 && m == 1 ? 112 + c16 : 16 * (t0 + (m & 1)) + c16);
        zl[m] = L_AF + ((kq & ~31) + ((kq & 4) << 2) + ((kq & 24) >> 1) + (kq & 3)) * 32 + g * 8;
        pin(zl[m]);
        if (to[m] == OOB) xs[m] = sv;
      }
      bar();
      if (isG) wait_flags(ws, hro, tag);
      {
        const int src = (blk * 5 + wave * 13) & 63;
        const int po = __builtin_amdgcn_readlane(to[(0)], 0) + (src & 15) * 64 + (src >> 4) * 16;
        const u32 old = __builtin_amdgcn_ds_bpermute(src * 4, xs[0].x);
        _Pragma("nounroll") for (int s = 0; s < SPIN; ++s) {
          if (wall(ld1c(ws, fresh(po)) != old)) break;
          __builtin_amdgcn_s_sleep(SL2);
        }
      }
      STAMP(13);
      v4u z[4];
      _Pragma("nounroll") for (int s = 0; s < SPIN; ++s) {
        u32 mn = 1;
#pragma unroll
        for (int m = 0; m < 4; ++m) z[m] = ld4c(ws, fresh(to[m]));
#pragma unroll
        for (int m = 0; m < 4; ++m) { const v4u d = z[m] ^ xs[m]; mn = min(min(mn, d.x), min(min(d.y, d.z), d.w)); }
        if (wall(mn != 0)) break;
      }
      STAMP(14);
#pragma unroll
      for (int m = 0; m < 4; ++m) {
        char LAS* p = L + zl[m];
        *(LAS v2u*)(p) = v2u{__builtin_amdgcn_perm(z[m].y, z[m].x, 0x05040100u), __builtin_amdgcn_perm(z[m].w, z[m].z, 0x05040100u)};
        *(LAS v2u*)(p + ZPL) = v2u{__builtin_amdgcn_perm(z[m].y, z[m].x, 0x07060302u), __builtin_amdgcn_perm(z[m].w, z[m].z, 0x07060302u)};
      }
    }
    bar();
    STAMP(5);
    {
      v4u zf[4][4];
#pragma unroll
      for (int ty = 0; ty < 4; ++ty)
#pragma unroll
        for (int s = 0; s < 4; ++s) zf[ty][s] = ldtr2(L + zr, ty * 4096 + s * 1024, ty * 4096 + s * 1024 + 512);
      v4f acc[2][4], x2[2][4], gav[2][4];
      v2f s1 = {0.f, 0.f}, s2 = {0.f, 0.f};
#define P3M(t, ty) mf8(acc[t][ty], acc[t][ty + 1], cf[4 * t + ty], zf[ty], cf[4 * t + ty + 1], zf[ty + 1]);
#define P3X(t, m, yv) { x2[t][m] = fma4(gav[t][m], yv, xo[t][m]); \
      if (t == 0 || wave != 0) { s1 += lo2(x2[t][m]) + hi2(x2[t][m]); \
        s2 = fma2(lo2(x2[t][m]), lo2(x2[t][m]), s2); s2 = fma2(hi2(x2[t][m]), hi2(x2[t][m]), s2); } }
      P3M(1, 0) P3M(1, 2)
#pragma unroll
      for (int m = 0; m < 4; ++m) { gav[0][m] = lds4f(L + ro, m * 64); gav[1][m] = lds4f(L + ro1, m * 64); }
      sb();
      P3M(0, 0) P3M(0, 2)
      mfence<0>(acc[1][0], acc[1][1], acc[1][2], acc[1][3]);
      const v4f A1 = acc[1][0] + acc[1][1], B1 = acc[1][0] - acc[1][1], P1 = acc[1][3] - acc[1][2], Q1 = acc[1][2] + acc[1][3];
      P3X(1, 0, A1 - P1) P3X(1, 1, A1 + P1) P3X(1, 2, B1 - Q1) P3X(1, 3, B1 + Q1)
      sb();
      mfence<0>(acc[0][0], acc[0][1], acc[0][2], acc[0][3]);
      {
        const v4f A = acc[0][0] + acc[0][1], B = acc[0][0] - acc[0][1], P = acc[0][3] - acc[0][2], Q = acc[0][2] + acc[0][3];
        v4f y[4] = {A - P, A + P, B - Q, B + Q};
        const bool sp = tid < 16;
        y[0].x = sp ? A.x : y[0].x; y[2].x = sp ? B.x : y[2].x;
        y[1].x = sp ? acc[1][0].x - acc[0][3].x : y[1].x; y[3].x = sp ? acc[1][0].x + acc[0][3].x : y[3].x;
        P3X(0, 0, y[0]) P3X(0, 1, y[1]) P3X(0, 2, y[2]) P3X(0, 3, y[3])
      }
      float sm = s1.x + s1.y, sq = s2.x + s2.y;
      sumg2(sm, sq);
      if (g == 0) *(LAS v2f*)(L + ex + wave * 128) = v2f{sm, sq};
      v4f sbv[2][4], cbv[2][4];
#pragma unroll
      for (int m = 0; m < 4; ++m) {
        sbv[0][m] = lds4f(L + ro, 1792 + m * 64); cbv[0][m] = lds4f(L + ro, 2 * 1792 + m * 64);
        sbv[1][m] = lds4f(L + ro1, 1792 + m * 64); cbv[1][m] = lds4f(L + ro1, 2 * 1792 + m * 64);
      }
      bar();
      v2f e = *(const LAS v2f*)(L + ex);
#pragma unroll
      for (int w = 1; w < 4; ++w) e += *(const LAS v2f*)(L + ex + w * 128);
      const float mc = e.x * (1.f / D), rstd = __builtin_amdgcn_rsqf(fmaxf(e.y * (1.f / D) - mc * mc, 0.f) + 1e-5f), nb = -mc * rstd;
      const int ho = WS_H2 + blk * 14336 + 2 * t0 * 1024 + l16, ho1 = wave == 0 ? OOB : ho + 2048;
#pragma unroll
      for (int t = 0; t < 2; ++t)
#pragma unroll
        for (int a = 0; a < 2; ++a) {
          v4u o;
#pragma unroll
          for (int hh = 0; hh < 2; ++hh) {
            const int m = 2 * a + hh;
            const v4f hv = fma4(fma4(x2[t][m], splat(rstd), splat(nb)), cbv[t][m], sbv[t][m]);
            o[2 * hh] = pk2(lo2(hv)); o[2 * hh + 1] = pk2(hi2(hv));
          }
          st4c(ws, (t ? ho1 : ho) + a * 1024, o);
        }
      STAMP(6);
      sb();
#pragma unroll
      for (int f = WD_EARLY; f < WD_MID; ++f) wdn[f >> 2][f & 3] = ld4c(ws, (isG ? wl16 : OOB) + (f & 3) * 1024, wdb + (f >> 2) * 4096);
      sb();
#pragma unroll
      for (int t = 0; t < 2; ++t)
        if (t == 0 || wave != 0)
#pragma unroll
          for (int m = 0; m < 4; ++m)
#pragma unroll
            for (int r = 0; r < 4; r += 2) {
              const u32 p = pk(x2[t][m][r] + st.x, x2[t][m][r + 1] + st.x);
              *(LAS u16*)(L + XOFF(t, m, r)) = (u16)p;
              *(LAS u16*)(L + XOFF(t, m, r + 1)) = (u16)(p >> 16);
            }
      sb();
    }
    bar();
    x2_read(L, x2r, tid);
    bar();
    if (!isG) {
      x2_store(ws, x2r, blk, tid);
      DUMP(); return;
    }
  } else {
    const int sj = j >= 30 ? j - 30 : j + 2;
    const int idx = sj * 8 + grp;
    const bool isC = j < 14 || idx == 0;
    const int pair = j < 14 ? grp * 14 + j : 112;
    const int lc = grp + 8 * (sj >> 1);
    const int qu = sj < 4 && lc < 14 ? 2 * lc + (sj & 1) : -1;
    const int t1o = WS_T1 + (32 * wave + (lane >> 3)) * T1_BLK + pair * 128 + (lane & 7) * 16;
    ModLoad ml;
    epoch = ld1c(ws, WS_EPOCH);
    if (qu >= 0) mod_load(ml, cin, wm, qu, tid);
#pragma unroll
    for (int q = 0; q < 4; ++q) st4c(ws, isC ? t1o + 8 * q * T1_BLK : OOB, sv);
    u32 pf0, pf1, pf2;
    {
      const rsrc_t mr = mkrs(wm, D * 6 * D * 2), xr = mkrs(xin, S * D * 2), fr_ = mkrs((wave & 1) ? wi : wr, S * DF * 2);
      const int row = (sj * 4 + wave) * 7 + (lane >> 1), l7 = lane / 7;
      pf0 = __builtin_amdgcn_raw_buffer_load_b32(mr, lane < 14 && (grp < 6 || !(lane & 1)) ? row * (6 * D * 2) + (grp + 8 * (lane & 1)) * 128 : OOB, 0, PF_AUX);
      pf1 = __builtin_amdgcn_raw_buffer_load_b32(xr, lane < (!XBAL ? 28 : sj < 4 ? 0 : sj < 12 ? 28 : 56) ? tok_row(16 * grp + (lane < 28 ? sj : sj - 12), 4 * wave + (l7 & 3)) * (D * 2) + (lane - 7 * l7) * 128 : OOB, 0, PF_AUX);
    }
    sb();
    epoch = __builtin_amdgcn_readfirstlane(epoch);
    tag = mktag(epoch);
    if (qu >= 0) mod_part(ml, L, tid);
    asm volatile("s_waitcnt vmcnt(%0)" :: "i"(W_LATE ? 2 : 3) : "memory");
    { const rsrc_t fr_ = mkrs((wave & 1) ? wi : wr, S * DF * 2);
      pf2 = __builtin_amdgcn_raw_buffer_load_b32(fr_, (lane & 31) < 29 && lane < (!XBAL ? 32 : sj < 4 ? 0 : sj < 12 ? 32 : 64) ? (((16 * grp + (lane < 32 ? sj : sj - 12)) * 7200) & ~127) + ((wave >> 1) * 29 + (lane & 31)) * 128 : OOB, 0, PF_AUX);
    }
    bar();
    if (wave == 0 && qu >= 0) mod_publish(L, ws, qu, lane, tag);
    if (wave == 1 && lane < 8) st2c(ws, isC ? WS_RDY + lane * 1024 + pair * 8 : OOB, v2u{tag, ~tag});
    STAMP(1);
    pin(pf0); pin(pf1); pin(pf2); pin(pfa); pin(pfb);
    const int qn = sj >= 4 && sj < 10 && grp < 7 ? 28 + grp * 6 + (sj - 4) : !isC && idx < 15 ? 69 + idx : -1;
    const int tw = sj >= 10 ? (sj - 10) * 8 + grp : sj == 4 && grp == 7 ? 48 : -1;
    v2u dw[2][8];
    if (qn >= 0) mod_load(ml, cin, wm, qn, tid);
    if (tw >= 0) {
      const rsrc_t dr = mkrs(wd, F * D * 2);
      const int u0 = tw * 4 + wave, uc = u0 / 28, kap = 2 * (u0 - uc * 28);
#pragma unroll
      for (int e = 0; e < 16; ++e)
        dw[e >> 3][e & 7] = __builtin_amdgcn_raw_buffer_load_b64(dr, (4 * g * D + 4 * c16) * 2 + (e & 3) * D * 2, ((32 * kap + 16 * (e >> 2)) * D + 64 * uc) * 2, PF_AUX);
    }
    sb();
    u32 pw[4];
    {
      const rsrc_t rg = mkrs(wg, D * F * 2), ru = mkrs(wu, D * F * 2);
      const int r7 = lane / 7, r0 = W1EVEN ? (j * 4 + wave) * 8 : sj < 4 ? (sj * 4 + wave) * 16 : 256 + ((sj - 4) * 4 + wave) * 4;
      const int po = (r0 + r7) * (F * 2) + ((grp & 3) * 7 + lane - r7 * 7) * 128;
      const int o0 = lane < (W1EVEN ? (j < 14 ? 56 : 0) : sj < 4 ? 56 : 28) ? po : OOB, o1 = !W1EVEN && lane < 56 && sj < 4 ? po + 8 * (F * 2) : OOB;
      pw[0] = ld1(rg, o0); pw[1] = ld1(ru, o0); pw[2] = ld1(rg, o1); pw[3] = ld1(ru, o1);
    }
    sb();
    STAMP(20);
    v4f t1c[2], t1s[2], t2c[2], t2s[2];
#pragma unroll
    for (int t = 0; t < 2; ++t) {
#pragma unroll
      for (int r = 0; r < 4; ++r) {
        const float a1 = (float)((16 * t + 4 * g + r) * (16 * wave + c16)) * (1.f / S);
        const float a2 = (float)((16 * t + c16) * (16 * wave + 4 * g + r)) * (1.f / S);
        t1c[t][r] = __builtin_amdgcn_cosf(a1); t1s[t][r] = __builtin_amdgcn_sinf(a1);
        t2c[t][r] = __builtin_amdgcn_cosf(a2); t2s[t][r] = __builtin_amdgcn_sinf(a2);
      }
      pin(t1c[t]); pin(t1s[t]); pin(t2c[t]); pin(t2s[t]);
    }
    if (tw >= 0) {
#pragma unroll
      for (int tt = 0; tt < 8; ++tt) {
        v4u o;
#pragma unroll
        for (int m = 0; m < 4; ++m)
          o[m] = __builtin_amdgcn_perm(dw[tt >> 2][2 * m + 1][(tt & 3) >> 1], dw[tt >> 2][2 * m][(tt & 3) >> 1], (tt & 1) ? 0x07060302u : 0x05040100u);
        st4c(ws, WS_WD + (tw * 4 + wave) * 8192 + tt * 1024 + l16, o);
      }
    }
    if (qn >= 0) {
      bar();
      mod_part(ml, L, tid);
      bar();
      if (wave == 0) mod_publish(L, ws, qn, lane, tag);
    }
    pin(pw[0]); pin(pw[1]); pin(pw[2]); pin(pw[3]);
    STAMP(21);
    if (!isC) { DUMP(); return; }
    v4u c64[2][4], c32[2][2][2];
#pragma unroll
    for (int cp = 0; cp < 2; ++cp)
#pragma unroll
      for (int s = 0; s < 4; ++s) c64[cp][s] = ld4(tab, l16 + s * 1024, (TAB_P2 + wave * 2 + cp) * 4096);
#pragma unroll
    for (int t = 0; t < 2; ++t)
#pragma unroll
      for (int f = 0; f < 4; ++f) c32[t][f >> 1][f & 1] = ld4(tab, l16 + f * 1024, (TAB_P2 + 8 + t) * 4096);
    sb();
    if (tw >= 0) {
      asm volatile("s_waitcnt vmcnt(16)" ::: "memory");
      bar();
      if (tid < 8) st2c(ws, WS_DRDY + tid * 1024 + tw * 8, v2u{tag, ~tag});
    }
    STAMP(2);
    int wfo = WS_WT + pair * 16384 + (16 * g + wave) * 128 + c16 * 8;
    pin(wfo);
    v2u wf[2][4];
    {
      int wl = L_CI + ((lane >> 2) & 1) * 8256 + wave * 2048 + ((lane >> 3) * 4 + (lane & 3)) * 16, lo = t1o;
      pin(wl); pin(lo);
      light_poll(ws, WS_T1 + (32 * wave + (pair & 31)) * T1_BLK + pair * 128);
      STAMP(13);
      v4u z[4];
      _Pragma("nounroll") for (int s = 0; s < SPIN; ++s) {
        lo = fresh(lo);
#pragma unroll
        for (int q = 0; q < 4; ++q) z[q] = ld4c(ws, lo, 8 * q * T1_BLK);
        if (wall(max(max(max4(z[0]), max4(z[1])), max(max4(z[2]), max4(z[3]))) != SENT)) break;
      }
      STAMP(14);
#pragma unroll
      for (int q = 0; q < 4; ++q) *(LAS v4u*)(L + wl + q * 512) = z[q];
    }
    bar();
#pragma unroll
    for (int t = 0; t < 2; ++t)
#pragma unroll
      for (int r = 0; r < 4; ++r) wf[t][r] = ld2c(ws, wfo + r * 512, t * 8192);
    v2u fd = ld2c(ws, WS_DRDY + grp * 1024 + min(lane, 48) * 8);
    sb();
    STAMP(4);
    {
      v4u a[2][2][4];
#pragma unroll
      for (int c = 0; c < 2; ++c)
#pragma unroll
        for (int mt = 0; mt < 2; ++mt)
#pragma unroll
          for (int s = 0; s < 4; ++s) a[c][mt][s] = lds4(LB, L_CI + c * 8256 + (s * 2 + mt) * 1024);
      v4u old[2][2];
#pragma unroll
      for (int c = 0; c < 2; ++c)
#pragma unroll
        for (int mt = 0; mt < 2; ++mt) old[c][mt] = lds4(LB + wave * 2048, L_CI + c * 8256 + mt * 1024);
      v4f re[2][2], im[2][2], ur[2][2], ui[2][2], fr_[2][2], fi_[2][2];
      u32 f2[2][2][4], f3[2][2][4], f4[4];
      char LAS* xw = L + lb + wave * 1024 - L_B;
#define V4(x) v4u{x[0], x[1], x[2], x[3]}
#define S1K(h) c64[0][2 * h], c64[0][2 * h + 1], c64[1][2 * h], c64[1][2 * h + 1]
#define S1X(c, t, h) re[c][t], im[c][t], a[c][t][2 * h], a[c][t][2 * h + 1], S1K(h)
#define S2X(c, t) ur[c][t], ui[c][t], V4(f2[c][0]), V4(f2[c][1]), c32[t][0][0], c32[t][0][1], c32[t][1][0], c32[t][1][1]
#define S3X(c, t) im[c][t], re[c][t], V4(f3[c][0]), V4(f3[c][1]), c32[t][0][0], c32[t][0][1], c32[t][1][0], c32[t][1][1]
#define CM(o, P, Q, C, S_, h) o[2 * h], o[2 * h + 1], P[2 * h], Q[2 * h], C[2 * h], S_[2 * h], P[2 * h + 1], Q[2 * h + 1], C[2 * h + 1], S_[2 * h + 1]
#define TW1(c, t, h) CM(f2[c][t], re[c][t], im[c][t], t1c[t], t1s[t], h)
#define FLT(c, t, h) CM(f3[c][t], ur[c][t], ui[c][t], fr_[c][t], fi_[c][t], h)
#define TW2(c, t, h) CM(f4, re[c][t], im[c][t], t2c[t], t2s[t], h)
#define HV(x, h) (h ? hi2(x) : lo2(x))
#define CP(PL, o, P, Q, C, S_, h) cmul2<PL>(o[2 * h], o[2 * h + 1], HV(P, h), HV(Q, h), HV(C, h), HV(S_, h));
#define XW(c, t) *(LAS v4u*)(xw + L_CX + (c * 2 + t) * 4096) = V4(f4);
#define UNP(c, t) { _Pragma("unroll") for (int r = 0; r < 4; ++r) { fr_[c][t][r] = blo(wf[t][r][c]); fi_[c][t][r] = bhi(wf[t][r][c]); } \
      pin(fr_[c][t]); pin(fi_[c][t]); }
      nop2();
      m4_a0(S1X(0, 0, 0)); m4_a1(S1X(0, 0, 1)); m4_a0(S1X(0, 1, 0)); mfence<3>(re[0][0], im[0][0]);
      h4_a1m(S1X(0, 1, 1), TW1(0, 0, 0)); h4_a0m(S1X(1, 0, 0), TW1(0, 0, 1));
      h4_a1m(S1X(1, 0, 1), TW1(0, 1, 0)); h4_a0m(S1X(1, 1, 0), TW1(0, 1, 1));
      h4_a1m(S1X(1, 1, 1), TW1(1, 0, 0)); h4_b0m(S2X(0, 0), TW1(1, 0, 1));
      h4_b0m(S2X(0, 1), TW1(1, 1, 0));
      sb();
      CP(false, f2[1][1], re[1][1], im[1][1], t1c[1], t1s[1], 1)
      UNP(0, 0) UNP(0, 1)
      sb();
      h4_b0p(S2X(1, 0), FLT(0, 0, 0)); h4_b0p(S2X(1, 1), FLT(0, 0, 1));
      sb();
      CP(true, f3[0][1], ur[0][1], ui[0][1], fr_[0][1], fi_[0][1], 0) CP(true, f3[0][1], ur[0][1], ui[0][1], fr_[0][1], fi_[0][1], 1)
      UNP(1, 0) UNP(1, 1)
      sb();
      h4_a0p(S3X(0, 0), FLT(1, 0, 0)); h4_a0p(S3X(0, 1), FLT(1, 0, 1));
      sb();
      CP(true, f3[1][1], ur[1][1], ui[1][1], fr_[1][1], fi_[1][1], 0) CP(true, f3[1][1], ur[1][1], ui[1][1], fr_[1][1], fi_[1][1], 1)
#pragma unroll
      for (int e = 0; e < 4; ++e) pin(f3[1][1][e]);
      nop2();
      h4_a0p(S3X(1, 0), TW2(0, 0, 0)); h4_a0p(S3X(1, 1), TW2(0, 0, 1));
      XW(0, 0)
      sb();
      CP(true, f4, re[0][1], im[0][1], t2c[1], t2s[1], 0) CP(true, f4, re[0][1], im[0][1], t2c[1], t2s[1], 1) XW(0, 1)
      sb();
      mfence<0>(re[1][0], im[1][0], re[1][1], im[1][1]);
      CP(true, f4, re[1][0], im[1][0], t2c[0], t2s[0], 0) CP(true, f4, re[1][0], im[1][0], t2c[0], t2s[0], 1) XW(1, 0)
      CP(true, f4, re[1][1], im[1][1], t2c[1], t2s[1], 0) CP(true, f4, re[1][1], im[1][1], t2c[1], t2s[1], 1) XW(1, 1)
      bar();
#pragma unroll
      for (int c = 0; c < 2; ++c)
#pragma unroll
        for (int nt = 0; nt < 2; ++nt)
#pragma unroll
          for (int s = 0; s < 4; ++s) a[c][nt][s] = lds4(LB, L_CX + ((c * 2 + nt) * 4 + s) * 1024);
      const int o2 = WS_T1 + (32 * wave + (lane >> 2)) * T1_BLK + pair * 128 + (lane & 3) * 16;
#define S4(c, t) mf8(im[c][t], re[c][t], c64[0], a[c][t], c64[1], a[c][t]);
#define NEQ(x, y) ((x) == (y) ? (x) ^ 1u : (x))
#define OUT(c, t) { const v4u o = {pk(re[c][t].x, im[c][t].x), pk(re[c][t].y, im[c][t].y), pk(re[c][t].z, im[c][t].z), pk(re[c][t].w, im[c][t].w)}; \
      st4c(ws, o2 + c * 64 + t * 16 * T1_BLK, v4u{NEQ(o.x, old[c][t].x), NEQ(o.y, old[c][t].y), NEQ(o.z, old[c][t].z), NEQ(o.w, old[c][t].w)}); sb(); }
      S4(0, 0) S4(0, 1) mfence<0>(re[0][0], im[0][0]); OUT(0, 0)
      S4(1, 0) mfence<0>(re[0][1], im[0][1]); OUT(0, 1)
      S4(1, 1) mfence<0>(re[1][0], im[1][0]); OUT(1, 0)
      mfence<0>(re[1][1], im[1][1]); OUT(1, 1)
    }
    STAMP(5);
    sb();
    if (!isG) { DUMP(); return; }
    if (!wall(fd.x == tag && fd.y == ~tag)) wait_flags(ws, WS_DRDY + grp * 1024 + min(lane, 48) * 8, tag);
    dma_w1(wg, wu, slice, wave, lane, lbase);
#pragma unroll
    for (int f = 0; f < WD_MID; ++f) wdn[f >> 2][f & 3] = ld4c(ws, wl16 + (f & 3) * 1024, wdb + (f >> 2) * 4096);
    fd = ld2c(ws, hro);
    sb();
#pragma unroll
    for (int m = 0; m < 16; ++m)
      st4c(ws, WS_ACT + (16 * (tile + 2 * (m >> 3)) + 4 * (grp & 3)) * 57344 + ct * 32768 + (m & 7) * 4096 + tid * 16, sv);
    sb();
    if (!wall(fd.x == tag && fd.y == ~tag)) wait_flags(ws, hro, tag);
    bar();
  }

  STAMP(7);
  const int blk0 = 16 * tile + 4 * (grp & 3);
  const int ab = WS_ACT + blk0 * 57344 + wave * 14 * 1024;
  v4u h[14][2];
  {
    const int hb = WS_H2 + (16 * tile + 4 * wave) * 14336;
    int tb[2][4];
#pragma unroll
    for (int u = 0; u < 8; ++u) {
      tb[u >> 2][u & 3] = (((lane >> 2) * 128 + ((lane >> 3) & 3) * 32 + (lane & 3) * 8) ^ ((u & 3) << 5)) + (u >> 2) * 57344;
      pin(tb[u >> 2][u & 3]);
    }
#define W_RD(dst, k, u) dst = ldtr2(L + tb[k >= 7][u & 3], (k % 7) * 8192 + (u >> 2) * 4096, (k % 7) * 8192 + (u >> 2) * 4096 + 2048)
#define H_LD(v, tt) hh_[v][tt] = ld4c(ws, l16 + (((v) % 14) & 3) * 1024, hb + (2 * ((v) / 14) + (tt)) * 14336 + (((v) % 14) >> 2) * 4096)
    const int abx[2] = {ab + (wave >> 1) * 57344, ab + (1 - (wave >> 1)) * 57344};
#define G_LD(e) h[(e) >> 1][(e) & 1] = ld4c(ws, l16 + (((e) >> 1) & 3) * 1024, abx[(e) & 1] + ((e) >> 3) * 4096)
    v4u wfr[2][8], hh_[28][2];
#pragma unroll
    for (int u = 0; u < 8; ++u) W_RD(wfr[0][u], 0, u);
    light_poll(ws, hb + (lane & 1) * 14336 + 13 * 1024);
    STAMP(15);
#pragma unroll
    for (int v = 0; v < LEAD; ++v) { H_LD(v, 0); H_LD(v, 1); }
    sb();
    if (!isR && lane < 2)
      st2c(ws, WS_CRDY + ((tile + 2 * lane) * 4 + (grp & 3)) * 256 + (ct * 4 + wave) * 8, v2u{tag, ~tag});
    sb();
#define BAD2(x0, x1) wany(max(max(max(max(x0.x, x0.y), x0.z), max(max(x0.w, x1.x), x1.y)), max(x1.z, x1.w)) == SENT)
#define RETRY2(x0, x1, o0, o1) \
      _Pragma("nounroll") for (int s_ = 0; s_ < SPIN; ++s_) { \
        const int lf_ = fresh(l16); SLOWCNT \
        x0 = ld4c(ws, lf_, o0); x1 = ld4c(ws, lf_, o1); \
        if (wall(max(max4(x0), max4(x1)) != SENT)) break; \
      }
    if (__builtin_expect(BAD2(hh_[0][0], hh_[0][1]), 0)) { RETRY2(hh_[0][0], hh_[0][1], hb, hb + 14336) }
    STAMP(8); CSTAMP(28);
    v4f acc[8][2];
    v2u fa;
    const int fx = WS_CRDY + (tile * 4 + (lane < 32 ? wave : grp & 3)) * 256 + min(lane & 31, 27) * 8;
    const int ao = WS_ACT + (16 * tile + 4 * wave) * 57344 + 2 * slice * 1024 + l16;
#define ACT_OUT(ps, tt, f) { v4u o; \
      _Pragma("unroll") for (int hh = 0; hh < 2; ++hh) { \
        const v4f av = relu4(acc[2 * f + hh][tt]) * acc[4 + 2 * f + hh][tt]; \
        o[2 * hh] = pk2(lo2(av)); o[2 * hh + 1] = pk2(hi2(av)); } \
      st4c(ws, ao + (2 * ps + tt) * 57344 + f * 1024, o); }
#define ACT_ALL(ps) { mfence<0>(acc[0][0], acc[1][0], acc[2][0], acc[3][0]); mfence<0>(acc[4][0], acc[5][0], acc[6][0], acc[7][0]); \
      ACT_OUT(ps, 0, 0) ACT_OUT(ps, 0, 1) sb(); \
      mfence<0>(acc[0][1], acc[1][1], acc[2][1], acc[3][1]); mfence<0>(acc[4][1], acc[5][1], acc[6][1], acc[7][1]); \
      ACT_OUT(ps, 1, 0) ACT_OUT(ps, 1, 1) sb(); }
#define W_RH(dst, k, u, hf) { const v2u t_ = ldtr(L + tb[k >= 7][u & 3], (k % 7) * 8192 + (u >> 2) * 4096 + hf * 2048); \
      dst[2 * hf] = t_.x; dst[2 * hf + 1] = t_.y; }
#pragma unroll
    for (int ps = 0; ps < 2; ++ps)
#pragma unroll
    for (int ks = 0; ks < 14; ++ks) {
      const int v = ps * 14 + ks, cur = v & 1, nk = (v + 1) % 14, vn = v < 27 ? v + 1 : v;
      const bool more = v < 27;
      u32 m0, m1;
      __builtin_amdgcn_s_waitcnt(0xC07F);
      sb();
#pragma unroll
      for (int i = 0; i < 16; ++i) {
        const int u = i & 7, tt = i >> 3;
        if (ks == 0) mv0(acc[u][tt], wfr[cur][u], hh_[v][tt]); else mv(acc[u][tt], wfr[cur][u], hh_[v][tt]);
        if (more) {
          if (i < 4) { W_RH(wfr[cur ^ 1][i], nk, i, 0) W_RH(wfr[cur ^ 1][i], nk, i, 1) }
          else if (i < 12) W_RH(wfr[cur ^ 1][4 + ((i - 4) >> 1)], nk, (4 + ((i - 4) >> 1)), (i & 1))
        }
        if (i == 5 && v + LEAD < 28) H_LD(v + LEAD, 0);
        if (i == 7 && v + LEAD < 28) H_LD(v + LEAD, 1);
        if (i == 9 || i == 11 || i == 13) {
          const int e = (i - 9) >> 1, f = WD_MID + (v < 14 ? v - 2 : 12 + WD_PER * (v - 14) + e);
          if (f < 56 && ((v >= 2 && v < 14 && e == 0) || (v >= 14 && e < WD_PER))) wdn[f >> 2][f & 3] = ld4c(ws, wl16 + (f & 3) * 1024, wdb + (f >> 2) * 4096);
        }
        if (v > A_V && (i & 1) && i >= 5 && ((i - 5) >> 1) < A_PER) G_LD(A_PER * (v - A_V - 1) + ((i - 5) >> 1));
        if (v == 8 && i == 13) fa = ld2c(ws, fx);
        if (more && i == 12) { m0 = max(max(hh_[vn][0].x, hh_[vn][0].y), hh_[vn][0].z); pin(m0); }
        if (more && i == 13) { m1 = max(max(hh_[vn][0].w, hh_[vn][1].x), hh_[vn][1].y); pin(m1); }
        if (more && i == 14) { m0 = max(max(m0, hh_[vn][1].z), hh_[vn][1].w); m0 = max(m0, m1); pin(m0); }
        sb();
      }
      if (v == 12 && !wall(fa.x == tag && fa.y == ~tag)) wait_flags(ws, fx, tag);
      if (more && __builtin_expect(wany(m0 == SENT), 0)) {
        const int pb = 2 * ((v + 1) / 14) * 14336 + nk * 1024;
        RETRY2(hh_[vn][0], hh_[vn][1], hb + pb, hb + pb + 14336)
      }
      if (v == 13) {
        STAMP(16); CSTAMP(29);
        ACT_ALL(0)
        if (isR) x2_store(ws, x2r, 16 * grp + j - 14, tid);
        nop2();
      }
      sb();
    }
    STAMP(27);
    ACT_ALL(1)
    CSTAMP(30);
#pragma unroll
    for (int e = A_PER * (27 - A_V); e < 28; ++e) G_LD(e);
    sb();
    STAMP(9);
    v4u a[14][2];
    v4f ac2[4][4];
#define A_LD(e) a[(e) >> 1][(e) & 1] = ld4c(ws, l16 + (((e) >> 1) & 3) * 1024, abx[(e) & 1] + 2 * 57344 + ((e) >> 3) * 4096)
    const int po = ab + 3 * 57344 + min(lane, 6) * 2048 + 1024;
    u32 probe;
#pragma unroll
    for (int ks = 0; ks < 14; ++ks) {
      if (__builtin_expect(BAD2(h[ks][0], h[ks][1]), 0)) { RETRY2(h[ks][0], h[ks][1], abx[0] + ks * 1024, abx[1] + ks * 1024) }
#pragma unroll
      for (int tt = 0; tt < 2; ++tt)
#pragma unroll
        for (int n = 0; n < 4; ++n) ac2[tt][n] = mfma(wdn[ks][n], h[ks][tt], ks ? ac2[tt][n] : splat(0.f));
      if (ks == P_AT) probe = ld1c(ws, po);
      sb();
      if (ks == B_AT2) {
        STAMP(25);
        if (wany(probe == SENT)) light_poll(ws, po);
        STAMP(26);
      }
      if (ks >= B_AT2) {
#pragma unroll
        for (int e = 4 * (ks - B_AT2); e < 4 * (ks - B_AT2 + 1); ++e) A_LD(e);
      }
      sb();
    }
    STAMP(17);
#pragma unroll
    for (int e = 4 * (14 - B_AT2); e < 4 * (14 - B_AT2) + 6; ++e) A_LD(e);
    sb();
    int rw[3], rr_ = L_B + lane * 8 + wave * 6144, oo[2];
#pragma unroll
    for (int x = 1; x < 4; ++x) { rw[x - 1] = L_B + lane * 8 + (wave ^ x) * 6144 + (x - 1) * 2048; pin(rw[x - 1]); }
    pin(rr_);
    const int blk = blk0 + (wave >> 1);
#pragma unroll
    for (int ph = 0; ph < 2; ++ph) { oo[ph] = (tok_row(blk + 2 * ph, c16) * D + ct * 64 + g * 16 + (wave & 1) * 8) * 2; pin(oo[ph]); }
#define RED_W1(ph, x, n) *(LAS v2f*)(L + rw[(x) - 1] + (ph) * 24576 + (n) * 512) = ((x) & 1) ? hi2(ac2[2 * (ph) + ((x) >> 1)][n]) : lo2(ac2[2 * (ph) + ((x) >> 1)][n]);
    const int xo = WS_X2 + blk * 14336 + c16 * 896 + ct * 128 + g * 32 + (wave & 1) * 16;
    const int go = WS_MOD + grp * MOD_REP + (70 + 2 * ct) * 128 + g * 64 + (wave & 1) * 32;
    v4u xv[2], gv[2];
    STAMP(18);
    sb();
#pragma unroll
    for (int ks = 0; ks < 14; ++ks) {
      if (__builtin_expect(BAD2(a[ks][0], a[ks][1]), 0)) { RETRY2(a[ks][0], a[ks][1], abx[0] + 2 * 57344 + ks * 1024, abx[1] + 2 * 57344 + ks * 1024) }
#pragma unroll
      for (int tt = 0; tt < 2; ++tt)
#pragma unroll
        for (int n = 0; n < 4; ++n) ac2[2 + tt][n] = mfma(wdn[ks][n], a[ks][tt], ks ? ac2[2 + tt][n] : splat(0.f));
#pragma unroll
      for (int e = 4 * (14 - B_AT2) + 6 + 3 * ks; e < 4 * (14 - B_AT2) + 9 + 3 * ks; ++e)
        if (e < 28) A_LD(e);
      if (ks == 6) { xv[0] = ld4c(ws, xo); xv[1] = ld4c(ws, xo + 2 * 14336); gv[0] = ld4c(ws, go); gv[1] = ld4c(ws, go + 16); }
      if (ks < 12) RED_W1(0, 1 + (ks >> 2), (ks & 3))
#pragma unroll
      for (int u = 0; u < 4; ++u) {
        __builtin_amdgcn_sched_group_barrier(0x8, 2, 0);
        __builtin_amdgcn_sched_group_barrier(0x20, 1, 0);
        if (u == 2) __builtin_amdgcn_sched_group_barrier(0x200, 1, 0);
      }
      sb();
    }
    STAMP(10);
#pragma unroll
    for (int e = 0; e < 12; ++e) RED_W1(1, 3 - (e >> 2), (e & 3))
#define FIN_OK() wall(max(max4(xv[0]), max4(xv[1])) != SENT && gv[0].y == tag && gv[0].w == tag && gv[1].y == tag && gv[1].w == tag)
    if (__builtin_expect(!FIN_OK(), 0)) {
      _Pragma("nounroll") for (int s = 0; s < SPIN; ++s) {
        const int gf = fresh(go), xf = fresh(xo);
        gv[0] = ld4c(ws, gf); gv[1] = ld4c(ws, gf + 16); xv[0] = ld4c(ws, xf); xv[1] = ld4c(ws, xf + 2 * 14336);
        if (FIN_OK()) break;
      }
    }
    bar();
#pragma unroll
    for (int ph = 0; ph < 2; ++ph) {
      v2f r[4];
#pragma unroll
      for (int n = 0; n < 4; ++n) {
        r[n] = lo2(ac2[2 * ph][n]);
#pragma unroll
        for (int w = 0; w < 3; ++w) r[n] += *(const LAS v2f*)(L + rr_ + w * 2048 + ph * 24576 + n * 512);
      }
      v4u o;
#pragma unroll
      for (int q = 0; q < 2; ++q) {
        o[2 * q] = pk2(fma2(bun(gv[q].x), v2f{r[0][q], r[1][q]}, bun(xv[ph][2 * q])));
        o[2 * q + 1] = pk2(fma2(bun(gv[q].z), v2f{r[2][q], r[3][q]}, bun(xv[ph][2 * q + 1])));
      }
      st4c(mkrs(out, S * D * 2), oo[ph], o);
    }
    if (blockIdx.x == 0 && wave == 0) {
      _Pragma("nounroll") for (int s = 0; s < SPIN; ++s) {
        const v4u v = ld4c(ws, fresh(WS_MOD + (70 + (lane < 14 ? lane : 0)) * 128));
        if (wall(v.y == tag)) break;
      }
      if (lane == 0) __builtin_amdgcn_raw_buffer_store_b32(epoch + 1u, ws, WS_EPOCH, 0, SC1);
    }
  }
  STAMP(11);
  DUMP();
}

extern "C" void kernel_launch(void* const* d_in, const int* in_sizes, int n_in, void* d_out, int out_size, void* d_ws,
                              size_t ws_size, hipStream_t stream) {
  fused<<<256, 256, 0, stream>>>((const u16*)d_in[0], (const u16*)d_in[1], (const u16*)d_in[2], (const u16*)d_in[4],
                                 (const u16*)d_in[5], (const u16*)d_in[6], (const u16*)d_in[7], (const u16*)d_in[8],
                                 (u16*)d_out, (char*)d_ws);
}
```

```cpp
#define PRE_OUT 0
#define OUT_AUX 0
#include <hip/hip_runtime.h>

typedef unsigned short u16;
typedef unsigned int u32;
typedef unsigned long long u64;
typedef u32 v4u __attribute__((ext_vector_type(4)));
typedef u32 v2u __attribute__((ext_vector_type(2)));
typedef float v4f __attribute__((ext_vector_type(4)));
typedef float v2f __attribute__((ext_vector_type(2)));
typedef short v8s __attribute__((ext_vector_type(8)));
typedef short v4s __attribute__((ext_vector_type(4)));
typedef __bf16 v8b __attribute__((ext_vector_type(8)));
typedef __bf16 v2b __attribute__((ext_vector_type(2)));
#define LAS __attribute__((address_space(3)))
#define GAS __attribute__((address_space(1)))
#define DEV __device__ __forceinline__

#define C_W1_EARLY 0
#define CLR_DELAY 0
#define W_DELAY 0
#define CLR2_DELAY 0
#define PF_POS 2
#define SL1 1
#define SL2 2
#define SL4 4
#define X_AFTER 8
#define PF_AUX 2
#define W_LATE 1
#define W1_PF 1
#define DMA_MOD ""
#define LEAD 6
#define A_V 22
#define A_PER 4
#define WDV(f) ((f) - WD_MID + 2 < 14 ? (f) - WD_MID + 2 : ((f) - WD_MID - 12) / 2 + 16)
#define B_AT2 12
#define RE_PF 0
#define PF2_DELAY 0
#define C_DMA_EARLY 0
#define EVEN_PF 0
#define XBAL 1
#define W1EVEN 1
#define P_AT 7
#define WD_PER 3
#define A_FIRST 10
#define B_FIRST 10
#define A_AT 9
#define F_AT 4
#define CODE_BYTES 45056
#define WD_EARLY 16
#define WD_MID 24
#define B_AT 9
constexpr int S = 2048, D = 448, DF = 225, F = 1792;

struct alignas(16) Chain { u16 v[2048]; };
struct Cos448 { u16 v[448]; };
constexpr double c_cos_t(double a) {
  double a2 = a * a, t = 1.0, s = 1.0;
  for (int i = 1; i <= 10; ++i) { t = -t * a2 / ((2 * i - 1) * (2 * i)); s += t; }
  return s;
}
constexpr double c_sin_t(double a) {
  double a2 = a * a, t = a, s = a;
  for (int i = 1; i <= 10; ++i) { t = -t * a2 / ((2 * i) * (2 * i + 1)); s += t; }
  return s;
}
constexpr double c_cos448(int m) {
  if (m > 224) m = 448 - m;
  double sg = 1.0;
  if (m > 112) { m = 224 - m; sg = -1.0; }
  const double w = 6.283185307179586476925286766559 / 448.0;
  return m == 112 ? 0.0 : (m <= 56 ? sg * c_cos_t(w * m) : sg * c_sin_t(w * (112 - m)));
}
constexpr u16 c_bf16(double d) {
  const u32 u = __builtin_bit_cast(u32, (float)d);
  return (u16)((u + 0x7FFFu + ((u >> 16) & 1u)) >> 16);
}
constexpr Cos448 mk_cos() { Cos448 c{}; for (int m = 0; m < 448; ++m) c.v[m] = c_bf16(c_cos448(m)); return c; }
constexpr Cos448 CTAB = mk_cos();
constexpr u16 bcos(int m) { return CTAB.v[m % 448]; }
constexpr u16 bsin(int m) { return CTAB.v[(m % 448 + 336) % 448]; }
constexpr u16 bneg(u16 b) { return b ? (u16)(b ^ 0x8000u) : b; }
constexpr u16 bscale(u16 b, int e) { return b ? (u16)(b + e * 0x80) : b; }

constexpr Chain mk_p1(int c) {
  const int tile = c >> 2, par = c & 1, sn = (c >> 1) & 1;
  Chain ch{};
  for (int s = 0; s < 4; ++s)
    for (int l = 0; l < 64; ++l) {
      const int kp = 16 * tile + (l & 15), k = 2 * kp + par;
      if (k > 224) continue;
      for (int j = 0; j < 8; ++j) {
        const int dl = 32 * s + 8 * (l >> 4) + j;
        if (dl > 112) continue;
        u16 b = sn ? bneg(bsin(k * dl)) : bcos(k * dl);
        if (dl == 0 || dl == 112) b = bscale(b, -1);
        ch.v[(s * 64 + l) * 8 + j] = b;
      }
    }
  return ch;
}
constexpr Chain mk_p3(int c) {
  const int tile = c >> 1, sn = c & 1;
  Chain ch{};
  for (int s = 0; s < 4; ++s)
    for (int l = 0; l < 64; ++l) {
      int dl = 16 * tile + (l & 15);
      if (c == 1 && dl == 0) dl = 112;
      for (int j = 0; j < 8; ++j) {
        const int k = 2 * (32 * s + 8 * (l >> 4) + j) + 1;
        if (k < 224) ch.v[(s * 64 + l) * 8 + j] = sn ? bsin(k * dl) : bcos(k * dl);
      }
    }
  return ch;
}
constexpr Chain mk_p2(int c) {
  Chain ch{};
  for (int f = 0; f < 4; ++f)
    for (int l = 0; l < 64; ++l)
      for (int j = 0; j < 8; ++j) {
        const int comp = c < 8 ? (c & 1) : (f >> 1), s = c < 8 ? f : (f & 1);
        const int o = 16 * (c < 8 ? (c >> 1) : (c - 8)) + (l & 15), i = 16 * s + 4 * (l >> 4) + (j >> 1);
        const int m = (c < 8 ? 7 : 14) * ((i * o) % 64);
        u16 b = 0;
        if (comp == 0) b = (j & 1) ? bsin(m) : bcos(m);
        else b = (j & 1) ? bcos(m) : bneg(bsin(m));
        ch.v[(f * 64 + l) * 8 + j] = b;
      }
  return ch;
}
template <int I> constexpr Chain P1C = mk_p1(I);
template <int I> constexpr Chain P3C = mk_p3(I);
template <int I> constexpr Chain P2C = mk_p2(I);
#define L10(T, b) T<b>, T<b + 1>, T<b + 2>, T<b + 3>, T<b + 4>, T<b + 5>, T<b + 6>, T<b + 7>, T<b + 8>, T<b + 9>
#define L29(T) L10(T, 0), L10(T, 10), T<20>, T<21>, T<22>, T<23>, T<24>, T<25>, T<26>, T<27>, T<28>
constexpr int TAB_P1 = 0, TAB_P3 = 29, TAB_P2 = 43, TAB_N = 53;
static __device__ const Chain TAB[TAB_N] = {L29(P1C), L10(P3C, 0), P3C<10>, P3C<11>, P3C<12>, P3C<13>, L10(P2C, 0)};

constexpr int WS_EPOCH = 0;
constexpr int WS_RDY = 4096;
constexpr int WS_HRDY = 4096 + 8192;
constexpr int WS_DRDY = 4096 + 16384;
constexpr int WS_XRDY = 131072 + 65536;
constexpr int WS_ARDY = 131072;
constexpr int WS_CRDY = 131072 + 32768;
constexpr int WS_MOD = 32768, MOD_REP = 12288;
constexpr int T1_BLK = 226 * 64;
constexpr int WS_T1 = 1 << 18;
constexpr int WS_WT = WS_T1 + 128 * T1_BLK;
constexpr int WS_H2 = WS_WT + 113 * 16384;
constexpr int WS_X2 = WS_H2 + 128 * 14336;
constexpr int WS_WD = WS_X2 + 128 * 14336;
constexpr int WS_ACT = WS_WD + 7 * 56 * 4096;
constexpr int WS_END = WS_ACT + 128 * 57344;
constexpr int WS_DBG = 32 << 20;

constexpr int L_W1 = 0;
constexpr int L_B = 114688;
constexpr int XP = 912;
constexpr int L_XS = L_B;
constexpr int L_ST = L_XS + 16 * XP;
constexpr int L_M1 = L_ST + 128;
constexpr int L_AF = L_M1 + 4 * 1792;
constexpr int L_M2 = L_AF + 16384;
constexpr int L_EX = L_M2 + 3 * 1792;
constexpr int L_REND = L_EX + 512;
constexpr int ZPL = 8192;
constexpr int L_CI = L_B;
constexpr int L_CX = L_CI + 2 * 8256;
constexpr int L_RED = L_CX + 16384;
constexpr int L_DBG = 163840 - 1024;
constexpr int L_SZ = 163840;
static_assert(L_REND <= L_DBG && L_RED + 2048 <= L_DBG && 2 * ZPL <= 16384, "lds");

constexpr u32 SENT = 0xFFFFFFFFu;
constexpr int SPIN = 1 << 14;
constexpr int SC1 = 16;
constexpr int OOB = 0x7FFF0000;

typedef __amdgpu_buffer_rsrc_t rsrc_t;
DEV rsrc_t mkrs(const void* p, int bytes) { return __builtin_amdgcn_make_buffer_rsrc((void*)p, 0, bytes, 0x00020000); }
DEV v4u ld4(rsrc_t r, int off, int so = 0) { return __builtin_amdgcn_raw_buffer_load_b128(r, off, so, 0); }
DEV v2u ld2(rsrc_t r, int off, int so = 0) { return __builtin_amdgcn_raw_buffer_load_b64(r, off, so, 0); }
DEV v4u ld4c(rsrc_t r, int off, int so = 0) { return __builtin_amdgcn_raw_buffer_load_b128(r, off, so, SC1); }
DEV v2u ld2c(rsrc_t r, int off, int so = 0) { return __builtin_amdgcn_raw_buffer_load_b64(r, off, so, SC1); }
DEV u32 ld1c(rsrc_t r, int off, int so = 0) { return __builtin_amdgcn_raw_buffer_load_b32(r, off, so, SC1); }
DEV void st4c(rsrc_t r, int off, v4u v) { __builtin_amdgcn_raw_buffer_store_b128(v, r, off, 0, SC1); }
DEV void st2c(rsrc_t r, int off, v2u v) { __builtin_amdgcn_raw_buffer_store_b64(v, r, off, 0, SC1); }
DEV float blo(u32 v) { return __builtin_bit_cast(float, v << 16); }
DEV float bhi(u32 v) { return __builtin_bit_cast(float, v & 0xFFFF0000u); }
DEV v2f bun(u32 v) { return v2f{blo(v), bhi(v)}; }
DEV u32 pk(float lo, float hi) { return __builtin_bit_cast(u32, __builtin_convertvector(v2f{lo, hi}, v2b)); }
DEV u32 pk2(v2f v) { return __builtin_bit_cast(u32, __builtin_convertvector(v, v2b)); }
DEV float rb(float f) { return blo(pk(f, 0.f)); }
DEV v2f lo2(v4f v) { return __builtin_shufflevector(v, v, 0, 1); }
DEV v2f hi2(v4f v) { return __builtin_shufflevector(v, v, 2, 3); }
DEV v2f fma2(v2f a, v2f b, v2f c) { return __builtin_elementwise_fma(a, b, c); }
DEV v4f fma4(v4f a, v4f b, v4f c) { return __builtin_elementwise_fma(a, b, c); }
typedef int v4i __attribute__((ext_vector_type(4)));
DEV v4f relu4(v4f v) { return __builtin_bit_cast(v4f, __builtin_elementwise_max(__builtin_bit_cast(v4i, v), v4i{0, 0, 0, 0})); }
DEV v4f splat(float f) { return v4f{f, f, f, f}; }
DEV v4f mfma(v4u a, v4u b, v4f c) {
  return __builtin_amdgcn_mfma_f32_16x16x32_bf16(__builtin_bit_cast(v8b, a), __builtin_bit_cast(v8b, b), c, 0, 0, 0);
}
DEV void mfa(v4f& c, v4u a, v4u b) { asm volatile("s_nop 1\n\tv_mfma_f32_16x16x32_bf16 %0, %1, %2, %0" : "+v"(c) : "v"(a), "v"(b)); }
DEV void mf0(v4f& c, v4u a, v4u b) { asm volatile("s_nop 1\n\tv_mfma_f32_16x16x32_bf16 %0, %1, %2, 0" : "=&v"(c) : "v"(a), "v"(b)); }
template <int N> DEV void mfence(v4f& a, v4f& b) { asm volatile("s_nop %2" : "+v"(a), "+v"(b) : "i"(N)); }
template <int N> DEV void mfence(v4f& a, v4f& b, v4f& c, v4f& d) { asm volatile("s_nop %4" : "+v"(a), "+v"(b), "+v"(c), "+v"(d) : "i"(N)); }
#define MF "v_mfma_f32_16x16x32_bf16 "
DEV void mf8(v4f& d0, v4f& d1, const v4u (&a0)[4], const v4u (&b0)[4], const v4u (&a1)[4], const v4u (&b1)[4]) {
  asm volatile("s_nop 1\n\t" MF "%0, %2, %6, 0\n\t" MF "%1, %10, %14, 0\n\t" MF "%0, %3, %7, %0\n\t" MF "%1, %11, %15, %1\n\t"
               MF "%0, %4, %8, %0\n\t" MF "%1, %12, %16, %1\n\t" MF "%0, %5, %9, %0\n\t" MF "%1, %13, %17, %1"
               : "=&v"(d0), "=&v"(d1)
               : "v"(a0[0]), "v"(a0[1]), "v"(a0[2]), "v"(a0[3]), "v"(b0[0]), "v"(b0[1]), "v"(b0[2]), "v"(b0[3]),
                 "v"(a1[0]), "v"(a1[1]), "v"(a1[2]), "v"(a1[3]), "v"(b1[0]), "v"(b1[1]), "v"(b1[2]), "v"(b1[3]));
}
#define H4_M(XA, D, X, K, C) MF D ", " XA(X, K) ", " C "\n\t"
#define H4_XA(X, K) X ", " K
#define H4_XB(X, K) K ", " X
#define H4_DEF(NAME, XA, C0, C1, CONS, N0, N1, PA, PB, PC, PD) \
DEV void NAME(v4f& d0, v4f& d1, v4u x0, v4u x1, v4u k00, v4u k01, v4u k10, v4u k11, \
              u32& o0, u32& o1, float p0, float q0, float c0, float s0, float p1, float q1, float c1, float s1) { \
  float t0, t1, t2, t3; \
  asm volatile(H4_M(XA, "%0", "%8", "%10", C0) "v_mul_f32 %4, %14, %16\n\tv_mul_f32 %5, %15, %16\n\t" \
               H4_M(XA, "%1", "%8", "%12", C1) "v_mul_f32 %6, %18, %20\n\tv_mul_f32 %7, %19, %20\n\t" \
               H4_M(XA, "%0", "%9", "%11", "%0") "v_fma_f32 %4, " N0 "%15, %17, %4\n\tv_fma_f32 %5, " N1 "%14, %17, %5\n\t" \
               H4_M(XA, "%1", "%9", "%13", "%1") "v_fma_f32 %6, " N0 "%19, %21, %6\n\tv_fma_f32 %7, " N1 "%18, %21, %7\n\t" \
               "v_cvt_pk_bf16_f32 %2, " PA ", " PB "\n\tv_cvt_pk_bf16_f32 %3, " PC ", " PD \
               : CONS(d0), CONS(d1), "=&v"(o0), "=&v"(o1), "=&v"(t0), "=&v"(t1), "=&v"(t2), "=&v"(t3) \
               : "v"(x0), "v"(x1), "v"(k00), "v"(k01), "v"(k10), "v"(k11), \
                 "v"(p0), "v"(q0), "v"(c0), "v"(s0), "v"(p1), "v"(q1), "v"(c1), "v"(s1)); \
}
H4_DEF(h4_a0m, H4_XA, "0", "0", "=&v", "", "-", "%4", "%5", "%6", "%7")
H4_DEF(h4_a1m, H4_XA, "%0", "%1", "+v", "", "-", "%4", "%5", "%6", "%7")
H4_DEF(h4_b0m, H4_XB, "0", "0", "=&v", "", "-", "%4", "%5", "%6", "%7")
H4_DEF(h4_b0p, H4_XB, "0", "0", "=&v", "-", "", "%5", "%4", "%7", "%6")
H4_DEF(h4_a0p, H4_XA, "0", "0", "=&v", "-", "", "%5", "%4", "%7", "%6")
#define M4_DEF(NAME, XA, C0, C1, CONS) \
DEV void NAME(v4f& d0, v4f& d1, v4u x0, v4u x1, v4u k00, v4u k01, v4u k10, v4u k11) { \
  asm volatile(H4_M(XA, "%0", "%2", "%4", C0) H4_M(XA, "%1", "%2", "%6", C1) H4_M(XA, "%0", "%3", "%5", "%0") MF "%1, " XA("%3", "%7") ", %1" \
               : CONS(d0), CONS(d1) : "v"(x0), "v"(x1), "v"(k00), "v"(k01), "v"(k10), "v"(k11)); \
}
M4_DEF(m4_a0, H4_XA, "0", "0", "=&v") M4_DEF(m4_a1, H4_XA, "%0", "%1", "+v")
M4_DEF(m4_b0, H4_XB, "0", "0", "=&v") M4_DEF(m4_b1, H4_XB, "%0", "%1", "+v")
DEV void mv(v4f& c, v4u a, v4u b) { asm volatile(MF "%0, %1, %2, %0" : "+v"(c) : "v"(a), "v"(b)); }
DEV void mv0(v4f& c, v4u a, v4u b) { asm volatile(MF "%0, %1, %2, 0" : "=&v"(c) : "v"(a), "v"(b)); }
DEV void nop2() { asm volatile("s_nop 1"); }
template <class T> DEV void pin(T& v) { asm volatile("" : "+v"(v)); }
DEV int fresh(int off) { asm volatile("" : "+v"(off) :: "memory"); return off; }
DEV bool wall(bool ok) { return __builtin_amdgcn_ballot_w64(!ok) == 0; }
DEV bool wany(bool b) { return __builtin_amdgcn_ballot_w64(b) != 0; }
DEV void sb() { __builtin_amdgcn_sched_barrier(0); }
DEV u32 max4(v4u v) { return max(max(v.x, v.y), max(v.z, v.w)); }
DEV void drain() { asm volatile("s_waitcnt vmcnt(0)" ::: "memory"); }
DEV void cfence() { asm volatile("" ::: "memory"); }
DEV void bar() { asm volatile("s_waitcnt lgkmcnt(0)\n\ts_barrier" ::: "memory"); }
template <int CTRL> DEV float dpp(float v) {
  return __builtin_bit_cast(float, __builtin_amdgcn_update_dpp(0, __builtin_bit_cast(int, v), CTRL, 0xF, 0xF, true));
}
DEV float sum16(float v) {
  v += dpp<0x128>(v); v += dpp<0x124>(v); v += dpp<0x122>(v); v += dpp<0x121>(v); return v;
}
DEV void sumg2(float& a, float& b) {
  float c = a, d = b;
  asm volatile("s_nop 1\n\tv_permlane16_swap_b32 %0, %1\n\tv_permlane16_swap_b32 %2, %3" : "+v"(a), "+v"(c), "+v"(b), "+v"(d));
  a += c; b += d; c = a; d = b;
  asm volatile("s_nop 1\n\tv_permlane32_swap_b32 %0, %1\n\tv_permlane32_swap_b32 %2, %3" : "+v"(a), "+v"(c), "+v"(b), "+v"(d));
  a += c; b += d;
}
DEV v4u lds4(const char LAS* L, int off) { return *(const LAS v4u*)(L + off); }
DEV v4f lds4f(const char LAS* L, int off) { return *(const LAS v4f*)(L + off); }
DEV v2u ldtr(const char LAS* L, int off) { return __builtin_bit_cast(v2u, __builtin_amdgcn_ds_read_tr16_b64_v4i16((LAS v4s*)(L + off))); }
DEV v4u ldtr2(const char LAS* L, int o0, int o1) { const v2u a = ldtr(L, o0), b = ldtr(L, o1); return v4u{a.x, a.y, b.x, b.y}; }
DEV void dma16(rsrc_t r, int voff, int soff, u32 lds_dst) {
  u32 keep;
  asm volatile("s_mov_b32 %0, m0\n\ts_mov_b32 m0, %1\n\ts_nop 0\n\tbuffer_load_dwordx4 %2, %3, %4 offen" DMA_MOD " lds\n\ts_mov_b32 m0, %0"
               : "=&s"(keep) : "s"(lds_dst), "v"(voff), "s"(r), "s"(soff) : "memory");
}
DEV void light_poll(rsrc_t ws, int off) {
  _Pragma("nounroll") for (int s = 0; s < SPIN; ++s) {
    if (wall(ld1c(ws, fresh(off)) != SENT)) break;
    __builtin_amdgcn_s_sleep(SL2);
  }
}
DEV int tok_row(int blk, int t) {
  const int n1 = 16 * (blk >> 5) + 4 * ((blk >> 2) & 3) + (t & 3), n2 = 16 * ((blk >> 4) & 1) + 4 * (blk & 3) + (t >> 2);
  return n2 + 32 * n1;
}
template <bool SWAP> DEV v4u cmul_pack(v4f a, v4f b, v4f c, v4f s) {
  const v4f re = fma4(b, s, a * c), im = fma4(b, c, -(a * s));
  return SWAP ? v4u{pk(im.x, re.x), pk(im.y, re.y), pk(im.z, re.z), pk(im.w, re.w)}
              : v4u{pk(re.x, im.x), pk(re.y, im.y), pk(re.z, im.z), pk(re.w, im.w)};
}

template <bool PLUS> DEV void cmul2(u32& o0, u32& o1, v2f p, v2f q, v2f c, v2f s) {
  const v2f re = fma2(PLUS ? -q : q, s, p * c), im = fma2(PLUS ? p : -p, s, q * c);
  o0 = PLUS ? pk(im.x, re.x) : pk(re.x, im.x); o1 = PLUS ? pk(im.y, re.y) : pk(re.y, im.y);
}

#define STAMP(i)
#define CSTAMP(i)
#define SLOWCNT
#define DUMP()

DEV void dma_w1(const u16* wg, const u16* wu, int slice, int wave, int lane, u32 lbase) {
  const rsrc_t rg = mkrs(wg, D * F * 2), ru = mkrs(wu, D * F * 2);
  const int r = lane >> 3, ch = ((((lane & 7) >> 1) ^ (lane >> 4)) & 3) * 32 + (lane & 1) * 16 + slice * 128;
  const int vp = r * (F * 2) + ch, vn = (7 - r) * (F * 2) + ch;
#pragma unroll
  for (int n = 0; n < 4; ++n) {
    const int ks = wave + 4 * n;
    if (n == 3 && wave >= 2) break;
    const int t16 = (ks >> 1) * 16, a = ks & 1;
#pragma unroll
    for (int q = 0; q < 4; ++q) {
      const int dl0 = t16 + 8 * (q & 1);
      const bool pos = (q >> 1) == a;
      const int d0 = q < 2 ? (a ? 224 - dl0 - 7 : dl0) : (a ? 224 + dl0 : 448 - dl0 - 7);
      int vo = pos ? vp : vn, so = d0 * (F * 2);
      if (n == 0 && q == 2) {
        vo += so; so = 0;
        if (wave < 2 && lane < 8) vo = (wave ? 336 : 112) * (F * 2) + ch;
      }
      dma16(rg, vo, so, lbase + L_W1 + ks * 8192 + q * 1024);
      dma16(ru, vo, so, lbase + L_W1 + ks * 8192 + 4096 + q * 1024);
    }
  }
}
struct ModLoad { u32 cv[7]; v4u mw[7]; };
DEV void mod_load(ModLoad& m, const u16* cin, const u16* wm, int q, int tid) {
  const rsrc_t cr = mkrs(cin, q >= 0 ? D * 2 : 0), mr = mkrs(wm, q >= 0 ? D * 6 * D * 2 : 0);
#pragma unroll
  for (int i = 0; i < 7; ++i) m.cv[i] = __builtin_amdgcn_raw_buffer_load_b16(cr, (tid >> 2) * 2 + i * 128, 0, 0);
#pragma unroll
  for (int i = 0; i < 7; ++i) m.mw[i] = __builtin_amdgcn_raw_buffer_load_b128(mr, ((tid >> 2) * (6 * D) + q * 32 + (tid & 3) * 8) * 2, i * 64 * 6 * D * 2, PF_AUX);
}
DEV void mod_part(ModLoad& m, char LAS* L, int tid) {
  float sl[7];
#pragma unroll
  for (int i = 0; i < 7; ++i) { const float cc = blo(m.cv[i]); sl[i] = rb(cc * __builtin_amdgcn_rcpf(1.f + __expf(-cc))); pin(sl[i]); }
  v2f a[4];
#pragma unroll
  for (int e = 0; e < 4; ++e) a[e] = v2f{0.f, 0.f};
#pragma unroll
  for (int i = 0; i < 7; ++i)
#pragma unroll
    for (int e = 0; e < 4; ++e) a[e] = fma2(bun(m.mw[i][e]), v2f{sl[i], sl[i]}, a[e]);
#pragma unroll
  for (int e = 0; e < 4; ++e) {
    a[e].x += dpp<0x128>(a[e].x); a[e].y += dpp<0x128>(a[e].y);
    a[e].x += dpp<0x124>(a[e].x); a[e].y += dpp<0x124>(a[e].y);
  }
  if ((tid & 15) < 4) {
    *(LAS v4f*)(L + L_RED + ((tid >> 4) * 32 + (tid & 15) * 8) * 4) = v4f{a[0].x, a[0].y, a[1].x, a[1].y};
    *(LAS v4f*)(L + L_RED + ((tid >> 4) * 32 + (tid & 15) * 8 + 4) * 4) = v4f{a[2].x, a[2].y, a[3].x, a[3].y};
  }
}
DEV void mod_publish(char LAS* L, rsrc_t ws, int q, int lane, u32 tag) {
  v4f s = {0.f, 0.f, 0.f, 0.f};
#pragma unroll
  for (int r = 0; r < 16; ++r) s += lds4f(L, L_RED + (r * 32 + (lane & 7) * 4) * 4);
  const float one = (q / 14 == 1 || q / 14 == 4) ? 1.f : 0.f;
  st4c(ws, WS_MOD + (lane >> 3) * MOD_REP + q * 128 + (lane & 7) * 16,
       v4u{pk(rb(s.x) + one, rb(s.y) + one), tag, pk(rb(s.z) + one, rb(s.w) + one), tag});
}
DEV u32 ld1(rsrc_t r, int off) { return __builtin_amdgcn_raw_buffer_load_b32(r, off, 0, 0); }
DEV u32 ld1n(rsrc_t r, int off) { return __builtin_amdgcn_raw_buffer_load_b32(r, off, 0, PF_AUX); }
DEV u32 mktag(u32 epoch) { return epoch * 0x9E3779B1u + 0x7F4A7C15u; }

DEV void x2_read(const char LAS* L, v4u (&r)[4], int tid) {
#pragma unroll
  for (int m = 0; m < 4; ++m) { const int ch = min(tid + 256 * m, 895), tk = ch / 56; r[m] = lds4(L, L_XS + tk * XP + (ch - tk * 56) * 16); }
}
DEV void x2_store(rsrc_t ws, const v4u (&r)[4], int blk, int tid) {
#pragma unroll
  for (int m = 0; m < 4; ++m) st4c(ws, tid + 256 * m < 896 ? WS_X2 + blk * 14336 + (tid + 256 * m) * 16 : OOB, r[m]);
}
DEV bool flags_ok(v4u rv, u32 tag) { return rv.x == tag && rv.y == ~tag && rv.z == tag && rv.w == ~tag; }
DEV void wait_flags(rsrc_t ws, int off, u32 tag) {
  _Pragma("nounroll") for (int s = 0; s < SPIN; ++s) {
    const v2u rv = ld2c(ws, fresh(off));
    if (wall(rv.x == tag && rv.y == ~tag)) break;
    __builtin_amdgcn_s_sleep(SL1);
  }
}

__global__ void __launch_bounds__(256, 1)
fused(const u16* __restrict__ xin, const u16* __restrict__ wr, const u16* __restrict__ wi, const u16* __restrict__ wg,
      const u16* __restrict__ wu, const u16* __restrict__ wd, const u16* __restrict__ cin, const u16* __restrict__ wm,
      u16* __restrict__ out, char* wsp) {
  __shared__ __attribute__((aligned(16))) char smem[L_SZ];
  char LAS* L = (char LAS*)smem;
  const u32 lbase = (u32)(size_t)L;
  const int tid = threadIdx.x, lane = tid & 63, wave = __builtin_amdgcn_readfirstlane(tid >> 6);
  const int g = lane >> 4, c16 = lane & 15, l16 = lane * 16;
  const int grp = blockIdx.x & 7, j = blockIdx.x >> 3;
  u64 pc;
  asm volatile("s_getpc_b64 %0" : "=s"(pc));
  STAMP(0); CSTAMP(31);
  asm volatile("" :: "s"(xin), "s"(wr), "s"(wi), "s"(wg), "s"(wu), "s"(wd), "s"(cin), "s"(wm), "s"(out), "s"(wsp));
  const rsrc_t ws = mkrs(wsp, WS_END);
  const rsrc_t tab = mkrs(TAB, TAB_N * 4096);
  const rsrc_t code = mkrs((const void*)(pc & ~127ull), CODE_BYTES);
  const bool isR = j >= 14 && j < 30;
  const bool isG = j < 28;
  const int gk = j / 7, ct = j - gk * 7;
  const int tile = 4 * (grp >> 2) + gk, slice = 7 * (grp & 3) + ct;
  const int wdb = WS_WD + (ct * 56 + wave * 14) * 4096;
  const v4u sv = {SENT, SENT, SENT, SENT};
  int lb = L_B + l16;
  pin(lb);
  const char LAS* LB = L + lb - L_B;
  const int pfo = ((j * 4 + wave) * 14 + lane) * 128, pfc = ((j * 4 + wave) * 4 + lane) * 128;
  u32 pfa = ld1(tab, lane < 14 ? pfo : OOB), pfb = ld1(code, lane < 4 ? pfc : OOB);
  const int hro = WS_HRDY + (16 * tile + 4 * (lane < 32 ? wave : grp & 3)) * 32 + c16 * 8;
  v4u x2r[4];
  const int wl16 = (lane ^ ((wave & 1) << 1)) * 16;
  v4u wdn[14][4];
  u32 epoch, tag;

  if (isR) {
    const int blk = 16 * grp + (j - 14);
    epoch = ld1c(ws, WS_EPOCH);
    const int t0 = wave == 0 ? 0 : 2 * wave - 1;
    v4u cf[8][4];
#define CF_LOAD(q) { const int chn = TAB_P1 + ((wave == 0 && q >= 4) ? 28 : 4 * t0 + q), vo = (q < 5 || wave != 0) ? l16 : OOB; \
      _Pragma("unroll") for (int s = 0; s < 4; ++s) cf[q][s] = ld4(tab, vo + s * 1024, chn * 4096); sb(); }
#pragma unroll
    for (int q = 0; q < X_AFTER; ++q) CF_LOAD(q)
    v2u xv[7];
    {
      const rsrc_t xr = mkrs(xin, S * D * 2);
      const int off = tok_row(blk, 4 * wave + g) * (D * 2) + c16 * 8;
#pragma unroll
      for (int m = 0; m < 7; ++m) xv[m] = ld2(xr, off + m * 128);
    }
    typedef u32 v3u __attribute__((ext_vector_type(3)));
    v2u fa_[2], fb_[2]; v3u fc_[2], fd_[2];
    {
      const rsrc_t rr = mkrs(wr, S * DF * 2), ri = mkrs(wi, S * DF * 2);
      const int fo_ = 16 * wave + c16 < 57 ? 2 * g * 450 + (16 * wave + c16) * 8 : OOB;
#pragma unroll
      for (int hh = 0; hh < 2; ++hh) {
        const int so = blk * 7200 + hh * 3600;
        fa_[hh] = ld2(rr, fo_, so); fb_[hh] = ld2(ri, fo_, so);
        fc_[hh] = __builtin_amdgcn_raw_buffer_load_b96(rr, fo_ + 448, so, 0); fd_[hh] = __builtin_amdgcn_raw_buffer_load_b96(ri, fo_ + 448, so, 0);
      }
    }
    sb();
#pragma unroll
    for (int q = X_AFTER; q < 8; ++q) CF_LOAD(q)
    STAMP(23);
    {
      v2f f[14], s2 = {0.f, 0.f};
#pragma unroll
      for (int m = 0; m < 7; ++m) {
        f[2 * m] = bun(xv[m].x); f[2 * m + 1] = bun(xv[m].y);
        s2 += f[2 * m] + f[2 * m + 1];
        *(LAS v2u*)(L + L_XS + (4 * wave + g) * XP + c16 * 8 + m * 128) = xv[m];
      }
      STAMP(21);
      sb();
      {
        const int q = 16 * wave + c16, o = WS_WT + q * 16384 + blk * 128 + g * 16;
#pragma unroll
        for (int hh = 0; hh < 2; ++hh) {
          const v2u a = fa_[hh], b = fb_[hh]; const v3u c = fc_[hh], d = fd_[hh];
          st4c(ws, q < 57 ? o + hh * 64 : OOB, v4u{__builtin_amdgcn_perm(b.x, a.x, 0x05040100u), __builtin_amdgcn_perm(b.y, a.y, 0x05040100u),
                                                   __builtin_amdgcn_perm(d.x, c.x, 0x07060302u), __builtin_amdgcn_perm(d.y, c.y, 0x07060302u)});
          st4c(ws, q < 56 ? o + hh * 64 + 57 * 16384 : OOB,
               v4u{__builtin_amdgcn_perm(b.x, a.x, 0x07060302u), __builtin_amdgcn_perm(b.y, a.y, 0x07060302u),
                   __builtin_amdgcn_perm(d.y, c.y, 0x05040100u), __builtin_amdgcn_perm(d.z, c.z, 0x05040100u)});
        }
      }
      sb();
      const float mean = sum16(s2.x + s2.y) * (1.f / D);
      v2f q2 = {0.f, 0.f};
#pragma unroll
      for (int m = 0; m < 14; ++m) { const v2f d = f[m] - mean; q2 = fma2(d, d, q2); }
      const float rstd = __builtin_amdgcn_rsqf(sum16(q2.x + q2.y) * (1.f / D) + 1e-5f);
      if (c16 == 0) *(LAS v2f*)(L + L_ST + (4 * wave + g) * 8) = v2f{mean, rstd};
    }
    pin(pfa); pin(pfb);
    epoch = __builtin_amdgcn_readfirstlane(epoch);
    tag = mktag(epoch);
    STAMP(22);
    bar();
    v4f n[4][2];
    const int d0 = 8 * (4 * wave + g);
    const v2f st = *(const LAS v2f*)(L + L_ST + c16 * 8);
    {
      int xr_ = L_XS + c16 * XP + d0 * 2, xb_ = L_XS + c16 * XP + (216 - d0) * 2, x0_ = d0 == 0 ? xr_ : xb_ + 464;
      pin(xr_); pin(xb_); pin(x0_);
      const v4u a = lds4(L + xr_, 0), e = lds4(L + xr_, 448), c = lds4(L + xb_, 0), b = lds4(L + xb_, 448);
      const u32 c0 = *(const LAS u16*)(L + xb_ + 16), b0 = *(const LAS u16*)(L + x0_);
#pragma unroll
      for (int i = 0; i < 8; ++i) {
        n[0][i >> 2][i & 3] = (i & 1) ? bhi(a[i >> 1]) : blo(a[i >> 1]);
        n[3][i >> 2][i & 3] = (i & 1) ? bhi(e[i >> 1]) : blo(e[i >> 1]);
        n[1][i >> 2][i & 3] = i == 0 ? blo(b0) : (i & 1) ? bhi(b[(8 - i) >> 1]) : blo(b[(8 - i) >> 1]);
        n[2][i >> 2][i & 3] = i == 0 ? blo(c0) : (i & 1) ? bhi(c[(8 - i) >> 1]) : blo(c[(8 - i) >> 1]);
      }
      const float nm = -st.x * st.y;
#pragma unroll
      for (int m = 0; m < 4; ++m) {
#pragma unroll
        for (int h = 0; h < 2; ++h) { n[m][h] = fma4(n[m][h], splat(st.y), splat(nm)); pin(n[m][h]); }
      }
    }
    sb();
    STAMP(1);
    int mb = L_M1 + d0 * 4;
    pin(mb);
    v4u xs[4];
    int to[4];
    to[0] = WS_T1 + blk * T1_BLK + (16 * t0 + c16) * 64 + g * 16;
    to[1] = wave != 0 ? to[0] + 1024 : c16 < 2 ? to[0] + 112 * 64 : OOB;
    to[2] = to[0] + 114 * 64;
    to[3] = wave != 0 ? to[0] + 1024 + 114 * 64 : OOB;
#pragma unroll
    for (int q = 0; q < 4; ++q) pin(to[q]);

    {
      const int kind = tid >= 112, d = 4 * (tid - 112 * kind);
      int wb = L_M1 + (kind ? 0 : 2 * 1792) + d * 4, wrv = L_M1 + (kind ? 0 : 2 * 1792) + 1792 + (445 - d) * 4;
      int wr0 = d == 0 ? wrv + 12 - 1792 : wrv + 12;
      const int mo = tid < 224 ? WS_MOD + grp * MOD_REP + tid * 16 : OOB;
      pin(wb); pin(wrv); pin(wr0);
      const int ro = WS_RDY + grp * 1024 + min(tid >> 1, 112) * 8;
      v4u mv;
      _Pragma("nounroll") for (int s = 0; s < SPIN; ++s) {
        mv = ld4c(ws, fresh(mo));
        const v2u rv = ld2c(ws, fresh(ro));
        if (wall((tid >= 224 || (mv.y == tag && mv.w == tag)) && rv.x == tag && rv.y == ~tag)) break;
        __builtin_amdgcn_s_sleep(SL1);
      }
      STAMP(2);
      if (tid < 224) {
        const v4f v = {blo(mv.x), bhi(mv.x), blo(mv.z), bhi(mv.z)};
        *(LAS v4f*)(L + wb) = v;
        *(LAS float*)(L + wr0) = v.x;
        *(LAS float*)(L + wrv + 8) = v.y;
        *(LAS float*)(L + wrv + 4) = v.z;
        *(LAS float*)(L + wrv) = v.w;
      }
    }
    bar();
    {
      v4u fr[4];
#pragma unroll
      for (int hh = 0; hh < 2; ++hh) {
        v4f h[4];
#pragma unroll
        for (int m = 0; m < 4; ++m) {
          const int o = ((m == 1 || m == 2) ? 1792 : 0) + (m >= 2 ? 896 : 0) + hh * 16;
          h[m] = fma4(n[m][hh], lds4f(L + mb, o), lds4f(L + mb, 2 * 1792 + o));
        }
        const v4f p = h[0] + h[1], q = h[2] + h[3], u = h[0] - h[1], v = h[3] - h[2];
        const v4f t[4] = {p + q, p - q, u + v, u - v};
#pragma unroll
        for (int m = 0; m < 4; ++m) { fr[m][2 * hh] = pk2(lo2(t[m])); fr[m][2 * hh + 1] = pk2(hi2(t[m])); }
      }
#pragma unroll
      for (int m = 0; m < 4; ++m) *(LAS v4u*)(LB + wave * 1024 + L_AF + m * 4096) = fr[m];
    }
    bar();
    {
      v4u a[4][4];
#pragma unroll
      for (int p = 0; p < 2; ++p)
#pragma unroll
        for (int s = 0; s < 4; ++s) { a[p][s] = lds4(LB, L_AF + (p * 4 + s) * 1024); a[2 + p][s] = lds4(LB, L_AF + ((2 + p) * 4 + s) * 1024); }
      v4f re[4], im[4];
#pragma unroll
      for (int q = 0; q < 5; ++q) {
        if (q < 4) mf8(re[q], im[q], a[q >> 1], cf[4 * (q & 1) + (q >> 1)], a[2 + (q >> 1)], cf[4 * (q & 1) + 2 + (q >> 1)]);
        if (q > 0) {
          if (q < 4) mfence<0>(re[q - 1], im[q - 1]); else mfence<7>(re[3], im[3]);
          xs[q - 1] = v4u{pk(re[q - 1].x, im[q - 1].x), pk(re[q - 1].y, im[q - 1].y), pk(re[q - 1].z, im[q - 1].z), pk(re[q - 1].w, im[q - 1].w)};
          st4c(ws, to[q - 1], xs[q - 1]);
          sb();
        }
      }
    }
    STAMP(3);
    sb();
    sb();
    if (isG) dma_w1(wg, wu, slice, wave, lane, lbase);
#pragma unroll
    for (int q = 0; q < 4; ++q) {
      const int vo = (q < 2 || wave != 0) ? l16 : OOB;
#pragma unroll
      for (int s = 0; s < 4; ++s) cf[2 * q + 1][s] = ld4(tab, vo + s * 1024, (TAB_P3 + 2 * t0 + q) * 4096);
    }
#pragma unroll
    for (int m = 0; m < 4; ++m) {
      st4c(ws, tid + 256 * m < 896 ? WS_H2 + blk * 14336 + 4096 * m + tid * 16 : OOB, sv);
      st4c(ws, tid + 256 * m < 896 ? WS_X2 + blk * 14336 + 4096 * m + tid * 16 : OOB, sv);
    }
    sb();
    STAMP(20);
    v4f xo[2][4];
    int xa = L_XS + c16 * XP + (16 * t0 + 4 * g) * 2, xd = L_XS + c16 * XP + (192 - 16 * t0 - 4 * g) * 2;
    int xs1 = tid < 16 ? L_XS + c16 * XP + 224 : xd + 512, xs3 = tid < 16 ? L_XS + c16 * XP + 672 : xa + 448;
    pin(xa); pin(xd); pin(xs1); pin(xs3);
#define XOFF(t, m, r) ((t) == 0 && (r) == 0 && (m) == 1 ? xs1 : (t) == 0 && (r) == 0 && (m) == 3 ? xs3 : \
    (m) == 0 ? xa + 32 * (t) + 2 * (r) : (m) == 3 ? xa + 448 + 32 * (t) + 2 * (r) : (m) == 1 ? xd + 512 - 32 * (t) - 2 * (r) : xd + 64 - 32 * (t) - 2 * (r))
    {
      v2u w[2][4]; u32 e[2][2], e3;
#pragma unroll
      for (int t = 0; t < 2; ++t) {
        w[t][0] = *(const LAS v2u*)(L + xa + 32 * t); w[t][3] = *(const LAS v2u*)(L + xa + 448 + 32 * t);
        w[t][1] = *(const LAS v2u*)(L + xd + 504 - 32 * t); w[t][2] = *(const LAS v2u*)(L + xd + 56 - 32 * t);
        e[t][0] = *(const LAS u16*)(L + XOFF(t, 1, 0)); e[t][1] = *(const LAS u16*)(L + XOFF(t, 2, 0));
      }
      e3 = *(const LAS u16*)(L + xs3);
      sb();
#pragma unroll
      for (int t = 0; t < 2; ++t) {
        xo[t][0] = v4f{blo(w[t][0].x), bhi(w[t][0].x), blo(w[t][0].y), bhi(w[t][0].y)};
        xo[t][3] = v4f{t == 0 ? blo(e3) : blo(w[t][3].x), bhi(w[t][3].x), blo(w[t][3].y), bhi(w[t][3].y)};
        xo[t][1] = v4f{blo(e[t][0]), bhi(w[t][1].y), blo(w[t][1].y), bhi(w[t][1].x)};
        xo[t][2] = v4f{blo(e[t][1]), bhi(w[t][2].y), blo(w[t][2].y), bhi(w[t][2].x)};
#pragma unroll
        for (int m = 0; m < 4; ++m) { xo[t][m] -= st.x; pin(xo[t][m]); }
      }
    }
    int m0_[2], m1_[2], md_[2];
#pragma unroll
    for (int r = 0; r < 2; ++r) {
      const int q = r == 0 || tid < 80 ? tid + 256 * r : tid;
      const int kind = (q * 586) >> 16, d = 4 * (q - 112 * kind);
      const int reg = (d >= 112) + (d >= 224) + (d >= 336), cc = ((reg + 1) >> 1) * 224, m1 = (0x1320 >> (4 * reg)) & 3;
      const int d1 = abs(d + 1 - cc), d0_ = abs(d - cc), edge = d == 112 * reg && reg > 0;
      m1_[r] = L_M2 + kind * 1792 + (((d1 >> 4) << 6) | (m1 << 4) | (d1 & 15)) * 4;
      m0_[r] = L_M2 + kind * 1792 + (edge ? reg << 4 : ((d0_ >> 4) << 6) | (m1 << 4) | (d0_ & 15)) * 4;
      md_[r] = (reg & 1) ? -4 : 4;
      pin(m0_[r]); pin(m1_[r]); pin(md_[r]);
    }
    {
      v4u mv[2];
      _Pragma("nounroll") for (int s = 0; s < SPIN; ++s) {
        mv[0] = ld4c(ws, fresh(WS_MOD + grp * MOD_REP + (224 + tid) * 16));
        mv[1] = ld4c(ws, fresh(WS_MOD + grp * MOD_REP + (224 + (tid < 80 ? 256 + tid : tid)) * 16));
        const v2u rv = ld2c(ws, fresh(WS_DRDY + grp * 1024 + min(lane, 48) * 8));
        if (wall(mv[0].y == tag && mv[0].w == tag && mv[1].y == tag && mv[1].w == tag && rv.x == tag && rv.y == ~tag)) break;
        __builtin_amdgcn_s_sleep(SL4);
      }
      if (lane == 0) st2c(ws, WS_HRDY + blk * 32 + wave * 8, v2u{tag, ~tag});
      STAMP(12);
#pragma unroll
      for (int f = 0; f < WD_EARLY; ++f) wdn[f >> 2][f & 3] = ld4c(ws, (isG ? wl16 : OOB) + (f & 3) * 1024, wdb + (f >> 2) * 4096);
      sb();
#pragma unroll
      for (int r = 0; r < 2; ++r) {
        const float sc = m0_[r] < L_M2 + 1792 ? 2.f / (float)(S * D) : 1.f;
        *(LAS float*)(L + m0_[r]) = blo(mv[r].x) * sc;
        *(LAS float*)(L + m1_[r]) = bhi(mv[r].x) * sc;
        *(LAS float*)(L + m1_[r] + md_[r]) = blo(mv[r].z) * sc;
        *(LAS float*)(L + m1_[r] + 2 * md_[r]) = bhi(mv[r].z) * sc;
      }
    }
    bar();
    *(LAS v4u*)(L + L_AF + (tid >> 6) * 4096 + 3072 + l16) = v4u{0u, 0u, 0u, 0u};
    STAMP(4);
    int zr = L_AF + lane * 8, ro = L_M2 + (t0 * 64 + 4 * g) * 4, ro1 = wave == 0 ? ro : ro + 256, ex = L_EX + c16 * 8;
    pin(zr); pin(ro); pin(ro1); pin(ex);
    {
      int zl[4];
#pragma unroll
      for (int m = 0; m < 4; ++m) {
        const int kq = (m >> 1) * 128 + (to[m] == OOB ? 127 : wave == 0 && m == 1 ? 112 + c16 : 16 * (t0 + (m & 1)) + c16);
        zl[m] = L_AF + ((kq & ~31) + ((kq & 4) << 2) + ((kq & 24) >> 1) + (kq & 3)) * 32 + g * 8;
        pin(zl[m]);
        if (to[m] == OOB) xs[m] = sv;
      }
      bar();
      if (isG) wait_flags(ws, hro, tag);
      {
        const int src = (blk * 5 + wave * 13) & 63;
        const int po = __builtin_amdgcn_readlane(to[(0)], 0) + (src & 15) * 64 + (src >> 4) * 16;
        const u32 old = __builtin_amdgcn_ds_bpermute(src * 4, xs[0].x);
        _Pragma("nounroll") for (int s = 0; s < SPIN; ++s) {
          if (wall(ld1c(ws, fresh(po)) != old)) break;
          __builtin_amdgcn_s_sleep(SL2);
        }
      }
      STAMP(13);
      v4u z[4];
      _Pragma("nounroll") for (int s = 0; s < SPIN; ++s) {
        u32 mn = 1;
#pragma unroll
        for (int m = 0; m < 4; ++m) z[m] = ld4c(ws, fresh(to[m]));
#pragma unroll
        for (int m = 0; m < 4; ++m) { const v4u d = z[m] ^ xs[m]; mn = min(min(mn, d.x), min(min(d.y, d.z), d.w)); }
        if (wall(mn != 0)) break;
      }
      STAMP(14);
#pragma unroll
      for (int m = 0; m < 4; ++m) {
        char LAS* p = L + zl[m];
        *(LAS v2u*)(p) = v2u{__builtin_amdgcn_perm(z[m].y, z[m].x, 0x05040100u), __builtin_amdgcn_perm(z[m].w, z[m].z, 0x05040100u)};
        *(LAS v2u*)(p + ZPL) = v2u{__builtin_amdgcn_perm(z[m].y, z[m].x, 0x07060302u), __builtin_amdgcn_perm(z[m].w, z[m].z, 0x07060302u)};
      }
    }
    bar();
    STAMP(5);
    {
      v4u zf[4][4];
#pragma unroll
      for (int ty = 0; ty < 4; ++ty)
#pragma unroll
        for (int s = 0; s < 4; ++s) zf[ty][s] = ldtr2(L + zr, ty * 4096 + s * 1024, ty * 4096 + s * 1024 + 512);
      v4f acc[2][4], x2[2][4], gav[2][4];
      v2f s1 = {0.f, 0.f}, s2 = {0.f, 0.f};
#define P3M(t, ty) mf8(acc[t][ty], acc[t][ty + 1], cf[4 * t + ty], zf[ty], cf[4 * t + ty + 1], zf[ty + 1]);
#define P3X(t, m, yv) { x2[t][m] = fma4(gav[t][m], yv, xo[t][m]); \
      if (t == 0 || wave != 0) { s1 += lo2(x2[t][m]) + hi2(x2[t][m]); \
        s2 = fma2(lo2(x2[t][m]), lo2(x2[t][m]), s2); s2 = fma2(hi2(x2[t][m]), hi2(x2[t][m]), s2); } }
      P3M(1, 0) P3M(1, 2)
#pragma unroll
      for (int m = 0; m < 4; ++m) { gav[0][m] = lds4f(L + ro, m * 64); gav[1][m] = lds4f(L + ro1, m * 64); }
      sb();
      P3M(0, 0) P3M(0, 2)
      mfence<0>(acc[1][0], acc[1][1], acc[1][2], acc[1][3]);
      const v4f A1 = acc[1][0] + acc[1][1], B1 = acc[1][0] - acc[1][1], P1 = acc[1][3] - acc[1][2], Q1 = acc[1][2] + acc[1][3];
      P3X(1, 0, A1 - P1) P3X(1, 1, A1 + P1) P3X(1, 2, B1 - Q1) P3X(1, 3, B1 + Q1)
      sb();
      mfence<0>(acc[0][0], acc[0][1], acc[0][2], acc[0][3]);
      {
        const v4f A = acc[0][0] + acc[0][1], B = acc[0][0] - acc[0][1], P = acc[0][3] - acc[0][2], Q = acc[0][2] + acc[0][3];
        v4f y[4] = {A - P, A + P, B - Q, B + Q};
        const bool sp = tid < 16;
        y[0].x = sp ? A.x : y[0].x; y[2].x = sp ? B.x : y[2].x;
        y[1].x = sp ? acc[1][0].x - acc[0][3].x : y[1].x; y[3].x = sp ? acc[1][0].x + acc[0][3].x : y[3].x;
        P3X(0, 0, y[0]) P3X(0, 1, y[1]) P3X(0, 2, y[2]) P3X(0, 3, y[3])
      }
      float sm = s1.x + s1.y, sq = s2.x + s2.y;
      sumg2(sm, sq);
      if (g == 0) *(LAS v2f*)(L + ex + wave * 128) = v2f{sm, sq};
      v4f sbv[2][4], cbv[2][4];
#pragma unroll
      for (int m = 0; m < 4; ++m) {
        sbv[0][m] = lds4f(L + ro, 1792 + m * 64); cbv[0][m] = lds4f(L + ro, 2 * 1792 + m * 64);
        sbv[1][m] = lds4f(L + ro1, 1792 + m * 64); cbv[1][m] = lds4f(L + ro1, 2 * 1792 + m * 64);
      }
      bar();
      v2f e = *(const LAS v2f*)(L + ex);
#pragma unroll
      for (int w = 1; w < 4; ++w) e += *(const LAS v2f*)(L + ex + w * 128);
      const float mc = e.x * (1.f / D), rstd = __builtin_amdgcn_rsqf(fmaxf(e.y * (1.f / D) - mc * mc, 0.f) + 1e-5f), nb = -mc * rstd;
      const int ho = WS_H2 + blk * 14336 + 2 * t0 * 1024 + l16, ho1 = wave == 0 ? OOB : ho + 2048;
#pragma unroll
      for (int t = 0; t < 2; ++t)
#pragma unroll
        for (int a = 0; a < 2; ++a) {
          v4u o;
#pragma unroll
          for (int hh = 0; hh < 2; ++hh) {
            const int m = 2 * a + hh;
            const v4f hv = fma4(fma4(x2[t][m], splat(rstd), splat(nb)), cbv[t][m], sbv[t][m]);
            o[2 * hh] = pk2(lo2(hv)); o[2 * hh + 1] = pk2(hi2(hv));
          }
          st4c(ws, (t ? ho1 : ho) + a * 1024, o);
        }
      STAMP(6);
      sb();
#pragma unroll
      for (int f = WD_EARLY; f < WD_MID; ++f) wdn[f >> 2][f & 3] = ld4c(ws, (isG ? wl16 : OOB) + (f & 3) * 1024, wdb + (f >> 2) * 4096);
      sb();
#pragma unroll
      for (int t = 0; t < 2; ++t)
        if (t == 0 || wave != 0)
#pragma unroll
          for (int m = 0; m < 4; ++m)
#pragma unroll
            for (int r = 0; r < 4; r += 2) {
              const u32 p = pk(x2[t][m][r] + st.x, x2[t][m][r + 1] + st.x);
              *(LAS u16*)(L + XOFF(t, m, r)) = (u16)p;
              *(LAS u16*)(L + XOFF(t, m, r + 1)) = (u16)(p >> 16);
            }
      sb();
    }
    bar();
    x2_read(L, x2r, tid);
    bar();
    if (!isG) {
      x2_store(ws, x2r, blk, tid);
      DUMP(); return;
    }
  } else {
    const int sj = j >= 30 ? j - 30 : j + 2;
    const int idx = sj * 8 + grp;
    const bool isC = j < 14 || idx == 0;
    const int pair = j < 14 ? grp * 14 + j : 112;
    const int lc = grp + 8 * (sj >> 1);
    const int qu = sj < 4 && lc < 14 ? 2 * lc + (sj & 1) : -1;
    const int t1o = WS_T1 + (32 * wave + (lane >> 3)) * T1_BLK + pair * 128 + (lane & 7) * 16;
    ModLoad ml;
    epoch = ld1c(ws, WS_EPOCH);
    if (qu >= 0) mod_load(ml, cin, wm, qu, tid);
#pragma unroll
    for (int q = 0; q < 4; ++q) st4c(ws, isC ? t1o + 8 * q * T1_BLK : OOB, sv);
    u32 pf0, pf1, pf2;
    {
      const rsrc_t mr = mkrs(wm, D * 6 * D * 2), xr = mkrs(xin, S * D * 2), fr_ = mkrs((wave & 1) ? wi : wr, S * DF * 2);
      const int row = (sj * 4 + wave) * 7 + (lane >> 1), l7 = lane / 7;
      pf0 = __builtin_amdgcn_raw_buffer_load_b32(mr, lane < 14 && (grp < 6 || !(lane & 1)) ? row * (6 * D * 2) + (grp + 8 * (lane & 1)) * 128 : OOB, 0, PF_AUX);
      pf1 = __builtin_amdgcn_raw_buffer_load_b32(xr, lane < (!XBAL ? 28 : sj < 4 ? 0 : sj < 12 ? 28 : 56) ? tok_row(16 * grp + (lane < 28 ? sj : sj - 12), 4 * wave + (l7 & 3)) * (D * 2) + (lane - 7 * l7) * 128 : OOB, 0, PF_AUX);
    }
    sb();
    epoch = __builtin_amdgcn_readfirstlane(epoch);
    tag = mktag(epoch);
    if (qu >= 0) mod_part(ml, L, tid);
    asm volatile("s_waitcnt vmcnt(%0)" :: "i"(W_LATE ? 2 : 3) : "memory");
    { const rsrc_t fr_ = mkrs((wave & 1) ? wi : wr, S * DF * 2);
      pf2 = __builtin_amdgcn_raw_buffer_load_b32(fr_, (lane & 31) < 29 && lane < (!XBAL ? 32 : sj < 4 ? 0 : sj < 12 ? 32 : 64) ? (((16 * grp + (lane < 32 ? sj : sj - 12)) * 7200) & ~127) + ((wave >> 1) * 29 + (lane & 31)) * 128 : OOB, 0, PF_AUX);
    }
    bar();
    if (wave == 0 && qu >= 0) mod_publish(L, ws, qu, lane, tag);
    if (wave == 1 && lane < 8) st2c(ws, isC ? WS_RDY + lane * 1024 + pair * 8 : OOB, v2u{tag, ~tag});
    STAMP(1);
    pin(pf0); pin(pf1); pin(pf2); pin(pfa); pin(pfb);
    const int qn = sj >= 4 && sj < 10 && grp < 7 ? 28 + grp * 6 + (sj - 4) : !isC && idx < 15 ? 69 + idx : -1;
    const int tw = sj >= 10 ? (sj - 10) * 8 + grp : sj == 4 && grp == 7 ? 48 : -1;
    v2u dw[2][8];
    if (qn >= 0) mod_load(ml, cin, wm, qn, tid);
    if (tw >= 0) {
      const rsrc_t dr = mkrs(wd, F * D * 2);
      const int u0 = tw * 4 + wave, uc = u0 / 28, kap = 2 * (u0 - uc * 28);
#pragma unroll
      for (int e = 0; e < 16; ++e)
        dw[e >> 3][e & 7] = __builtin_amdgcn_raw_buffer_load_b64(dr, (4 * g * D + 4 * c16) * 2 + (e & 3) * D * 2, ((32 * kap + 16 * (e >> 2)) * D + 64 * uc) * 2, PF_AUX);
    }
    sb();
    u32 pw[4];
    {
      const rsrc_t rg = mkrs(wg, D * F * 2), ru = mkrs(wu, D * F * 2);
      const int r7 = lane / 7, r0 = W1EVEN ? (j * 4 + wave) * 8 : sj < 4 ? (sj * 4 + wave) * 16 : 256 + ((sj - 4) * 4 + wave) * 4;
      const int po = (r0 + r7) * (F * 2) + ((grp & 3) * 7 + lane - r7 * 7) * 128;
      const int o0 = lane < (W1EVEN ? (j < 14 ? 56 : 0) : sj < 4 ? 56 : 28) ? po : OOB, o1 = !W1EVEN && lane < 56 && sj < 4 ? po + 8 * (F * 2) : OOB;
      pw[0] = ld1(rg, o0); pw[1] = ld1(ru, o0); pw[2] = ld1(rg, o1); pw[3] = ld1(ru, o1);
    }
    sb();
    STAMP(20);
    v4f t1c[2], t1s[2], t2c[2], t2s[2];
#pragma unroll
    for (int t = 0; t < 2; ++t) {
#pragma unroll
      for (int r = 0; r < 4; ++r) {
        const float a1 = (float)((16 * t + 4 * g + r) * (16 * wave + c16)) * (1.f / S);
        const float a2 = (float)((16 * t + c16) * (16 * wave + 4 * g + r)) * (1.f / S);
        t1c[t][r] = __builtin_amdgcn_cosf(a1); t1s[t][r] = __builtin_amdgcn_sinf(a1);
        t2c[t][r] = __builtin_amdgcn_cosf(a2); t2s[t][r] = __builtin_amdgcn_sinf(a2);
      }
      pin(t1c[t]); pin(t1s[t]); pin(t2c[t]); pin(t2s[t]);
    }
    if (tw >= 0) {
#pragma unroll
      for (int tt = 0; tt < 8; ++tt) {
        v4u o;
#pragma unroll
        for (int m = 0; m < 4; ++m)
          o[m] = __builtin_amdgcn_perm(dw[tt >> 2][2 * m + 1][(tt & 3) >> 1], dw[tt >> 2][2 * m][(tt & 3) >> 1], (tt & 1) ? 0x07060302u : 0x05040100u);
        st4c(ws, WS_WD + (tw * 4 + wave) * 8192 + tt * 1024 + l16, o);
      }
    }
    if (qn >= 0) {
      bar();
      mod_part(ml, L, tid);
      bar();
      if (wave == 0) mod_publish(L, ws, qn, lane, tag);
    }
    pin(pw[0]); pin(pw[1]); pin(pw[2]); pin(pw[3]);
    STAMP(21);
    if (!isC) { DUMP(); return; }
    v4u c64[2][4], c32[2][2][2];
#pragma unroll
    for (int cp = 0; cp < 2; ++cp)
#pragma unroll
      for (int s = 0; s < 4; ++s) c64[cp][s] = ld4(tab, l16 + s * 1024, (TAB_P2 + wave * 2 + cp) * 4096);
#pragma unroll
    for (int t = 0; t < 2; ++t)
#pragma unroll
      for (int f = 0; f < 4; ++f) c32[t][f >> 1][f & 1] = ld4(tab, l16 + f * 1024, (TAB_P2 + 8 + t) * 4096);
    sb();
    if (tw >= 0) {
      asm volatile("s_waitcnt vmcnt(16)" ::: "memory");
      bar();
      if (tid < 8) st2c(ws, WS_DRDY + tid * 1024 + tw * 8, v2u{tag, ~tag});
    }
    STAMP(2);
    int wfo = WS_WT + pair * 16384 + (16 * g + wave) * 128 + c16 * 8;
    pin(wfo);
    v2u wf[2][4];
    {
      int wl = L_CI + ((lane >> 2) & 1) * 8256 + wave * 2048 + ((lane >> 3) * 4 + (lane & 3)) * 16, lo = t1o;
      pin(wl); pin(lo);
      light_poll(ws, WS_T1 + (32 * wave + (pair & 31)) * T1_BLK + pair * 128);
      STAMP(13);
      v4u z[4];
      _Pragma("nounroll") for (int s = 0; s < SPIN; ++s) {
        lo = fresh(lo);
#pragma unroll
        for (int q = 0; q < 4; ++q) z[q] = ld4c(ws, lo, 8 * q * T1_BLK);
        if (wall(max(max(max4(z[0]), max4(z[1])), max(max4(z[2]), max4(z[3]))) != SENT)) break;
      }
      STAMP(14);
#pragma unroll
      for (int q = 0; q < 4; ++q) *(LAS v4u*)(L + wl + q * 512) = z[q];
    }
    bar();
#pragma unroll
    for (int t = 0; t < 2; ++t)
#pragma unroll
      for (int r = 0; r < 4; ++r) wf[t][r] = ld2c(ws, wfo + r * 512, t * 8192);
    v2u fd = ld2c(ws, WS_DRDY + grp * 1024 + min(lane, 48) * 8);
    sb();
    STAMP(4);
    {
      v4u a[2][2][4];
#pragma unroll
      for (int c = 0; c < 2; ++c)
#pragma unroll
        for (int mt = 0; mt < 2; ++mt)
#pragma unroll
          for (int s = 0; s < 4; ++s) a[c][mt][s] = lds4(LB, L_CI + c * 8256 + (s * 2 + mt) * 1024);
      v4u old[2][2];
#pragma unroll
      for (int c = 0; c < 2; ++c)
#pragma unroll
        for (int mt = 0; mt < 2; ++mt) old[c][mt] = lds4(LB + wave * 2048, L_CI + c * 8256 + mt * 1024);
      v4f re[2][2], im[2][2], ur[2][2], ui[2][2], fr_[2][2], fi_[2][2];
      u32 f2[2][2][4], f3[2][2][4], f4[4];
      char LAS* xw = L + lb + wave * 1024 - L_B;
#define V4(x) v4u{x[0], x[1], x[2], x[3]}
#define S1K(h) c64[0][2 * h], c64[0][2 * h + 1], c64[1][2 * h], c64[1][2 * h + 1]
#define S1X(c, t, h) re[c][t], im[c][t], a[c][t][2 * h], a[c][t][2 * h + 1], S1K(h)
#define S2X(c, t) ur[c][t], ui[c][t], V4(f2[c][0]), V4(f2[c][1]), c32[t][0][0], c32[t][0][1], c32[t][1][0], c32[t][1][1]
#define S3X(c, t) im[c][t], re[c][t], V4(f3[c][0]), V4(f3[c][1]), c32[t][0][0], c32[t][0][1], c32[t][1][0], c32[t][1][1]
#define CM(o, P, Q, C, S_, h) o[2 * h], o[2 * h + 1], P[2 * h], Q[2 * h], C[2 * h], S_[2 * h], P[2 * h + 1], Q[2 * h + 1], C[2 * h + 1], S_[2 * h + 1]
#define TW1(c, t, h) CM(f2[c][t], re[c][t], im[c][t], t1c[t], t1s[t], h)
#define FLT(c, t, h) CM(f3[c][t], ur[c][t], ui[c][t], fr_[c][t], fi_[c][t], h)
#define TW2(c, t, h) CM(f4, re[c][t], im[c][t], t2c[t], t2s[t], h)
#define HV(x, h) (h ? hi2(x) : lo2(x))
#define CP(PL, o, P, Q, C, S_, h) cmul2<PL>(o[2 * h], o[2 * h + 1], HV(P, h), HV(Q, h), HV(C, h), HV(S_, h));
#define XW(c, t) *(LAS v4u*)(xw + L_CX + (c * 2 + t) * 4096) = V4(f4);
#define UNP(c, t) { _Pragma("unroll") for (int r = 0; r < 4; ++r) { fr_[c][t][r] = blo(wf[t][r][c]); fi_[c][t][r] = bhi(wf[t][r][c]); } \
      pin(fr_[c][t]); pin(fi_[c][t]); }
      nop2();
      m4_a0(S1X(0, 0, 0)); m4_a1(S1X(0, 0, 1)); m4_a0(S1X(0, 1, 0)); mfence<3>(re[0][0], im[0][0]);
      h4_a1m(S1X(0, 1, 1), TW1(0, 0, 0)); h4_a0m(S1X(1, 0, 0), TW1(0, 0, 1));
      h4_a1m(S1X(1, 0, 1), TW1(0, 1, 0)); h4_a0m(S1X(1, 1, 0), TW1(0, 1, 1));
      h4_a1m(S1X(1, 1, 1), TW1(1, 0, 0)); h4_b0m(S2X(0, 0), TW1(1, 0, 1));
      h4_b0m(S2X(0, 1), TW1(1, 1, 0));
      sb();
      CP(false, f2[1][1], re[1][1], im[1][1], t1c[1], t1s[1], 1)
      UNP(0, 0) UNP(0, 1)
      sb();
      h4_b0p(S2X(1, 0), FLT(0, 0, 0)); h4_b0p(S2X(1, 1), FLT(0, 0, 1));
      sb();
      CP(true, f3[0][1], ur[0][1], ui[0][1], fr_[0][1], fi_[0][1], 0) CP(true, f3[0][1], ur[0][1], ui[0][1], fr_[0][1], fi_[0][1], 1)
      UNP(1, 0) UNP(1, 1)
      sb();
      h4_a0p(S3X(0, 0), FLT(1, 0, 0)); h4_a0p(S3X(0, 1), FLT(1, 0, 1));
      sb();
      CP(true, f3[1][1], ur[1][1], ui[1][1], fr_[1][1], fi_[1][1], 0) CP(true, f3[1][1], ur[1][1], ui[1][1], fr_[1][1], fi_[1][1], 1)
#pragma unroll
      for (int e = 0; e < 4; ++e) pin(f3[1][1][e]);
      nop2();
      h4_a0p(S3X(1, 0), TW2(0, 0, 0)); h4_a0p(S3X(1, 1), TW2(0, 0, 1));
      XW(0, 0)
      sb();
      CP(true, f4, re[0][1], im[0][1], t2c[1], t2s[1], 0) CP(true, f4, re[0][1], im[0][1], t2c[1], t2s[1], 1) XW(0, 1)
      sb();
      mfence<0>(re[1][0], im[1][0], re[1][1], im[1][1]);
      CP(true, f4, re[1][0], im[1][0], t2c[0], t2s[0], 0) CP(true, f4, re[1][0], im[1][0], t2c[0], t2s[0], 1) XW(1, 0)
      CP(true, f4, re[1][1], im[1][1], t2c[1], t2s[1], 0) CP(true, f4, re[1][1], im[1][1], t2c[1], t2s[1], 1) XW(1, 1)
      bar();
#pragma unroll
      for (int c = 0; c < 2; ++c)
#pragma unroll
        for (int nt = 0; nt < 2; ++nt)
#pragma unroll
          for (int s = 0; s < 4; ++s) a[c][nt][s] = lds4(LB, L_CX + ((c * 2 + nt) * 4 + s) * 1024);
      const int o2 = WS_T1 + (32 * wave + (lane >> 2)) * T1_BLK + pair * 128 + (lane & 3) * 16;
#define S4(c, t) mf8(im[c][t], re[c][t], c64[0], a[c][t], c64[1], a[c][t]);
#define NEQ(x, y) ((x) == (y) ? (x) ^ 1u : (x))
#define OUT(c, t) { const v4u o = {pk(re[c][t].x, im[c][t].x), pk(re[c][t].y, im[c][t].y), pk(re[c][t].z, im[c][t].z), pk(re[c][t].w, im[c][t].w)}; \
      st4c(ws, o2 + c * 64 + t * 16 * T1_BLK, v4u{NEQ(o.x, old[c][t].x), NEQ(o.y, old[c][t].y), NEQ(o.z, old[c][t].z), NEQ(o.w, old[c][t].w)}); sb(); }
      S4(0, 0) S4(0, 1) mfence<0>(re[0][0], im[0][0]); OUT(0, 0)
      S4(1, 0) mfence<0>(re[0][1], im[0][1]); OUT(0, 1)
      S4(1, 1) mfence<0>(re[1][0], im[1][0]); OUT(1, 0)
      mfence<0>(re[1][1], im[1][1]); OUT(1, 1)
    }
    STAMP(5);
    sb();
    if (!isG) { DUMP(); return; }
    if (!wall(fd.x == tag && fd.y == ~tag)) wait_flags(ws, WS_DRDY + grp * 1024 + min(lane, 48) * 8, tag);
    dma_w1(wg, wu, slice, wave, lane, lbase);
#pragma unroll
    for (int f = 0; f < WD_MID; ++f) wdn[f >> 2][f & 3] = ld4c(ws, wl16 + (f & 3) * 1024, wdb + (f >> 2) * 4096);
    fd = ld2c(ws, hro);
    sb();
#pragma unroll
    for (int m = 0; m < 16; ++m)
      st4c(ws, WS_ACT + (16 * (tile + 2 * (m >> 3)) + 4 * (grp & 3)) * 57344 + ct * 32768 + (m & 7) * 4096 + tid * 16, sv);
    sb();
    if (!wall(fd.x == tag && fd.y == ~tag)) wait_flags(ws, hro, tag);
    bar();
  }

  STAMP(7);
  const int blk0 = 16 * tile + 4 * (grp & 3);
  const int ab = WS_ACT + blk0 * 57344 + wave * 14 * 1024;
  v4u h[14][2];
  {
    const int hb = WS_H2 + (16 * tile + 4 * wave) * 14336;
    int tb[2][4];
#pragma unroll
    for (int u = 0; u < 8; ++u) {
      tb[u >> 2][u & 3] = (((lane >> 2) * 128 + ((lane >> 3) & 3) * 32 + (lane & 3) * 8) ^ ((u & 3) << 5)) + (u >> 2) * 57344;
      pin(tb[u >> 2][u & 3]);
    }
#define W_RD(dst, k, u) dst = ldtr2(L + tb[k >= 7][u & 3], (k % 7) * 8192 + (u >> 2) * 4096, (k % 7) * 8192 + (u >> 2) * 4096 + 2048)
#define H_LD(v, tt) hh_[v][tt] = ld4c(ws, l16 + (((v) % 14) & 3) * 1024, hb + (2 * ((v) / 14) + (tt)) * 14336 + (((v) % 14) >> 2) * 4096)
    const int abx[2] = {ab + (wave >> 1) * 57344, ab + (1 - (wave >> 1)) * 57344};
#define G_LD(e) h[(e) >> 1][(e) & 1] = ld4c(ws, l16 + (((e) >> 1) & 3) * 1024, abx[(e) & 1] + ((e) >> 3) * 4096)
    v4u wfr[2][8], hh_[28][2];
#pragma unroll
    for (int u = 0; u < 8; ++u) W_RD(wfr[0][u], 0, u);
    light_poll(ws, hb + (lane & 1) * 14336 + 13 * 1024);
    STAMP(15);
#pragma unroll
    for (int v = 0; v < LEAD; ++v) { H_LD(v, 0); H_LD(v, 1); }
    sb();
    if (!isR && lane < 2)
      st2c(ws, WS_CRDY + ((tile + 2 * lane) * 4 + (grp & 3)) * 256 + (ct * 4 + wave) * 8, v2u{tag, ~tag});
    sb();
#define BAD2(x0, x1) wany(max(max(max(max(x0.x, x0.y), x0.z), max(max(x0.w, x1.x), x1.y)), max(x1.z, x1.w)) == SENT)
#define RETRY2(x0, x1, o0, o1) \
      _Pragma("nounroll") for (int s_ = 0; s_ < SPIN; ++s_) { \
        const int lf_ = fresh(l16); SLOWCNT \
        x0 = ld4c(ws, lf_, o0); x1 = ld4c(ws, lf_, o1); \
        if (wall(max(max4(x0), max4(x1)) != SENT)) break; \
      }
    if (__builtin_expect(BAD2(hh_[0][0], hh_[0][1]), 0)) { RETRY2(hh_[0][0], hh_[0][1], hb, hb + 14336) }
    STAMP(8); CSTAMP(28);
    v4f acc[8][2];
    v2u fa;
    const int fx = WS_CRDY + (tile * 4 + (lane < 32 ? wave : grp & 3)) * 256 + min(lane & 31, 27) * 8;
    const int ao = WS_ACT + (16 * tile + 4 * wave) * 57344 + 2 * slice * 1024 + l16;
#define ACT_OUT(ps, tt, f) { v4u o; \
      _Pragma("unroll") for (int hh = 0; hh < 2; ++hh) { \
        const v4f av = relu4(acc[2 * f + hh][tt]) * acc[4 + 2 * f + hh][tt]; \
        o[2 * hh] = pk2(lo2(av)); o[2 * hh + 1] = pk2(hi2(av)); } \
      st4c(ws, ao + (2 * ps + tt) * 57344 + f * 1024, o); }
#define ACT_ALL(ps) { mfence<0>(acc[0][0], acc[1][0], acc[2][0], acc[3][0]); mfence<0>(acc[4][0], acc[5][0], acc[6][0], acc[7][0]); \
      ACT_OUT(ps, 0, 0) ACT_OUT(ps, 0, 1) sb(); \
      mfence<0>(acc[0][1], acc[1][1], acc[2][1], acc[3][1]); mfence<0>(acc[4][1], acc[5][1], acc[6][1], acc[7][1]); \
      ACT_OUT(ps, 1, 0) ACT_OUT(ps, 1, 1) sb(); }
#define W_RH(dst, k, u, hf) { const v2u t_ = ldtr(L + tb[k >= 7][u & 3], (k % 7) * 8192 + (u >> 2) * 4096 + hf * 2048); \
      dst[2 * hf] = t_.x; dst[2 * hf + 1] = t_.y; }
#pragma unroll
    for (int ps = 0; ps < 2; ++ps)
#pragma unroll
    for (int ks = 0; ks < 14; ++ks) {
      const int v = ps * 14 + ks, cur = v & 1, nk = (v + 1) % 14, vn = v < 27 ? v + 1 : v;
      const bool more = v < 27;
      u32 m0, m1;
      __builtin_amdgcn_s_waitcnt(0xC07F);
      sb();
#pragma unroll
      for (int i = 0; i < 16; ++i) {
        const int u = i & 7, tt = i >> 3;
        if (ks == 0) mv0(acc[u][tt], wfr[cur][u], hh_[v][tt]); else mv(acc[u][tt], wfr[cur][u], hh_[v][tt]);
        if (more) {
          if (i < 4) { W_RH(wfr[cur ^ 1][i], nk, i, 0) W_RH(wfr[cur ^ 1][i], nk, i, 1) }
          else if (i < 12) W_RH(wfr[cur ^ 1][4 + ((i - 4) >> 1)], nk, (4 + ((i - 4) >> 1)), (i & 1))
        }
        if (i == 5 && v + LEAD < 28) H_LD(v + LEAD, 0);
        if (i == 7 && v + LEAD < 28) H_LD(v + LEAD, 1);
        if (i == 9 || i == 11 || i == 13) {
          const int e = (i - 9) >> 1, f = WD_MID + (v < 14 ? v - 2 : 12 + WD_PER * (v - 14) + e);
          if (f < 56 && ((v >= 2 && v < 14 && e == 0) || (v >= 14 && e < WD_PER))) wdn[f >> 2][f & 3] = ld4c(ws, wl16 + (f & 3) * 1024, wdb + (f >> 2) * 4096);
        }
        if (v > A_V && (i & 1) && i >= 5 && ((i - 5) >> 1) < A_PER) G_LD(A_PER * (v - A_V - 1) + ((i - 5) >> 1));
        if (v == 8 && i == 13) fa = ld2c(ws, fx);
        if (more && i == 12) { m0 = max(max(hh_[vn][0].x, hh_[vn][0].y), hh_[vn][0].z); pin(m0); }
        if (more && i == 13) { m1 = max(max(hh_[vn][0].w, hh_[vn][1].x), hh_[vn][1].y); pin(m1); }
        if (more && i == 14) { m0 = max(max(m0, hh_[vn][1].z), hh_[vn][1].w); m0 = max(m0, m1); pin(m0); }
        sb();
      }
      if (v == 12 && !wall(fa.x == tag && fa.y == ~tag)) wait_flags(ws, fx, tag);
      if (more && __builtin_expect(wany(m0 == SENT), 0)) {
        const int pb = 2 * ((v + 1) / 14) * 14336 + nk * 1024;
        RETRY2(hh_[vn][0], hh_[vn][1], hb + pb, hb + pb + 14336)
      }
      if (v == 13) {
        STAMP(16); CSTAMP(29);
        ACT_ALL(0)
        if (isR) x2_store(ws, x2r, 16 * grp + j - 14, tid);
        nop2();
      }
      sb();
    }
    STAMP(27);
    ACT_ALL(1)
    CSTAMP(30);
#pragma unroll
    for (int e = A_PER * (27 - A_V); e < 28; ++e) G_LD(e);
    sb();
    STAMP(9);
    v4u a[14][2];
    v4f ac2[4][4];
#define A_LD(e) a[(e) >> 1][(e) & 1] = ld4c(ws, l16 + (((e) >> 1) & 3) * 1024, abx[(e) & 1] + 2 * 57344 + ((e) >> 3) * 4096)
    const int po = ab + 3 * 57344 + min(lane, 6) * 2048 + 1024;
    u32 probe;
#pragma unroll
    for (int ks = 0; ks < 14; ++ks) {
      if (__builtin_expect(BAD2(h[ks][0], h[ks][1]), 0)) { RETRY2(h[ks][0], h[ks][1], abx[0] + ks * 1024, abx[1] + ks * 1024) }
#pragma unroll
      for (int tt = 0; tt < 2; ++tt)
#pragma unroll
        for (int n = 0; n < 4; ++n) ac2[tt][n] = mfma(wdn[ks][n], h[ks][tt], ks ? ac2[tt][n] : splat(0.f));
      if (ks == P_AT) probe = ld1c(ws, po);
      sb();
      if (ks == B_AT2) {
        STAMP(25);
        if (wany(probe == SENT)) light_poll(ws, po);
        STAMP(26);
      }
      if (ks >= B_AT2) {
#pragma unroll
        for (int e = 4 * (ks - B_AT2); e < 4 * (ks - B_AT2 + 1); ++e) A_LD(e);
      }
      sb();
    }
    STAMP(17);
#pragma unroll
    for (int e = 4 * (14 - B_AT2); e < 4 * (14 - B_AT2) + 6; ++e) A_LD(e);
    sb();
    int rw[3], rr_ = L_B + lane * 8 + wave * 6144, oo[2];
#pragma unroll
    for (int x = 1; x < 4; ++x) { rw[x - 1] = L_B + lane * 8 + (wave ^ x) * 6144 + (x - 1) * 2048; pin(rw[x - 1]); }
    pin(rr_);
    const int blk = blk0 + (wave >> 1);
#pragma unroll
    for (int ph = 0; ph < 2; ++ph) { oo[ph] = (tok_row(blk + 2 * ph, c16) * D + ct * 64 + g * 16 + (wave & 1) * 8) * 2; pin(oo[ph]); }
#define RED_W1(ph, x, n) *(LAS v2f*)(L + rw[(x) - 1] + (ph) * 24576 + (n) * 512) = ((x) & 1) ? hi2(ac2[2 * (ph) + ((x) >> 1)][n]) : lo2(ac2[2 * (ph) + ((x) >> 1)][n]);
    const int xo = WS_X2 + blk * 14336 + c16 * 896 + ct * 128 + g * 32 + (wave & 1) * 16;
    const int go = WS_MOD + grp * MOD_REP + (70 + 2 * ct) * 128 + g * 64 + (wave & 1) * 32;
    v4u xv[2], gv[2];
    STAMP(18);
    sb();
#pragma unroll
    for (int ks = 0; ks < 14; ++ks) {
      if (__builtin_expect(BAD2(a[ks][0], a[ks][1]), 0)) { RETRY2(a[ks][0], a[ks][1], abx[0] + 2 * 57344 + ks * 1024, abx[1] + 2 * 57344 + ks * 1024) }
#pragma unroll
      for (int tt = 0; tt < 2; ++tt)
#pragma unroll
        for (int n = 0; n < 4; ++n) ac2[2 + tt][n] = mfma(wdn[ks][n], a[ks][tt], ks ? ac2[2 + tt][n] : splat(0.f));
#pragma unroll
      for (int e = 4 * (14 - B_AT2) + 6 + 3 * ks; e < 4 * (14 - B_AT2) + 9 + 3 * ks; ++e)
        if (e < 28) A_LD(e);
      if (ks == 6) { xv[0] = ld4c(ws, xo); xv[1] = ld4c(ws, xo + 2 * 14336); gv[0] = ld4c(ws, go); gv[1] = ld4c(ws, go + 16); }
      if (ks < 12) RED_W1(0, 1 + (ks >> 2), (ks & 3))
#pragma unroll
      for (int u = 0; u < 4; ++u) {
        __builtin_amdgcn_sched_group_barrier(0x8, 2, 0);
        __builtin_amdgcn_sched_group_barrier(0x20, 1, 0);
        if (u == 2) __builtin_amdgcn_sched_group_barrier(0x200, 1, 0);
      }
      sb();
    }
    STAMP(10);
#pragma unroll
    for (int e = 0; e < 12; ++e) RED_W1(1, 3 - (e >> 2), (e & 3))
#define FIN_OK() wall(max(max4(xv[0]), max4(xv[1])) != SENT && gv[0].y == tag && gv[0].w == tag && gv[1].y == tag && gv[1].w == tag)
    if (__builtin_expect(!FIN_OK(), 0)) {
      _Pragma("nounroll") for (int s = 0; s < SPIN; ++s) {
        const int gf = fresh(go), xf = fresh(xo);
        gv[0] = ld4c(ws, gf); gv[1] = ld4c(ws, gf + 16); xv[0] = ld4c(ws, xf); xv[1] = ld4c(ws, xf + 2 * 14336);
        if (FIN_OK()) break;
      }
    }
    bar();
#pragma unroll
    for (int ph = 0; ph < 2; ++ph) {
      v2f r[4];
#pragma unroll
      for (int n = 0; n < 4; ++n) {
        r[n] = lo2(ac2[2 * ph][n]);
#pragma unroll
        for (int w = 0; w < 3; ++w) r[n] += *(const LAS v2f*)(L + rr_ + w * 2048 + ph * 24576 + n * 512);
      }
      v4u o;
#pragma unroll
      for (int q = 0; q < 2; ++q) {
        o[2 * q] = pk2(fma2(bun(gv[q].x), v2f{r[0][q], r[1][q]}, bun(xv[ph][2 * q])));
        o[2 * q + 1] = pk2(fma2(bun(gv[q].z), v2f{r[2][q], r[3][q]}, bun(xv[ph][2 * q + 1])));
      }
      __builtin_amdgcn_raw_buffer_store_b128(o, mkrs(out, S * D * 2), oo[ph], 0, OUT_AUX);
    }
    if (blockIdx.x == 0 && wave == 0) {
      _Pragma("nounroll") for (int s = 0; s < SPIN; ++s) {
        const v4u v = ld4c(ws, fresh(WS_MOD + (70 + (lane < 14 ? lane : 0)) * 128));
        if (wall(v.y == tag)) break;
      }
      if (lane == 0) __builtin_amdgcn_raw_buffer_store_b32(epoch + 1u, ws, WS_EPOCH, 0, SC1);
    }
  }
  STAMP(11);
  DUMP();
}

extern "C" void kernel_launch(void* const* d_in, const int* in_sizes, int n_in, void* d_out, int out_size, void* d_ws,
                              size_t ws_size, hipStream_t stream) {
  fused<<<256, 256, 0, stream>>>((const u16*)d_in[0], (const u16*)d_in[1], (const u16*)d_in[2], (const u16*)d_in[4],
                                 (const u16*)d_in[5], (const u16*)d_in[6], (const u16*)d_in[7], (const u16*)d_in[8],
                                 (u16*)d_out, (char*)d_ws);
}
```

```cpp
#define PADI 17
#define PADB 42
#define PADT 1000
#include <hip/hip_runtime.h>

typedef unsigned short u16;
typedef unsigned int u32;
typedef unsigned long long u64;
typedef u32 v4u __attribute__((ext_vector_type(4)));
typedef u32 v2u __attribute__((ext_vector_type(2)));
typedef float v4f __attribute__((ext_vector_type(4)));
typedef float v2f __attribute__((ext_vector_type(2)));
typedef short v8s __attribute__((ext_vector_type(8)));
typedef short v4s __attribute__((ext_vector_type(4)));
typedef __bf16 v8b __attribute__((ext_vector_type(8)));
typedef __bf16 v2b __attribute__((ext_vector_type(2)));
#define LAS __attribute__((address_space(3)))
#define GAS __attribute__((address_space(1)))
#define DEV __device__ __forceinline__

#define C_W1_EARLY 0
#define CLR_DELAY 0
#define W_DELAY 0
#define CLR2_DELAY 0
#define PF_POS 2
#define SL1 1
#define SL2 2
#define SL4 4
#define X_AFTER 8
#define PF_AUX 2
#define W_LATE 1
#define W1_PF 1
#define DMA_MOD ""
#define LEAD 6
#define A_V 22
#define A_PER 4
#define WDV(f) ((f) - WD_MID + 2 < 14 ? (f) - WD_MID + 2 : ((f) - WD_MID - 12) / 2 + 16)
#define B_AT2 12
#define RE_PF 0
#define PF2_DELAY 0
#define C_DMA_EARLY 0
#define EVEN_PF 0
#define XBAL 1
#define W1EVEN 1
#define PRE_OUT 0
#define OUT_AUX 16
#define P_AT 7
#define WD_PER 3
#define A_FIRST 10
#define B_FIRST 10
#define A_AT 9
#define F_AT 4
#define CODE_BYTES 45056
#define WD_EARLY 16
#define WD_MID 24
#define B_AT 9
constexpr int S = 2048, D = 448, DF = 225, F = 1792;

struct alignas(16) Chain { u16 v[2048]; };
struct Cos448 { u16 v[448]; };
constexpr double c_cos_t(double a) {
  double a2 = a * a, t = 1.0, s = 1.0;
  for (int i = 1; i <= 10; ++i) { t = -t * a2 / ((2 * i - 1) * (2 * i)); s += t; }
  return s;
}
constexpr double c_sin_t(double a) {
  double a2 = a * a, t = a, s = a;
  for (int i = 1; i <= 10; ++i) { t = -t * a2 / ((2 * i) * (2 * i + 1)); s += t; }
  return s;
}
constexpr double c_cos448(int m) {
  if (m > 224) m = 448 - m;
  double sg = 1.0;
  if (m > 112) { m = 224 - m; sg = -1.0; }
  const double w = 6.283185307179586476925286766559 / 448.0;
  return m == 112 ? 0.0 : (m <= 56 ? sg * c_cos_t(w * m) : sg * c_sin_t(w * (112 - m)));
}
constexpr u16 c_bf16(double d) {
  const u32 u = __builtin_bit_cast(u32, (float)d);
  return (u16)((u + 0x7FFFu + ((u >> 16) & 1u)) >> 16);
}
constexpr Cos448 mk_cos() { Cos448 c{}; for (int m = 0; m < 448; ++m) c.v[m] = c_bf16(c_cos448(m)); return c; }
constexpr Cos448 CTAB = mk_cos();
constexpr u16 bcos(int m) { return CTAB.v[m % 448]; }
constexpr u16 bsin(int m) { return CTAB.v[(m % 448 + 336) % 448]; }
constexpr u16 bneg(u16 b) { return b ? (u16)(b ^ 0x8000u) : b; }
constexpr u16 bscale(u16 b, int e) { return b ? (u16)(b + e * 0x80) : b; }

constexpr Chain mk_p1(int c) {
  const int tile = c >> 2, par = c & 1, sn = (c >> 1) & 1;
  Chain ch{};
  for (int s = 0; s < 4; ++s)
    for (int l = 0; l < 64; ++l) {
      const int kp = 16 * tile + (l & 15), k = 2 * kp + par;
      if (k > 224) continue;
      for (int j = 0; j < 8; ++j) {
        const int dl = 32 * s + 8 * (l >> 4) + j;
        if (dl > 112) continue;
        u16 b = sn ? bneg(bsin(k * dl)) : bcos(k * dl);
        if (dl == 0 || dl == 112) b = bscale(b, -1);
        ch.v[(s * 64 + l) * 8 + j] = b;
      }
    }
  return ch;
}
constexpr Chain mk_p3(int c) {
  const int tile = c >> 1, sn = c & 1;
  Chain ch{};
  for (int s = 0; s < 4; ++s)
    for (int l = 0; l < 64; ++l) {
      int dl = 16 * tile + (l & 15);
      if (c == 1 && dl == 0) dl = 112;
      for (int j = 0; j < 8; ++j) {
        const int k = 2 * (32 * s + 8 * (l >> 4) + j) + 1;
        if (k < 224) ch.v[(s * 64 + l) * 8 + j] = sn ? bsin(k * dl) : bcos(k * dl);
      }
    }
  return ch;
}
constexpr Chain mk_p2(int c) {
  Chain ch{};
  for (int f = 0; f < 4; ++f)
    for (int l = 0; l < 64; ++l)
      for (int j = 0; j < 8; ++j) {
        const int comp = c < 8 ? (c & 1) : (f >> 1), s = c < 8 ? f : (f & 1);
        const int o = 16 * (c < 8 ? (c >> 1) : (c - 8)) + (l & 15), i = 16 * s + 4 * (l >> 4) + (j >> 1);
        const int m = (c < 8 ? 7 : 14) * ((i * o) % 64);
        u16 b = 0;
        if (comp == 0) b = (j & 1) ? bsin(m) : bcos(m);
        else b = (j & 1) ? bcos(m) : bneg(bsin(m));
        ch.v[(f * 64 + l) * 8 + j] = b;
      }
  return ch;
}
template <int I> constexpr Chain P1C = mk_p1(I);
template <int I> constexpr Chain P3C = mk_p3(I);
template <int I> constexpr Chain P2C = mk_p2(I);
#define L10(T, b) T<b>, T<b + 1>, T<b + 2>, T<b + 3>, T<b + 4>, T<b + 5>, T<b + 6>, T<b + 7>, T<b + 8>, T<b + 9>
#define L29(T) L10(T, 0), L10(T, 10), T<20>, T<21>, T<22>, T<23>, T<24>, T<25>, T<26>, T<27>, T<28>
constexpr int TAB_P1 = 0, TAB_P3 = 29, TAB_P2 = 43, TAB_N = 53;
static __device__ const Chain TAB[TAB_N] = {L29(P1C), L10(P3C, 0), P3C<10>, P3C<11>, P3C<12>, P3C<13>, L10(P2C, 0)};

constexpr int WS_EPOCH = 0;
constexpr int WS_RDY = 4096;
constexpr int WS_HRDY = 4096 + 8192;
constexpr int WS_DRDY = 4096 + 16384;
constexpr int WS_XRDY = 131072 + 65536;
constexpr int WS_ARDY = 131072;
constexpr int WS_CRDY = 131072 + 32768;
constexpr int WS_MOD = 32768, MOD_REP = 12288;
constexpr int T1_BLK = 226 * 64;
constexpr int WS_T1 = 1 << 18;
constexpr int WS_WT = WS_T1 + 128 * T1_BLK;
constexpr int WS_H2 = WS_WT + 113 * 16384;
constexpr int WS_X2 = WS_H2 + 128 * 14336;
constexpr int WS_WD = WS_X2 + 128 * 14336;
constexpr int WS_ACT = WS_WD + 7 * 56 * 4096;
constexpr int WS_END = WS_ACT + 128 * 57344;
constexpr int WS_DBG = 32 << 20;

constexpr int L_W1 = 0;
constexpr int L_B = 114688;
constexpr int XP = 912;
constexpr int L_XS = L_B;
constexpr int L_ST = L_XS + 16 * XP;
constexpr int L_M1 = L_ST + 128;
constexpr int L_AF = L_M1 + 4 * 1792;
constexpr int L_M2 = L_AF + 16384;
constexpr int L_EX = L_M2 + 3 * 1792;
constexpr int L_REND = L_EX + 512;
constexpr int ZPL = 8192;
constexpr int L_CI = L_B;
constexpr int L_CX = L_CI + 2 * 8256;
constexpr int L_RED = L_CX + 16384;
constexpr int L_DBG = 163840 - 1024;
constexpr int L_SZ = 163840;
static_assert(L_REND <= L_DBG && L_RED + 2048 <= L_DBG && 2 * ZPL <= 16384, "lds");

constexpr u32 SENT = 0xFFFFFFFFu;
constexpr int SPIN = 1 << 14;
constexpr int SC1 = 16;
constexpr int OOB = 0x7FFF0000;

typedef __amdgpu_buffer_rsrc_t rsrc_t;
DEV rsrc_t mkrs(const void* p, int bytes) { return __builtin_amdgcn_make_buffer_rsrc((void*)p, 0, bytes, 0x00020000); }
DEV v4u ld4(rsrc_t r, int off, int so = 0) { return __builtin_amdgcn_raw_buffer_load_b128(r, off, so, 0); }
DEV v2u ld2(rsrc_t r, int off, int so = 0) { return __builtin_amdgcn_raw_buffer_load_b64(r, off, so, 0); }
DEV v4u ld4c(rsrc_t r, int off, int so = 0) { return __builtin_amdgcn_raw_buffer_load_b128(r, off, so, SC1); }
DEV v2u ld2c(rsrc_t r, int off, int so = 0) { return __builtin_amdgcn_raw_buffer_load_b64(r, off, so, SC1); }
DEV u32 ld1c(rsrc_t r, int off, int so = 0) { return __builtin_amdgcn_raw_buffer_load_b32(r, off, so, SC1); }
DEV void st4c(rsrc_t r, int off, v4u v) { __builtin_amdgcn_raw_buffer_store_b128(v, r, off, 0, SC1); }
DEV void st2c(rsrc_t r, int off, v2u v) { __builtin_amdgcn_raw_buffer_store_b64(v, r, off, 0, SC1); }
DEV float blo(u32 v) { return __builtin_bit_cast(float, v << 16); }
DEV float bhi(u32 v) { return __builtin_bit_cast(float, v & 0xFFFF0000u); }
DEV v2f bun(u32 v) { return v2f{blo(v), bhi(v)}; }
DEV u32 pk(float lo, float hi) { return __builtin_bit_cast(u32, __builtin_convertvector(v2f{lo, hi}, v2b)); }
DEV u32 pk2(v2f v) { return __builtin_bit_cast(u32, __builtin_convertvector(v, v2b)); }
DEV float rb(float f) { return blo(pk(f, 0.f)); }
DEV v2f lo2(v4f v) { return __builtin_shufflevector(v, v, 0, 1); }
DEV v2f hi2(v4f v) { return __builtin_shufflevector(v, v, 2, 3); }
DEV v2f fma2(v2f a, v2f b, v2f c) { return __builtin_elementwise_fma(a, b, c); }
DEV v4f fma4(v4f a, v4f b, v4f c) { return __builtin_elementwise_fma(a, b, c); }
typedef int v4i __attribute__((ext_vector_type(4)));
DEV v4f relu4(v4f v) { return __builtin_bit_cast(v4f, __builtin_elementwise_max(__builtin_bit_cast(v4i, v), v4i{0, 0, 0, 0})); }
DEV v4f splat(float f) { return v4f{f, f, f, f}; }
DEV v4f mfma(v4u a, v4u b, v4f c) {
  return __builtin_amdgcn_mfma_f32_16x16x32_bf16(__builtin_bit_cast(v8b, a), __builtin_bit_cast(v8b, b), c, 0, 0, 0);
}
DEV void mfa(v4f& c, v4u a, v4u b) { asm volatile("s_nop 1\n\tv_mfma_f32_16x16x32_bf16 %0, %1, %2, %0" : "+v"(c) : "v"(a), "v"(b)); }
DEV void mf0(v4f& c, v4u a, v4u b) { asm volatile("s_nop 1\n\tv_mfma_f32_16x16x32_bf16 %0, %1, %2, 0" : "=&v"(c) : "v"(a), "v"(b)); }
template <int N> DEV void mfence(v4f& a, v4f& b) { asm volatile("s_nop %2" : "+v"(a), "+v"(b) : "i"(N)); }
template <int N> DEV void mfence(v4f& a, v4f& b, v4f& c, v4f& d) { asm volatile("s_nop %4" : "+v"(a), "+v"(b), "+v"(c), "+v"(d) : "i"(N)); }
#define MF "v_mfma_f32_16x16x32_bf16 "
DEV void mf8(v4f& d0, v4f& d1, const v4u (&a0)[4], const v4u (&b0)[4], const v4u (&a1)[4], const v4u (&b1)[4]) {
  asm volatile("s_nop 1\n\t" MF "%0, %2, %6, 0\n\t" MF "%1, %10, %14, 0\n\t" MF "%0, %3, %7, %0\n\t" MF "%1, %11, %15, %1\n\t"
               MF "%0, %4, %8, %0\n\t" MF "%1, %12, %16, %1\n\t" MF "%0, %5, %9, %0\n\t" MF "%1, %13, %17, %1"
               : "=&v"(d0), "=&v"(d1)
               : "v"(a0[0]), "v"(a0[1]), "v"(a0[2]), "v"(a0[3]), "v"(b0[0]), "v"(b0[1]), "v"(b0[2]), "v"(b0[3]),
                 "v"(a1[0]), "v"(a1[1]), "v"(a1[2]), "v"(a1[3]), "v"(b1[0]), "v"(b1[1]), "v"(b1[2]), "v"(b1[3]));
}
#define H4_M(XA, D, X, K, C) MF D ", " XA(X, K) ", " C "\n\t"
#define H4_XA(X, K) X ", " K
#define H4_XB(X, K) K ", " X
#define H4_DEF(NAME, XA, C0, C1, CONS, N0, N1, PA, PB, PC, PD) \
DEV void NAME(v4f& d0, v4f& d1, v4u x0, v4u x1, v4u k00, v4u k01, v4u k10, v4u k11, \
              u32& o0, u32& o1, float p0, float q0, float c0, float s0, float p1, float q1, float c1, float s1) { \
  float t0, t1, t2, t3; \
  asm volatile(H4_M(XA, "%0", "%8", "%10", C0) "v_mul_f32 %4, %14, %16\n\tv_mul_f32 %5, %15, %16\n\t" \
               H4_M(XA, "%1", "%8", "%12", C1) "v_mul_f32 %6, %18, %20\n\tv_mul_f32 %7, %19, %20\n\t" \
               H4_M(XA, "%0", "%9", "%11", "%0") "v_fma_f32 %4, " N0 "%15, %17, %4\n\tv_fma_f32 %5, " N1 "%14, %17, %5\n\t" \
               H4_M(XA, "%1", "%9", "%13", "%1") "v_fma_f32 %6, " N0 "%19, %21, %6\n\tv_fma_f32 %7, " N1 "%18, %21, %7\n\t" \
               "v_cvt_pk_bf16_f32 %2, " PA ", " PB "\n\tv_cvt_pk_bf16_f32 %3, " PC ", " PD \
               : CONS(d0), CONS(d1), "=&v"(o0), "=&v"(o1), "=&v"(t0), "=&v"(t1), "=&v"(t2), "=&v"(t3) \
               : "v"(x0), "v"(x1), "v"(k00), "v"(k01), "v"(k10), "v"(k11), \
                 "v"(p0), "v"(q0), "v"(c0), "v"(s0), "v"(p1), "v"(q1), "v"(c1), "v"(s1)); \
}
H4_DEF(h4_a0m, H4_XA, "0", "0", "=&v", "", "-", "%4", "%5", "%6", "%7")
H4_DEF(h4_a1m, H4_XA, "%0", "%1", "+v", "", "-", "%4", "%5", "%6", "%7")
H4_DEF(h4_b0m, H4_XB, "0", "0", "=&v", "", "-", "%4", "%5", "%6", "%7")
H4_DEF(h4_b0p, H4_XB, "0", "0", "=&v", "-", "", "%5", "%4", "%7", "%6")
H4_DEF(h4_a0p, H4_XA, "0", "0", "=&v", "-", "", "%5", "%4", "%7", "%6")
#define M4_DEF(NAME, XA, C0, C1, CONS) \
DEV void NAME(v4f& d0, v4f& d1, v4u x0, v4u x1, v4u k00, v4u k01, v4u k10, v4u k11) { \
  asm volatile(H4_M(XA, "%0", "%2", "%4", C0) H4_M(XA, "%1", "%2", "%6", C1) H4_M(XA, "%0", "%3", "%5", "%0") MF "%1, " XA("%3", "%7") ", %1" \
               : CONS(d0), CONS(d1) : "v"(x0), "v"(x1), "v"(k00), "v"(k01), "v"(k10), "v"(k11)); \
}
M4_DEF(m4_a0, H4_XA, "0", "0", "=&v") M4_DEF(m4_a1, H4_XA, "%0", "%1", "+v")
M4_DEF(m4_b0, H4_XB, "0", "0", "=&v") M4_DEF(m4_b1, H4_XB, "%0", "%1", "+v")
DEV void mv(v4f& c, v4u a, v4u b) { asm volatile(MF "%0, %1, %2, %0" : "+v"(c) : "v"(a), "v"(b)); }
DEV void mv0(v4f& c, v4u a, v4u b) { asm volatile(MF "%0, %1, %2, 0" : "=&v"(c) : "v"(a), "v"(b)); }
DEV void nop2() { asm volatile("s_nop 1"); }
template <class T> DEV void pin(T& v) { asm volatile("" : "+v"(v)); }
DEV int fresh(int off) { asm volatile("" : "+v"(off) :: "memory"); return off; }
DEV bool wall(bool ok) { return __builtin_amdgcn_ballot_w64(!ok) == 0; }
DEV bool wany(bool b) { return __builtin_amdgcn_ballot_w64(b) != 0; }
DEV void sb() { __builtin_amdgcn_sched_barrier(0); }
DEV u32 max4(v4u v) { return max(max(v.x, v.y), max(v.z, v.w)); }
DEV void drain() { asm volatile("s_waitcnt vmcnt(0)" ::: "memory"); }
DEV void cfence() { asm volatile("" ::: "memory"); }
DEV void bar() { asm volatile("s_waitcnt lgkmcnt(0)\n\ts_barrier" ::: "memory"); }
template <int CTRL> DEV float dpp(float v) {
  return __builtin_bit_cast(float, __builtin_amdgcn_update_dpp(0, __builtin_bit_cast(int, v), CTRL, 0xF, 0xF, true));
}
DEV float sum16(float v) {
  v += dpp<0x128>(v); v += dpp<0x124>(v); v += dpp<0x122>(v); v += dpp<0x121>(v); return v;
}
DEV void sumg2(float& a, float& b) {
  float c = a, d = b;
  asm volatile("s_nop 1\n\tv_permlane16_swap_b32 %0, %1\n\tv_permlane16_swap_b32 %2, %3" : "+v"(a), "+v"(c), "+v"(b), "+v"(d));
  a += c; b += d; c = a; d = b;
  asm volatile("s_nop 1\n\tv_permlane32_swap_b32 %0, %1\n\tv_permlane32_swap_b32 %2, %3" : "+v"(a), "+v"(c), "+v"(b), "+v"(d));
  a += c; b += d;
}
DEV v4u lds4(const char LAS* L, int off) { return *(const LAS v4u*)(L + off); }
DEV v4f lds4f(const char LAS* L, int off) { return *(const LAS v4f*)(L + off); }
DEV v2u ldtr(const char LAS* L, int off) { return __builtin_bit_cast(v2u, __builtin_amdgcn_ds_read_tr16_b64_v4i16((LAS v4s*)(L + off))); }
DEV v4u ldtr2(const char LAS* L, int o0, int o1) { const v2u a = ldtr(L, o0), b = ldtr(L, o1); return v4u{a.x, a.y, b.x, b.y}; }
DEV void dma16(rsrc_t r, int voff, int soff, u32 lds_dst) {
  u32 keep;
  asm volatile("s_mov_b32 %0, m0\n\ts_mov_b32 m0, %1\n\ts_nop 0\n\tbuffer_load_dwordx4 %2, %3, %4 offen" DMA_MOD " lds\n\ts_mov_b32 m0, %0"
               : "=&s"(keep) : "s"(lds_dst), "v"(voff), "s"(r), "s"(soff) : "memory");
}
DEV void light_poll(rsrc_t ws, int off) {
  _Pragma("nounroll") for (int s = 0; s < SPIN; ++s) {
    if (wall(ld1c(ws, fresh(off)) != SENT)) break;
    __builtin_amdgcn_s_sleep(SL2);
  }
}
DEV int tok_row(int blk, int t) {
  const int n1 = 16 * (blk >> 5) + 4 * ((blk >> 2) & 3) + (t & 3), n2 = 16 * ((blk >> 4) & 1) + 4 * (blk & 3) + (t >> 2);
  return n2 + 32 * n1;
}
template <bool SWAP> DEV v4u cmul_pack(v4f a, v4f b, v4f c, v4f s) {
  const v4f re = fma4(b, s, a * c), im = fma4(b, c, -(a * s));
  return SWAP ? v4u{pk(im.x, re.x), pk(im.y, re.y), pk(im.z, re.z), pk(im.w, re.w)}
              : v4u{pk(re.x, im.x), pk(re.y, im.y), pk(re.z, im.z), pk(re.w, im.w)};
}

template <bool PLUS> DEV void cmul2(u32& o0, u32& o1, v2f p, v2f q, v2f c, v2f s) {
  const v2f re = fma2(PLUS ? -q : q, s, p * c), im = fma2(PLUS ? p : -p, s, q * c);
  o0 = PLUS ? pk(im.x, re.x) : pk(re.x, im.x); o1 = PLUS ? pk(im.y, re.y) : pk(re.y, im.y);
}

#define STAMP(i) if ((i) == PADI) padt = __builtin_amdgcn_s_memrealtime();
#define CSTAMP(i)
#define SLOWCNT
#define DUMP()

DEV void dma_w1(const u16* wg, const u16* wu, int slice, int wave, int lane, u32 lbase) {
  const rsrc_t rg = mkrs(wg, D * F * 2), ru = mkrs(wu, D * F * 2);
  const int r = lane >> 3, ch = ((((lane & 7) >> 1) ^ (lane >> 4)) & 3) * 32 + (lane & 1) * 16 + slice * 128;
  const int vp = r * (F * 2) + ch, vn = (7 - r) * (F * 2) + ch;
#pragma unroll
  for (int n = 0; n < 4; ++n) {
    const int ks = wave + 4 * n;
    if (n == 3 && wave >= 2) break;
    const int t16 = (ks >> 1) * 16, a = ks & 1;
#pragma unroll
    for (int q = 0; q < 4; ++q) {
      const int dl0 = t16 + 8 * (q & 1);
      const bool pos = (q >> 1) == a;
      const int d0 = q < 2 ? (a ? 224 - dl0 - 7 : dl0) : (a ? 224 + dl0 : 448 - dl0 - 7);
      int vo = pos ? vp : vn, so = d0 * (F * 2);
      if (n == 0 && q == 2) {
        vo += so; so = 0;
        if (wave < 2 && lane < 8) vo = (wave ? 336 : 112) * (F * 2) + ch;
      }
      dma16(rg, vo, so, lbase + L_W1 + ks * 8192 + q * 1024);
      dma16(ru, vo, so, lbase + L_W1 + ks * 8192 + 4096 + q * 1024);
    }
  }
}
struct ModLoad { u32 cv[7]; v4u mw[7]; };
DEV void mod_load(ModLoad& m, const u16* cin, const u16* wm, int q, int tid) {
  const rsrc_t cr = mkrs(cin, q >= 0 ? D * 2 : 0), mr = mkrs(wm, q >= 0 ? D * 6 * D * 2 : 0);
#pragma unroll
  for (int i = 0; i < 7; ++i) m.cv[i] = __builtin_amdgcn_raw_buffer_load_b16(cr, (tid >> 2) * 2 + i * 128, 0, 0);
#pragma unroll
  for (int i = 0; i < 7; ++i) m.mw[i] = __builtin_amdgcn_raw_buffer_load_b128(mr, ((tid >> 2) * (6 * D) + q * 32 + (tid & 3) * 8) * 2, i * 64 * 6 * D * 2, PF_AUX);
}
DEV void mod_part(ModLoad& m, char LAS* L, int tid) {
  float sl[7];
#pragma unroll
  for (int i = 0; i < 7; ++i) { const float cc = blo(m.cv[i]); sl[i] = rb(cc * __builtin_amdgcn_rcpf(1.f + __expf(-cc))); pin(sl[i]); }
  v2f a[4];
#pragma unroll
  for (int e = 0; e < 4; ++e) a[e] = v2f{0.f, 0.f};
#pragma unroll
  for (int i = 0; i < 7; ++i)
#pragma unroll
    for (int e = 0; e < 4; ++e) a[e] = fma2(bun(m.mw[i][e]), v2f{sl[i], sl[i]}, a[e]);
#pragma unroll
  for (int e = 0; e < 4; ++e) {
    a[e].x += dpp<0x128>(a[e].x); a[e].y += dpp<0x128>(a[e].y);
    a[e].x += dpp<0x124>(a[e].x); a[e].y += dpp<0x124>(a[e].y);
  }
  if ((tid & 15) < 4) {
    *(LAS v4f*)(L + L_RED + ((tid >> 4) * 32 + (tid & 15) * 8) * 4) = v4f{a[0].x, a[0].y, a[1].x, a[1].y};
    *(LAS v4f*)(L + L_RED + ((tid >> 4) * 32 + (tid & 15) * 8 + 4) * 4) = v4f{a[2].x, a[2].y, a[3].x, a[3].y};
  }
}
DEV void mod_publish(char LAS* L, rsrc_t ws, int q, int lane, u32 tag) {
  v4f s = {0.f, 0.f, 0.f, 0.f};
#pragma unroll
  for (int r = 0; r < 16; ++r) s += lds4f(L, L_RED + (r * 32 + (lane & 7) * 4) * 4);
  const float one = (q / 14 == 1 || q / 14 == 4) ? 1.f : 0.f;
  st4c(ws, WS_MOD + (lane >> 3) * MOD_REP + q * 128 + (lane & 7) * 16,
       v4u{pk(rb(s.x) + one, rb(s.y) + one), tag, pk(rb(s.z) + one, rb(s.w) + one), tag});
}
DEV u32 ld1(rsrc_t r, int off) { return __builtin_amdgcn_raw_buffer_load_b32(r, off, 0, 0); }
DEV u32 ld1n(rsrc_t r, int off) { return __builtin_amdgcn_raw_buffer_load_b32(r, off, 0, PF_AUX); }
DEV u32 mktag(u32 epoch) { return epoch * 0x9E3779B1u + 0x7F4A7C15u; }

DEV void x2_read(const char LAS* L, v4u (&r)[4], int tid) {
#pragma unroll
  for (int m = 0; m < 4; ++m) { const int ch = min(tid + 256 * m, 895), tk = ch / 56; r[m] = lds4(L, L_XS + tk * XP + (ch - tk * 56) * 16); }
}
DEV void x2_store(rsrc_t ws, const v4u (&r)[4], int blk, int tid) {
#pragma unroll
  for (int m = 0; m < 4; ++m) st4c(ws, tid + 256 * m < 896 ? WS_X2 + blk * 14336 + (tid + 256 * m) * 16 : OOB, r[m]);
}
DEV bool flags_ok(v4u rv, u32 tag) { return rv.x == tag && rv.y == ~tag && rv.z == tag && rv.w == ~tag; }
DEV void wait_flags(rsrc_t ws, int off, u32 tag) {
  _Pragma("nounroll") for (int s = 0; s < SPIN; ++s) {
    const v2u rv = ld2c(ws, fresh(off));
    if (wall(rv.x == tag && rv.y == ~tag)) break;
    __builtin_amdgcn_s_sleep(SL1);
  }
}

__global__ void __launch_bounds__(256, 1)
fused(const u16* __restrict__ xin, const u16* __restrict__ wr, const u16* __restrict__ wi, const u16* __restrict__ wg,
      const u16* __restrict__ wu, const u16* __restrict__ wd, const u16* __restrict__ cin, const u16* __restrict__ wm,
      u16* __restrict__ out, char* wsp) {
  __shared__ __attribute__((aligned(16))) char smem[L_SZ];
  char LAS* L = (char LAS*)smem;
  const u32 lbase = (u32)(size_t)L;
  const int tid = threadIdx.x, lane = tid & 63, wave = __builtin_amdgcn_readfirstlane(tid >> 6);
  const int g = lane >> 4, c16 = lane & 15, l16 = lane * 16;
  const int grp = blockIdx.x & 7, j = blockIdx.x >> 3;
  u64 pc, padt = 0;
  asm volatile("s_getpc_b64 %0" : "=s"(pc));
  STAMP(0); CSTAMP(31);
  asm volatile("" :: "s"(xin), "s"(wr), "s"(wi), "s"(wg), "s"(wu), "s"(wd), "s"(cin), "s"(wm), "s"(out), "s"(wsp));
  const rsrc_t ws = mkrs(wsp, WS_END);
  const rsrc_t tab = mkrs(TAB, TAB_N * 4096);
  const rsrc_t code = mkrs((const void*)(pc & ~127ull), CODE_BYTES);
  const bool isR = j >= 14 && j < 30;
  const bool isG = j < 28;
  const int gk = j / 7, ct = j - gk * 7;
  const int tile = 4 * (grp >> 2) + gk, slice = 7 * (grp & 3) + ct;
  const int wdb = WS_WD + (ct * 56 + wave * 14) * 4096;
  const v4u sv = {SENT, SENT, SENT, SENT};
  int lb = L_B + l16;
  pin(lb);
  const char LAS* LB = L + lb - L_B;
  const int pfo = ((j * 4 + wave) * 14 + lane) * 128, pfc = ((j * 4 + wave) * 4 + lane) * 128;
  u32 pfa = ld1(tab, lane < 14 ? pfo : OOB), pfb = ld1(code, lane < 4 ? pfc : OOB);
  const int hro = WS_HRDY + (16 * tile + 4 * (lane < 32 ? wave : grp & 3)) * 32 + c16 * 8;
  v4u x2r[4];
  const int wl16 = (lane ^ ((wave & 1) << 1)) * 16;
  v4u wdn[14][4];
  u32 epoch, tag;

  if (isR) {
    const int blk = 16 * grp + (j - 14);
    epoch = ld1c(ws, WS_EPOCH);
    const int t0 = wave == 0 ? 0 : 2 * wave - 1;
    v4u cf[8][4];
#define CF_LOAD(q) { const int chn = TAB_P1 + ((wave == 0 && q >= 4) ? 28 : 4 * t0 + q), vo = (q < 5 || wave != 0) ? l16 : OOB; \
      _Pragma("unroll") for (int s = 0; s < 4; ++s) cf[q][s] = ld4(tab, vo + s * 1024, chn * 4096); sb(); }
#pragma unroll
    for (int q = 0; q < X_AFTER; ++q) CF_LOAD(q)
    v2u xv[7];
    {
      const rsrc_t xr = mkrs(xin, S * D * 2);
      const int off = tok_row(blk, 4 * wave + g) * (D * 2) + c16 * 8;
#pragma unroll
      for (int m = 0; m < 7; ++m) xv[m] = ld2(xr, off + m * 128);
    }
    typedef u32 v3u __attribute__((ext_vector_type(3)));
    v2u fa_[2], fb_[2]; v3u fc_[2], fd_[2];
    {
      const rsrc_t rr = mkrs(wr, S * DF * 2), ri = mkrs(wi, S * DF * 2);
      const int fo_ = 16 * wave + c16 < 57 ? 2 * g * 450 + (16 * wave + c16) * 8 : OOB;
#pragma unroll
      for (int hh = 0; hh < 2; ++hh) {
        const int so = blk * 7200 + hh * 3600;
        fa_[hh] = ld2(rr, fo_, so); fb_[hh] = ld2(ri, fo_, so);
        fc_[hh] = __builtin_amdgcn_raw_buffer_load_b96(rr, fo_ + 448, so, 0); fd_[hh] = __builtin_amdgcn_raw_buffer_load_b96(ri, fo_ + 448, so, 0);
      }
    }
    sb();
#pragma unroll
    for (int q = X_AFTER; q < 8; ++q) CF_LOAD(q)
    STAMP(23);
    {
      v2f f[14], s2 = {0.f, 0.f};
#pragma unroll
      for (int m = 0; m < 7; ++m) {
        f[2 * m] = bun(xv[m].x); f[2 * m + 1] = bun(xv[m].y);
        s2 += f[2 * m] + f[2 * m + 1];
        *(LAS v2u*)(L + L_XS + (4 * wave + g) * XP + c16 * 8 + m * 128) = xv[m];
      }
      STAMP(21);
      sb();
      {
        const int q = 16 * wave + c16, o = WS_WT + q * 16384 + blk * 128 + g * 16;
#pragma unroll
        for (int hh = 0; hh < 2; ++hh) {
          const v2u a = fa_[hh], b = fb_[hh]; const v3u c = fc_[hh], d = fd_[hh];
          st4c(ws, q < 57 ? o + hh * 64 : OOB, v4u{__builtin_amdgcn_perm(b.x, a.x, 0x05040100u), __builtin_amdgcn_perm(b.y, a.y, 0x05040100u),
                                                   __builtin_amdgcn_perm(d.x, c.x, 0x07060302u), __builtin_amdgcn_perm(d.y, c.y, 0x07060302u)});
          st4c(ws, q < 56 ? o + hh * 64 + 57 * 16384 : OOB,
               v4u{__builtin_amdgcn_perm(b.x, a.x, 0x07060302u), __builtin_amdgcn_perm(b.y, a.y, 0x07060302u),
                   __builtin_amdgcn_perm(d.y, c.y, 0x05040100u), __builtin_amdgcn_perm(d.z, c.z, 0x05040100u)});
        }
      }
      sb();
      const float mean = sum16(s2.x + s2.y) * (1.f / D);
      v2f q2 = {0.f, 0.f};
#pragma unroll
      for (int m = 0; m < 14; ++m) { const v2f d = f[m] - mean; q2 = fma2(d, d, q2); }
      const float rstd = __builtin_amdgcn_rsqf(sum16(q2.x + q2.y) * (1.f / D) + 1e-5f);
      if (c16 == 0) *(LAS v2f*)(L + L_ST + (4 * wave + g) * 8) = v2f{mean, rstd};
    }
    pin(pfa); pin(pfb);
    epoch = __builtin_amdgcn_readfirstlane(epoch);
    tag = mktag(epoch);
    STAMP(22);
    bar();
    v4f n[4][2];
    const int d0 = 8 * (4 * wave + g);
    const v2f st = *(const LAS v2f*)(L + L_ST + c16 * 8);
    {
      int xr_ = L_XS + c16 * XP + d0 * 2, xb_ = L_XS + c16 * XP + (216 - d0) * 2, x0_ = d0 == 0 ? xr_ : xb_ + 464;
      pin(xr_); pin(xb_); pin(x0_);
      const v4u a = lds4(L + xr_, 0), e = lds4(L + xr_, 448), c = lds4(L + xb_, 0), b = lds4(L + xb_, 448);
      const u32 c0 = *(const LAS u16*)(L + xb_ + 16), b0 = *(const LAS u16*)(L + x0_);
#pragma unroll
      for (int i = 0; i < 8; ++i) {
        n[0][i >> 2][i & 3] = (i & 1) ? bhi(a[i >> 1]) : blo(a[i >> 1]);
        n[3][i >> 2][i & 3] = (i & 1) ? bhi(e[i >> 1]) : blo(e[i >> 1]);
        n[1][i >> 2][i & 3] = i == 0 ? blo(b0) : (i & 1) ? bhi(b[(8 - i) >> 1]) : blo(b[(8 - i) >> 1]);
        n[2][i >> 2][i & 3] = i == 0 ? blo(c0) : (i & 1) ? bhi(c[(8 - i) >> 1]) : blo(c[(8 - i) >> 1]);
      }
      const float nm = -st.x * st.y;
#pragma unroll
      for (int m = 0; m < 4; ++m) {
#pragma unroll
        for (int h = 0; h < 2; ++h) { n[m][h] = fma4(n[m][h], splat(st.y), splat(nm)); pin(n[m][h]); }
      }
    }
    sb();
    STAMP(1);
    int mb = L_M1 + d0 * 4;
    pin(mb);
    v4u xs[4];
    int to[4];
    to[0] = WS_T1 + blk * T1_BLK + (16 * t0 + c16) * 64 + g * 16;
    to[1] = wave != 0 ? to[0] + 1024 : c16 < 2 ? to[0] + 112 * 64 : OOB;
    to[2] = to[0] + 114 * 64;
    to[3] = wave != 0 ? to[0] + 1024 + 114 * 64 : OOB;
#pragma unroll
    for (int q = 0; q < 4; ++q) pin(to[q]);

    {
      const int kind = tid >= 112, d = 4 * (tid - 112 * kind);
      int wb = L_M1 + (kind ? 0 : 2 * 1792) + d * 4, wrv = L_M1 + (kind ? 0 : 2 * 1792) + 1792 + (445 - d) * 4;
      int wr0 = d == 0 ? wrv + 12 - 1792 : wrv + 12;
      const int mo = tid < 224 ? WS_MOD + grp * MOD_REP + tid * 16 : OOB;
      pin(wb); pin(wrv); pin(wr0);
      const int ro = WS_RDY + grp * 1024 + min(tid >> 1, 112) * 8;
      v4u mv;
      _Pragma("nounroll") for (int s = 0; s < SPIN; ++s) {
        mv = ld4c(ws, fresh(mo));
        const v2u rv = ld2c(ws, fresh(ro));
        if (wall((tid >= 224 || (mv.y == tag && mv.w == tag)) && rv.x == tag && rv.y == ~tag)) break;
        __builtin_amdgcn_s_sleep(SL1);
      }
      STAMP(2);
      if (tid < 224) {
        const v4f v = {blo(mv.x), bhi(mv.x), blo(mv.z), bhi(mv.z)};
        *(LAS v4f*)(L + wb) = v;
        *(LAS float*)(L + wr0) = v.x;
        *(LAS float*)(L + wrv + 8) = v.y;
        *(LAS float*)(L + wrv + 4) = v.z;
        *(LAS float*)(L + wrv) = v.w;
      }
    }
    bar();
    {
      v4u fr[4];
#pragma unroll
      for (int hh = 0; hh < 2; ++hh) {
        v4f h[4];
#pragma unroll
        for (int m = 0; m < 4; ++m) {
          const int o = ((m == 1 || m == 2) ? 1792 : 0) + (m >= 2 ? 896 : 0) + hh * 16;
          h[m] = fma4(n[m][hh], lds4f(L + mb, o), lds4f(L + mb, 2 * 1792 + o));
        }
        const v4f p = h[0] + h[1], q = h[2] + h[3], u = h[0] - h[1], v = h[3] - h[2];
        const v4f t[4] = {p + q, p - q, u + v, u - v};
#pragma unroll
        for (int m = 0; m < 4; ++m) { fr[m][2 * hh] = pk2(lo2(t[m])); fr[m][2 * hh + 1] = pk2(hi2(t[m])); }
      }
#pragma unroll
      for (int m = 0; m < 4; ++m) *(LAS v4u*)(LB + wave * 1024 + L_AF + m * 4096) = fr[m];
    }
    bar();
    {
      v4u a[4][4];
#pragma unroll
      for (int p = 0; p < 2; ++p)
#pragma unroll
        for (int s = 0; s < 4; ++s) { a[p][s] = lds4(LB, L_AF + (p * 4 + s) * 1024); a[2 + p][s] = lds4(LB, L_AF + ((2 + p) * 4 + s) * 1024); }
      v4f re[4], im[4];
#pragma unroll
      for (int q = 0; q < 5; ++q) {
        if (q < 4) mf8(re[q], im[q], a[q >> 1], cf[4 * (q & 1) + (q >> 1)], a[2 + (q >> 1)], cf[4 * (q & 1) + 2 + (q >> 1)]);
        if (q > 0) {
          if (q < 4) mfence<0>(re[q - 1], im[q - 1]); else mfence<7>(re[3], im[3]);
          xs[q - 1] = v4u{pk(re[q - 1].x, im[q - 1].x), pk(re[q - 1].y, im[q - 1].y), pk(re[q - 1].z, im[q - 1].z), pk(re[q - 1].w, im[q - 1].w)};
          st4c(ws, to[q - 1], xs[q - 1]);
          sb();
        }
      }
    }
    STAMP(3);
    sb();
    sb();
    if (isG) dma_w1(wg, wu, slice, wave, lane, lbase);
#pragma unroll
    for (int q = 0; q < 4; ++q) {
      const int vo = (q < 2 || wave != 0) ? l16 : OOB;
#pragma unroll
      for (int s = 0; s < 4; ++s) cf[2 * q + 1][s] = ld4(tab, vo + s * 1024, (TAB_P3 + 2 * t0 + q) * 4096);
    }
#pragma unroll
    for (int m = 0; m < 4; ++m) {
      st4c(ws, tid + 256 * m < 896 ? WS_H2 + blk * 14336 + 4096 * m + tid * 16 : OOB, sv);
      st4c(ws, tid + 256 * m < 896 ? WS_X2 + blk * 14336 + 4096 * m + tid * 16 : OOB, sv);
    }
    sb();
    STAMP(20);
    v4f xo[2][4];
    int xa = L_XS + c16 * XP + (16 * t0 + 4 * g) * 2, xd = L_XS + c16 * XP + (192 - 16 * t0 - 4 * g) * 2;
    int xs1 = tid < 16 ? L_XS + c16 * XP + 224 : xd + 512, xs3 = tid < 16 ? L_XS + c16 * XP + 672 : xa + 448;
    pin(xa); pin(xd); pin(xs1); pin(xs3);
#define XOFF(t, m, r) ((t) == 0 && (r) == 0 && (m) == 1 ? xs1 : (t) == 0 && (r) == 0 && (m) == 3 ? xs3 : \
    (m) == 0 ? xa + 32 * (t) + 2 * (r) : (m) == 3 ? xa + 448 + 32 * (t) + 2 * (r) : (m) == 1 ? xd + 512 - 32 * (t) - 2 * (r) : xd + 64 - 32 * (t) - 2 * (r))
    {
      v2u w[2][4]; u32 e[2][2], e3;
#pragma unroll
      for (int t = 0; t < 2; ++t) {
        w[t][0] = *(const LAS v2u*)(L + xa + 32 * t); w[t][3] = *(const LAS v2u*)(L + xa + 448 + 32 * t);
        w[t][1] = *(const LAS v2u*)(L + xd + 504 - 32 * t); w[t][2] = *(const LAS v2u*)(L + xd + 56 - 32 * t);
        e[t][0] = *(const LAS u16*)(L + XOFF(t, 1, 0)); e[t][1] = *(const LAS u16*)(L + XOFF(t, 2, 0));
      }
      e3 = *(const LAS u16*)(L + xs3);
      sb();
#pragma unroll
      for (int t = 0; t < 2; ++t) {
        xo[t][0] = v4f{blo(w[t][0].x), bhi(w[t][0].x), blo(w[t][0].y), bhi(w[t][0].y)};
        xo[t][3] = v4f{t == 0 ? blo(e3) : blo(w[t][3].x), bhi(w[t][3].x), blo(w[t][3].y), bhi(w[t][3].y)};
        xo[t][1] = v4f{blo(e[t][0]), bhi(w[t][1].y), blo(w[t][1].y), bhi(w[t][1].x)};
        xo[t][2] = v4f{blo(e[t][1]), bhi(w[t][2].y), blo(w[t][2].y), bhi(w[t][2].x)};
#pragma unroll
        for (int m = 0; m < 4; ++m) { xo[t][m] -= st.x; pin(xo[t][m]); }
      }
    }
    int m0_[2], m1_[2], md_[2];
#pragma unroll
    for (int r = 0; r < 2; ++r) {
      const int q = r == 0 || tid < 80 ? tid + 256 * r : tid;
      const int kind = (q * 586) >> 16, d = 4 * (q - 112 * kind);
      const int reg = (d >= 112) + (d >= 224) + (d >= 336), cc = ((reg + 1) >> 1) * 224, m1 = (0x1320 >> (4 * reg)) & 3;
      const int d1 = abs(d + 1 - cc), d0_ = abs(d - cc), edge = d == 112 * reg && reg > 0;
      m1_[r] = L_M2 + kind * 1792 + (((d1 >> 4) << 6) | (m1 << 4) | (d1 & 15)) * 4;
      m0_[r] = L_M2 + kind * 1792 + (edge ? reg << 4 : ((d0_ >> 4) << 6) | (m1 << 4) | (d0_ & 15)) * 4;
      md_[r] = (reg & 1) ? -4 : 4;
      pin(m0_[r]); pin(m1_[r]); pin(md_[r]);
    }
    {
      v4u mv[2];
      _Pragma("nounroll") for (int s = 0; s < SPIN; ++s) {
        mv[0] = ld4c(ws, fresh(WS_MOD + grp * MOD_REP + (224 + tid) * 16));
        mv[1] = ld4c(ws, fresh(WS_MOD + grp * MOD_REP + (224 + (tid < 80 ? 256 + tid : tid)) * 16));
        const v2u rv = ld2c(ws, fresh(WS_DRDY + grp * 1024 + min(lane, 48) * 8));
        if (wall(mv[0].y == tag && mv[0].w == tag && mv[1].y == tag && mv[1].w == tag && rv.x == tag && rv.y == ~tag)) break;
        __builtin_amdgcn_s_sleep(SL4);
      }
      if (lane == 0) st2c(ws, WS_HRDY + blk * 32 + wave * 8, v2u{tag, ~tag});
      STAMP(12);
#pragma unroll
      for (int f = 0; f < WD_EARLY; ++f) wdn[f >> 2][f & 3] = ld4c(ws, (isG ? wl16 : OOB) + (f & 3) * 1024, wdb + (f >> 2) * 4096);
      sb();
#pragma unroll
      for (int r = 0; r < 2; ++r) {
        const float sc = m0_[r] < L_M2 + 1792 ? 2.f / (float)(S * D) : 1.f;
        *(LAS float*)(L + m0_[r]) = blo(mv[r].x) * sc;
        *(LAS float*)(L + m1_[r]) = bhi(mv[r].x) * sc;
        *(LAS float*)(L + m1_[r] + md_[r]) = blo(mv[r].z) * sc;
        *(LAS float*)(L + m1_[r] + 2 * md_[r]) = bhi(mv[r].z) * sc;
      }
    }
    bar();
    *(LAS v4u*)(L + L_AF + (tid >> 6) * 4096 + 3072 + l16) = v4u{0u, 0u, 0u, 0u};
    STAMP(4);
    int zr = L_AF + lane * 8, ro = L_M2 + (t0 * 64 + 4 * g) * 4, ro1 = wave == 0 ? ro : ro + 256, ex = L_EX + c16 * 8;
    pin(zr); pin(ro); pin(ro1); pin(ex);
    {
      int zl[4];
#pragma unroll
      for (int m = 0; m < 4; ++m) {
        const int kq = (m >> 1) * 128 + (to[m] == OOB ? 127 : wave == 0 && m == 1 ? 112 + c16 : 16 * (t0 + (m & 1)) + c16);
        zl[m] = L_AF + ((kq & ~31) + ((kq & 4) << 2) + ((kq & 24) >> 1) + (kq & 3)) * 32 + g * 8;
        pin(zl[m]);
        if (to[m] == OOB) xs[m] = sv;
      }
      bar();
      if (isG) wait_flags(ws, hro, tag);
      {
        const int src = (blk * 5 + wave * 13) & 63;
        const int po = __builtin_amdgcn_readlane(to[(0)], 0) + (src & 15) * 64 + (src >> 4) * 16;
        const u32 old = __builtin_amdgcn_ds_bpermute(src * 4, xs[0].x);
        _Pragma("nounroll") for (int s = 0; s < SPIN; ++s) {
          if (wall(ld1c(ws, fresh(po)) != old)) break;
          __builtin_amdgcn_s_sleep(SL2);
        }
      }
      STAMP(13);
      v4u z[4];
      _Pragma("nounroll") for (int s = 0; s < SPIN; ++s) {
        u32 mn = 1;
#pragma unroll
        for (int m = 0; m < 4; ++m) z[m] = ld4c(ws, fresh(to[m]));
#pragma unroll
        for (int m = 0; m < 4; ++m) { const v4u d = z[m] ^ xs[m]; mn = min(min(mn, d.x), min(min(d.y, d.z), d.w)); }
        if (wall(mn != 0)) break;
      }
      STAMP(14);
#pragma unroll
      for (int m = 0; m < 4; ++m) {
        char LAS* p = L + zl[m];
        *(LAS v2u*)(p) = v2u{__builtin_amdgcn_perm(z[m].y, z[m].x, 0x05040100u), __builtin_amdgcn_perm(z[m].w, z[m].z, 0x05040100u)};
        *(LAS v2u*)(p + ZPL) = v2u{__builtin_amdgcn_perm(z[m].y, z[m].x, 0x07060302u), __builtin_amdgcn_perm(z[m].w, z[m].z, 0x07060302u)};
      }
    }
    bar();
    STAMP(5);
    {
      v4u zf[4][4];
#pragma unroll
      for (int ty = 0; ty < 4; ++ty)
#pragma unroll
        for (int s = 0; s < 4; ++s) zf[ty][s] = ldtr2(L + zr, ty * 4096 + s * 1024, ty * 4096 + s * 1024 + 512);
      v4f acc[2][4], x2[2][4], gav[2][4];
      v2f s1 = {0.f, 0.f}, s2 = {0.f, 0.f};
#define P3M(t, ty) mf8(acc[t][ty], acc[t][ty + 1], cf[4 * t + ty], zf[ty], cf[4 * t + ty + 1], zf[ty + 1]);
#define P3X(t, m, yv) { x2[t][m] = fma4(gav[t][m], yv, xo[t][m]); \
      if (t == 0 || wave != 0) { s1 += lo2(x2[t][m]) + hi2(x2[t][m]); \
        s2 = fma2(lo2(x2[t][m]), lo2(x2[t][m]), s2); s2 = fma2(hi2(x2[t][m]), hi2(x2[t][m]), s2); } }
      P3M(1, 0) P3M(1, 2)
#pragma unroll
      for (int m = 0; m < 4; ++m) { gav[0][m] = lds4f(L + ro, m * 64); gav[1][m] = lds4f(L + ro1, m * 64); }
      sb();
      P3M(0, 0) P3M(0, 2)
      mfence<0>(acc[1][0], acc[1][1], acc[1][2], acc[1][3]);
      const v4f A1 = acc[1][0] + acc[1][1], B1 = acc[1][0] - acc[1][1], P1 = acc[1][3] - acc[1][2], Q1 = acc[1][2] + acc[1][3];
      P3X(1, 0, A1 - P1) P3X(1, 1, A1 + P1) P3X(1, 2, B1 - Q1) P3X(1, 3, B1 + Q1)
      sb();
      mfence<0>(acc[0][0], acc[0][1], acc[0][2], acc[0][3]);
      {
        const v4f A = acc[0][0] + acc[0][1], B = acc[0][0] - acc[0][1], P = acc[0][3] - acc[0][2], Q = acc[0][2] + acc[0][3];
        v4f y[4] = {A - P, A + P, B - Q, B + Q};
        const bool sp = tid < 16;
        y[0].x = sp ? A.x : y[0].x; y[2].x = sp ? B.x : y[2].x;
        y[1].x = sp ? acc[1][0].x - acc[0][3].x : y[1].x; y[3].x = sp ? acc[1][0].x + acc[0][3].x : y[3].x;
        P3X(0, 0, y[0]) P3X(0, 1, y[1]) P3X(0, 2, y[2]) P3X(0, 3, y[3])
      }
      float sm = s1.x + s1.y, sq = s2.x + s2.y;
      sumg2(sm, sq);
      if (g == 0) *(LAS v2f*)(L + ex + wave * 128) = v2f{sm, sq};
      v4f sbv[2][4], cbv[2][4];
#pragma unroll
      for (int m = 0; m < 4; ++m) {
        sbv[0][m] = lds4f(L + ro, 1792 + m * 64); cbv[0][m] = lds4f(L + ro, 2 * 1792 + m * 64);
        sbv[1][m] = lds4f(L + ro1, 1792 + m * 64); cbv[1][m] = lds4f(L + ro1, 2 * 1792 + m * 64);
      }
      bar();
      v2f e = *(const LAS v2f*)(L + ex);
#pragma unroll
      for (int w = 1; w < 4; ++w) e += *(const LAS v2f*)(L + ex + w * 128);
      const float mc = e.x * (1.f / D), rstd = __builtin_amdgcn_rsqf(fmaxf(e.y * (1.f / D) - mc * mc, 0.f) + 1e-5f), nb = -mc * rstd;
      const int ho = WS_H2 + blk * 14336 + 2 * t0 * 1024 + l16, ho1 = wave == 0 ? OOB : ho + 2048;
#pragma unroll
      for (int t = 0; t < 2; ++t)
#pragma unroll
        for (int a = 0; a < 2; ++a) {
          v4u o;
#pragma unroll
          for (int hh = 0; hh < 2; ++hh) {
            const int m = 2 * a + hh;
            const v4f hv = fma4(fma4(x2[t][m], splat(rstd), splat(nb)), cbv[t][m], sbv[t][m]);
            o[2 * hh] = pk2(lo2(hv)); o[2 * hh + 1] = pk2(hi2(hv));
          }
          st4c(ws, (t ? ho1 : ho) + a * 1024, o);
        }
      STAMP(6);
      sb();
#pragma unroll
      for (int f = WD_EARLY; f < WD_MID; ++f) wdn[f >> 2][f & 3] = ld4c(ws, (isG ? wl16 : OOB) + (f & 3) * 1024, wdb + (f >> 2) * 4096);
      sb();
#pragma unroll
      for (int t = 0; t < 2; ++t)
        if (t == 0 || wave != 0)
#pragma unroll
          for (int m = 0; m < 4; ++m)
#pragma unroll
            for (int r = 0; r < 4; r += 2) {
              const u32 p = pk(x2[t][m][r] + st.x, x2[t][m][r + 1] + st.x);
              *(LAS u16*)(L + XOFF(t, m, r)) = (u16)p;
              *(LAS u16*)(L + XOFF(t, m, r + 1)) = (u16)(p >> 16);
            }
      sb();
    }
    bar();
    x2_read(L, x2r, tid);
    bar();
    if (!isG) {
      x2_store(ws, x2r, blk, tid);
      DUMP(); return;
    }
  } else {
    const int sj = j >= 30 ? j - 30 : j + 2;
    const int idx = sj * 8 + grp;
    const bool isC = j < 14 || idx == 0;
    const int pair = j < 14 ? grp * 14 + j : 112;
    const int lc = grp + 8 * (sj >> 1);
    const int qu = sj < 4 && lc < 14 ? 2 * lc + (sj & 1) : -1;
    const int t1o = WS_T1 + (32 * wave + (lane >> 3)) * T1_BLK + pair * 128 + (lane & 7) * 16;
    ModLoad ml;
    epoch = ld1c(ws, WS_EPOCH);
    if (qu >= 0) mod_load(ml, cin, wm, qu, tid);
#pragma unroll
    for (int q = 0; q < 4; ++q) st4c(ws, isC ? t1o + 8 * q * T1_BLK : OOB, sv);
    u32 pf0, pf1, pf2;
    {
      const rsrc_t mr = mkrs(wm, D * 6 * D * 2), xr = mkrs(xin, S * D * 2), fr_ = mkrs((wave & 1) ? wi : wr, S * DF * 2);
      const int row = (sj * 4 + wave) * 7 + (lane >> 1), l7 = lane / 7;
      pf0 = __builtin_amdgcn_raw_buffer_load_b32(mr, lane < 14 && (grp < 6 || !(lane & 1)) ? row * (6 * D * 2) + (grp + 8 * (lane & 1)) * 128 : OOB, 0, PF_AUX);
      pf1 = __builtin_amdgcn_raw_buffer_load_b32(xr, lane < (!XBAL ? 28 : sj < 4 ? 0 : sj < 12 ? 28 : 56) ? tok_row(16 * grp + (lane < 28 ? sj : sj - 12), 4 * wave + (l7 & 3)) * (D * 2) + (lane - 7 * l7) * 128 : OOB, 0, PF_AUX);
    }
    sb();
    epoch = __builtin_amdgcn_readfirstlane(epoch);
    tag = mktag(epoch);
    if (qu >= 0) mod_part(ml, L, tid);
    asm volatile("s_waitcnt vmcnt(%0)" :: "i"(W_LATE ? 2 : 3) : "memory");
    { const rsrc_t fr_ = mkrs((wave & 1) ? wi : wr, S * DF * 2);
      pf2 = __builtin_amdgcn_raw_buffer_load_b32(fr_, (lane & 31) < 29 && lane < (!XBAL ? 32 : sj < 4 ? 0 : sj < 12 ? 32 : 64) ? (((16 * grp + (lane < 32 ? sj : sj - 12)) * 7200) & ~127) + ((wave >> 1) * 29 + (lane & 31)) * 128 : OOB, 0, PF_AUX);
    }
    bar();
    if (wave == 0 && qu >= 0) mod_publish(L, ws, qu, lane, tag);
    if (wave == 1 && lane < 8) st2c(ws, isC ? WS_RDY + lane * 1024 + pair * 8 : OOB, v2u{tag, ~tag});
    STAMP(1);
    pin(pf0); pin(pf1); pin(pf2); pin(pfa); pin(pfb);
    const int qn = sj >= 4 && sj < 10 && grp < 7 ? 28 + grp * 6 + (sj - 4) : !isC && idx < 15 ? 69 + idx : -1;
    const int tw = sj >= 10 ? (sj - 10) * 8 + grp : sj == 4 && grp == 7 ? 48 : -1;
    v2u dw[2][8];
    if (qn >= 0) mod_load(ml, cin, wm, qn, tid);
    if (tw >= 0) {
      const rsrc_t dr = mkrs(wd, F * D * 2);
      const int u0 = tw * 4 + wave, uc = u0 / 28, kap = 2 * (u0 - uc * 28);
#pragma unroll
      for (int e = 0; e < 16; ++e)
        dw[e >> 3][e & 7] = __builtin_amdgcn_raw_buffer_load_b64(dr, (4 * g * D + 4 * c16) * 2 + (e & 3) * D * 2, ((32 * kap + 16 * (e >> 2)) * D + 64 * uc) * 2, PF_AUX);
    }
    sb();
    u32 pw[4];
    {
      const rsrc_t rg = mkrs(wg, D * F * 2), ru = mkrs(wu, D * F * 2);
      const int r7 = lane / 7, r0 = W1EVEN ? (j * 4 + wave) * 8 : sj < 4 ? (sj * 4 + wave) * 16 : 256 + ((sj - 4) * 4 + wave) * 4;
      const int po = (r0 + r7) * (F * 2) + ((grp & 3) * 7 + lane - r7 * 7) * 128;
      const int o0 = lane < (W1EVEN ? (j < 14 ? 56 : 0) : sj < 4 ? 56 : 28) ? po : OOB, o1 = !W1EVEN && lane < 56 && sj < 4 ? po + 8 * (F * 2) : OOB;
      pw[0] = ld1(rg, o0); pw[1] = ld1(ru, o0); pw[2] = ld1(rg, o1); pw[3] = ld1(ru, o1);
    }
    sb();
    STAMP(20);
    v4f t1c[2], t1s[2], t2c[2], t2s[2];
#pragma unroll
    for (int t = 0; t < 2; ++t) {
#pragma unroll
      for (int r = 0; r < 4; ++r) {
        const float a1 = (float)((16 * t + 4 * g + r) * (16 * wave + c16)) * (1.f / S);
        const float a2 = (float)((16 * t + c16) * (16 * wave + 4 * g + r)) * (1.f / S);
        t1c[t][r] = __builtin_amdgcn_cosf(a1); t1s[t][r] = __builtin_amdgcn_sinf(a1);
        t2c[t][r] = __builtin_amdgcn_cosf(a2); t2s[t][r] = __builtin_amdgcn_sinf(a2);
      }
      pin(t1c[t]); pin(t1s[t]); pin(t2c[t]); pin(t2s[t]);
    }
    if (tw >= 0) {
#pragma unroll
      for (int tt = 0; tt < 8; ++tt) {
        v4u o;
#pragma unroll
        for (int m = 0; m < 4; ++m)
          o[m] = __builtin_amdgcn_perm(dw[tt >> 2][2 * m + 1][(tt & 3) >> 1], dw[tt >> 2][2 * m][(tt & 3) >> 1], (tt & 1) ? 0x07060302u : 0x05040100u);
        st4c(ws, WS_WD + (tw * 4 + wave) * 8192 + tt * 1024 + l16, o);
      }
    }
    if (qn >= 0) {
      bar();
      mod_part(ml, L, tid);
      bar();
      if (wave == 0) mod_publish(L, ws, qn, lane, tag);
    }
    pin(pw[0]); pin(pw[1]); pin(pw[2]); pin(pw[3]);
    STAMP(21);
    if (!isC) { DUMP(); return; }
    v4u c64[2][4], c32[2][2][2];
#pragma unroll
    for (int cp = 0; cp < 2; ++cp)
#pragma unroll
      for (int s = 0; s < 4; ++s) c64[cp][s] = ld4(tab, l16 + s * 1024, (TAB_P2 + wave * 2 + cp) * 4096);
#pragma unroll
    for (int t = 0; t < 2; ++t)
#pragma unroll
      for (int f = 0; f < 4; ++f) c32[t][f >> 1][f & 1] = ld4(tab, l16 + f * 1024, (TAB_P2 + 8 + t) * 4096);
    sb();
    if (tw >= 0) {
      asm volatile("s_waitcnt vmcnt(16)" ::: "memory");
      bar();
      if (tid < 8) st2c(ws, WS_DRDY + tid * 1024 + tw * 8, v2u{tag, ~tag});
    }
    STAMP(2);
    int wfo = WS_WT + pair * 16384 + (16 * g + wave) * 128 + c16 * 8;
    pin(wfo);
    v2u wf[2][4];
    {
      int wl = L_CI + ((lane >> 2) & 1) * 8256 + wave * 2048 + ((lane >> 3) * 4 + (lane & 3)) * 16, lo = t1o;
      pin(wl); pin(lo);
      light_poll(ws, WS_T1 + (32 * wave + (pair & 31)) * T1_BLK + pair * 128);
      STAMP(13);
      v4u z[4];
      _Pragma("nounroll") for (int s = 0; s < SPIN; ++s) {
        lo = fresh(lo);
#pragma unroll
        for (int q = 0; q < 4; ++q) z[q] = ld4c(ws, lo, 8 * q * T1_BLK);
        if (wall(max(max(max4(z[0]), max4(z[1])), max(max4(z[2]), max4(z[3]))) != SENT)) break;
      }
      STAMP(14);
#pragma unroll
      for (int q = 0; q < 4; ++q) *(LAS v4u*)(L + wl + q * 512) = z[q];
    }
    bar();
#pragma unroll
    for (int t = 0; t < 2; ++t)
#pragma unroll
      for (int r = 0; r < 4; ++r) wf[t][r] = ld2c(ws, wfo + r * 512, t * 8192);
    v2u fd = ld2c(ws, WS_DRDY + grp * 1024 + min(lane, 48) * 8);
    sb();
    STAMP(4);
    {
      v4u a[2][2][4];
#pragma unroll
      for (int c = 0; c < 2; ++c)
#pragma unroll
        for (int mt = 0; mt < 2; ++mt)
#pragma unroll
          for (int s = 0; s < 4; ++s) a[c][mt][s] = lds4(LB, L_CI + c * 8256 + (s * 2 + mt) * 1024);
      v4u old[2][2];
#pragma unroll
      for (int c = 0; c < 2; ++c)
#pragma unroll
        for (int mt = 0; mt < 2; ++mt) old[c][mt] = lds4(LB + wave * 2048, L_CI + c * 8256 + mt * 1024);
      v4f re[2][2], im[2][2], ur[2][2], ui[2][2], fr_[2][2], fi_[2][2];
      u32 f2[2][2][4], f3[2][2][4], f4[4];
      char LAS* xw = L + lb + wave * 1024 - L_B;
#define V4(x) v4u{x[0], x[1], x[2], x[3]}
#define S1K(h) c64[0][2 * h], c64[0][2 * h + 1], c64[1][2 * h], c64[1][2 * h + 1]
#define S1X(c, t, h) re[c][t], im[c][t], a[c][t][2 * h], a[c][t][2 * h + 1], S1K(h)
#define S2X(c, t) ur[c][t], ui[c][t], V4(f2[c][0]), V4(f2[c][1]), c32[t][0][0], c32[t][0][1], c32[t][1][0], c32[t][1][1]
#define S3X(c, t) im[c][t], re[c][t], V4(f3[c][0]), V4(f3[c][1]), c32[t][0][0], c32[t][0][1], c32[t][1][0], c32[t][1][1]
#define CM(o, P, Q, C, S_, h) o[2 * h], o[2 * h + 1], P[2 * h], Q[2 * h], C[2 * h], S_[2 * h], P[2 * h + 1], Q[2 * h + 1], C[2 * h + 1], S_[2 * h + 1]
#define TW1(c, t, h) CM(f2[c][t], re[c][t], im[c][t], t1c[t], t1s[t], h)
#define FLT(c, t, h) CM(f3[c][t], ur[c][t], ui[c][t], fr_[c][t], fi_[c][t], h)
#define TW2(c, t, h) CM(f4, re[c][t], im[c][t], t2c[t], t2s[t], h)
#define HV(x, h) (h ? hi2(x) : lo2(x))
#define CP(PL, o, P, Q, C, S_, h) cmul2<PL>(o[2 * h], o[2 * h + 1], HV(P, h), HV(Q, h), HV(C, h), HV(S_, h));
#define XW(c, t) *(LAS v4u*)(xw + L_CX + (c * 2 + t) * 4096) = V4(f4);
#define UNP(c, t) { _Pragma("unroll") for (int r = 0; r < 4; ++r) { fr_[c][t][r] = blo(wf[t][r][c]); fi_[c][t][r] = bhi(wf[t][r][c]); } \
      pin(fr_[c][t]); pin(fi_[c][t]); }
      nop2();
      m4_a0(S1X(0, 0, 0)); m4_a1(S1X(0, 0, 1)); m4_a0(S1X(0, 1, 0)); mfence<3>(re[0][0], im[0][0]);
      h4_a1m(S1X(0, 1, 1), TW1(0, 0, 0)); h4_a0m(S1X(1, 0, 0), TW1(0, 0, 1));
      h4_a1m(S1X(1, 0, 1), TW1(0, 1, 0)); h4_a0m(S1X(1, 1, 0), TW1(0, 1, 1));
      h4_a1m(S1X(1, 1, 1), TW1(1, 0, 0)); h4_b0m(S2X(0, 0), TW1(1, 0, 1));
      h4_b0m(S2X(0, 1), TW1(1, 1, 0));
      sb();
      CP(false, f2[1][1], re[1][1], im[1][1], t1c[1], t1s[1], 1)
      UNP(0, 0) UNP(0, 1)
      sb();
      h4_b0p(S2X(1, 0), FLT(0, 0, 0)); h4_b0p(S2X(1, 1), FLT(0, 0, 1));
      sb();
      CP(true, f3[0][1], ur[0][1], ui[0][1], fr_[0][1], fi_[0][1], 0) CP(true, f3[0][1], ur[0][1], ui[0][1], fr_[0][1], fi_[0][1], 1)
      UNP(1, 0) UNP(1, 1)
      sb();
      h4_a0p(S3X(0, 0), FLT(1, 0, 0)); h4_a0p(S3X(0, 1), FLT(1, 0, 1));
      sb();
      CP(true, f3[1][1], ur[1][1], ui[1][1], fr_[1][1], fi_[1][1], 0) CP(true, f3[1][1], ur[1][1], ui[1][1], fr_[1][1], fi_[1][1], 1)
#pragma unroll
      for (int e = 0; e < 4; ++e) pin(f3[1][1][e]);
      nop2();
      h4_a0p(S3X(1, 0), TW2(0, 0, 0)); h4_a0p(S3X(1, 1), TW2(0, 0, 1));
      XW(0, 0)
      sb();
      CP(true, f4, re[0][1], im[0][1], t2c[1], t2s[1], 0) CP(true, f4, re[0][1], im[0][1], t2c[1], t2s[1], 1) XW(0, 1)
      sb();
      mfence<0>(re[1][0], im[1][0], re[1][1], im[1][1]);
      CP(true, f4, re[1][0], im[1][0], t2c[0], t2s[0], 0) CP(true, f4, re[1][0], im[1][0], t2c[0], t2s[0], 1) XW(1, 0)
      CP(true, f4, re[1][1], im[1][1], t2c[1], t2s[1], 0) CP(true, f4, re[1][1], im[1][1], t2c[1], t2s[1], 1) XW(1, 1)
      bar();
#pragma unroll
      for (int c = 0; c < 2; ++c)
#pragma unroll
        for (int nt = 0; nt < 2; ++nt)
#pragma unroll
          for (int s = 0; s < 4; ++s) a[c][nt][s] = lds4(LB, L_CX + ((c * 2 + nt) * 4 + s) * 1024);
      const int o2 = WS_T1 + (32 * wave + (lane >> 2)) * T1_BLK + pair * 128 + (lane & 3) * 16;
#define S4(c, t) mf8(im[c][t], re[c][t], c64[0], a[c][t], c64[1], a[c][t]);
#define NEQ(x, y) ((x) == (y) ? (x) ^ 1u : (x))
#define OUT(c, t) { const v4u o = {pk(re[c][t].x, im[c][t].x), pk(re[c][t].y, im[c][t].y), pk(re[c][t].z, im[c][t].z), pk(re[c][t].w, im[c][t].w)}; \
      st4c(ws, o2 + c * 64 + t * 16 * T1_BLK, v4u{NEQ(o.x, old[c][t].x), NEQ(o.y, old[c][t].y), NEQ(o.z, old[c][t].z), NEQ(o.w, old[c][t].w)}); sb(); }
      S4(0, 0) S4(0, 1) mfence<0>(re[0][0], im[0][0]); OUT(0, 0)
      S4(1, 0) mfence<0>(re[0][1], im[0][1]); OUT(0, 1)
      S4(1, 1) mfence<0>(re[1][0], im[1][0]); OUT(1, 0)
      mfence<0>(re[1][1], im[1][1]); OUT(1, 1)
    }
    STAMP(5);
    sb();
    if (!isG) { DUMP(); return; }
    if (!wall(fd.x == tag && fd.y == ~tag)) wait_flags(ws, WS_DRDY + grp * 1024 + min(lane, 48) * 8, tag);
    dma_w1(wg, wu, slice, wave, lane, lbase);
#pragma unroll
    for (int f = 0; f < WD_MID; ++f) wdn[f >> 2][f & 3] = ld4c(ws, wl16 + (f & 3) * 1024, wdb + (f >> 2) * 4096);
    fd = ld2c(ws, hro);
    sb();
#pragma unroll
    for (int m = 0; m < 16; ++m)
      st4c(ws, WS_ACT + (16 * (tile + 2 * (m >> 3)) + 4 * (grp & 3)) * 57344 + ct * 32768 + (m & 7) * 4096 + tid * 16, sv);
    sb();
    if (!wall(fd.x == tag && fd.y == ~tag)) wait_flags(ws, hro, tag);
    bar();
  }

  STAMP(7);
  const int blk0 = 16 * tile + 4 * (grp & 3);
  const int ab = WS_ACT + blk0 * 57344 + wave * 14 * 1024;
  v4u h[14][2];
  {
    const int hb = WS_H2 + (16 * tile + 4 * wave) * 14336;
    int tb[2][4];
#pragma unroll
    for (int u = 0; u < 8; ++u) {
      tb[u >> 2][u & 3] = (((lane >> 2) * 128 + ((lane >> 3) & 3) * 32 + (lane & 3) * 8) ^ ((u & 3) << 5)) + (u >> 2) * 57344;
      pin(tb[u >> 2][u & 3]);
    }
#define W_RD(dst, k, u) dst = ldtr2(L + tb[k >= 7][u & 3], (k % 7) * 8192 + (u >> 2) * 4096, (k % 7) * 8192 + (u >> 2) * 4096 + 2048)
#define H_LD(v, tt) hh_[v][tt] = ld4c(ws, l16 + (((v) % 14) & 3) * 1024, hb + (2 * ((v) / 14) + (tt)) * 14336 + (((v) % 14) >> 2) * 4096)
    const int abx[2] = {ab + (wave >> 1) * 57344, ab + (1 - (wave >> 1)) * 57344};
#define G_LD(e) h[(e) >> 1][(e) & 1] = ld4c(ws, l16 + (((e) >> 1) & 3) * 1024, abx[(e) & 1] + ((e) >> 3) * 4096)
    v4u wfr[2][8], hh_[28][2];
#pragma unroll
    for (int u = 0; u < 8; ++u) W_RD(wfr[0][u], 0, u);
    light_poll(ws, hb + (lane & 1) * 14336 + 13 * 1024);
    STAMP(15);
#pragma unroll
    for (int v = 0; v < LEAD; ++v) { H_LD(v, 0); H_LD(v, 1); }
    sb();
    if (!isR && lane < 2)
      st2c(ws, WS_CRDY + ((tile + 2 * lane) * 4 + (grp & 3)) * 256 + (ct * 4 + wave) * 8, v2u{tag, ~tag});
    sb();
#define BAD2(x0, x1) wany(max(max(max(max(x0.x, x0.y), x0.z), max(max(x0.w, x1.x), x1.y)), max(x1.z, x1.w)) == SENT)
#define RETRY2(x0, x1, o0, o1) \
      _Pragma("nounroll") for (int s_ = 0; s_ < SPIN; ++s_) { \
        const int lf_ = fresh(l16); SLOWCNT \
        x0 = ld4c(ws, lf_, o0); x1 = ld4c(ws, lf_, o1); \
        if (wall(max(max4(x0), max4(x1)) != SENT)) break; \
      }
    if (__builtin_expect(BAD2(hh_[0][0], hh_[0][1]), 0)) { RETRY2(hh_[0][0], hh_[0][1], hb, hb + 14336) }
    STAMP(8); CSTAMP(28);
    v4f acc[8][2];
    v2u fa;
    const int fx = WS_CRDY + (tile * 4 + (lane < 32 ? wave : grp & 3)) * 256 + min(lane & 31, 27) * 8;
    const int ao = WS_ACT + (16 * tile + 4 * wave) * 57344 + 2 * slice * 1024 + l16;
#define ACT_OUT(ps, tt, f) { v4u o; \
      _Pragma("unroll") for (int hh = 0; hh < 2; ++hh) { \
        const v4f av = relu4(acc[2 * f + hh][tt]) * acc[4 + 2 * f + hh][tt]; \
        o[2 * hh] = pk2(lo2(av)); o[2 * hh + 1] = pk2(hi2(av)); } \
      st4c(ws, ao + (2 * ps + tt) * 57344 + f * 1024, o); }
#define ACT_ALL(ps) { mfence<0>(acc[0][0], acc[1][0], acc[2][0], acc[3][0]); mfence<0>(acc[4][0], acc[5][0], acc[6][0], acc[7][0]); \
      ACT_OUT(ps, 0, 0) ACT_OUT(ps, 0, 1) sb(); \
      mfence<0>(acc[0][1], acc[1][1], acc[2][1], acc[3][1]); mfence<0>(acc[4][1], acc[5][1], acc[6][1], acc[7][1]); \
      ACT_OUT(ps, 1, 0) ACT_OUT(ps, 1, 1) sb(); }
#define W_RH(dst, k, u, hf) { const v2u t_ = ldtr(L + tb[k >= 7][u & 3], (k % 7) * 8192 + (u >> 2) * 4096 + hf * 2048); \
      dst[2 * hf] = t_.x; dst[2 * hf + 1] = t_.y; }
#pragma unroll
    for (int ps = 0; ps < 2; ++ps)
#pragma unroll
    for (int ks = 0; ks < 14; ++ks) {
      const int v = ps * 14 + ks, cur = v & 1, nk = (v + 1) % 14, vn = v < 27 ? v + 1 : v;
      const bool more = v < 27;
      u32 m0, m1;
      __builtin_amdgcn_s_waitcnt(0xC07F);
      sb();
#pragma unroll
      for (int i = 0; i < 16; ++i) {
        const int u = i & 7, tt = i >> 3;
        if (ks == 0) mv0(acc[u][tt], wfr[cur][u], hh_[v][tt]); else mv(acc[u][tt], wfr[cur][u], hh_[v][tt]);
        if (more) {
          if (i < 4) { W_RH(wfr[cur ^ 1][i], nk, i, 0) W_RH(wfr[cur ^ 1][i], nk, i, 1) }
          else if (i < 12) W_RH(wfr[cur ^ 1][4 + ((i - 4) >> 1)], nk, (4 + ((i - 4) >> 1)), (i & 1))
        }
        if (i == 5 && v + LEAD < 28) H_LD(v + LEAD, 0);
        if (i == 7 && v + LEAD < 28) H_LD(v + LEAD, 1);
        if (i == 9 || i == 11 || i == 13) {
          const int e = (i - 9) >> 1, f = WD_MID + (v < 14 ? v - 2 : 12 + WD_PER * (v - 14) + e);
          if (f < 56 && ((v >= 2 && v < 14 && e == 0) || (v >= 14 && e < WD_PER))) wdn[f >> 2][f & 3] = ld4c(ws, wl16 + (f & 3) * 1024, wdb + (f >> 2) * 4096);
        }
        if (v > A_V && (i & 1) && i >= 5 && ((i - 5) >> 1) < A_PER) G_LD(A_PER * (v - A_V - 1) + ((i - 5) >> 1));
        if (v == 8 && i == 13) fa = ld2c(ws, fx);
        if (more && i == 12) { m0 = max(max(hh_[vn][0].x, hh_[vn][0].y), hh_[vn][0].z); pin(m0); }
        if (more && i == 13) { m1 = max(max(hh_[vn][0].w, hh_[vn][1].x), hh_[vn][1].y); pin(m1); }
        if (more && i == 14) { m0 = max(max(m0, hh_[vn][1].z), hh_[vn][1].w); m0 = max(m0, m1); pin(m0); }
        sb();
      }
      if (v == 12 && !wall(fa.x == tag && fa.y == ~tag)) wait_flags(ws, fx, tag);
      if (more && __builtin_expect(wany(m0 == SENT), 0)) {
        const int pb = 2 * ((v + 1) / 14) * 14336 + nk * 1024;
        RETRY2(hh_[vn][0], hh_[vn][1], hb + pb, hb + pb + 14336)
      }
      if (v == 13) {
        STAMP(16); CSTAMP(29);
        ACT_ALL(0)
        if (isR) x2_store(ws, x2r, 16 * grp + j - 14, tid);
        nop2();
      }
      sb();
    }
    STAMP(27);
    ACT_ALL(1)
    CSTAMP(30);
#pragma unroll
    for (int e = A_PER * (27 - A_V); e < 28; ++e) G_LD(e);
    sb();
    STAMP(9);
    v4u a[14][2];
    v4f ac2[4][4];
#define A_LD(e) a[(e) >> 1][(e) & 1] = ld4c(ws, l16 + (((e) >> 1) & 3) * 1024, abx[(e) & 1] + 2 * 57344 + ((e) >> 3) * 4096)
    const int po = ab + 3 * 57344 + min(lane, 6) * 2048 + 1024;
    u32 probe;
#pragma unroll
    for (int ks = 0; ks < 14; ++ks) {
      if (__builtin_expect(BAD2(h[ks][0], h[ks][1]), 0)) { RETRY2(h[ks][0], h[ks][1], abx[0] + ks * 1024, abx[1] + ks * 1024) }
#pragma unroll
      for (int tt = 0; tt < 2; ++tt)
#pragma unroll
        for (int n = 0; n < 4; ++n) ac2[tt][n] = mfma(wdn[ks][n], h[ks][tt], ks ? ac2[tt][n] : splat(0.f));
      if (ks == P_AT) probe = ld1c(ws, po);
      sb();
      if (ks == B_AT2) {
        STAMP(25);
        if (wany(probe == SENT)) light_poll(ws, po);
        STAMP(26);
      }
      if (ks >= B_AT2) {
#pragma unroll
        for (int e = 4 * (ks - B_AT2); e < 4 * (ks - B_AT2 + 1); ++e) A_LD(e);
      }
      sb();
    }
    STAMP(17);
#pragma unroll
    for (int e = 4 * (14 - B_AT2); e < 4 * (14 - B_AT2) + 6; ++e) A_LD(e);
    sb();
    int rw[3], rr_ = L_B + lane * 8 + wave * 6144, oo[2];
#pragma unroll
    for (int x = 1; x < 4; ++x) { rw[x - 1] = L_B + lane * 8 + (wave ^ x) * 6144 + (x - 1) * 2048; pin(rw[x - 1]); }
    pin(rr_);
    const int blk = blk0 + (wave >> 1);
#pragma unroll
    for (int ph = 0; ph < 2; ++ph) { oo[ph] = (tok_row(blk + 2 * ph, c16) * D + ct * 64 + g * 16 + (wave & 1) * 8) * 2; pin(oo[ph]); }
#define RED_W1(ph, x, n) *(LAS v2f*)(L + rw[(x) - 1] + (ph) * 24576 + (n) * 512) = ((x) & 1) ? hi2(ac2[2 * (ph) + ((x) >> 1)][n]) : lo2(ac2[2 * (ph) + ((x) >> 1)][n]);
    const int xo = WS_X2 + blk * 14336 + c16 * 896 + ct * 128 + g * 32 + (wave & 1) * 16;
    const int go = WS_MOD + grp * MOD_REP + (70 + 2 * ct) * 128 + g * 64 + (wave & 1) * 32;
    v4u xv[2], gv[2];
    STAMP(18);
    sb();
#pragma unroll
    for (int ks = 0; ks < 14; ++ks) {
      if (__builtin_expect(BAD2(a[ks][0], a[ks][1]), 0)) { RETRY2(a[ks][0], a[ks][1], abx[0] + 2 * 57344 + ks * 1024, abx[1] + 2 * 57344 + ks * 1024) }
#pragma unroll
      for (int tt = 0; tt < 2; ++tt)
#pragma unroll
        for (int n = 0; n < 4; ++n) ac2[2 + tt][n] = mfma(wdn[ks][n], a[ks][tt], ks ? ac2[2 + tt][n] : splat(0.f));
#pragma unroll
      for (int e = 4 * (14 - B_AT2) + 6 + 3 * ks; e < 4 * (14 - B_AT2) + 9 + 3 * ks; ++e)
        if (e < 28) A_LD(e);
      if (ks == 6) { xv[0] = ld4c(ws, xo); xv[1] = ld4c(ws, xo + 2 * 14336); gv[0] = ld4c(ws, go); gv[1] = ld4c(ws, go + 16); }
      if (ks < 12) RED_W1(0, 1 + (ks >> 2), (ks & 3))
#pragma unroll
      for (int u = 0; u < 4; ++u) {
        __builtin_amdgcn_sched_group_barrier(0x8, 2, 0);
        __builtin_amdgcn_sched_group_barrier(0x20, 1, 0);
        if (u == 2) __builtin_amdgcn_sched_group_barrier(0x200, 1, 0);
      }
      sb();
    }
    STAMP(10);
#pragma unroll
    for (int e = 0; e < 12; ++e) RED_W1(1, 3 - (e >> 2), (e & 3))
#define FIN_OK() wall(max(max4(xv[0]), max4(xv[1])) != SENT && gv[0].y == tag && gv[0].w == tag && gv[1].y == tag && gv[1].w == tag)
    if (__builtin_expect(!FIN_OK(), 0)) {
      _Pragma("nounroll") for (int s = 0; s < SPIN; ++s) {
        const int gf = fresh(go), xf = fresh(xo);
        gv[0] = ld4c(ws, gf); gv[1] = ld4c(ws, gf + 16); xv[0] = ld4c(ws, xf); xv[1] = ld4c(ws, xf + 2 * 14336);
        if (FIN_OK()) break;
      }
    }
    bar();
#pragma unroll
    for (int ph = 0; ph < 2; ++ph) {
      v2f r[4];
#pragma unroll
      for (int n = 0; n < 4; ++n) {
        r[n] = lo2(ac2[2 * ph][n]);
#pragma unroll
        for (int w = 0; w < 3; ++w) r[n] += *(const LAS v2f*)(L + rr_ + w * 2048 + ph * 24576 + n * 512);
      }
      v4u o;
#pragma unroll
      for (int q = 0; q < 2; ++q) {
        o[2 * q] = pk2(fma2(bun(gv[q].x), v2f{r[0][q], r[1][q]}, bun(xv[ph][2 * q])));
        o[2 * q + 1] = pk2(fma2(bun(gv[q].z), v2f{r[2][q], r[3][q]}, bun(xv[ph][2 * q + 1])));
      }
      __builtin_amdgcn_raw_buffer_store_b128(o, mkrs(out, S * D * 2), oo[ph], 0, OUT_AUX);
    }
    if (blockIdx.x == 0 && wave == 0) {
      _Pragma("nounroll") for (int s = 0; s < SPIN; ++s) {
        const v4u v = ld4c(ws, fresh(WS_MOD + (70 + (lane < 14 ? lane : 0)) * 128));
        if (wall(v.y == tag)) break;
      }
      if (lane == 0) __builtin_amdgcn_raw_buffer_store_b32(epoch + 1u, ws, WS_EPOCH, 0, SC1);
    }
  }
  if (blockIdx.x == PADB) { while (__builtin_amdgcn_s_memrealtime() < padt + PADT) __builtin_amdgcn_s_sleep(1); }
  DUMP();
}

extern "C" void kernel_launch(void* const* d_in, const int* in_sizes, int n_in, void* d_out, int out_size, void* d_ws,
                              size_t ws_size, hipStream_t stream) {
  fused<<<256, 256, 0, stream>>>((const u16*)d_in[0], (const u16*)d_in[1], (const u16*)d_in[2], (const u16*)d_in[4],
                                 (const u16*)d_in[5], (const u16*)d_in[6], (const u16*)d_in[7], (const u16*)d_in[8],
                                 (u16*)d_out, (char*)d_ws);
}
```

```cpp
#define CLR_MODE 0
#include <hip/hip_runtime.h>

typedef unsigned short u16;
typedef unsigned int u32;
typedef unsigned long long u64;
typedef u32 v4u __attribute__((ext_vector_type(4)));
typedef u32 v2u __attribute__((ext_vector_type(2)));
typedef float v4f __attribute__((ext_vector_type(4)));
typedef float v2f __attribute__((ext_vector_type(2)));
typedef short v8s __attribute__((ext_vector_type(8)));
typedef short v4s __attribute__((ext_vector_type(4)));
typedef __bf16 v8b __attribute__((ext_vector_type(8)));
typedef __bf16 v2b __attribute__((ext_vector_type(2)));
#define LAS __attribute__((address_space(3)))
#define GAS __attribute__((address_space(1)))
#define DEV __device__ __forceinline__

#define C_W1_EARLY 0
#define CLR_DELAY 0
#define W_DELAY 0
#define CLR2_DELAY 0
#define PF_POS 2
#define SL1 1
#define SL2 2
#define SL4 4
#define X_AFTER 8
#define PF_AUX 2
#define W_LATE 1
#define W1_PF 1
#define DMA_MOD ""
#define LEAD 6
#define A_V 22
#define A_PER 4
#define WDV(f) ((f) - WD_MID + 2 < 14 ? (f) - WD_MID + 2 : ((f) - WD_MID - 12) / 2 + 16)
#define B_AT2 12
#define RE_PF 0
#define PF2_DELAY 0
#define C_DMA_EARLY 0
#define EVEN_PF 0
#define XBAL 1
#define W1EVEN 1
#define PRE_OUT 0
#define OUT_AUX 16
#define P_AT 7
#define WD_PER 3
#define A_FIRST 10
#define B_FIRST 10
#define A_AT 9
#define F_AT 4
#define CODE_BYTES 45056
#define WD_EARLY 16
#define WD_MID 24
#define B_AT 9
constexpr int S = 2048, D = 448, DF = 225, F = 1792;

struct alignas(16) Chain { u16 v[2048]; };
struct Cos448 { u16 v[448]; };
constexpr double c_cos_t(double a) {
  double a2 = a * a, t = 1.0, s = 1.0;
  for (int i = 1; i <= 10; ++i) { t = -t * a2 / ((2 * i - 1) * (2 * i)); s += t; }
  return s;
}
constexpr double c_sin_t(double a) {
  double a2 = a * a, t = a, s = a;
  for (int i = 1; i <= 10; ++i) { t = -t * a2 / ((2 * i) * (2 * i + 1)); s += t; }
  return s;
}
constexpr double c_cos448(int m) {
  if (m > 224) m = 448 - m;
  double sg = 1.0;
  if (m > 112) { m = 224 - m; sg = -1.0; }
  const double w = 6.283185307179586476925286766559 / 448.0;
  return m == 112 ? 0.0 : (m <= 56 ? sg * c_cos_t(w * m) : sg * c_sin_t(w * (112 - m)));
}
constexpr u16 c_bf16(double d) {
  const u32 u = __builtin_bit_cast(u32, (float)d);
  return (u16)((u + 0x7FFFu + ((u >> 16) & 1u)) >> 16);
}
constexpr Cos448 mk_cos() { Cos448 c{}; for (int m = 0; m < 448; ++m) c.v[m] = c_bf16(c_cos448(m)); return c; }
constexpr Cos448 CTAB = mk_cos();
constexpr u16 bcos(int m) { return CTAB.v[m % 448]; }
constexpr u16 bsin(int m) { return CTAB.v[(m % 448 + 336) % 448]; }
constexpr u16 bneg(u16 b) { return b ? (u16)(b ^ 0x8000u) : b; }
constexpr u16 bscale(u16 b, int e) { return b ? (u16)(b + e * 0x80) : b; }

constexpr Chain mk_p1(int c) {
  const int tile = c >> 2, par = c & 1, sn = (c >> 1) & 1;
  Chain ch{};
  for (int s = 0; s < 4; ++s)
    for (int l = 0; l < 64; ++l) {
      const int kp = 16 * tile + (l & 15), k = 2 * kp + par;
      if (k > 224) continue;
      for (int j = 0; j < 8; ++j) {
        const int dl = 32 * s + 8 * (l >> 4) + j;
        if (dl > 112) continue;
        u16 b = sn ? bneg(bsin(k * dl)) : bcos(k * dl);
        if (dl == 0 || dl == 112) b = bscale(b, -1);
        ch.v[(s * 64 + l) * 8 + j] = b;
      }
    }
  return ch;
}
constexpr Chain mk_p3(int c) {
  const int tile = c >> 1, sn = c & 1;
  Chain ch{};
  for (int s = 0; s < 4; ++s)
    for (int l = 0; l < 64; ++l) {
      int dl = 16 * tile + (l & 15);
      if (c == 1 && dl == 0) dl = 112;
      for (int j = 0; j < 8; ++j) {
        const int k = 2 * (32 * s + 8 * (l >> 4) + j) + 1;
        if (k < 224) ch.v[(s * 64 + l) * 8 + j] = sn ? bsin(k * dl) : bcos(k * dl);
      }
    }
  return ch;
}
constexpr Chain mk_p2(int c) {
  Chain ch{};
  for (int f = 0; f < 4; ++f)
    for (int l = 0; l < 64; ++l)
      for (int j = 0; j < 8; ++j) {
        const int comp = c < 8 ? (c & 1) : (f >> 1), s = c < 8 ? f : (f & 1);
        const int o = 16 * (c < 8 ? (c >> 1) : (c - 8)) + (l & 15), i = 16 * s + 4 * (l >> 4) + (j >> 1);
        const int m = (c < 8 ? 7 : 14) * ((i * o) % 64);
        u16 b = 0;
        if (comp == 0) b = (j & 1) ? bsin(m) : bcos(m);
        else b = (j & 1) ? bcos(m) : bneg(bsin(m));
        ch.v[(f * 64 + l) * 8 + j] = b;
      }
  return ch;
}
template <int I> constexpr Chain P1C = mk_p1(I);
template <int I> constexpr Chain P3C = mk_p3(I);
template <int I> constexpr Chain P2C = mk_p2(I);
#define L10(T, b) T<b>, T<b + 1>, T<b + 2>, T<b + 3>, T<b + 4>, T<b + 5>, T<b + 6>, T<b + 7>, T<b + 8>, T<b + 9>
#define L29(T) L10(T, 0), L10(T, 10), T<20>, T<21>, T<22>, T<23>, T<24>, T<25>, T<26>, T<27>, T<28>
constexpr int TAB_P1 = 0, TAB_P3 = 29, TAB_P2 = 43, TAB_N = 53;
static __device__ const Chain TAB[TAB_N] = {L29(P1C), L10(P3C, 0), P3C<10>, P3C<11>, P3C<12>, P3C<13>, L10(P2C, 0)};

constexpr int WS_EPOCH = 0;
constexpr int WS_RDY = 4096;
constexpr int WS_HRDY = 4096 + 8192;
constexpr int WS_DRDY = 4096 + 16384;
constexpr int WS_XRDY = 131072 + 65536;
constexpr int WS_ARDY = 131072;
constexpr int WS_CRDY = 131072 + 32768;
constexpr int WS_MOD = 32768, MOD_REP = 12288;
constexpr int T1_BLK = 226 * 64;
constexpr int WS_T1 = 1 << 18;
constexpr int WS_WT = WS_T1 + 128 * T1_BLK;
constexpr int WS_H2 = WS_WT + 113 * 16384;
constexpr int WS_X2 = WS_H2 + 128 * 14336;
constexpr int WS_WD = WS_X2 + 128 * 14336;
constexpr int WS_ACT = WS_WD + 7 * 56 * 4096;
constexpr int WS_END = WS_ACT + 128 * 57344;
constexpr int WS_DBG = 32 << 20;

constexpr int L_W1 = 0;
constexpr int L_B = 114688;
constexpr int XP = 912;
constexpr int L_XS = L_B;
constexpr int L_ST = L_XS + 16 * XP;
constexpr int L_M1 = L_ST + 128;
constexpr int L_AF = L_M1 + 4 * 1792;
constexpr int L_M2 = L_AF + 16384;
constexpr int L_EX = L_M2 + 3 * 1792;
constexpr int L_REND = L_EX + 512;
constexpr int ZPL = 8192;
constexpr int L_CI = L_B;
constexpr int L_CX = L_CI + 2 * 8256;
constexpr int L_RED = L_CX + 16384;
constexpr int L_DBG = 163840 - 1024;
constexpr int L_SZ = 163840;
static_assert(L_REND <= L_DBG && L_RED + 2048 <= L_DBG && 2 * ZPL <= 16384, "lds");

constexpr u32 SENT = 0xFFFFFFFFu;
constexpr int SPIN = 1 << 14;
constexpr int SC1 = 16;
constexpr int OOB = 0x7FFF0000;

typedef __amdgpu_buffer_rsrc_t rsrc_t;
DEV rsrc_t mkrs(const void* p, int bytes) { return __builtin_amdgcn_make_buffer_rsrc((void*)p, 0, bytes, 0x00020000); }
DEV v4u ld4(rsrc_t r, int off, int so = 0) { return __builtin_amdgcn_raw_buffer_load_b128(r, off, so, 0); }
DEV v2u ld2(rsrc_t r, int off, int so = 0) { return __builtin_amdgcn_raw_buffer_load_b64(r, off, so, 0); }
DEV v4u ld4c(rsrc_t r, int off, int so = 0) { return __builtin_amdgcn_raw_buffer_load_b128(r, off, so, SC1); }
DEV v2u ld2c(rsrc_t r, int off, int so = 0) { return __builtin_amdgcn_raw_buffer_load_b64(r, off, so, SC1); }
DEV u32 ld1c(rsrc_t r, int off, int so = 0) { return __builtin_amdgcn_raw_buffer_load_b32(r, off, so, SC1); }
DEV void st4c(rsrc_t r, int off, v4u v) { __builtin_amdgcn_raw_buffer_store_b128(v, r, off, 0, SC1); }
DEV void st2c(rsrc_t r, int off, v2u v) { __builtin_amdgcn_raw_buffer_store_b64(v, r, off, 0, SC1); }
DEV float blo(u32 v) { return __builtin_bit_cast(float, v << 16); }
DEV float bhi(u32 v) { return __builtin_bit_cast(float, v & 0xFFFF0000u); }
DEV v2f bun(u32 v) { return v2f{blo(v), bhi(v)}; }
DEV u32 pk(float lo, float hi) { return __builtin_bit_cast(u32, __builtin_convertvector(v2f{lo, hi}, v2b)); }
DEV u32 pk2(v2f v) { return __builtin_bit_cast(u32, __builtin_convertvector(v, v2b)); }
DEV float rb(float f) { return blo(pk(f, 0.f)); }
DEV v2f lo2(v4f v) { return __builtin_shufflevector(v, v, 0, 1); }
DEV v2f hi2(v4f v) { return __builtin_shufflevector(v, v, 2, 3); }
DEV v2f fma2(v2f a, v2f b, v2f c) { return __builtin_elementwise_fma(a, b, c); }
DEV v4f fma4(v4f a, v4f b, v4f c) { return __builtin_elementwise_fma(a, b, c); }
typedef int v4i __attribute__((ext_vector_type(4)));
DEV v4f relu4(v4f v) { return __builtin_bit_cast(v4f, __builtin_elementwise_max(__builtin_bit_cast(v4i, v), v4i{0, 0, 0, 0})); }
DEV v4f splat(float f) { return v4f{f, f, f, f}; }
DEV v4f mfma(v4u a, v4u b, v4f c) {
  return __builtin_amdgcn_mfma_f32_16x16x32_bf16(__builtin_bit_cast(v8b, a), __builtin_bit_cast(v8b, b), c, 0, 0, 0);
}
DEV void mfa(v4f& c, v4u a, v4u b) { asm volatile("s_nop 1\n\tv_mfma_f32_16x16x32_bf16 %0, %1, %2, %0" : "+v"(c) : "v"(a), "v"(b)); }
DEV void mf0(v4f& c, v4u a, v4u b) { asm volatile("s_nop 1\n\tv_mfma_f32_16x16x32_bf16 %0, %1, %2, 0" : "=&v"(c) : "v"(a), "v"(b)); }
template <int N> DEV void mfence(v4f& a, v4f& b) { asm volatile("s_nop %2" : "+v"(a), "+v"(b) : "i"(N)); }
template <int N> DEV void mfence(v4f& a, v4f& b, v4f& c, v4f& d) { asm volatile("s_nop %4" : "+v"(a), "+v"(b), "+v"(c), "+v"(d) : "i"(N)); }
#define MF "v_mfma_f32_16x16x32_bf16 "
DEV void mf8(v4f& d0, v4f& d1, const v4u (&a0)[4], const v4u (&b0)[4], const v4u (&a1)[4], const v4u (&b1)[4]) {
  asm volatile("s_nop 1\n\t" MF "%0, %2, %6, 0\n\t" MF "%1, %10, %14, 0\n\t" MF "%0, %3, %7, %0\n\t" MF "%1, %11, %15, %1\n\t"
               MF "%0, %4, %8, %0\n\t" MF "%1, %12, %16, %1\n\t" MF "%0, %5, %9, %0\n\t" MF "%1, %13, %17, %1"
               : "=&v"(d0), "=&v"(d1)
               : "v"(a0[0]), "v"(a0[1]), "v"(a0[2]), "v"(a0[3]), "v"(b0[0]), "v"(b0[1]), "v"(b0[2]), "v"(b0[3]),
                 "v"(a1[0]), "v"(a1[1]), "v"(a1[2]), "v"(a1[3]), "v"(b1[0]), "v"(b1[1]), "v"(b1[2]), "v"(b1[3]));
}
#define H4_M(XA, D, X, K, C) MF D ", " XA(X, K) ", " C "\n\t"
#define H4_XA(X, K) X ", " K
#define H4_XB(X, K) K ", " X
#define H4_DEF(NAME, XA, C0, C1, CONS, N0, N1, PA, PB, PC, PD) \
DEV void NAME(v4f& d0, v4f& d1, v4u x0, v4u x1, v4u k00, v4u k01, v4u k10, v4u k11, \
              u32& o0, u32& o1, float p0, float q0, float c0, float s0, float p1, float q1, float c1, float s1) { \
  float t0, t1, t2, t3; \
  asm volatile(H4_M(XA, "%0", "%8", "%10", C0) "v_mul_f32 %4, %14, %16\n\tv_mul_f32 %5, %15, %16\n\t" \
               H4_M(XA, "%1", "%8", "%12", C1) "v_mul_f32 %6, %18, %20\n\tv_mul_f32 %7, %19, %20\n\t" \
               H4_M(XA, "%0", "%9", "%11", "%0") "v_fma_f32 %4, " N0 "%15, %17, %4\n\tv_fma_f32 %5, " N1 "%14, %17, %5\n\t" \
               H4_M(XA, "%1", "%9", "%13", "%1") "v_fma_f32 %6, " N0 "%19, %21, %6\n\tv_fma_f32 %7, " N1 "%18, %21, %7\n\t" \
               "v_cvt_pk_bf16_f32 %2, " PA ", " PB "\n\tv_cvt_pk_bf16_f32 %3, " PC ", " PD \
               : CONS(d0), CONS(d1), "=&v"(o0), "=&v"(o1), "=&v"(t0), "=&v"(t1), "=&v"(t2), "=&v"(t3) \
               : "v"(x0), "v"(x1), "v"(k00), "v"(k01), "v"(k10), "v"(k11), \
                 "v"(p0), "v"(q0), "v"(c0), "v"(s0), "v"(p1), "v"(q1), "v"(c1), "v"(s1)); \
}
H4_DEF(h4_a0m, H4_XA, "0", "0", "=&v", "", "-", "%4", "%5", "%6", "%7")
H4_DEF(h4_a1m, H4_XA, "%0", "%1", "+v", "", "-", "%4", "%5", "%6", "%7")
H4_DEF(h4_b0m, H4_XB, "0", "0", "=&v", "", "-", "%4", "%5", "%6", "%7")
H4_DEF(h4_b0p, H4_XB, "0", "0", "=&v", "-", "", "%5", "%4", "%7", "%6")
H4_DEF(h4_a0p, H4_XA, "0", "0", "=&v", "-", "", "%5", "%4", "%7", "%6")
#define M4_DEF(NAME, XA, C0, C1, CONS) \
DEV void NAME(v4f& d0, v4f& d1, v4u x0, v4u x1, v4u k00, v4u k01, v4u k10, v4u k11) { \
  asm volatile(H4_M(XA, "%0", "%2", "%4", C0) H4_M(XA, "%1", "%2", "%6", C1) H4_M(XA, "%0", "%3", "%5", "%0") MF "%1, " XA("%3", "%7") ", %1" \
               : CONS(d0), CONS(d1) : "v"(x0), "v"(x1), "v"(k00), "v"(k01), "v"(k10), "v"(k11)); \
}
M4_DEF(m4_a0, H4_XA, "0", "0", "=&v") M4_DEF(m4_a1, H4_XA, "%0", "%1", "+v")
M4_DEF(m4_b0, H4_XB, "0", "0", "=&v") M4_DEF(m4_b1, H4_XB, "%0", "%1", "+v")
DEV void mv(v4f& c, v4u a, v4u b) { asm volatile(MF "%0, %1, %2, %0" : "+v"(c) : "v"(a), "v"(b)); }
DEV void mv0(v4f& c, v4u a, v4u b) { asm volatile(MF "%0, %1, %2, 0" : "=&v"(c) : "v"(a), "v"(b)); }
DEV void nop2() { asm volatile("s_nop 1"); }
template <class T> DEV void pin(T& v) { asm volatile("" : "+v"(v)); }
DEV int fresh(int off) { asm volatile("" : "+v"(off) :: "memory"); return off; }
DEV bool wall(bool ok) { return __builtin_amdgcn_ballot_w64(!ok) == 0; }
DEV bool wany(bool b) { return __builtin_amdgcn_ballot_w64(b) != 0; }
DEV void sb() { __builtin_amdgcn_sched_barrier(0); }
DEV u32 max4(v4u v) { return max(max(v.x, v.y), max(v.z, v.w)); }
DEV void drain() { asm volatile("s_waitcnt vmcnt(0)" ::: "memory"); }
DEV void cfence() { asm volatile("" ::: "memory"); }
DEV void bar() { asm volatile("s_waitcnt lgkmcnt(0)\n\ts_barrier" ::: "memory"); }
template <int CTRL> DEV float dpp(float v) {
  return __builtin_bit_cast(float, __builtin_amdgcn_update_dpp(0, __builtin_bit_cast(int, v), CTRL, 0xF, 0xF, true));
}
DEV float sum16(float v) {
  v += dpp<0x128>(v); v += dpp<0x124>(v); v += dpp<0x122>(v); v += dpp<0x121>(v); return v;
}
DEV void sumg2(float& a, float& b) {
  float c = a, d = b;
  asm volatile("s_nop 1\n\tv_permlane16_swap_b32 %0, %1\n\tv_permlane16_swap_b32 %2, %3" : "+v"(a), "+v"(c), "+v"(b), "+v"(d));
  a += c; b += d; c = a; d = b;
  asm volatile("s_nop 1\n\tv_permlane32_swap_b32 %0, %1\n\tv_permlane32_swap_b32 %2, %3" : "+v"(a), "+v"(c), "+v"(b), "+v"(d));
  a += c; b += d;
}
DEV v4u lds4(const char LAS* L, int off) { return *(const LAS v4u*)(L + off); }
DEV v4f lds4f(const char LAS* L, int off) { return *(const LAS v4f*)(L + off); }
DEV v2u ldtr(const char LAS* L, int off) { return __builtin_bit_cast(v2u, __builtin_amdgcn_ds_read_tr16_b64_v4i16((LAS v4s*)(L + off))); }
DEV v4u ldtr2(const char LAS* L, int o0, int o1) { const v2u a = ldtr(L, o0), b = ldtr(L, o1); return v4u{a.x, a.y, b.x, b.y}; }
DEV void dma16(rsrc_t r, int voff, int soff, u32 lds_dst) {
  u32 keep;
  asm volatile("s_mov_b32 %0, m0\n\ts_mov_b32 m0, %1\n\ts_nop 0\n\tbuffer_load_dwordx4 %2, %3, %4 offen" DMA_MOD " lds\n\ts_mov_b32 m0, %0"
               : "=&s"(keep) : "s"(lds_dst), "v"(voff), "s"(r), "s"(soff) : "memory");
}
DEV void light_poll(rsrc_t ws, int off) {
  _Pragma("nounroll") for (int s = 0; s < SPIN; ++s) {
    if (wall(ld1c(ws, fresh(off)) != SENT)) break;
    __builtin_amdgcn_s_sleep(SL2);
  }
}
DEV int tok_row(int blk, int t) {
  const int n1 = 16 * (blk >> 5) + 4 * ((blk >> 2) & 3) + (t & 3), n2 = 16 * ((blk >> 4) & 1) + 4 * (blk & 3) + (t >> 2);
  return n2 + 32 * n1;
}
template <bool SWAP> DEV v4u cmul_pack(v4f a, v4f b, v4f c, v4f s) {
  const v4f re = fma4(b, s, a * c), im = fma4(b, c, -(a * s));
  return SWAP ? v4u{pk(im.x, re.x), pk(im.y, re.y), pk(im.z, re.z), pk(im.w, re.w)}
              : v4u{pk(re.x, im.x), pk(re.y, im.y), pk(re.z, im.z), pk(re.w, im.w)};
}

template <bool PLUS> DEV void cmul2(u32& o0, u32& o1, v2f p, v2f q, v2f c, v2f s) {
  const v2f re = fma2(PLUS ? -q : q, s, p * c), im = fma2(PLUS ? p : -p, s, q * c);
  o0 = PLUS ? pk(im.x, re.x) : pk(re.x, im.x); o1 = PLUS ? pk(im.y, re.y) : pk(re.y, im.y);
}

#define STAMP(i)
#define CSTAMP(i)
#define SLOWCNT
#define DUMP()

DEV void dma_w1(const u16* wg, const u16* wu, int slice, int wave, int lane, u32 lbase) {
  const rsrc_t rg = mkrs(wg, D * F * 2), ru = mkrs(wu, D * F * 2);
  const int r = lane >> 3, ch = ((((lane & 7) >> 1) ^ (lane >> 4)) & 3) * 32 + (lane & 1) * 16 + slice * 128;
  const int vp = r * (F * 2) + ch, vn = (7 - r) * (F * 2) + ch;
#pragma unroll
  for (int n = 0; n < 4; ++n) {
    const int ks = wave + 4 * n;
    if (n == 3 && wave >= 2) break;
    const int t16 = (ks >> 1) * 16, a = ks & 1;
#pragma unroll
    for (int q = 0; q < 4; ++q) {
      const int dl0 = t16 + 8 * (q & 1);
      const bool pos = (q >> 1) == a;
      const int d0 = q < 2 ? (a ? 224 - dl0 - 7 : dl0) : (a ? 224 + dl0 : 448 - dl0 - 7);
      int vo = pos ? vp : vn, so = d0 * (F * 2);
      if (n == 0 && q == 2) {
        vo += so; so = 0;
        if (wave < 2 && lane < 8) vo = (wave ? 336 : 112) * (F * 2) + ch;
      }
      dma16(rg, vo, so, lbase + L_W1 + ks * 8192 + q * 1024);
      dma16(ru, vo, so, lbase + L_W1 + ks * 8192 + 4096 + q * 1024);
    }
  }
}
struct ModLoad { u32 cv[7]; v4u mw[7]; };
DEV void mod_load(ModLoad& m, const u16* cin, const u16* wm, int q, int tid) {
  const rsrc_t cr = mkrs(cin, q >= 0 ? D * 2 : 0), mr = mkrs(wm, q >= 0 ? D * 6 * D * 2 : 0);
#pragma unroll
  for (int i = 0; i < 7; ++i) m.cv[i] = __builtin_amdgcn_raw_buffer_load_b16(cr, (tid >> 2) * 2 + i * 128, 0, 0);
#pragma unroll
  for (int i = 0; i < 7; ++i) m.mw[i] = __builtin_amdgcn_raw_buffer_load_b128(mr, ((tid >> 2) * (6 * D) + q * 32 + (tid & 3) * 8) * 2, i * 64 * 6 * D * 2, PF_AUX);
}
DEV void mod_part(ModLoad& m, char LAS* L, int tid) {
  float sl[7];
#pragma unroll
  for (int i = 0; i < 7; ++i) { const float cc = blo(m.cv[i]); sl[i] = rb(cc * __builtin_amdgcn_rcpf(1.f + __expf(-cc))); pin(sl[i]); }
  v2f a[4];
#pragma unroll
  for (int e = 0; e < 4; ++e) a[e] = v2f{0.f, 0.f};
#pragma unroll
  for (int i = 0; i < 7; ++i)
#pragma unroll
    for (int e = 0; e < 4; ++e) a[e] = fma2(bun(m.mw[i][e]), v2f{sl[i], sl[i]}, a[e]);
#pragma unroll
  for (int e = 0; e < 4; ++e) {
    a[e].x += dpp<0x128>(a[e].x); a[e].y += dpp<0x128>(a[e].y);
    a[e].x += dpp<0x124>(a[e].x); a[e].y += dpp<0x124>(a[e].y);
  }
  if ((tid & 15) < 4) {
    *(LAS v4f*)(L + L_RED + ((tid >> 4) * 32 + (tid & 15) * 8) * 4) = v4f{a[0].x, a[0].y, a[1].x, a[1].y};
    *(LAS v4f*)(L + L_RED + ((tid >> 4) * 32 + (tid & 15) * 8 + 4) * 4) = v4f{a[2].x, a[2].y, a[3].x, a[3].y};
  }
}
DEV void mod_publish(char LAS* L, rsrc_t ws, int q, int lane, u32 tag) {
  v4f s = {0.f, 0.f, 0.f, 0.f};
#pragma unroll
  for (int r = 0; r < 16; ++r) s += lds4f(L, L_RED + (r * 32 + (lane & 7) * 4) * 4);
  const float one = (q / 14 == 1 || q / 14 == 4) ? 1.f : 0.f;
  st4c(ws, WS_MOD + (lane >> 3) * MOD_REP + q * 128 + (lane & 7) * 16,
       v4u{pk(rb(s.x) + one, rb(s.y) + one), tag, pk(rb(s.z) + one, rb(s.w) + one), tag});
}
DEV u32 ld1(rsrc_t r, int off) { return __builtin_amdgcn_raw_buffer_load_b32(r, off, 0, 0); }
DEV u32 ld1n(rsrc_t r, int off) { return __builtin_amdgcn_raw_buffer_load_b32(r, off, 0, PF_AUX); }
DEV u32 mktag(u32 epoch) { return epoch * 0x9E3779B1u + 0x7F4A7C15u; }

DEV void x2_read(const char LAS* L, v4u (&r)[4], int tid) {
#pragma unroll
  for (int m = 0; m < 4; ++m) { const int ch = min(tid + 256 * m, 895), tk = ch / 56; r[m] = lds4(L, L_XS + tk * XP + (ch - tk * 56) * 16); }
}
DEV void x2_store(rsrc_t ws, const v4u (&r)[4], int blk, int tid) {
#pragma unroll
  for (int m = 0; m < 4; ++m) st4c(ws, tid + 256 * m < 896 ? WS_X2 + blk * 14336 + (tid + 256 * m) * 16 : OOB, r[m]);
}
DEV bool flags_ok(v4u rv, u32 tag) { return rv.x == tag && rv.y == ~tag && rv.z == tag && rv.w == ~tag; }
DEV void wait_flags(rsrc_t ws, int off, u32 tag) {
  _Pragma("nounroll") for (int s = 0; s < SPIN; ++s) {
    const v2u rv = ld2c(ws, fresh(off));
    if (wall(rv.x == tag && rv.y == ~tag)) break;
    __builtin_amdgcn_s_sleep(SL1);
  }
}

__global__ void __launch_bounds__(256, 1)
fused(const u16* __restrict__ xin, const u16* __restrict__ wr, const u16* __restrict__ wi, const u16* __restrict__ wg,
      const u16* __restrict__ wu, const u16* __restrict__ wd, const u16* __restrict__ cin, const u16* __restrict__ wm,
      u16* __restrict__ out, char* wsp) {
  __shared__ __attribute__((aligned(16))) char smem[L_SZ];
  char LAS* L = (char LAS*)smem;
  const u32 lbase = (u32)(size_t)L;
  const int tid = threadIdx.x, lane = tid & 63, wave = __builtin_amdgcn_readfirstlane(tid >> 6);
  const int g = lane >> 4, c16 = lane & 15, l16 = lane * 16;
  const int grp = blockIdx.x & 7, j = blockIdx.x >> 3;
  u64 pc;
  asm volatile("s_getpc_b64 %0" : "=s"(pc));
  STAMP(0); CSTAMP(31);
  asm volatile("" :: "s"(xin), "s"(wr), "s"(wi), "s"(wg), "s"(wu), "s"(wd), "s"(cin), "s"(wm), "s"(out), "s"(wsp));
  const rsrc_t ws = mkrs(wsp, WS_END);
  const rsrc_t tab = mkrs(TAB, TAB_N * 4096);
  const rsrc_t code = mkrs((const void*)(pc & ~127ull), CODE_BYTES);
  const bool isR = j >= 14 && j < 30;
  const bool isG = j < 28;
  const int gk = j / 7, ct = j - gk * 7;
  const int tile = 4 * (grp >> 2) + gk, slice = 7 * (grp & 3) + ct;
  const int wdb = WS_WD + (ct * 56 + wave * 14) * 4096;
  const v4u sv = {SENT, SENT, SENT, SENT};
  int lb = L_B + l16;
  pin(lb);
  const char LAS* LB = L + lb - L_B;
  const int pfo = ((j * 4 + wave) * 14 + lane) * 128, pfc = ((j * 4 + wave) * 4 + lane) * 128;
  u32 pfa = ld1(tab, lane < 14 ? pfo : OOB), pfb = ld1(code, lane < 4 ? pfc : OOB);
  const int hro = WS_HRDY + (16 * tile + 4 * (lane < 32 ? wave : grp & 3)) * 32 + c16 * 8;
  v4u x2r[4];
  const int wl16 = (lane ^ ((wave & 1) << 1)) * 16;
  v4u wdn[14][4];
  u32 epoch, tag;

#define ACT_CLEAR(lo, hi) _Pragma("unroll") for (int m = lo; m < hi; ++m) \
      st4c(ws, isG ? WS_ACT + (16 * (tile + 2 * (m >> 3)) + 4 * (grp & 3)) * 57344 + ct * 32768 + (m & 7) * 4096 + tid * 16 : OOB, sv);
  if (isR) {
    const int blk = 16 * grp + (j - 14);
    epoch = ld1c(ws, WS_EPOCH);
    const int t0 = wave == 0 ? 0 : 2 * wave - 1;
    v4u cf[8][4];
#define CF_LOAD(q) { const int chn = TAB_P1 + ((wave == 0 && q >= 4) ? 28 : 4 * t0 + q), vo = (q < 5 || wave != 0) ? l16 : OOB; \
      _Pragma("unroll") for (int s = 0; s < 4; ++s) cf[q][s] = ld4(tab, vo + s * 1024, chn * 4096); sb(); }
#pragma unroll
    for (int q = 0; q < X_AFTER; ++q) CF_LOAD(q)
    v2u xv[7];
    {
      const rsrc_t xr = mkrs(xin, S * D * 2);
      const int off = tok_row(blk, 4 * wave + g) * (D * 2) + c16 * 8;
#pragma unroll
      for (int m = 0; m < 7; ++m) xv[m] = ld2(xr, off + m * 128);
    }
    typedef u32 v3u __attribute__((ext_vector_type(3)));
    v2u fa_[2], fb_[2]; v3u fc_[2], fd_[2];
    {
      const rsrc_t rr = mkrs(wr, S * DF * 2), ri = mkrs(wi, S * DF * 2);
      const int fo_ = 16 * wave + c16 < 57 ? 2 * g * 450 + (16 * wave + c16) * 8 : OOB;
#pragma unroll
      for (int hh = 0; hh < 2; ++hh) {
        const int so = blk * 7200 + hh * 3600;
        fa_[hh] = ld2(rr, fo_, so); fb_[hh] = ld2(ri, fo_, so);
        fc_[hh] = __builtin_amdgcn_raw_buffer_load_b96(rr, fo_ + 448, so, 0); fd_[hh] = __builtin_amdgcn_raw_buffer_load_b96(ri, fo_ + 448, so, 0);
      }
    }
    sb();
#pragma unroll
    for (int q = X_AFTER; q < 8; ++q) CF_LOAD(q)
    STAMP(23);
    {
      v2f f[14], s2 = {0.f, 0.f};
#pragma unroll
      for (int m = 0; m < 7; ++m) {
        f[2 * m] = bun(xv[m].x); f[2 * m + 1] = bun(xv[m].y);
        s2 += f[2 * m] + f[2 * m + 1];
        *(LAS v2u*)(L + L_XS + (4 * wave + g) * XP + c16 * 8 + m * 128) = xv[m];
      }
      STAMP(21);
      sb();
      {
        const int q = 16 * wave + c16, o = WS_WT + q * 16384 + blk * 128 + g * 16;
#pragma unroll
        for (int hh = 0; hh < 2; ++hh) {
          const v2u a = fa_[hh], b = fb_[hh]; const v3u c = fc_[hh], d = fd_[hh];
          st4c(ws, q < 57 ? o + hh * 64 : OOB, v4u{__builtin_amdgcn_perm(b.x, a.x, 0x05040100u), __builtin_amdgcn_perm(b.y, a.y, 0x05040100u),
                                                   __builtin_amdgcn_perm(d.x, c.x, 0x07060302u), __builtin_amdgcn_perm(d.y, c.y, 0x07060302u)});
          st4c(ws, q < 56 ? o + hh * 64 + 57 * 16384 : OOB,
               v4u{__builtin_amdgcn_perm(b.x, a.x, 0x07060302u), __builtin_amdgcn_perm(b.y, a.y, 0x07060302u),
                   __builtin_amdgcn_perm(d.y, c.y, 0x05040100u), __builtin_amdgcn_perm(d.z, c.z, 0x05040100u)});
        }
      }
      sb();
      const float mean = sum16(s2.x + s2.y) * (1.f / D);
      v2f q2 = {0.f, 0.f};
#pragma unroll
      for (int m = 0; m < 14; ++m) { const v2f d = f[m] - mean; q2 = fma2(d, d, q2); }
      const float rstd = __builtin_amdgcn_rsqf(sum16(q2.x + q2.y) * (1.f / D) + 1e-5f);
      if (c16 == 0) *(LAS v2f*)(L + L_ST + (4 * wave + g) * 8) = v2f{mean, rstd};
    }
    pin(pfa); pin(pfb);
    epoch = __builtin_amdgcn_readfirstlane(epoch);
    tag = mktag(epoch);
    STAMP(22);
    bar();
    v4f n[4][2];
    const int d0 = 8 * (4 * wave + g);
    const v2f st = *(const LAS v2f*)(L + L_ST + c16 * 8);
    {
      int xr_ = L_XS + c16 * XP + d0 * 2, xb_ = L_XS + c16 * XP + (216 - d0) * 2, x0_ = d0 == 0 ? xr_ : xb_ + 464;
      pin(xr_); pin(xb_); pin(x0_);
      const v4u a = lds4(L + xr_, 0), e = lds4(L + xr_, 448), c = lds4(L + xb_, 0), b = lds4(L + xb_, 448);
      const u32 c0 = *(const LAS u16*)(L + xb_ + 16), b0 = *(const LAS u16*)(L + x0_);
#pragma unroll
      for (int i = 0; i < 8; ++i) {
        n[0][i >> 2][i & 3] = (i & 1) ? bhi(a[i >> 1]) : blo(a[i >> 1]);
        n[3][i >> 2][i & 3] = (i & 1) ? bhi(e[i >> 1]) : blo(e[i >> 1]);
        n[1][i >> 2][i & 3] = i == 0 ? blo(b0) : (i & 1) ? bhi(b[(8 - i) >> 1]) : blo(b[(8 - i) >> 1]);
        n[2][i >> 2][i & 3] = i == 0 ? blo(c0) : (i & 1) ? bhi(c[(8 - i) >> 1]) : blo(c[(8 - i) >> 1]);
      }
      const float nm = -st.x * st.y;
#pragma unroll
      for (int m = 0; m < 4; ++m) {
#pragma unroll
        for (int h = 0; h < 2; ++h) { n[m][h] = fma4(n[m][h], splat(st.y), splat(nm)); pin(n[m][h]); }
      }
    }
    sb();
    STAMP(1);
    int mb = L_M1 + d0 * 4;
    pin(mb);
    v4u xs[4];
    int to[4];
    to[0] = WS_T1 + blk * T1_BLK + (16 * t0 + c16) * 64 + g * 16;
    to[1] = wave != 0 ? to[0] + 1024 : c16 < 2 ? to[0] + 112 * 64 : OOB;
    to[2] = to[0] + 114 * 64;
    to[3] = wave != 0 ? to[0] + 1024 + 114 * 64 : OOB;
#pragma unroll
    for (int q = 0; q < 4; ++q) pin(to[q]);

    {
      const int kind = tid >= 112, d = 4 * (tid - 112 * kind);
      int wb = L_M1 + (kind ? 0 : 2 * 1792) + d * 4, wrv = L_M1 + (kind ? 0 : 2 * 1792) + 1792 + (445 - d) * 4;
      int wr0 = d == 0 ? wrv + 12 - 1792 : wrv + 12;
      const int mo = tid < 224 ? WS_MOD + grp * MOD_REP + tid * 16 : OOB;
      pin(wb); pin(wrv); pin(wr0);
      const int ro = WS_RDY + grp * 1024 + min(tid >> 1, 112) * 8;
      v4u mv;
      _Pragma("nounroll") for (int s = 0; s < SPIN; ++s) {
        mv = ld4c(ws, fresh(mo));
        const v2u rv = ld2c(ws, fresh(ro));
        if (wall((tid >= 224 || (mv.y == tag && mv.w == tag)) && rv.x == tag && rv.y == ~tag)) break;
        __builtin_amdgcn_s_sleep(SL1);
      }
      STAMP(2);
      if (tid < 224) {
        const v4f v = {blo(mv.x), bhi(mv.x), blo(mv.z), bhi(mv.z)};
        *(LAS v4f*)(L + wb) = v;
        *(LAS float*)(L + wr0) = v.x;
        *(LAS float*)(L + wrv + 8) = v.y;
        *(LAS float*)(L + wrv + 4) = v.z;
        *(LAS float*)(L + wrv) = v.w;
      }
    }
    bar();
    {
      v4u fr[4];
#pragma unroll
      for (int hh = 0; hh < 2; ++hh) {
        v4f h[4];
#pragma unroll
        for (int m = 0; m < 4; ++m) {
          const int o = ((m == 1 || m == 2) ? 1792 : 0) + (m >= 2 ? 896 : 0) + hh * 16;
          h[m] = fma4(n[m][hh], lds4f(L + mb, o), lds4f(L + mb, 2 * 1792 + o));
        }
        const v4f p = h[0] + h[1], q = h[2] + h[3], u = h[0] - h[1], v = h[3] - h[2];
        const v4f t[4] = {p + q, p - q, u + v, u - v};
#pragma unroll
        for (int m = 0; m < 4; ++m) { fr[m][2 * hh] = pk2(lo2(t[m])); fr[m][2 * hh + 1] = pk2(hi2(t[m])); }
      }
#pragma unroll
      for (int m = 0; m < 4; ++m) *(LAS v4u*)(LB + wave * 1024 + L_AF + m * 4096) = fr[m];
    }
    bar();
    {
      v4u a[4][4];
#pragma unroll
      for (int p = 0; p < 2; ++p)
#pragma unroll
        for (int s = 0; s < 4; ++s) { a[p][s] = lds4(LB, L_AF + (p * 4 + s) * 1024); a[2 + p][s] = lds4(LB, L_AF + ((2 + p) * 4 + s) * 1024); }
      v4f re[4], im[4];
#pragma unroll
      for (int q = 0; q < 5; ++q) {
        if (q < 4) mf8(re[q], im[q], a[q >> 1], cf[4 * (q & 1) + (q >> 1)], a[2 + (q >> 1)], cf[4 * (q & 1) + 2 + (q >> 1)]);
        if (q > 0) {
          if (q < 4) mfence<0>(re[q - 1], im[q - 1]); else mfence<7>(re[3], im[3]);
          xs[q - 1] = v4u{pk(re[q - 1].x, im[q - 1].x), pk(re[q - 1].y, im[q - 1].y), pk(re[q - 1].z, im[q - 1].z), pk(re[q - 1].w, im[q - 1].w)};
          st4c(ws, to[q - 1], xs[q - 1]);
          sb();
        }
      }
    }
    STAMP(3);
    sb();
    sb();
    if (isG) dma_w1(wg, wu, slice, wave, lane, lbase);
#pragma unroll
    for (int q = 0; q < 4; ++q) {
      const int vo = (q < 2 || wave != 0) ? l16 : OOB;
#pragma unroll
      for (int s = 0; s < 4; ++s) cf[2 * q + 1][s] = ld4(tab, vo + s * 1024, (TAB_P3 + 2 * t0 + q) * 4096);
    }
#pragma unroll
    for (int m = 0; m < 4; ++m) {
      st4c(ws, tid + 256 * m < 896 ? WS_H2 + blk * 14336 + 4096 * m + tid * 16 : OOB, sv);
      st4c(ws, tid + 256 * m < 896 ? WS_X2 + blk * 14336 + 4096 * m + tid * 16 : OOB, sv);
    }
    sb();
    STAMP(20);
    v4f xo[2][4];
    int xa = L_XS + c16 * XP + (16 * t0 + 4 * g) * 2, xd = L_XS + c16 * XP + (192 - 16 * t0 - 4 * g) * 2;
    int xs1 = tid < 16 ? L_XS + c16 * XP + 224 : xd + 512, xs3 = tid < 16 ? L_XS + c16 * XP + 672 : xa + 448;
    pin(xa); pin(xd); pin(xs1); pin(xs3);
#define XOFF(t, m, r) ((t) == 0 && (r) == 0 && (m) == 1 ? xs1 : (t) == 0 && (r) == 0 && (m) == 3 ? xs3 : \
    (m) == 0 ? xa + 32 * (t) + 2 * (r) : (m) == 3 ? xa + 448 + 32 * (t) + 2 * (r) : (m) == 1 ? xd + 512 - 32 * (t) - 2 * (r) : xd + 64 - 32 * (t) - 2 * (r))
    {
      v2u w[2][4]; u32 e[2][2], e3;
#pragma unroll
      for (int t = 0; t < 2; ++t) {
        w[t][0] = *(const LAS v2u*)(L + xa + 32 * t); w[t][3] = *(const LAS v2u*)(L + xa + 448 + 32 * t);
        w[t][1] = *(const LAS v2u*)(L + xd + 504 - 32 * t); w[t][2] = *(const LAS v2u*)(L + xd + 56 - 32 * t);
        e[t][0] = *(const LAS u16*)(L + XOFF(t, 1, 0)); e[t][1] = *(const LAS u16*)(L + XOFF(t, 2, 0));
      }
      e3 = *(const LAS u16*)(L + xs3);
      sb();
#pragma unroll
      for (int t = 0; t < 2; ++t) {
        xo[t][0] = v4f{blo(w[t][0].x), bhi(w[t][0].x), blo(w[t][0].y), bhi(w[t][0].y)};
        xo[t][3] = v4f{t == 0 ? blo(e3) : blo(w[t][3].x), bhi(w[t][3].x), blo(w[t][3].y), bhi(w[t][3].y)};
        xo[t][1] = v4f{blo(e[t][0]), bhi(w[t][1].y), blo(w[t][1].y), bhi(w[t][1].x)};
        xo[t][2] = v4f{blo(e[t][1]), bhi(w[t][2].y), blo(w[t][2].y), bhi(w[t][2].x)};
#pragma unroll
        for (int m = 0; m < 4; ++m) { xo[t][m] -= st.x; pin(xo[t][m]); }
      }
    }
    int m0_[2], m1_[2], md_[2];
#pragma unroll
    for (int r = 0; r < 2; ++r) {
      const int q = r == 0 || tid < 80 ? tid + 256 * r : tid;
      const int kind = (q * 586) >> 16, d = 4 * (q - 112 * kind);
      const int reg = (d >= 112) + (d >= 224) + (d >= 336), cc = ((reg + 1) >> 1) * 224, m1 = (0x1320 >> (4 * reg)) & 3;
      const int d1 = abs(d + 1 - cc), d0_ = abs(d - cc), edge = d == 112 * reg && reg > 0;
      m1_[r] = L_M2 + kind * 1792 + (((d1 >> 4) << 6) | (m1 << 4) | (d1 & 15)) * 4;
      m0_[r] = L_M2 + kind * 1792 + (edge ? reg << 4 : ((d0_ >> 4) << 6) | (m1 << 4) | (d0_ & 15)) * 4;
      md_[r] = (reg & 1) ? -4 : 4;
      pin(m0_[r]); pin(m1_[r]); pin(md_[r]);
    }
    {
      v4u mv[2];
      _Pragma("nounroll") for (int s = 0; s < SPIN; ++s) {
        mv[0] = ld4c(ws, fresh(WS_MOD + grp * MOD_REP + (224 + tid) * 16));
        mv[1] = ld4c(ws, fresh(WS_MOD + grp * MOD_REP + (224 + (tid < 80 ? 256 + tid : tid)) * 16));
        const v2u rv = ld2c(ws, fresh(WS_DRDY + grp * 1024 + min(lane, 48) * 8));
        if (wall(mv[0].y == tag && mv[0].w == tag && mv[1].y == tag && mv[1].w == tag && rv.x == tag && rv.y == ~tag)) break;
        __builtin_amdgcn_s_sleep(SL4);
      }
      if (lane < (CLR_MODE == 1 ? 2 : 1)) st2c(ws, lane == 0 ? WS_HRDY + blk * 32 + wave * 8 : isG ? WS_CRDY + (tile * 4 + (grp & 3)) * 256 + (ct * 4 + wave) * 8 : OOB, v2u{tag, ~tag});
      STAMP(12);
#pragma unroll
      for (int f = 0; f < WD_EARLY; ++f) wdn[f >> 2][f & 3] = ld4c(ws, (isG ? wl16 : OOB) + (f & 3) * 1024, wdb + (f >> 2) * 4096);
      sb();
#pragma unroll
      for (int r = 0; r < 2; ++r) {
        const float sc = m0_[r] < L_M2 + 1792 ? 2.f / (float)(S * D) : 1.f;
        *(LAS float*)(L + m0_[r]) = blo(mv[r].x) * sc;
        *(LAS float*)(L + m1_[r]) = bhi(mv[r].x) * sc;
        *(LAS float*)(L + m1_[r] + md_[r]) = blo(mv[r].z) * sc;
        *(LAS float*)(L + m1_[r] + 2 * md_[r]) = bhi(mv[r].z) * sc;
      }
    }
    bar();
    *(LAS v4u*)(L + L_AF + (tid >> 6) * 4096 + 3072 + l16) = v4u{0u, 0u, 0u, 0u};
    STAMP(4);
    int zr = L_AF + lane * 8, ro = L_M2 + (t0 * 64 + 4 * g) * 4, ro1 = wave == 0 ? ro : ro + 256, ex = L_EX + c16 * 8;
    pin(zr); pin(ro); pin(ro1); pin(ex);
    {
      int zl[4];
#pragma unroll
      for (int m = 0; m < 4; ++m) {
        const int kq = (m >> 1) * 128 + (to[m] == OOB ? 127 : wave == 0 && m == 1 ? 112 + c16 : 16 * (t0 + (m & 1)) + c16);
        zl[m] = L_AF + ((kq & ~31) + ((kq & 4) << 2) + ((kq & 24) >> 1) + (kq & 3)) * 32 + g * 8;
        pin(zl[m]);
        if (to[m] == OOB) xs[m] = sv;
      }
      bar();
      if (isG) wait_flags(ws, hro, tag);
      {
        const int src = (blk * 5 + wave * 13) & 63;
        const int po = __builtin_amdgcn_readlane(to[(0)], 0) + (src & 15) * 64 + (src >> 4) * 16;
        const u32 old = __builtin_amdgcn_ds_bpermute(src * 4, xs[0].x);
        _Pragma("nounroll") for (int s = 0; s < SPIN; ++s) {
          if (wall(ld1c(ws, fresh(po)) != old)) break;
          __builtin_amdgcn_s_sleep(SL2);
        }
      }
      STAMP(13);
      v4u z[4];
      _Pragma("nounroll") for (int s = 0; s < SPIN; ++s) {
        u32 mn = 1;
#pragma unroll
        for (int m = 0; m < 4; ++m) z[m] = ld4c(ws, fresh(to[m]));
#pragma unroll
        for (int m = 0; m < 4; ++m) { const v4u d = z[m] ^ xs[m]; mn = min(min(mn, d.x), min(min(d.y, d.z), d.w)); }
        if (wall(mn != 0)) break;
      }
      STAMP(14);
#pragma unroll
      for (int m = 0; m < 4; ++m) {
        char LAS* p = L + zl[m];
        *(LAS v2u*)(p) = v2u{__builtin_amdgcn_perm(z[m].y, z[m].x, 0x05040100u), __builtin_amdgcn_perm(z[m].w, z[m].z, 0x05040100u)};
        *(LAS v2u*)(p + ZPL) = v2u{__builtin_amdgcn_perm(z[m].y, z[m].x, 0x07060302u), __builtin_amdgcn_perm(z[m].w, z[m].z, 0x07060302u)};
      }
    }
    bar();
    STAMP(5);
    {
      v4u zf[4][4];
#pragma unroll
      for (int ty = 0; ty < 4; ++ty)
#pragma unroll
        for (int s = 0; s < 4; ++s) zf[ty][s] = ldtr2(L + zr, ty * 4096 + s * 1024, ty * 4096 + s * 1024 + 512);
      v4f acc[2][4], x2[2][4], gav[2][4];
      v2f s1 = {0.f, 0.f}, s2 = {0.f, 0.f};
#define P3M(t, ty) mf8(acc[t][ty], acc[t][ty + 1], cf[4 * t + ty], zf[ty], cf[4 * t + ty + 1], zf[ty + 1]);
#define P3X(t, m, yv) { x2[t][m] = fma4(gav[t][m], yv, xo[t][m]); \
      if (t == 0 || wave != 0) { s1 += lo2(x2[t][m]) + hi2(x2[t][m]); \
        s2 = fma2(lo2(x2[t][m]), lo2(x2[t][m]), s2); s2 = fma2(hi2(x2[t][m]), hi2(x2[t][m]), s2); } }
      P3M(1, 0) P3M(1, 2)
#pragma unroll
      for (int m = 0; m < 4; ++m) { gav[0][m] = lds4f(L + ro, m * 64); gav[1][m] = lds4f(L + ro1, m * 64); }
      sb();
      P3M(0, 0) P3M(0, 2)
      mfence<0>(acc[1][0], acc[1][1], acc[1][2], acc[1][3]);
      const v4f A1 = acc[1][0] + acc[1][1], B1 = acc[1][0] - acc[1][1], P1 = acc[1][3] - acc[1][2], Q1 = acc[1][2] + acc[1][3];
      P3X(1, 0, A1 - P1) P3X(1, 1, A1 + P1) P3X(1, 2, B1 - Q1) P3X(1, 3, B1 + Q1)
      sb();
      mfence<0>(acc[0][0], acc[0][1], acc[0][2], acc[0][3]);
      {
        const v4f A = acc[0][0] + acc[0][1], B = acc[0][0] - acc[0][1], P = acc[0][3] - acc[0][2], Q = acc[0][2] + acc[0][3];
        v4f y[4] = {A - P, A + P, B - Q, B + Q};
        const bool sp = tid < 16;
        y[0].x = sp ? A.x : y[0].x; y[2].x = sp ? B.x : y[2].x;
        y[1].x = sp ? acc[1][0].x - acc[0][3].x : y[1].x; y[3].x = sp ? acc[1][0].x + acc[0][3].x : y[3].x;
        P3X(0, 0, y[0]) P3X(0, 1, y[1]) P3X(0, 2, y[2]) P3X(0, 3, y[3])
      }
      float sm = s1.x + s1.y, sq = s2.x + s2.y;
      sumg2(sm, sq);
      if (g == 0) *(LAS v2f*)(L + ex + wave * 128) = v2f{sm, sq};
      v4f sbv[2][4], cbv[2][4];
#pragma unroll
      for (int m = 0; m < 4; ++m) {
        sbv[0][m] = lds4f(L + ro, 1792 + m * 64); cbv[0][m] = lds4f(L + ro, 2 * 1792 + m * 64);
        sbv[1][m] = lds4f(L + ro1, 1792 + m * 64); cbv[1][m] = lds4f(L + ro1, 2 * 1792 + m * 64);
      }
      bar();
      v2f e = *(const LAS v2f*)(L + ex);
#pragma unroll
      for (int w = 1; w < 4; ++w) e += *(const LAS v2f*)(L + ex + w * 128);
      const float mc = e.x * (1.f / D), rstd = __builtin_amdgcn_rsqf(fmaxf(e.y * (1.f / D) - mc * mc, 0.f) + 1e-5f), nb = -mc * rstd;
      const int ho = WS_H2 + blk * 14336 + 2 * t0 * 1024 + l16, ho1 = wave == 0 ? OOB : ho + 2048;
#pragma unroll
      for (int t = 0; t < 2; ++t)
#pragma unroll
        for (int a = 0; a < 2; ++a) {
          v4u o;
#pragma unroll
          for (int hh = 0; hh < 2; ++hh) {
            const int m = 2 * a + hh;
            const v4f hv = fma4(fma4(x2[t][m], splat(rstd), splat(nb)), cbv[t][m], sbv[t][m]);
            o[2 * hh] = pk2(lo2(hv)); o[2 * hh + 1] = pk2(hi2(hv));
          }
          st4c(ws, (t ? ho1 : ho) + a * 1024, o);
        }
      STAMP(6);
      sb();
#pragma unroll
      for (int f = WD_EARLY; f < WD_MID; ++f) wdn[f >> 2][f & 3] = ld4c(ws, (isG ? wl16 : OOB) + (f & 3) * 1024, wdb + (f >> 2) * 4096);
      sb();
#pragma unroll
      for (int t = 0; t < 2; ++t)
        if (t == 0 || wave != 0)
#pragma unroll
          for (int m = 0; m < 4; ++m)
#pragma unroll
            for (int r = 0; r < 4; r += 2) {
              const u32 p = pk(x2[t][m][r] + st.x, x2[t][m][r + 1] + st.x);
              *(LAS u16*)(L + XOFF(t, m, r)) = (u16)p;
              *(LAS u16*)(L + XOFF(t, m, r + 1)) = (u16)(p >> 16);
            }
      sb();
    }
    bar();
    x2_read(L, x2r, tid);
    bar();
    if (!isG) {
      x2_store(ws, x2r, blk, tid);
      DUMP(); return;
    }
  } else {
    const int sj = j >= 30 ? j - 30 : j + 2;
    const int idx = sj * 8 + grp;
    const bool isC = j < 14 || idx == 0;
    const int pair = j < 14 ? grp * 14 + j : 112;
    const int lc = grp + 8 * (sj >> 1);
    const int qu = sj < 4 && lc < 14 ? 2 * lc + (sj & 1) : -1;
    const int t1o = WS_T1 + (32 * wave + (lane >> 3)) * T1_BLK + pair * 128 + (lane & 7) * 16;
    ModLoad ml;
    epoch = ld1c(ws, WS_EPOCH);
    if (qu >= 0) mod_load(ml, cin, wm, qu, tid);
#pragma unroll
    for (int q = 0; q < 4; ++q) st4c(ws, isC ? t1o + 8 * q * T1_BLK : OOB, sv);
    u32 pf0, pf1, pf2;
    {
      const rsrc_t mr = mkrs(wm, D * 6 * D * 2), xr = mkrs(xin, S * D * 2), fr_ = mkrs((wave & 1) ? wi : wr, S * DF * 2);
      const int row = (sj * 4 + wave) * 7 + (lane >> 1), l7 = lane / 7;
      pf0 = __builtin_amdgcn_raw_buffer_load_b32(mr, lane < 14 && (grp < 6 || !(lane & 1)) ? row * (6 * D * 2) + (grp + 8 * (lane & 1)) * 128 : OOB, 0, PF_AUX);
      pf1 = __builtin_amdgcn_raw_buffer_load_b32(xr, lane < (!XBAL ? 28 : sj < 4 ? 0 : sj < 12 ? 28 : 56) ? tok_row(16 * grp + (lane < 28 ? sj : sj - 12), 4 * wave + (l7 & 3)) * (D * 2) + (lane - 7 * l7) * 128 : OOB, 0, PF_AUX);
    }
    sb();
    epoch = __builtin_amdgcn_readfirstlane(epoch);
    tag = mktag(epoch);
    if (qu >= 0) mod_part(ml, L, tid);
    asm volatile("s_waitcnt vmcnt(%0)" :: "i"(W_LATE ? 2 : 3) : "memory");
    { const rsrc_t fr_ = mkrs((wave & 1) ? wi : wr, S * DF * 2);
      pf2 = __builtin_amdgcn_raw_buffer_load_b32(fr_, (lane & 31) < 29 && lane < (!XBAL ? 32 : sj < 4 ? 0 : sj < 12 ? 32 : 64) ? (((16 * grp + (lane < 32 ? sj : sj - 12)) * 7200) & ~127) + ((wave >> 1) * 29 + (lane & 31)) * 128 : OOB, 0, PF_AUX);
    }
    bar();
    if (wave == 0 && qu >= 0) mod_publish(L, ws, qu, lane, tag);
    if (wave == 1 && lane < 8) st2c(ws, isC ? WS_RDY + lane * 1024 + pair * 8 : OOB, v2u{tag, ~tag});
    STAMP(1);
    pin(pf0); pin(pf1); pin(pf2); pin(pfa); pin(pfb);
    const int qn = sj >= 4 && sj < 10 && grp < 7 ? 28 + grp * 6 + (sj - 4) : !isC && idx < 15 ? 69 + idx : -1;
    const int tw = sj >= 10 ? (sj - 10) * 8 + grp : sj == 4 && grp == 7 ? 48 : -1;
    v2u dw[2][8];
    if (qn >= 0) mod_load(ml, cin, wm, qn, tid);
    if (tw >= 0) {
      const rsrc_t dr = mkrs(wd, F * D * 2);
      const int u0 = tw * 4 + wave, uc = u0 / 28, kap = 2 * (u0 - uc * 28);
#pragma unroll
      for (int e = 0; e < 16; ++e)
        dw[e >> 3][e & 7] = __builtin_amdgcn_raw_buffer_load_b64(dr, (4 * g * D + 4 * c16) * 2 + (e & 3) * D * 2, ((32 * kap + 16 * (e >> 2)) * D + 64 * uc) * 2, PF_AUX);
    }
    sb();
    u32 pw[4];
    {
      const rsrc_t rg = mkrs(wg, D * F * 2), ru = mkrs(wu, D * F * 2);
      const int r7 = lane / 7, r0 = W1EVEN ? (j * 4 + wave) * 8 : sj < 4 ? (sj * 4 + wave) * 16 : 256 + ((sj - 4) * 4 + wave) * 4;
      const int po = (r0 + r7) * (F * 2) + ((grp & 3) * 7 + lane - r7 * 7) * 128;
      const int o0 = lane < (W1EVEN ? (j < 14 ? 56 : 0) : sj < 4 ? 56 : 28) ? po : OOB, o1 = !W1EVEN && lane < 56 && sj < 4 ? po + 8 * (F * 2) : OOB;
      pw[0] = ld1(rg, o0); pw[1] = ld1(ru, o0); pw[2] = ld1(rg, o1); pw[3] = ld1(ru, o1);
    }
    sb();
    STAMP(20);
    v4f t1c[2], t1s[2], t2c[2], t2s[2];
#pragma unroll
    for (int t = 0; t < 2; ++t) {
#pragma unroll
      for (int r = 0; r < 4; ++r) {
        const float a1 = (float)((16 * t + 4 * g + r) * (16 * wave + c16)) * (1.f / S);
        const float a2 = (float)((16 * t + c16) * (16 * wave + 4 * g + r)) * (1.f / S);
        t1c[t][r] = __builtin_amdgcn_cosf(a1); t1s[t][r] = __builtin_amdgcn_sinf(a1);
        t2c[t][r] = __builtin_amdgcn_cosf(a2); t2s[t][r] = __builtin_amdgcn_sinf(a2);
      }
      pin(t1c[t]); pin(t1s[t]); pin(t2c[t]); pin(t2s[t]);
    }
    if (tw >= 0) {
#pragma unroll
      for (int tt = 0; tt < 8; ++tt) {
        v4u o;
#pragma unroll
        for (int m = 0; m < 4; ++m)
          o[m] = __builtin_amdgcn_perm(dw[tt >> 2][2 * m + 1][(tt & 3) >> 1], dw[tt >> 2][2 * m][(tt & 3) >> 1], (tt & 1) ? 0x07060302u : 0x05040100u);
        st4c(ws, WS_WD + (tw * 4 + wave) * 8192 + tt * 1024 + l16, o);
      }
    }
    if (qn >= 0) {
      bar();
      mod_part(ml, L, tid);
      bar();
      if (wave == 0) mod_publish(L, ws, qn, lane, tag);
    }
    pin(pw[0]); pin(pw[1]); pin(pw[2]); pin(pw[3]);
    STAMP(21);
    if (!isC) { DUMP(); return; }
    v4u c64[2][4], c32[2][2][2];
#pragma unroll
    for (int cp = 0; cp < 2; ++cp)
#pragma unroll
      for (int s = 0; s < 4; ++s) c64[cp][s] = ld4(tab, l16 + s * 1024, (TAB_P2 + wave * 2 + cp) * 4096);
#pragma unroll
    for (int t = 0; t < 2; ++t)
#pragma unroll
      for (int f = 0; f < 4; ++f) c32[t][f >> 1][f & 1] = ld4(tab, l16 + f * 1024, (TAB_P2 + 8 + t) * 4096);
    sb();
    if (tw >= 0) {
      asm volatile("s_waitcnt vmcnt(16)" ::: "memory");
      bar();
      if (tid < 8) st2c(ws, WS_DRDY + tid * 1024 + tw * 8, v2u{tag, ~tag});
    }
    STAMP(2);
    int wfo = WS_WT + pair * 16384 + (16 * g + wave) * 128 + c16 * 8;
    pin(wfo);
    v2u wf[2][4];
    {
      int wl = L_CI + ((lane >> 2) & 1) * 8256 + wave * 2048 + ((lane >> 3) * 4 + (lane & 3)) * 16, lo = t1o;
      pin(wl); pin(lo);
      light_poll(ws, WS_T1 + (32 * wave + (pair & 31)) * T1_BLK + pair * 128);
      STAMP(13);
      v4u z[4];
      _Pragma("nounroll") for (int s = 0; s < SPIN; ++s) {
        lo = fresh(lo);
#pragma unroll
        for (int q = 0; q < 4; ++q) z[q] = ld4c(ws, lo, 8 * q * T1_BLK);
        if (wall(max(max(max4(z[0]), max4(z[1])), max(max4(z[2]), max4(z[3]))) != SENT)) break;
      }
      STAMP(14);
#pragma unroll
      for (int q = 0; q < 4; ++q) *(LAS v4u*)(L + wl + q * 512) = z[q];
    }
    bar();
#pragma unroll
    for (int t = 0; t < 2; ++t)
#pragma unroll
      for (int r = 0; r < 4; ++r) wf[t][r] = ld2c(ws, wfo + r * 512, t * 8192);
    v2u fd = ld2c(ws, WS_DRDY + grp * 1024 + min(lane, 48) * 8);
    sb();
    STAMP(4);
    {
      v4u a[2][2][4];
#pragma unroll
      for (int c = 0; c < 2; ++c)
#pragma unroll
        for (int mt = 0; mt < 2; ++mt)
#pragma unroll
          for (int s = 0; s < 4; ++s) a[c][mt][s] = lds4(LB, L_CI + c * 8256 + (s * 2 + mt) * 1024);
      v4u old[2][2];
#pragma unroll
      for (int c = 0; c < 2; ++c)
#pragma unroll
        for (int mt = 0; mt < 2; ++mt) old[c][mt] = lds4(LB + wave * 2048, L_CI + c * 8256 + mt * 1024);
      v4f re[2][2], im[2][2], ur[2][2], ui[2][2], fr_[2][2], fi_[2][2];
      u32 f2[2][2][4], f3[2][2][4], f4[4];
      char LAS* xw = L + lb + wave * 1024 - L_B;
#define V4(x) v4u{x[0], x[1], x[2], x[3]}
#define S1K(h) c64[0][2 * h], c64[0][2 * h + 1], c64[1][2 * h], c64[1][2 * h + 1]
#define S1X(c, t, h) re[c][t], im[c][t], a[c][t][2 * h], a[c][t][2 * h + 1], S1K(h)
#define S2X(c, t) ur[c][t], ui[c][t], V4(f2[c][0]), V4(f2[c][1]), c32[t][0][0], c32[t][0][1], c32[t][1][0], c32[t][1][1]
#define S3X(c, t) im[c][t], re[c][t], V4(f3[c][0]), V4(f3[c][1]), c32[t][0][0], c32[t][0][1], c32[t][1][0], c32[t][1][1]
#define CM(o, P, Q, C, S_, h) o[2 * h], o[2 * h + 1], P[2 * h], Q[2 * h], C[2 * h], S_[2 * h], P[2 * h + 1], Q[2 * h + 1], C[2 * h + 1], S_[2 * h + 1]
#define TW1(c, t, h) CM(f2[c][t], re[c][t], im[c][t], t1c[t], t1s[t], h)
#define FLT(c, t, h) CM(f3[c][t], ur[c][t], ui[c][t], fr_[c][t], fi_[c][t], h)
#define TW2(c, t, h) CM(f4, re[c][t], im[c][t], t2c[t], t2s[t], h)
#define HV(x, h) (h ? hi2(x) : lo2(x))
#define CP(PL, o, P, Q, C, S_, h) cmul2<PL>(o[2 * h], o[2 * h + 1], HV(P, h), HV(Q, h), HV(C, h), HV(S_, h));
#define XW(c, t) *(LAS v4u*)(xw + L_CX + (c * 2 + t) * 4096) = V4(f4);
#define UNP(c, t) { _Pragma("unroll") for (int r = 0; r < 4; ++r) { fr_[c][t][r] = blo(wf[t][r][c]); fi_[c][t][r] = bhi(wf[t][r][c]); } \
      pin(fr_[c][t]); pin(fi_[c][t]); }
      nop2();
      m4_a0(S1X(0, 0, 0)); m4_a1(S1X(0, 0, 1)); m4_a0(S1X(0, 1, 0)); mfence<3>(re[0][0], im[0][0]);
      h4_a1m(S1X(0, 1, 1), TW1(0, 0, 0)); h4_a0m(S1X(1, 0, 0), TW1(0, 0, 1));
      h4_a1m(S1X(1, 0, 1), TW1(0, 1, 0)); h4_a0m(S1X(1, 1, 0), TW1(0, 1, 1));
      h4_a1m(S1X(1, 1, 1), TW1(1, 0, 0)); h4_b0m(S2X(0, 0), TW1(1, 0, 1));
      h4_b0m(S2X(0, 1), TW1(1, 1, 0));
      sb();
      CP(false, f2[1][1], re[1][1], im[1][1], t1c[1], t1s[1], 1)
      UNP(0, 0) UNP(0, 1)
      sb();
      h4_b0p(S2X(1, 0), FLT(0, 0, 0)); h4_b0p(S2X(1, 1), FLT(0, 0, 1));
      sb();
      CP(true, f3[0][1], ur[0][1], ui[0][1], fr_[0][1], fi_[0][1], 0) CP(true, f3[0][1], ur[0][1], ui[0][1], fr_[0][1], fi_[0][1], 1)
      UNP(1, 0) UNP(1, 1)
      sb();
      h4_a0p(S3X(0, 0), FLT(1, 0, 0)); h4_a0p(S3X(0, 1), FLT(1, 0, 1));
      sb();
      CP(true, f3[1][1], ur[1][1], ui[1][1], fr_[1][1], fi_[1][1], 0) CP(true, f3[1][1], ur[1][1], ui[1][1], fr_[1][1], fi_[1][1], 1)
#pragma unroll
      for (int e = 0; e < 4; ++e) pin(f3[1][1][e]);
      nop2();
      h4_a0p(S3X(1, 0), TW2(0, 0, 0)); h4_a0p(S3X(1, 1), TW2(0, 0, 1));
      XW(0, 0)
      sb();
      CP(true, f4, re[0][1], im[0][1], t2c[1], t2s[1], 0) CP(true, f4, re[0][1], im[0][1], t2c[1], t2s[1], 1) XW(0, 1)
      sb();
      mfence<0>(re[1][0], im[1][0], re[1][1], im[1][1]);
      CP(true, f4, re[1][0], im[1][0], t2c[0], t2s[0], 0) CP(true, f4, re[1][0], im[1][0], t2c[0], t2s[0], 1) XW(1, 0)
      CP(true, f4, re[1][1], im[1][1], t2c[1], t2s[1], 0) CP(true, f4, re[1][1], im[1][1], t2c[1], t2s[1], 1) XW(1, 1)
      bar();
#pragma unroll
      for (int c = 0; c < 2; ++c)
#pragma unroll
        for (int nt = 0; nt < 2; ++nt)
#pragma unroll
          for (int s = 0; s < 4; ++s) a[c][nt][s] = lds4(LB, L_CX + ((c * 2 + nt) * 4 + s) * 1024);
      const int o2 = WS_T1 + (32 * wave + (lane >> 2)) * T1_BLK + pair * 128 + (lane & 3) * 16;
#define S4(c, t) mf8(im[c][t], re[c][t], c64[0], a[c][t], c64[1], a[c][t]);
#define NEQ(x, y) ((x) == (y) ? (x) ^ 1u : (x))
#define OUT(c, t) { const v4u o = {pk(re[c][t].x, im[c][t].x), pk(re[c][t].y, im[c][t].y), pk(re[c][t].z, im[c][t].z), pk(re[c][t].w, im[c][t].w)}; \
      st4c(ws, o2 + c * 64 + t * 16 * T1_BLK, v4u{NEQ(o.x, old[c][t].x), NEQ(o.y, old[c][t].y), NEQ(o.z, old[c][t].z), NEQ(o.w, old[c][t].w)}); sb(); }
      S4(0, 0) S4(0, 1) mfence<0>(re[0][0], im[0][0]); OUT(0, 0)
      S4(1, 0) mfence<0>(re[0][1], im[0][1]); OUT(0, 1)
      S4(1, 1) mfence<0>(re[1][0], im[1][0]); OUT(1, 0)
      mfence<0>(re[1][1], im[1][1]); OUT(1, 1)
    }
    STAMP(5);
    sb();
    if (!isG) { DUMP(); return; }
    if (!wall(fd.x == tag && fd.y == ~tag)) wait_flags(ws, WS_DRDY + grp * 1024 + min(lane, 48) * 8, tag);
    dma_w1(wg, wu, slice, wave, lane, lbase);
#pragma unroll
    for (int f = 0; f < WD_MID; ++f) wdn[f >> 2][f & 3] = ld4c(ws, wl16 + (f & 3) * 1024, wdb + (f >> 2) * 4096);
    fd = ld2c(ws, hro);
    sb();
    ACT_CLEAR(0, 16)
    sb();
    if (!wall(fd.x == tag && fd.y == ~tag)) wait_flags(ws, hro, tag);
    bar();
  }

  STAMP(7);
  const int blk0 = 16 * tile + 4 * (grp & 3);
  const int ab = WS_ACT + blk0 * 57344 + wave * 14 * 1024;
  v4u h[14][2];
  {
    const int hb = WS_H2 + (16 * tile + 4 * wave) * 14336;
    int tb[2][4];
#pragma unroll
    for (int u = 0; u < 8; ++u) {
      tb[u >> 2][u & 3] = (((lane >> 2) * 128 + ((lane >> 3) & 3) * 32 + (lane & 3) * 8) ^ ((u & 3) << 5)) + (u >> 2) * 57344;
      pin(tb[u >> 2][u & 3]);
    }
#define W_RD(dst, k, u) dst = ldtr2(L + tb[k >= 7][u & 3], (k % 7) * 8192 + (u >> 2) * 4096, (k % 7) * 8192 + (u >> 2) * 4096 + 2048)
#define H_LD(v, tt) hh_[v][tt] = ld4c(ws, l16 + (((v) % 14) & 3) * 1024, hb + (2 * ((v) / 14) + (tt)) * 14336 + (((v) % 14) >> 2) * 4096)
    const int abx[2] = {ab + (wave >> 1) * 57344, ab + (1 - (wave >> 1)) * 57344};
#define G_LD(e) h[(e) >> 1][(e) & 1] = ld4c(ws, l16 + (((e) >> 1) & 3) * 1024, abx[(e) & 1] + ((e) >> 3) * 4096)
    v4u wfr[2][8], hh_[28][2];
#pragma unroll
    for (int u = 0; u < 8; ++u) W_RD(wfr[0][u], 0, u);
    light_poll(ws, hb + (lane & 1) * 14336 + 13 * 1024);
    STAMP(15);
#pragma unroll
    for (int v = 0; v < LEAD; ++v) { H_LD(v, 0); H_LD(v, 1); }
    sb();
    if (!isR && lane < (CLR_MODE == 1 ? 1 : 2))
      st2c(ws, WS_CRDY + ((tile + 2 * lane) * 4 + (grp & 3)) * 256 + (ct * 4 + wave) * 8, v2u{tag, ~tag});
    sb();
#define BAD2(x0, x1) wany(max(max(max(max(x0.x, x0.y), x0.z), max(max(x0.w, x1.x), x1.y)), max(x1.z, x1.w)) == SENT)
#define RETRY2(x0, x1, o0, o1) \
      _Pragma("nounroll") for (int s_ = 0; s_ < SPIN; ++s_) { \
        const int lf_ = fresh(l16); SLOWCNT \
        x0 = ld4c(ws, lf_, o0); x1 = ld4c(ws, lf_, o1); \
        if (wall(max(max4(x0), max4(x1)) != SENT)) break; \
      }
    if (__builtin_expect(BAD2(hh_[0][0], hh_[0][1]), 0)) { RETRY2(hh_[0][0], hh_[0][1], hb, hb + 14336) }
    STAMP(8); CSTAMP(28);
    v4f acc[8][2];
    v2u fa;
    const int fx = WS_CRDY + (tile * 4 + (lane < 32 ? wave : grp & 3)) * 256 + min(lane & 31, 27) * 8;
    const int ao = WS_ACT + (16 * tile + 4 * wave) * 57344 + 2 * slice * 1024 + l16;
#define ACT_OUT(ps, tt, f) { v4u o; \
      _Pragma("unroll") for (int hh = 0; hh < 2; ++hh) { \
        const v4f av = relu4(acc[2 * f + hh][tt]) * acc[4 + 2 * f + hh][tt]; \
        o[2 * hh] = pk2(lo2(av)); o[2 * hh + 1] = pk2(hi2(av)); } \
      st4c(ws, ao + (2 * ps + tt) * 57344 + f * 1024, o); }
#define ACT_ALL(ps) { mfence<0>(acc[0][0], acc[1][0], acc[2][0], acc[3][0]); mfence<0>(acc[4][0], acc[5][0], acc[6][0], acc[7][0]); \
      ACT_OUT(ps, 0, 0) ACT_OUT(ps, 0, 1) sb(); \
      mfence<0>(acc[0][1], acc[1][1], acc[2][1], acc[3][1]); mfence<0>(acc[4][1], acc[5][1], acc[6][1], acc[7][1]); \
      ACT_OUT(ps, 1, 0) ACT_OUT(ps, 1, 1) sb(); }
#define W_RH(dst, k, u, hf) { const v2u t_ = ldtr(L + tb[k >= 7][u & 3], (k % 7) * 8192 + (u >> 2) * 4096 + hf * 2048); \
      dst[2 * hf] = t_.x; dst[2 * hf + 1] = t_.y; }
#pragma unroll
    for (int ps = 0; ps < 2; ++ps)
#pragma unroll
    for (int ks = 0; ks < 14; ++ks) {
      const int v = ps * 14 + ks, cur = v & 1, nk = (v + 1) % 14, vn = v < 27 ? v + 1 : v;
      const bool more = v < 27;
      u32 m0, m1;
      __builtin_amdgcn_s_waitcnt(0xC07F);
      sb();
#pragma unroll
      for (int i = 0; i < 16; ++i) {
        const int u = i & 7, tt = i >> 3;
        if (ks == 0) mv0(acc[u][tt], wfr[cur][u], hh_[v][tt]); else mv(acc[u][tt], wfr[cur][u], hh_[v][tt]);
        if (more) {
          if (i < 4) { W_RH(wfr[cur ^ 1][i], nk, i, 0) W_RH(wfr[cur ^ 1][i], nk, i, 1) }
          else if (i < 12) W_RH(wfr[cur ^ 1][4 + ((i - 4) >> 1)], nk, (4 + ((i - 4) >> 1)), (i & 1))
        }
        if (i == 5 && v + LEAD < 28) H_LD(v + LEAD, 0);
        if (i == 7 && v + LEAD < 28) H_LD(v + LEAD, 1);
        if (i == 9 || i == 11 || i == 13) {
          const int e = (i - 9) >> 1, f = WD_MID + (v < 14 ? v - 2 : 12 + WD_PER * (v - 14) + e);
          if (f < 56 && ((v >= 2 && v < 14 && e == 0) || (v >= 14 && e < WD_PER))) wdn[f >> 2][f & 3] = ld4c(ws, wl16 + (f & 3) * 1024, wdb + (f >> 2) * 4096);
        }
        if (v > A_V && (i & 1) && i >= 5 && ((i - 5) >> 1) < A_PER) G_LD(A_PER * (v - A_V - 1) + ((i - 5) >> 1));
        if (v == 8 && i == 13) fa = ld2c(ws, fx);
        if (more && i == 12) { m0 = max(max(hh_[vn][0].x, hh_[vn][0].y), hh_[vn][0].z); pin(m0); }
        if (more && i == 13) { m1 = max(max(hh_[vn][0].w, hh_[vn][1].x), hh_[vn][1].y); pin(m1); }
        if (more && i == 14) { m0 = max(max(m0, hh_[vn][1].z), hh_[vn][1].w); m0 = max(m0, m1); pin(m0); }
        sb();
      }
      if (v == 12 && !wall(fa.x == tag && fa.y == ~tag)) wait_flags(ws, fx, tag);
      if (more && __builtin_expect(wany(m0 == SENT), 0)) {
        const int pb = 2 * ((v + 1) / 14) * 14336 + nk * 1024;
        RETRY2(hh_[vn][0], hh_[vn][1], hb + pb, hb + pb + 14336)
      }
      if (v == 13) {
        STAMP(16); CSTAMP(29);
        ACT_ALL(0)
        if (isR) x2_store(ws, x2r, 16 * grp + j - 14, tid);
        nop2();
      }
      sb();
    }
    STAMP(27);
    ACT_ALL(1)
    CSTAMP(30);
#pragma unroll
    for (int e = A_PER * (27 - A_V); e < 28; ++e) G_LD(e);
    sb();
    STAMP(9);
    v4u a[14][2];
    v4f ac2[4][4];
#define A_LD(e) a[(e) >> 1][(e) & 1] = ld4c(ws, l16 + (((e) >> 1) & 3) * 1024, abx[(e) & 1] + 2 * 57344 + ((e) >> 3) * 4096)
    const int po = ab + 3 * 57344 + min(lane, 6) * 2048 + 1024;
    u32 probe;
#pragma unroll
    for (int ks = 0; ks < 14; ++ks) {
      if (__builtin_expect(BAD2(h[ks][0], h[ks][1]), 0)) { RETRY2(h[ks][0], h[ks][1], abx[0] + ks * 1024, abx[1] + ks * 1024) }
#pragma unroll
      for (int tt = 0; tt < 2; ++tt)
#pragma unroll
        for (int n = 0; n < 4; ++n) ac2[tt][n] = mfma(wdn[ks][n], h[ks][tt], ks ? ac2[tt][n] : splat(0.f));
      if (ks == P_AT) probe = ld1c(ws, po);
      sb();
      if (ks == B_AT2) {
        STAMP(25);
        if (wany(probe == SENT)) light_poll(ws, po);
        STAMP(26);
      }
      if (ks >= B_AT2) {
#pragma unroll
        for (int e = 4 * (ks - B_AT2); e < 4 * (ks - B_AT2 + 1); ++e) A_LD(e);
      }
      sb();
    }
    STAMP(17);
#pragma unroll
    for (int e = 4 * (14 - B_AT2); e < 4 * (14 - B_AT2) + 6; ++e) A_LD(e);
    sb();
    int rw[3], rr_ = L_B + lane * 8 + wave * 6144, oo[2];
#pragma unroll
    for (int x = 1; x < 4; ++x) { rw[x - 1] = L_B + lane * 8 + (wave ^ x) * 6144 + (x - 1) * 2048; pin(rw[x - 1]); }
    pin(rr_);
    const int blk = blk0 + (wave >> 1);
#pragma unroll
    for (int ph = 0; ph < 2; ++ph) { oo[ph] = (tok_row(blk + 2 * ph, c16) * D + ct * 64 + g * 16 + (wave & 1) * 8) * 2; pin(oo[ph]); }
#define RED_W1(ph, x, n) *(LAS v2f*)(L + rw[(x) - 1] + (ph) * 24576 + (n) * 512) = ((x) & 1) ? hi2(ac2[2 * (ph) + ((x) >> 1)][n]) : lo2(ac2[2 * (ph) + ((x) >> 1)][n]);
    const int xo = WS_X2 + blk * 14336 + c16 * 896 + ct * 128 + g * 32 + (wave & 1) * 16;
    const int go = WS_MOD + grp * MOD_REP + (70 + 2 * ct) * 128 + g * 64 + (wave & 1) * 32;
    v4u xv[2], gv[2];
    STAMP(18);
    sb();
#pragma unroll
    for (int ks = 0; ks < 14; ++ks) {
      if (__builtin_expect(BAD2(a[ks][0], a[ks][1]), 0)) { RETRY2(a[ks][0], a[ks][1], abx[0] + 2 * 57344 + ks * 1024, abx[1] + 2 * 57344 + ks * 1024) }
#pragma unroll
      for (int tt = 0; tt < 2; ++tt)
#pragma unroll
        for (int n = 0; n < 4; ++n) ac2[2 + tt][n] = mfma(wdn[ks][n], a[ks][tt], ks ? ac2[2 + tt][n] : splat(0.f));
#pragma unroll
      for (int e = 4 * (14 - B_AT2) + 6 + 3 * ks; e < 4 * (14 - B_AT2) + 9 + 3 * ks; ++e)
        if (e < 28) A_LD(e);
      if (ks == 6) { xv[0] = ld4c(ws, xo); xv[1] = ld4c(ws, xo + 2 * 14336); gv[0] = ld4c(ws, go); gv[1] = ld4c(ws, go + 16); }
      if (ks < 12) RED_W1(0, 1 + (ks >> 2), (ks & 3))
#pragma unroll
      for (int u = 0; u < 4; ++u) {
        __builtin_amdgcn_sched_group_barrier(0x8, 2, 0);
        __builtin_amdgcn_sched_group_barrier(0x20, 1, 0);
        if (u == 2) __builtin_amdgcn_sched_group_barrier(0x200, 1, 0);
      }
      sb();
    }
    STAMP(10);
#pragma unroll
    for (int e = 0; e < 12; ++e) RED_W1(1, 3 - (e >> 2), (e & 3))
#define FIN_OK() wall(max(max4(xv[0]), max4(xv[1])) != SENT && gv[0].y == tag && gv[0].w == tag && gv[1].y == tag && gv[1].w == tag)
    if (__builtin_expect(!FIN_OK(), 0)) {
      _Pragma("nounroll") for (int s = 0; s < SPIN; ++s) {
        const int gf = fresh(go), xf = fresh(xo);
        gv[0] = ld4c(ws, gf); gv[1] = ld4c(ws, gf + 16); xv[0] = ld4c(ws, xf); xv[1] = ld4c(ws, xf + 2 * 14336);
        if (FIN_OK()) break;
      }
    }
    bar();
#pragma unroll
    for (int ph = 0; ph < 2; ++ph) {
      v2f r[4];
#pragma unroll
      for (int n = 0; n < 4; ++n) {
        r[n] = lo2(ac2[2 * ph][n]);
#pragma unroll
        for (int w = 0; w < 3; ++w) r[n] += *(const LAS v2f*)(L + rr_ + w * 2048 + ph * 24576 + n * 512);
      }
      v4u o;
#pragma unroll
      for (int q = 0; q < 2; ++q) {
        o[2 * q] = pk2(fma2(bun(gv[q].x), v2f{r[0][q], r[1][q]}, bun(xv[ph][2 * q])));
        o[2 * q + 1] = pk2(fma2(bun(gv[q].z), v2f{r[2][q], r[3][q]}, bun(xv[ph][2 * q + 1])));
      }
      __builtin_amdgcn_raw_buffer_store_b128(o, mkrs(out, S * D * 2), oo[ph], 0, OUT_AUX);
    }
    if (blockIdx.x == 0 && wave == 0) {
      _Pragma("nounroll") for (int s = 0; s < SPIN; ++s) {
        const v4u v = ld4c(ws, fresh(WS_MOD + (70 + (lane < 14 ? lane : 0)) * 128));
        if (wall(v.y == tag)) break;
      }
      if (lane == 0) __builtin_amdgcn_raw_buffer_store_b32(epoch + 1u, ws, WS_EPOCH, 0, SC1);
    }
  }
  STAMP(11);
  DUMP();
}

extern "C" void kernel_launch(void* const* d_in, const int* in_sizes, int n_in, void* d_out, int out_size, void* d_ws,
                              size_t ws_size, hipStream_t stream) {
  fused<<<256, 256, 0, stream>>>((const u16*)d_in[0], (const u16*)d_in[1], (const u16*)d_in[2], (const u16*)d_in[4],
                                 (const u16*)d_in[5], (const u16*)d_in[6], (const u16*)d_in[7], (const u16*)d_in[8],
                                 (u16*)d_out, (char*)d_ws);
}
```

```cpp
#define F_V 17
#define A_V 25
#include <hip/hip_runtime.h>

typedef unsigned short u16;
typedef unsigned int u32;
typedef unsigned long long u64;
typedef u32 v4u __attribute__((ext_vector_type(4)));
typedef u32 v2u __attribute__((ext_vector_type(2)));
typedef float v4f __attribute__((ext_vector_type(4)));
typedef float v2f __attribute__((ext_vector_type(2)));
typedef short v8s __attribute__((ext_vector_type(8)));
typedef short v4s __attribute__((ext_vector_type(4)));
typedef __bf16 v8b __attribute__((ext_vector_type(8)));
typedef __bf16 v2b __attribute__((ext_vector_type(2)));
#define LAS __attribute__((address_space(3)))
#define GAS __attribute__((address_space(1)))
#define DEV __device__ __forceinline__

#define C_W1_EARLY 0
#define CLR_DELAY 0
#define W_DELAY 0
#define CLR2_DELAY 0
#define PF_POS 2
#define SL1 1
#define SL2 2
#define SL4 4
#define X_AFTER 8
#define PF_AUX 2
#define W_LATE 1
#define W1_PF 1
#define DMA_MOD ""
#define LEAD 6
#define A_PER 6
#define WDV(f) ((f) - WD_MID + 2 < 14 ? (f) - WD_MID + 2 : ((f) - WD_MID - 12) / 2 + 16)
#define B_AT2 12
#define RE_PF 0
#define PF2_DELAY 0
#define C_DMA_EARLY 0
#define EVEN_PF 0
#define XBAL 1
#define W1EVEN 1
#define PRE_OUT 0
#define OUT_AUX 16
#define CLR_MODE 0
#define A_PRE 20
#define P_AT 7
#define WD_PER 3
#define A_FIRST 10
#define B_FIRST 10
#define A_AT 9
#define F_AT 4
#define CODE_BYTES 45056
#define WD_EARLY 16
#define WD_MID 24
#define B_AT 9
constexpr int S = 2048, D = 448, DF = 225, F = 1792;

struct alignas(16) Chain { u16 v[2048]; };
struct Cos448 { u16 v[448]; };
constexpr double c_cos_t(double a) {
  double a2 = a * a, t = 1.0, s = 1.0;
  for (int i = 1; i <= 10; ++i) { t = -t * a2 / ((2 * i - 1) * (2 * i)); s += t; }
  return s;
}
constexpr double c_sin_t(double a) {
  double a2 = a * a, t = a, s = a;
  for (int i = 1; i <= 10; ++i) { t = -t * a2 / ((2 * i) * (2 * i + 1)); s += t; }
  return s;
}
constexpr double c_cos448(int m) {
  if (m > 224) m = 448 - m;
  double sg = 1.0;
  if (m > 112) { m = 224 - m; sg = -1.0; }
  const double w = 6.283185307179586476925286766559 / 448.0;
  return m == 112 ? 0.0 : (m <= 56 ? sg * c_cos_t(w * m) : sg * c_sin_t(w * (112 - m)));
}
constexpr u16 c_bf16(double d) {
  const u32 u = __builtin_bit_cast(u32, (float)d);
  return (u16)((u + 0x7FFFu + ((u >> 16) & 1u)) >> 16);
}
constexpr Cos448 mk_cos() { Cos448 c{}; for (int m = 0; m < 448; ++m) c.v[m] = c_bf16(c_cos448(m)); return c; }
constexpr Cos448 CTAB = mk_cos();
constexpr u16 bcos(int m) { return CTAB.v[m % 448]; }
constexpr u16 bsin(int m) { return CTAB.v[(m % 448 + 336) % 448]; }
constexpr u16 bneg(u16 b) { return b ? (u16)(b ^ 0x8000u) : b; }
constexpr u16 bscale(u16 b, int e) { return b ? (u16)(b + e * 0x80) : b; }

constexpr Chain mk_p1(int c) {
  const int tile = c >> 2, par = c & 1, sn = (c >> 1) & 1;
  Chain ch{};
  for (int s = 0; s < 4; ++s)
    for (int l = 0; l < 64; ++l) {
      const int kp = 16 * tile + (l & 15), k = 2 * kp + par;
      if (k > 224) continue;
      for (int j = 0; j < 8; ++j) {
        const int dl = 32 * s + 8 * (l >> 4) + j;
        if (dl > 112) continue;
        u16 b = sn ? bneg(bsin(k * dl)) : bcos(k * dl);
        if (dl == 0 || dl == 112) b = bscale(b, -1);
        ch.v[(s * 64 + l) * 8 + j] = b;
      }
    }
  return ch;
}
constexpr Chain mk_p3(int c) {
  const int tile = c >> 1, sn = c & 1;
  Chain ch{};
  for (int s = 0; s < 4; ++s)
    for (int l = 0; l < 64; ++l) {
      int dl = 16 * tile + (l & 15);
      if (c == 1 && dl == 0) dl = 112;
      for (int j = 0; j < 8; ++j) {
        const int k = 2 * (32 * s + 8 * (l >> 4) + j) + 1;
        if (k < 224) ch.v[(s * 64 + l) * 8 + j] = sn ? bsin(k * dl) : bcos(k * dl);
      }
    }
  return ch;
}
constexpr Chain mk_p2(int c) {
  Chain ch{};
  for (int f = 0; f < 4; ++f)
    for (int l = 0; l < 64; ++l)
      for (int j = 0; j < 8; ++j) {
        const int comp = c < 8 ? (c & 1) : (f >> 1), s = c < 8 ? f : (f & 1);
        const int o = 16 * (c < 8 ? (c >> 1) : (c - 8)) + (l & 15), i = 16 * s + 4 * (l >> 4) + (j >> 1);
        const int m = (c < 8 ? 7 : 14) * ((i * o) % 64);
        u16 b = 0;
        if (comp == 0) b = (j & 1) ? bsin(m) : bcos(m);
        else b = (j & 1) ? bcos(m) : bneg(bsin(m));
        ch.v[(f * 64 + l) * 8 + j] = b;
      }
  return ch;
}
template <int I> constexpr Chain P1C = mk_p1(I);
template <int I> constexpr Chain P3C = mk_p3(I);
template <int I> constexpr Chain P2C = mk_p2(I);
#define L10(T, b) T<b>, T<b + 1>, T<b + 2>, T<b + 3>, T<b + 4>, T<b + 5>, T<b + 6>, T<b + 7>, T<b + 8>, T<b + 9>
#define L29(T) L10(T, 0), L10(T, 10), T<20>, T<21>, T<22>, T<23>, T<24>, T<25>, T<26>, T<27>, T<28>
constexpr int TAB_P1 = 0, TAB_P3 = 29, TAB_P2 = 43, TAB_N = 53;
static __device__ const Chain TAB[TAB_N] = {L29(P1C), L10(P3C, 0), P3C<10>, P3C<11>, P3C<12>, P3C<13>, L10(P2C, 0)};

constexpr int WS_EPOCH = 0;
constexpr int WS_RDY = 4096;
constexpr int WS_HRDY = 4096 + 8192;
constexpr int WS_DRDY = 4096 + 16384;
constexpr int WS_XRDY = 131072 + 65536;
constexpr int WS_ARDY = 131072;
constexpr int WS_CRDY = 131072 + 32768;
constexpr int WS_MOD = 32768, MOD_REP = 12288;
constexpr int T1_BLK = 226 * 64;
constexpr int WS_T1 = 1 << 18;
constexpr int WS_WT = WS_T1 + 128 * T1_BLK;
constexpr int WS_H2 = WS_WT + 113 * 16384;
constexpr int WS_X2 = WS_H2 + 128 * 14336;
constexpr int WS_WD = WS_X2 + 128 * 14336;
constexpr int WS_ACT = WS_WD + 7 * 56 * 4096;
constexpr int WS_END = WS_ACT + 128 * 57344;
constexpr int WS_DBG = 32 << 20;

constexpr int L_W1 = 0;
constexpr int L_B = 114688;
constexpr int XP = 912;
constexpr int L_XS = L_B;
constexpr int L_ST = L_XS + 16 * XP;
constexpr int L_M1 = L_ST + 128;
constexpr int L_AF = L_M1 + 4 * 1792;
constexpr int L_M2 = L_AF + 16384;
constexpr int L_EX = L_M2 + 3 * 1792;
constexpr int L_REND = L_EX + 512;
constexpr int ZPL = 8192;
constexpr int L_CI = L_B;
constexpr int L_CX = L_CI + 2 * 8256;
constexpr int L_RED = L_CX + 16384;
constexpr int L_DBG = 163840 - 1024;
constexpr int L_SZ = 163840;
static_assert(L_REND <= L_DBG && L_RED + 2048 <= L_DBG && 2 * ZPL <= 16384, "lds");

constexpr u32 SENT = 0xFFFFFFFFu;
constexpr int SPIN = 1 << 14;
constexpr int SC1 = 16;
constexpr int OOB = 0x7FFF0000;

typedef __amdgpu_buffer_rsrc_t rsrc_t;
DEV rsrc_t mkrs(const void* p, int bytes) { return __builtin_amdgcn_make_buffer_rsrc((void*)p, 0, bytes, 0x00020000); }
DEV v4u ld4(rsrc_t r, int off, int so = 0) { return __builtin_amdgcn_raw_buffer_load_b128(r, off, so, 0); }
DEV v2u ld2(rsrc_t r, int off, int so = 0) { return __builtin_amdgcn_raw_buffer_load_b64(r, off, so, 0); }
DEV v4u ld4c(rsrc_t r, int off, int so = 0) { return __builtin_amdgcn_raw_buffer_load_b128(r, off, so, SC1); }
DEV v2u ld2c(rsrc_t r, int off, int so = 0) { return __builtin_amdgcn_raw_buffer_load_b64(r, off, so, SC1); }
DEV u32 ld1c(rsrc_t r, int off, int so = 0) { return __builtin_amdgcn_raw_buffer_load_b32(r, off, so, SC1); }
DEV void st4c(rsrc_t r, int off, v4u v) { __builtin_amdgcn_raw_buffer_store_b128(v, r, off, 0, SC1); }
DEV void st2c(rsrc_t r, int off, v2u v) { __builtin_amdgcn_raw_buffer_store_b64(v, r, off, 0, SC1); }
DEV float blo(u32 v) { return __builtin_bit_cast(float, v << 16); }
DEV float bhi(u32 v) { return __builtin_bit_cast(float, v & 0xFFFF0000u); }
DEV v2f bun(u32 v) { return v2f{blo(v), bhi(v)}; }
DEV u32 pk(float lo, float hi) { return __builtin_bit_cast(u32, __builtin_convertvector(v2f{lo, hi}, v2b)); }
DEV u32 pk2(v2f v) { return __builtin_bit_cast(u32, __builtin_convertvector(v, v2b)); }
DEV float rb(float f) { return blo(pk(f, 0.f)); }
DEV v2f lo2(v4f v) { return __builtin_shufflevector(v, v, 0, 1); }
DEV v2f hi2(v4f v) { return __builtin_shufflevector(v, v, 2, 3); }
DEV v2f fma2(v2f a, v2f b, v2f c) { return __builtin_elementwise_fma(a, b, c); }
DEV v4f fma4(v4f a, v4f b, v4f c) { return __builtin_elementwise_fma(a, b, c); }
typedef int v4i __attribute__((ext_vector_type(4)));
DEV v4f relu4(v4f v) { return __builtin_bit_cast(v4f, __builtin_elementwise_max(__builtin_bit_cast(v4i, v), v4i{0, 0, 0, 0})); }
DEV v4f splat(float f) { return v4f{f, f, f, f}; }
DEV v4f mfma(v4u a, v4u b, v4f c) {
  return __builtin_amdgcn_mfma_f32_16x16x32_bf16(__builtin_bit_cast(v8b, a), __builtin_bit_cast(v8b, b), c, 0, 0, 0);
}
DEV void mfa(v4f& c, v4u a, v4u b) { asm volatile("s_nop 1\n\tv_mfma_f32_16x16x32_bf16 %0, %1, %2, %0" : "+v"(c) : "v"(a), "v"(b)); }
DEV void mf0(v4f& c, v4u a, v4u b) { asm volatile("s_nop 1\n\tv_mfma_f32_16x16x32_bf16 %0, %1, %2, 0" : "=&v"(c) : "v"(a), "v"(b)); }
template <int N> DEV void mfence(v4f& a, v4f& b) { asm volatile("s_nop %2" : "+v"(a), "+v"(b) : "i"(N)); }
template <int N> DEV void mfence(v4f& a, v4f& b, v4f& c, v4f& d) { asm volatile("s_nop %4" : "+v"(a), "+v"(b), "+v"(c), "+v"(d) : "i"(N)); }
#define MF "v_mfma_f32_16x16x32_bf16 "
DEV void mf8(v4f& d0, v4f& d1, const v4u (&a0)[4], const v4u (&b0)[4], const v4u (&a1)[4], const v4u (&b1)[4]) {
  asm volatile("s_nop 1\n\t" MF "%0, %2, %6, 0\n\t" MF "%1, %10, %14, 0\n\t" MF "%0, %3, %7, %0\n\t" MF "%1, %11, %15, %1\n\t"
               MF "%0, %4, %8, %0\n\t" MF "%1, %12, %16, %1\n\t" MF "%0, %5, %9, %0\n\t" MF "%1, %13, %17, %1"
               : "=&v"(d0), "=&v"(d1)
               : "v"(a0[0]), "v"(a0[1]), "v"(a0[2]), "v"(a0[3]), "v"(b0[0]), "v"(b0[1]), "v"(b0[2]), "v"(b0[3]),
                 "v"(a1[0]), "v"(a1[1]), "v"(a1[2]), "v"(a1[3]), "v"(b1[0]), "v"(b1[1]), "v"(b1[2]), "v"(b1[3]));
}
#define H4_M(XA, D, X, K, C) MF D ", " XA(X, K) ", " C "\n\t"
#define H4_XA(X, K) X ", " K
#define H4_XB(X, K) K ", " X
#define H4_DEF(NAME, XA, C0, C1, CONS, N0, N1, PA, PB, PC, PD) \
DEV void NAME(v4f& d0, v4f& d1, v4u x0, v4u x1, v4u k00, v4u k01, v4u k10, v4u k11, \
              u32& o0, u32& o1, float p0, float q0, float c0, float s0, float p1, float q1, float c1, float s1) { \
  float t0, t1, t2, t3; \
  asm volatile(H4_M(XA, "%0", "%8", "%10", C0) "v_mul_f32 %4, %14, %16\n\tv_mul_f32 %5, %15, %16\n\t" \
               H4_M(XA, "%1", "%8", "%12", C1) "v_mul_f32 %6, %18, %20\n\tv_mul_f32 %7, %19, %20\n\t" \
               H4_M(XA, "%0", "%9", "%11", "%0") "v_fma_f32 %4, " N0 "%15, %17, %4\n\tv_fma_f32 %5, " N1 "%14, %17, %5\n\t" \
               H4_M(XA, "%1", "%9", "%13", "%1") "v_fma_f32 %6, " N0 "%19, %21, %6\n\tv_fma_f32 %7, " N1 "%18, %21, %7\n\t" \
               "v_cvt_pk_bf16_f32 %2, " PA ", " PB "\n\tv_cvt_pk_bf16_f32 %3, " PC ", " PD \
               : CONS(d0), CONS(d1), "=&v"(o0), "=&v"(o1), "=&v"(t0), "=&v"(t1), "=&v"(t2), "=&v"(t3) \
               : "v"(x0), "v"(x1), "v"(k00), "v"(k01), "v"(k10), "v"(k11), \
                 "v"(p0), "v"(q0), "v"(c0), "v"(s0), "v"(p1), "v"(q1), "v"(c1), "v"(s1)); \
}
H4_DEF(h4_a0m, H4_XA, "0", "0", "=&v", "", "-", "%4", "%5", "%6", "%7")
H4_DEF(h4_a1m, H4_XA, "%0", "%1", "+v", "", "-", "%4", "%5", "%6", "%7")
H4_DEF(h4_b0m, H4_XB, "0", "0", "=&v", "", "-", "%4", "%5", "%6", "%7")
H4_DEF(h4_b0p, H4_XB, "0", "0", "=&v", "-", "", "%5", "%4", "%7", "%6")
H4_DEF(h4_a0p, H4_XA, "0", "0", "=&v", "-", "", "%5", "%4", "%7", "%6")
#define M4_DEF(NAME, XA, C0, C1, CONS) \
DEV void NAME(v4f& d0, v4f& d1, v4u x0, v4u x1, v4u k00, v4u k01, v4u k10, v4u k11) { \
  asm volatile(H4_M(XA, "%0", "%2", "%4", C0) H4_M(XA, "%1", "%2", "%6", C1) H4_M(XA, "%0", "%3", "%5", "%0") MF "%1, " XA("%3", "%7") ", %1" \
               : CONS(d0), CONS(d1) : "v"(x0), "v"(x1), "v"(k00), "v"(k01), "v"(k10), "v"(k11)); \
}
M4_DEF(m4_a0, H4_XA, "0", "0", "=&v") M4_DEF(m4_a1, H4_XA, "%0", "%1", "+v")
M4_DEF(m4_b0, H4_XB, "0", "0", "=&v") M4_DEF(m4_b1, H4_XB, "%0", "%1", "+v")
DEV void mv(v4f& c, v4u a, v4u b) { asm volatile(MF "%0, %1, %2, %0" : "+v"(c) : "v"(a), "v"(b)); }
DEV void mv0(v4f& c, v4u a, v4u b) { asm volatile(MF "%0, %1, %2, 0" : "=&v"(c) : "v"(a), "v"(b)); }
DEV void nop2() { asm volatile("s_nop 1"); }
template <class T> DEV void pin(T& v) { asm volatile("" : "+v"(v)); }
DEV int fresh(int off) { asm volatile("" : "+v"(off) :: "memory"); return off; }
DEV bool wall(bool ok) { return __builtin_amdgcn_ballot_w64(!ok) == 0; }
DEV bool wany(bool b) { return __builtin_amdgcn_ballot_w64(b) != 0; }
DEV void sb() { __builtin_amdgcn_sched_barrier(0); }
DEV u32 max4(v4u v) { return max(max(v.x, v.y), max(v.z, v.w)); }
DEV void drain() { asm volatile("s_waitcnt vmcnt(0)" ::: "memory"); }
DEV void cfence() { asm volatile("" ::: "memory"); }
DEV void bar() { asm volatile("s_waitcnt lgkmcnt(0)\n\ts_barrier" ::: "memory"); }
template <int CTRL> DEV float dpp(float v) {
  return __builtin_bit_cast(float, __builtin_amdgcn_update_dpp(0, __builtin_bit_cast(int, v), CTRL, 0xF, 0xF, true));
}
DEV float sum16(float v) {
  v += dpp<0x128>(v); v += dpp<0x124>(v); v += dpp<0x122>(v); v += dpp<0x121>(v); return v;
}
DEV void sumg2(float& a, float& b) {
  float c = a, d = b;
  asm volatile("s_nop 1\n\tv_permlane16_swap_b32 %0, %1\n\tv_permlane16_swap_b32 %2, %3" : "+v"(a), "+v"(c), "+v"(b), "+v"(d));
  a += c; b += d; c = a; d = b;
  asm volatile("s_nop 1\n\tv_permlane32_swap_b32 %0, %1\n\tv_permlane32_swap_b32 %2, %3" : "+v"(a), "+v"(c), "+v"(b), "+v"(d));
  a += c; b += d;
}
DEV v4u lds4(const char LAS* L, int off) { return *(const LAS v4u*)(L + off); }
DEV v4f lds4f(const char LAS* L, int off) { return *(const LAS v4f*)(L + off); }
DEV v2u ldtr(const char LAS* L, int off) { return __builtin_bit_cast(v2u, __builtin_amdgcn_ds_read_tr16_b64_v4i16((LAS v4s*)(L + off))); }
DEV v4u ldtr2(const char LAS* L, int o0, int o1) { const v2u a = ldtr(L, o0), b = ldtr(L, o1); return v4u{a.x, a.y, b.x, b.y}; }
DEV void dma16(rsrc_t r, int voff, int soff, u32 lds_dst) {
  u32 keep;
  asm volatile("s_mov_b32 %0, m0\n\ts_mov_b32 m0, %1\n\ts_nop 0\n\tbuffer_load_dwordx4 %2, %3, %4 offen" DMA_MOD " lds\n\ts_mov_b32 m0, %0"
               : "=&s"(keep) : "s"(lds_dst), "v"(voff), "s"(r), "s"(soff) : "memory");
}
DEV void light_poll(rsrc_t ws, int off) {
  _Pragma("nounroll") for (int s = 0; s < SPIN; ++s) {
    if (wall(ld1c(ws, fresh(off)) != SENT)) break;
    __builtin_amdgcn_s_sleep(SL2);
  }
}
DEV int tok_row(int blk, int t) {
  const int n1 = 16 * (blk >> 5) + 4 * ((blk >> 2) & 3) + (t & 3), n2 = 16 * ((blk >> 4) & 1) + 4 * (blk & 3) + (t >> 2);
  return n2 + 32 * n1;
}
template <bool SWAP> DEV v4u cmul_pack(v4f a, v4f b, v4f c, v4f s) {
  const v4f re = fma4(b, s, a * c), im = fma4(b, c, -(a * s));
  return SWAP ? v4u{pk(im.x, re.x), pk(im.y, re.y), pk(im.z, re.z), pk(im.w, re.w)}
              : v4u{pk(re.x, im.x), pk(re.y, im.y), pk(re.z, im.z), pk(re.w, im.w)};
}

template <bool PLUS> DEV void cmul2(u32& o0, u32& o1, v2f p, v2f q, v2f c, v2f s) {
  const v2f re = fma2(PLUS ? -q : q, s, p * c), im = fma2(PLUS ? p : -p, s, q * c);
  o0 = PLUS ? pk(im.x, re.x) : pk(re.x, im.x); o1 = PLUS ? pk(im.y, re.y) : pk(re.y, im.y);
}

#define STAMP(i)
#define CSTAMP(i)
#define SLOWCNT
#define DUMP()

DEV void dma_w1(const u16* wg, const u16* wu, int slice, int wave, int lane, u32 lbase) {
  const rsrc_t rg = mkrs(wg, D * F * 2), ru = mkrs(wu, D * F * 2);
  const int r = lane >> 3, ch = ((((lane & 7) >> 1) ^ (lane >> 4)) & 3) * 32 + (lane & 1) * 16 + slice * 128;
  const int vp = r * (F * 2) + ch, vn = (7 - r) * (F * 2) + ch;
#pragma unroll
  for (int n = 0; n < 4; ++n) {
    const int ks = wave + 4 * n;
    if (n == 3 && wave >= 2) break;
    const int t16 = (ks >> 1) * 16, a = ks & 1;
#pragma unroll
    for (int q = 0; q < 4; ++q) {
      const int dl0 = t16 + 8 * (q & 1);
      const bool pos = (q >> 1) == a;
      const int d0 = q < 2 ? (a ? 224 - dl0 - 7 : dl0) : (a ? 224 + dl0 : 448 - dl0 - 7);
      int vo = pos ? vp : vn, so = d0 * (F * 2);
      if (n == 0 && q == 2) {
        vo += so; so = 0;
        if (wave < 2 && lane < 8) vo = (wave ? 336 : 112) * (F * 2) + ch;
      }
      dma16(rg, vo, so, lbase + L_W1 + ks * 8192 + q * 1024);
      dma16(ru, vo, so, lbase + L_W1 + ks * 8192 + 4096 + q * 1024);
    }
  }
}
struct ModLoad { u32 cv[7]; v4u mw[7]; };
DEV void mod_load(ModLoad& m, const u16* cin, const u16* wm, int q, int tid) {
  const rsrc_t cr = mkrs(cin, q >= 0 ? D * 2 : 0), mr = mkrs(wm, q >= 0 ? D * 6 * D * 2 : 0);
#pragma unroll
  for (int i = 0; i < 7; ++i) m.cv[i] = __builtin_amdgcn_raw_buffer_load_b16(cr, (tid >> 2) * 2 + i * 128, 0, 0);
#pragma unroll
  for (int i = 0; i < 7; ++i) m.mw[i] = __builtin_amdgcn_raw_buffer_load_b128(mr, ((tid >> 2) * (6 * D) + q * 32 + (tid & 3) * 8) * 2, i * 64 * 6 * D * 2, PF_AUX);
}
DEV void mod_part(ModLoad& m, char LAS* L, int tid) {
  float sl[7];
#pragma unroll
  for (int i = 0; i < 7; ++i) { const float cc = blo(m.cv[i]); sl[i] = rb(cc * __builtin_amdgcn_rcpf(1.f + __expf(-cc))); pin(sl[i]); }
  v2f a[4];
#pragma unroll
  for (int e = 0; e < 4; ++e) a[e] = v2f{0.f, 0.f};
#pragma unroll
  for (int i = 0; i < 7; ++i)
#pragma unroll
    for (int e = 0; e < 4; ++e) a[e] = fma2(bun(m.mw[i][e]), v2f{sl[i], sl[i]}, a[e]);
#pragma unroll
  for (int e = 0; e < 4; ++e) {
    a[e].x += dpp<0x128>(a[e].x); a[e].y += dpp<0x128>(a[e].y);
    a[e].x += dpp<0x124>(a[e].x); a[e].y += dpp<0x124>(a[e].y);
  }
  if ((tid & 15) < 4) {
    *(LAS v4f*)(L + L_RED + ((tid >> 4) * 32 + (tid & 15) * 8) * 4) = v4f{a[0].x, a[0].y, a[1].x, a[1].y};
    *(LAS v4f*)(L + L_RED + ((tid >> 4) * 32 + (tid & 15) * 8 + 4) * 4) = v4f{a[2].x, a[2].y, a[3].x, a[3].y};
  }
}
DEV void mod_publish(char LAS* L, rsrc_t ws, int q, int lane, u32 tag) {
  v4f s = {0.f, 0.f, 0.f, 0.f};
#pragma unroll
  for (int r = 0; r < 16; ++r) s += lds4f(L, L_RED + (r * 32 + (lane & 7) * 4) * 4);
  const float one = (q / 14 == 1 || q / 14 == 4) ? 1.f : 0.f;
  st4c(ws, WS_MOD + (lane >> 3) * MOD_REP + q * 128 + (lane & 7) * 16,
       v4u{pk(rb(s.x) + one, rb(s.y) + one), tag, pk(rb(s.z) + one, rb(s.w) + one), tag});
}
DEV u32 ld1(rsrc_t r, int off) { return __builtin_amdgcn_raw_buffer_load_b32(r, off, 0, 0); }
DEV u32 ld1n(rsrc_t r, int off) { return __builtin_amdgcn_raw_buffer_load_b32(r, off, 0, PF_AUX); }
DEV u32 mktag(u32 epoch) { return epoch * 0x9E3779B1u + 0x7F4A7C15u; }

DEV void x2_read(const char LAS* L, v4u (&r)[4], int tid) {
#pragma unroll
  for (int m = 0; m < 4; ++m) { const int ch = min(tid + 256 * m, 895), tk = ch / 56; r[m] = lds4(L, L_XS + tk * XP + (ch - tk * 56) * 16); }
}
DEV void x2_store(rsrc_t ws, const v4u (&r)[4], int blk, int tid) {
#pragma unroll
  for (int m = 0; m < 4; ++m) st4c(ws, tid + 256 * m < 896 ? WS_X2 + blk * 14336 + (tid + 256 * m) * 16 : OOB, r[m]);
}
DEV bool flags_ok(v4u rv, u32 tag) { return rv.x == tag && rv.y == ~tag && rv.z == tag && rv.w == ~tag; }
DEV void wait_flags(rsrc_t ws, int off, u32 tag) {
  _Pragma("nounroll") for (int s = 0; s < SPIN; ++s) {
    const v2u rv = ld2c(ws, fresh(off));
    if (wall(rv.x == tag && rv.y == ~tag)) break;
    __builtin_amdgcn_s_sleep(SL1);
  }
}

__global__ void __launch_bounds__(256, 1)
fused(const u16* __restrict__ xin, const u16* __restrict__ wr, const u16* __restrict__ wi, const u16* __restrict__ wg,
      const u16* __restrict__ wu, const u16* __restrict__ wd, const u16* __restrict__ cin, const u16* __restrict__ wm,
      u16* __restrict__ out, char* wsp) {
  __shared__ __attribute__((aligned(16))) char smem[L_SZ];
  char LAS* L = (char LAS*)smem;
  const u32 lbase = (u32)(size_t)L;
  const int tid = threadIdx.x, lane = tid & 63, wave = __builtin_amdgcn_readfirstlane(tid >> 6);
  const int g = lane >> 4, c16 = lane & 15, l16 = lane * 16;
  const int grp = blockIdx.x & 7, j = blockIdx.x >> 3;
  u64 pc;
  asm volatile("s_getpc_b64 %0" : "=s"(pc));
  STAMP(0); CSTAMP(31);
  asm volatile("" :: "s"(xin), "s"(wr), "s"(wi), "s"(wg), "s"(wu), "s"(wd), "s"(cin), "s"(wm), "s"(out), "s"(wsp));
  const rsrc_t ws = mkrs(wsp, WS_END);
  const rsrc_t tab = mkrs(TAB, TAB_N * 4096);
  const rsrc_t code = mkrs((const void*)(pc & ~127ull), CODE_BYTES);
  const bool isR = j >= 14 && j < 30;
  const bool isG = j < 28;
  const int gk = j / 7, ct = j - gk * 7;
  const int tile = 4 * (grp >> 2) + gk, slice = 7 * (grp & 3) + ct;
  const int wdb = WS_WD + (ct * 56 + wave * 14) * 4096;
  const v4u sv = {SENT, SENT, SENT, SENT};
  int lb = L_B + l16;
  pin(lb);
  const char LAS* LB = L + lb - L_B;
  const int pfo = ((j * 4 + wave) * 14 + lane) * 128, pfc = ((j * 4 + wave) * 4 + lane) * 128;
  u32 pfa = ld1(tab, lane < 14 ? pfo : OOB), pfb = ld1(code, lane < 4 ? pfc : OOB);
  const int hro = WS_HRDY + (16 * tile + 4 * (lane < 32 ? wave : grp & 3)) * 32 + c16 * 8;
  v4u x2r[4];
  const int wl16 = (lane ^ ((wave & 1) << 1)) * 16;
  v4u wdn[14][4];
  u32 epoch, tag;

#define ACT_CLEAR(lo, hi) _Pragma("unroll") for (int m = lo; m < hi; ++m) \
      st4c(ws, isG ? WS_ACT + (16 * (tile + 2 * (m >> 2)) + 4 * (grp & 3) + 2) * 57344 + ct * 16384 + (m & 3) * 4096 + tid * 16 : OOB, sv);
  if (isR) {
    const int blk = 16 * grp + (j - 14);
    epoch = ld1c(ws, WS_EPOCH);
    const int t0 = wave == 0 ? 0 : 2 * wave - 1;
    v4u cf[8][4];
#define CF_LOAD(q) { const int chn = TAB_P1 + ((wave == 0 && q >= 4) ? 28 : 4 * t0 + q), vo = (q < 5 || wave != 0) ? l16 : OOB; \
      _Pragma("unroll") for (int s = 0; s < 4; ++s) cf[q][s] = ld4(tab, vo + s * 1024, chn * 4096); sb(); }
#pragma unroll
    for (int q = 0; q < X_AFTER; ++q) CF_LOAD(q)
    v2u xv[7];
    {
      const rsrc_t xr = mkrs(xin, S * D * 2);
      const int off = tok_row(blk, 4 * wave + g) * (D * 2) + c16 * 8;
#pragma unroll
      for (int m = 0; m < 7; ++m) xv[m] = ld2(xr, off + m * 128);
    }
    typedef u32 v3u __attribute__((ext_vector_type(3)));
    v2u fa_[2], fb_[2]; v3u fc_[2], fd_[2];
    {
      const rsrc_t rr = mkrs(wr, S * DF * 2), ri = mkrs(wi, S * DF * 2);
      const int fo_ = 16 * wave + c16 < 57 ? 2 * g * 450 + (16 * wave + c16) * 8 : OOB;
#pragma unroll
      for (int hh = 0; hh < 2; ++hh) {
        const int so = blk * 7200 + hh * 3600;
        fa_[hh] = ld2(rr, fo_, so); fb_[hh] = ld2(ri, fo_, so);
        fc_[hh] = __builtin_amdgcn_raw_buffer_load_b96(rr, fo_ + 448, so, 0); fd_[hh] = __builtin_amdgcn_raw_buffer_load_b96(ri, fo_ + 448, so, 0);
      }
    }
    sb();
#pragma unroll
    for (int q = X_AFTER; q < 8; ++q) CF_LOAD(q)
    STAMP(23);
    {
      v2f f[14], s2 = {0.f, 0.f};
#pragma unroll
      for (int m = 0; m < 7; ++m) {
        f[2 * m] = bun(xv[m].x); f[2 * m + 1] = bun(xv[m].y);
        s2 += f[2 * m] + f[2 * m + 1];
        *(LAS v2u*)(L + L_XS + (4 * wave + g) * XP + c16 * 8 + m * 128) = xv[m];
      }
      STAMP(21);
      sb();
      {
        const int q = 16 * wave + c16, o = WS_WT + q * 16384 + blk * 128 + g * 16;
#pragma unroll
        for (int hh = 0; hh < 2; ++hh) {
          const v2u a = fa_[hh], b = fb_[hh]; const v3u c = fc_[hh], d = fd_[hh];
          st4c(ws, q < 57 ? o + hh * 64 : OOB, v4u{__builtin_amdgcn_perm(b.x, a.x, 0x05040100u), __builtin_amdgcn_perm(b.y, a.y, 0x05040100u),
                                                   __builtin_amdgcn_perm(d.x, c.x, 0x07060302u), __builtin_amdgcn_perm(d.y, c.y, 0x07060302u)});
          st4c(ws, q < 56 ? o + hh * 64 + 57 * 16384 : OOB,
               v4u{__builtin_amdgcn_perm(b.x, a.x, 0x07060302u), __builtin_amdgcn_perm(b.y, a.y, 0x07060302u),
                   __builtin_amdgcn_perm(d.y, c.y, 0x05040100u), __builtin_amdgcn_perm(d.z, c.z, 0x05040100u)});
        }
      }
      sb();
      const float mean = sum16(s2.x + s2.y) * (1.f / D);
      v2f q2 = {0.f, 0.f};
#pragma unroll
      for (int m = 0; m < 14; ++m) { const v2f d = f[m] - mean; q2 = fma2(d, d, q2); }
      const float rstd = __builtin_amdgcn_rsqf(sum16(q2.x + q2.y) * (1.f / D) + 1e-5f);
      if (c16 == 0) *(LAS v2f*)(L + L_ST + (4 * wave + g) * 8) = v2f{mean, rstd};
    }
    pin(pfa); pin(pfb);
    epoch = __builtin_amdgcn_readfirstlane(epoch);
    tag = mktag(epoch);
    STAMP(22);
    bar();
    v4f n[4][2];
    const int d0 = 8 * (4 * wave + g);
    const v2f st = *(const LAS v2f*)(L + L_ST + c16 * 8);
    {
      int xr_ = L_XS + c16 * XP + d0 * 2, xb_ = L_XS + c16 * XP + (216 - d0) * 2, x0_ = d0 == 0 ? xr_ : xb_ + 464;
      pin(xr_); pin(xb_); pin(x0_);
      const v4u a = lds4(L + xr_, 0), e = lds4(L + xr_, 448), c = lds4(L + xb_, 0), b = lds4(L + xb_, 448);
      const u32 c0 = *(const LAS u16*)(L + xb_ + 16), b0 = *(const LAS u16*)(L + x0_);
#pragma unroll
      for (int i = 0; i < 8; ++i) {
        n[0][i >> 2][i & 3] = (i & 1) ? bhi(a[i >> 1]) : blo(a[i >> 1]);
        n[3][i >> 2][i & 3] = (i & 1) ? bhi(e[i >> 1]) : blo(e[i >> 1]);
        n[1][i >> 2][i & 3] = i == 0 ? blo(b0) : (i & 1) ? bhi(b[(8 - i) >> 1]) : blo(b[(8 - i) >> 1]);
        n[2][i >> 2][i & 3] = i == 0 ? blo(c0) : (i & 1) ? bhi(c[(8 - i) >> 1]) : blo(c[(8 - i) >> 1]);
      }
      const float nm = -st.x * st.y;
#pragma unroll
      for (int m = 0; m < 4; ++m) {
#pragma unroll
        for (int h = 0; h < 2; ++h) { n[m][h] = fma4(n[m][h], splat(st.y), splat(nm)); pin(n[m][h]); }
      }
    }
    sb();
    STAMP(1);
    int mb = L_M1 + d0 * 4;
    pin(mb);
    v4u xs[4];
    int to[4];
    to[0] = WS_T1 + blk * T1_BLK + (16 * t0 + c16) * 64 + g * 16;
    to[1] = wave != 0 ? to[0] + 1024 : c16 < 2 ? to[0] + 112 * 64 : OOB;
    to[2] = to[0] + 114 * 64;
    to[3] = wave != 0 ? to[0] + 1024 + 114 * 64 : OOB;
#pragma unroll
    for (int q = 0; q < 4; ++q) pin(to[q]);

    {
      const int kind = tid >= 112, d = 4 * (tid - 112 * kind);
      int wb = L_M1 + (kind ? 0 : 2 * 1792) + d * 4, wrv = L_M1 + (kind ? 0 : 2 * 1792) + 1792 + (445 - d) * 4;
      int wr0 = d == 0 ? wrv + 12 - 1792 : wrv + 12;
      const int mo = tid < 224 ? WS_MOD + grp * MOD_REP + tid * 16 : OOB;
      pin(wb); pin(wrv); pin(wr0);
      const int ro = WS_RDY + grp * 1024 + min(tid >> 1, 112) * 8;
      v4u mv;
      _Pragma("nounroll") for (int s = 0; s < SPIN; ++s) {
        mv = ld4c(ws, fresh(mo));
        const v2u rv = ld2c(ws, fresh(ro));
        if (wall((tid >= 224 || (mv.y == tag && mv.w == tag)) && rv.x == tag && rv.y == ~tag)) break;
        __builtin_amdgcn_s_sleep(SL1);
      }
      STAMP(2);
      if (tid < 224) {
        const v4f v = {blo(mv.x), bhi(mv.x), blo(mv.z), bhi(mv.z)};
        *(LAS v4f*)(L + wb) = v;
        *(LAS float*)(L + wr0) = v.x;
        *(LAS float*)(L + wrv + 8) = v.y;
        *(LAS float*)(L + wrv + 4) = v.z;
        *(LAS float*)(L + wrv) = v.w;
      }
    }
    bar();
    {
      v4u fr[4];
#pragma unroll
      for (int hh = 0; hh < 2; ++hh) {
        v4f h[4];
#pragma unroll
        for (int m = 0; m < 4; ++m) {
          const int o = ((m == 1 || m == 2) ? 1792 : 0) + (m >= 2 ? 896 : 0) + hh * 16;
          h[m] = fma4(n[m][hh], lds4f(L + mb, o), lds4f(L + mb, 2 * 1792 + o));
        }
        const v4f p = h[0] + h[1], q = h[2] + h[3], u = h[0] - h[1], v = h[3] - h[2];
        const v4f t[4] = {p + q, p - q, u + v, u - v};
#pragma unroll
        for (int m = 0; m < 4; ++m) { fr[m][2 * hh] = pk2(lo2(t[m])); fr[m][2 * hh + 1] = pk2(hi2(t[m])); }
      }
#pragma unroll
      for (int m = 0; m < 4; ++m) *(LAS v4u*)(LB + wave * 1024 + L_AF + m * 4096) = fr[m];
    }
    bar();
    {
      v4u a[4][4];
#pragma unroll
      for (int p = 0; p < 2; ++p)
#pragma unroll
        for (int s = 0; s < 4; ++s) { a[p][s] = lds4(LB, L_AF + (p * 4 + s) * 1024); a[2 + p][s] = lds4(LB, L_AF + ((2 + p) * 4 + s) * 1024); }
      v4f re[4], im[4];
#pragma unroll
      for (int q = 0; q < 5; ++q) {
        if (q < 4) mf8(re[q], im[q], a[q >> 1], cf[4 * (q & 1) + (q >> 1)], a[2 + (q >> 1)], cf[4 * (q & 1) + 2 + (q >> 1)]);
        if (q > 0) {
          if (q < 4) mfence<0>(re[q - 1], im[q - 1]); else mfence<7>(re[3], im[3]);
          xs[q - 1] = v4u{pk(re[q - 1].x, im[q - 1].x), pk(re[q - 1].y, im[q - 1].y), pk(re[q - 1].z, im[q - 1].z), pk(re[q - 1].w, im[q - 1].w)};
          st4c(ws, to[q - 1], xs[q - 1]);
          sb();
        }
      }
    }
    STAMP(3);
    sb();
    sb();
    if (isG) dma_w1(wg, wu, slice, wave, lane, lbase);
#pragma unroll
    for (int q = 0; q < 4; ++q) {
      const int vo = (q < 2 || wave != 0) ? l16 : OOB;
#pragma unroll
      for (int s = 0; s < 4; ++s) cf[2 * q + 1][s] = ld4(tab, vo + s * 1024, (TAB_P3 + 2 * t0 + q) * 4096);
    }
#pragma unroll
    for (int m = 0; m < 4; ++m) {
      st4c(ws, tid + 256 * m < 896 ? WS_H2 + blk * 14336 + 4096 * m + tid * 16 : OOB, sv);
    }
    sb();
    STAMP(20);
    v4f xo[2][4];
    int xa = L_XS + c16 * XP + (16 * t0 + 4 * g) * 2, xd = L_XS + c16 * XP + (192 - 16 * t0 - 4 * g) * 2;
    int xs1 = tid < 16 ? L_XS + c16 * XP + 224 : xd + 512, xs3 = tid < 16 ? L_XS + c16 * XP + 672 : xa + 448;
    pin(xa); pin(xd); pin(xs1); pin(xs3);
#define XOFF(t, m, r) ((t) == 0 && (r) == 0 && (m) == 1 ? xs1 : (t) == 0 && (r) == 0 && (m) == 3 ? xs3 : \
    (m) == 0 ? xa + 32 * (t) + 2 * (r) : (m) == 3 ? xa + 448 + 32 * (t) + 2 * (r) : (m) == 1 ? xd + 512 - 32 * (t) - 2 * (r) : xd + 64 - 32 * (t) - 2 * (r))
    {
      v2u w[2][4]; u32 e[2][2], e3;
#pragma unroll
      for (int t = 0; t < 2; ++t) {
        w[t][0] = *(const LAS v2u*)(L + xa + 32 * t); w[t][3] = *(const LAS v2u*)(L + xa + 448 + 32 * t);
        w[t][1] = *(const LAS v2u*)(L + xd + 504 - 32 * t); w[t][2] = *(const LAS v2u*)(L + xd + 56 - 32 * t);
        e[t][0] = *(const LAS u16*)(L + XOFF(t, 1, 0)); e[t][1] = *(const LAS u16*)(L + XOFF(t, 2, 0));
      }
      e3 = *(const LAS u16*)(L + xs3);
      sb();
#pragma unroll
      for (int t = 0; t < 2; ++t) {
        xo[t][0] = v4f{blo(w[t][0].x), bhi(w[t][0].x), blo(w[t][0].y), bhi(w[t][0].y)};
        xo[t][3] = v4f{t == 0 ? blo(e3) : blo(w[t][3].x), bhi(w[t][3].x), blo(w[t][3].y), bhi(w[t][3].y)};
        xo[t][1] = v4f{blo(e[t][0]), bhi(w[t][1].y), blo(w[t][1].y), bhi(w[t][1].x)};
        xo[t][2] = v4f{blo(e[t][1]), bhi(w[t][2].y), blo(w[t][2].y), bhi(w[t][2].x)};
#pragma unroll
        for (int m = 0; m < 4; ++m) { xo[t][m] -= st.x; pin(xo[t][m]); }
      }
    }
    int m0_[2], m1_[2], md_[2];
#pragma unroll
    for (int r = 0; r < 2; ++r) {
      const int q = r == 0 || tid < 80 ? tid + 256 * r : tid;
      const int kind = (q * 586) >> 16, d = 4 * (q - 112 * kind);
      const int reg = (d >= 112) + (d >= 224) + (d >= 336), cc = ((reg + 1) >> 1) * 224, m1 = (0x1320 >> (4 * reg)) & 3;
      const int d1 = abs(d + 1 - cc), d0_ = abs(d - cc), edge = d == 112 * reg && reg > 0;
      m1_[r] = L_M2 + kind * 1792 + (((d1 >> 4) << 6) | (m1 << 4) | (d1 & 15)) * 4;
      m0_[r] = L_M2 + kind * 1792 + (edge ? reg << 4 : ((d0_ >> 4) << 6) | (m1 << 4) | (d0_ & 15)) * 4;
      md_[r] = (reg & 1) ? -4 : 4;
      pin(m0_[r]); pin(m1_[r]); pin(md_[r]);
    }
    {
      v4u mv[2];
      _Pragma("nounroll") for (int s = 0; s < SPIN; ++s) {
        mv[0] = ld4c(ws, fresh(WS_MOD + grp * MOD_REP + (224 + tid) * 16));
        mv[1] = ld4c(ws, fresh(WS_MOD + grp * MOD_REP + (224 + (tid < 80 ? 256 + tid : tid)) * 16));
        const v2u rv = ld2c(ws, fresh(WS_DRDY + grp * 1024 + min(lane, 48) * 8));
        if (wall(mv[0].y == tag && mv[0].w == tag && mv[1].y == tag && mv[1].w == tag && rv.x == tag && rv.y == ~tag)) break;
        __builtin_amdgcn_s_sleep(SL4);
      }
      if (lane < (CLR_MODE == 1 ? 2 : 1)) st2c(ws, lane == 0 ? WS_HRDY + blk * 32 + wave * 8 : isG ? WS_CRDY + (tile * 4 + (grp & 3)) * 256 + (ct * 4 + wave) * 8 : OOB, v2u{tag, ~tag});
      STAMP(12);
#pragma unroll
      for (int f = 0; f < WD_EARLY; ++f) wdn[f >> 2][f & 3] = ld4c(ws, (isG ? wl16 : OOB) + (f & 3) * 1024, wdb + (f >> 2) * 4096);
      sb();
#pragma unroll
      for (int r = 0; r < 2; ++r) {
        const float sc = m0_[r] < L_M2 + 1792 ? 2.f / (float)(S * D) : 1.f;
        *(LAS float*)(L + m0_[r]) = blo(mv[r].x) * sc;
        *(LAS float*)(L + m1_[r]) = bhi(mv[r].x) * sc;
        *(LAS float*)(L + m1_[r] + md_[r]) = blo(mv[r].z) * sc;
        *(LAS float*)(L + m1_[r] + 2 * md_[r]) = bhi(mv[r].z) * sc;
      }
    }
    bar();
    *(LAS v4u*)(L + L_AF + (tid >> 6) * 4096 + 3072 + l16) = v4u{0u, 0u, 0u, 0u};
    STAMP(4);
    int zr = L_AF + lane * 8, ro = L_M2 + (t0 * 64 + 4 * g) * 4, ro1 = wave == 0 ? ro : ro + 256, ex = L_EX + c16 * 8;
    pin(zr); pin(ro); pin(ro1); pin(ex);
    {
      int zl[4];
#pragma unroll
      for (int m = 0; m < 4; ++m) {
        const int kq = (m >> 1) * 128 + (to[m] == OOB ? 127 : wave == 0 && m == 1 ? 112 + c16 : 16 * (t0 + (m & 1)) + c16);
        zl[m] = L_AF + ((kq & ~31) + ((kq & 4) << 2) + ((kq & 24) >> 1) + (kq & 3)) * 32 + g * 8;
        pin(zl[m]);
        if (to[m] == OOB) xs[m] = sv;
      }
      bar();
      if (isG) wait_flags(ws, hro, tag);
      {
        const int src = (blk * 5 + wave * 13) & 63;
        const int po = __builtin_amdgcn_readlane(to[(0)], 0) + (src & 15) * 64 + (src >> 4) * 16;
        const u32 old = __builtin_amdgcn_ds_bpermute(src * 4, xs[0].x);
        _Pragma("nounroll") for (int s = 0; s < SPIN; ++s) {
          if (wall(ld1c(ws, fresh(po)) != old)) break;
          __builtin_amdgcn_s_sleep(SL2);
        }
      }
      STAMP(13);
      v4u z[4];
      _Pragma("nounroll") for (int s = 0; s < SPIN; ++s) {
        u32 mn = 1;
#pragma unroll
        for (int m = 0; m < 4; ++m) z[m] = ld4c(ws, fresh(to[m]));
#pragma unroll
        for (int m = 0; m < 4; ++m) { const v4u d = z[m] ^ xs[m]; mn = min(min(mn, d.x), min(min(d.y, d.z), d.w)); }
        if (wall(mn != 0)) break;
      }
      STAMP(14);
#pragma unroll
      for (int m = 0; m < 4; ++m) {
        char LAS* p = L + zl[m];
        *(LAS v2u*)(p) = v2u{__builtin_amdgcn_perm(z[m].y, z[m].x, 0x05040100u), __builtin_amdgcn_perm(z[m].w, z[m].z, 0x05040100u)};
        *(LAS v2u*)(p + ZPL) = v2u{__builtin_amdgcn_perm(z[m].y, z[m].x, 0x07060302u), __builtin_amdgcn_perm(z[m].w, z[m].z, 0x07060302u)};
      }
    }
    bar();
    STAMP(5);
    {
      v4u zf[4][4];
#pragma unroll
      for (int ty = 0; ty < 4; ++ty)
#pragma unroll
        for (int s = 0; s < 4; ++s) zf[ty][s] = ldtr2(L + zr, ty * 4096 + s * 1024, ty * 4096 + s * 1024 + 512);
      v4f acc[2][4], x2[2][4], gav[2][4];
      v2f s1 = {0.f, 0.f}, s2 = {0.f, 0.f};
#define P3M(t, ty) mf8(acc[t][ty], acc[t][ty + 1], cf[4 * t + ty], zf[ty], cf[4 * t + ty + 1], zf[ty + 1]);
#define P3X(t, m, yv) { x2[t][m] = fma4(gav[t][m], yv, xo[t][m]); \
      if (t == 0 || wave != 0) { s1 += lo2(x2[t][m]) + hi2(x2[t][m]); \
        s2 = fma2(lo2(x2[t][m]), lo2(x2[t][m]), s2); s2 = fma2(hi2(x2[t][m]), hi2(x2[t][m]), s2); } }
      P3M(1, 0) P3M(1, 2)
#pragma unroll
      for (int m = 0; m < 4; ++m) { gav[0][m] = lds4f(L + ro, m * 64); gav[1][m] = lds4f(L + ro1, m * 64); }
      sb();
      P3M(0, 0) P3M(0, 2)
      mfence<0>(acc[1][0], acc[1][1], acc[1][2], acc[1][3]);
      const v4f A1 = acc[1][0] + acc[1][1], B1 = acc[1][0] - acc[1][1], P1 = acc[1][3] - acc[1][2], Q1 = acc[1][2] + acc[1][3];
      P3X(1, 0, A1 - P1) P3X(1, 1, A1 + P1) P3X(1, 2, B1 - Q1) P3X(1, 3, B1 + Q1)
      sb();
      mfence<0>(acc[0][0], acc[0][1], acc[0][2], acc[0][3]);
      {
        const v4f A = acc[0][0] + acc[0][1], B = acc[0][0] - acc[0][1], P = acc[0][3] - acc[0][2], Q = acc[0][2] + acc[0][3];
        v4f y[4] = {A - P, A + P, B - Q, B + Q};
        const bool sp = tid < 16;
        y[0].x = sp ? A.x : y[0].x; y[2].x = sp ? B.x : y[2].x;
        y[1].x = sp ? acc[1][0].x - acc[0][3].x : y[1].x; y[3].x = sp ? acc[1][0].x + acc[0][3].x : y[3].x;
        P3X(0, 0, y[0]) P3X(0, 1, y[1]) P3X(0, 2, y[2]) P3X(0, 3, y[3])
      }
      float sm = s1.x + s1.y, sq = s2.x + s2.y;
      sumg2(sm, sq);
      if (g == 0) *(LAS v2f*)(L + ex + wave * 128) = v2f{sm, sq};
      v4f sbv[2][4], cbv[2][4];
#pragma unroll
      for (int m = 0; m < 4; ++m) {
        sbv[0][m] = lds4f(L + ro, 1792 + m * 64); cbv[0][m] = lds4f(L + ro, 2 * 1792 + m * 64);
        sbv[1][m] = lds4f(L + ro1, 1792 + m * 64); cbv[1][m] = lds4f(L + ro1, 2 * 1792 + m * 64);
      }
      bar();
      v2f e = *(const LAS v2f*)(L + ex);
#pragma unroll
      for (int w = 1; w < 4; ++w) e += *(const LAS v2f*)(L + ex + w * 128);
      const float mc = e.x * (1.f / D), rstd = __builtin_amdgcn_rsqf(fmaxf(e.y * (1.f / D) - mc * mc, 0.f) + 1e-5f), nb = -mc * rstd;
      const int ho = WS_H2 + blk * 14336 + 2 * t0 * 1024 + l16, ho1 = wave == 0 ? OOB : ho + 2048;
#pragma unroll
      for (int t = 0; t < 2; ++t)
#pragma unroll
        for (int a = 0; a < 2; ++a) {
          v4u o;
#pragma unroll
          for (int hh = 0; hh < 2; ++hh) {
            const int m = 2 * a + hh;
            const v4f hv = fma4(fma4(x2[t][m], splat(rstd), splat(nb)), cbv[t][m], sbv[t][m]);
            o[2 * hh] = pk2(lo2(hv)); o[2 * hh + 1] = pk2(hi2(hv));
          }
          st4c(ws, (t ? ho1 : ho) + a * 1024, o);
        }
      STAMP(6);
      sb();
#pragma unroll
      for (int f = WD_EARLY; f < WD_MID; ++f) wdn[f >> 2][f & 3] = ld4c(ws, (isG ? wl16 : OOB) + (f & 3) * 1024, wdb + (f >> 2) * 4096);
      sb();
#pragma unroll
      for (int t = 0; t < 2; ++t)
        if (t == 0 || wave != 0)
#pragma unroll
          for (int m = 0; m < 4; ++m)
#pragma unroll
            for (int r = 0; r < 4; r += 2) {
              const u32 p = pk(x2[t][m][r] + st.x, x2[t][m][r + 1] + st.x);
              *(LAS u16*)(L + XOFF(t, m, r)) = (u16)p;
              *(LAS u16*)(L + XOFF(t, m, r + 1)) = (u16)(p >> 16);
            }
      sb();
    }
    bar();
    x2_read(L, x2r, tid);
    bar();
    if (!isG) {
      x2_store(ws, x2r, blk, tid);
      drain();
      if (lane == 0) st2c(ws, WS_XRDY + blk * 64 + wave * 8, v2u{tag, ~tag});
      DUMP(); return;
    }
  } else {
    const int sj = j >= 30 ? j - 30 : j + 2;
    const int idx = sj * 8 + grp;
    const bool isC = j < 14 || idx == 0;
    const int pair = j < 14 ? grp * 14 + j : 112;
    const int lc = grp + 8 * (sj >> 1);
    const int qu = sj < 4 && lc < 14 ? 2 * lc + (sj & 1) : -1;
    const int t1o = WS_T1 + (32 * wave + (lane >> 3)) * T1_BLK + pair * 128 + (lane & 7) * 16;
    ModLoad ml;
    epoch = ld1c(ws, WS_EPOCH);
    if (qu >= 0) mod_load(ml, cin, wm, qu, tid);
#pragma unroll
    for (int q = 0; q < 4; ++q) st4c(ws, isC ? t1o + 8 * q * T1_BLK : OOB, sv);
    u32 pf0, pf1, pf2;
    {
      const rsrc_t mr = mkrs(wm, D * 6 * D * 2), xr = mkrs(xin, S * D * 2), fr_ = mkrs((wave & 1) ? wi : wr, S * DF * 2);
      const int row = (sj * 4 + wave) * 7 + (lane >> 1), l7 = lane / 7;
      pf0 = __builtin_amdgcn_raw_buffer_load_b32(mr, lane < 14 && (grp < 6 || !(lane & 1)) ? row * (6 * D * 2) + (grp + 8 * (lane & 1)) * 128 : OOB, 0, PF_AUX);
      pf1 = __builtin_amdgcn_raw_buffer_load_b32(xr, lane < (!XBAL ? 28 : sj < 4 ? 0 : sj < 12 ? 28 : 56) ? tok_row(16 * grp + (lane < 28 ? sj : sj - 12), 4 * wave + (l7 & 3)) * (D * 2) + (lane - 7 * l7) * 128 : OOB, 0, PF_AUX);
    }
    sb();
    epoch = __builtin_amdgcn_readfirstlane(epoch);
    tag = mktag(epoch);
    if (qu >= 0) mod_part(ml, L, tid);
    asm volatile("s_waitcnt vmcnt(%0)" :: "i"(W_LATE ? 2 : 3) : "memory");
    { const rsrc_t fr_ = mkrs((wave & 1) ? wi : wr, S * DF * 2);
      pf2 = __builtin_amdgcn_raw_buffer_load_b32(fr_, (lane & 31) < 29 && lane < (!XBAL ? 32 : sj < 4 ? 0 : sj < 12 ? 32 : 64) ? (((16 * grp + (lane < 32 ? sj : sj - 12)) * 7200) & ~127) + ((wave >> 1) * 29 + (lane & 31)) * 128 : OOB, 0, PF_AUX);
    }
    bar();
    if (wave == 0 && qu >= 0) mod_publish(L, ws, qu, lane, tag);
    if (wave == 1 && lane < 8) st2c(ws, isC ? WS_RDY + lane * 1024 + pair * 8 : OOB, v2u{tag, ~tag});
    STAMP(1);
    pin(pf0); pin(pf1); pin(pf2); pin(pfa); pin(pfb);
    const int qn = sj >= 4 && sj < 10 && grp < 7 ? 28 + grp * 6 + (sj - 4) : !isC && idx < 15 ? 69 + idx : -1;
    const int tw = sj >= 10 ? (sj - 10) * 8 + grp : sj == 4 && grp == 7 ? 48 : -1;
    v2u dw[2][8];
    if (qn >= 0) mod_load(ml, cin, wm, qn, tid);
    if (tw >= 0) {
      const rsrc_t dr = mkrs(wd, F * D * 2);
      const int u0 = tw * 4 + wave, uc = u0 / 28, kap = 2 * (u0 - uc * 28);
#pragma unroll
      for (int e = 0; e < 16; ++e)
        dw[e >> 3][e & 7] = __builtin_amdgcn_raw_buffer_load_b64(dr, (4 * g * D + 4 * c16) * 2 + (e & 3) * D * 2, ((32 * kap + 16 * (e >> 2)) * D + 64 * uc) * 2, PF_AUX);
    }
    sb();
    u32 pw[4];
    {
      const rsrc_t rg = mkrs(wg, D * F * 2), ru = mkrs(wu, D * F * 2);
      const int r7 = lane / 7, r0 = W1EVEN ? (j * 4 + wave) * 8 : sj < 4 ? (sj * 4 + wave) * 16 : 256 + ((sj - 4) * 4 + wave) * 4;
      const int po = (r0 + r7) * (F * 2) + ((grp & 3) * 7 + lane - r7 * 7) * 128;
      const int o0 = lane < (W1EVEN ? (j < 14 ? 56 : 0) : sj < 4 ? 56 : 28) ? po : OOB, o1 = !W1EVEN && lane < 56 && sj < 4 ? po + 8 * (F * 2) : OOB;
      pw[0] = ld1(rg, o0); pw[1] = ld1(ru, o0); pw[2] = ld1(rg, o1); pw[3] = ld1(ru, o1);
    }
    sb();
    STAMP(20);
    v4f t1c[2], t1s[2], t2c[2], t2s[2];
#pragma unroll
    for (int t = 0; t < 2; ++t) {
#pragma unroll
      for (int r = 0; r < 4; ++r) {
        const float a1 = (float)((16 * t + 4 * g + r) * (16 * wave + c16)) * (1.f / S);
        const float a2 = (float)((16 * t + c16) * (16 * wave + 4 * g + r)) * (1.f / S);
        t1c[t][r] = __builtin_amdgcn_cosf(a1); t1s[t][r] = __builtin_amdgcn_sinf(a1);
        t2c[t][r] = __builtin_amdgcn_cosf(a2); t2s[t][r] = __builtin_amdgcn_sinf(a2);
      }
      pin(t1c[t]); pin(t1s[t]); pin(t2c[t]); pin(t2s[t]);
    }
    if (tw >= 0) {
#pragma unroll
      for (int tt = 0; tt < 8; ++tt) {
        v4u o;
#pragma unroll
        for (int m = 0; m < 4; ++m)
          o[m] = __builtin_amdgcn_perm(dw[tt >> 2][2 * m + 1][(tt & 3) >> 1], dw[tt >> 2][2 * m][(tt & 3) >> 1], (tt & 1) ? 0x07060302u : 0x05040100u);
        st4c(ws, WS_WD + (tw * 4 + wave) * 8192 + tt * 1024 + l16, o);
      }
    }
    if (qn >= 0) {
      bar();
      mod_part(ml, L, tid);
      bar();
      if (wave == 0) mod_publish(L, ws, qn, lane, tag);
    }
    pin(pw[0]); pin(pw[1]); pin(pw[2]); pin(pw[3]);
    STAMP(21);
    if (!isC) { DUMP(); return; }
    v4u c64[2][4], c32[2][2][2];
#pragma unroll
    for (int cp = 0; cp < 2; ++cp)
#pragma unroll
      for (int s = 0; s < 4; ++s) c64[cp][s] = ld4(tab, l16 + s * 1024, (TAB_P2 + wave * 2 + cp) * 4096);
#pragma unroll
    for (int t = 0; t < 2; ++t)
#pragma unroll
      for (int f = 0; f < 4; ++f) c32[t][f >> 1][f & 1] = ld4(tab, l16 + f * 1024, (TAB_P2 + 8 + t) * 4096);
    sb();
    if (tw >= 0) {
      asm volatile("s_waitcnt vmcnt(16)" ::: "memory");
      bar();
      if (tid < 8) st2c(ws, WS_DRDY + tid * 1024 + tw * 8, v2u{tag, ~tag});
    }
    STAMP(2);
    int wfo = WS_WT + pair * 16384 + (16 * g + wave) * 128 + c16 * 8;
    pin(wfo);
    v2u wf[2][4];
    {
      int wl = L_CI + ((lane >> 2) & 1) * 8256 + wave * 2048 + ((lane >> 3) * 4 + (lane & 3)) * 16, lo = t1o;
      pin(wl); pin(lo);
      light_poll(ws, WS_T1 + (32 * wave + (pair & 31)) * T1_BLK + pair * 128);
      STAMP(13);
      v4u z[4];
      _Pragma("nounroll") for (int s = 0; s < SPIN; ++s) {
        lo = fresh(lo);
#pragma unroll
        for (int q = 0; q < 4; ++q) z[q] = ld4c(ws, lo, 8 * q * T1_BLK);
        if (wall(max(max(max4(z[0]), max4(z[1])), max(max4(z[2]), max4(z[3]))) != SENT)) break;
      }
      STAMP(14);
#pragma unroll
      for (int q = 0; q < 4; ++q) *(LAS v4u*)(L + wl + q * 512) = z[q];
    }
    bar();
#pragma unroll
    for (int t = 0; t < 2; ++t)
#pragma unroll
      for (int r = 0; r < 4; ++r) wf[t][r] = ld2c(ws, wfo + r * 512, t * 8192);
    v2u fd = ld2c(ws, WS_DRDY + grp * 1024 + min(lane, 48) * 8);
    sb();
    STAMP(4);
    {
      v4u a[2][2][4];
#pragma unroll
      for (int c = 0; c < 2; ++c)
#pragma unroll
        for (int mt = 0; mt < 2; ++mt)
#pragma unroll
          for (int s = 0; s < 4; ++s) a[c][mt][s] = lds4(LB, L_CI + c * 8256 + (s * 2 + mt) * 1024);
      v4u old[2][2];
#pragma unroll
      for (int c = 0; c < 2; ++c)
#pragma unroll
        for (int mt = 0; mt < 2; ++mt) old[c][mt] = lds4(LB + wave * 2048, L_CI + c * 8256 + mt * 1024);
      v4f re[2][2], im[2][2], ur[2][2], ui[2][2], fr_[2][2], fi_[2][2];
      u32 f2[2][2][4], f3[2][2][4], f4[4];
      char LAS* xw = L + lb + wave * 1024 - L_B;
#define V4(x) v4u{x[0], x[1], x[2], x[3]}
#define S1K(h) c64[0][2 * h], c64[0][2 * h + 1], c64[1][2 * h], c64[1][2 * h + 1]
#define S1X(c, t, h) re[c][t], im[c][t], a[c][t][2 * h], a[c][t][2 * h + 1], S1K(h)
#define S2X(c, t) ur[c][t], ui[c][t], V4(f2[c][0]), V4(f2[c][1]), c32[t][0][0], c32[t][0][1], c32[t][1][0], c32[t][1][1]
#define S3X(c, t) im[c][t], re[c][t], V4(f3[c][0]), V4(f3[c][1]), c32[t][0][0], c32[t][0][1], c32[t][1][0], c32[t][1][1]
#define CM(o, P, Q, C, S_, h) o[2 * h], o[2 * h + 1], P[2 * h], Q[2 * h], C[2 * h], S_[2 * h], P[2 * h + 1], Q[2 * h + 1], C[2 * h + 1], S_[2 * h + 1]
#define TW1(c, t, h) CM(f2[c][t], re[c][t], im[c][t], t1c[t], t1s[t], h)
#define FLT(c, t, h) CM(f3[c][t], ur[c][t], ui[c][t], fr_[c][t], fi_[c][t], h)
#define TW2(c, t, h) CM(f4, re[c][t], im[c][t], t2c[t], t2s[t], h)
#define HV(x, h) (h ? hi2(x) : lo2(x))
#define CP(PL, o, P, Q, C, S_, h) cmul2<PL>(o[2 * h], o[2 * h + 1], HV(P, h), HV(Q, h), HV(C, h), HV(S_, h));
#define XW(c, t) *(LAS v4u*)(xw + L_CX + (c * 2 + t) * 4096) = V4(f4);
#define UNP(c, t) { _Pragma("unroll") for (int r = 0; r < 4; ++r) { fr_[c][t][r] = blo(wf[t][r][c]); fi_[c][t][r] = bhi(wf[t][r][c]); } \
      pin(fr_[c][t]); pin(fi_[c][t]); }
      nop2();
      m4_a0(S1X(0, 0, 0)); m4_a1(S1X(0, 0, 1)); m4_a0(S1X(0, 1, 0)); mfence<3>(re[0][0], im[0][0]);
      h4_a1m(S1X(0, 1, 1), TW1(0, 0, 0)); h4_a0m(S1X(1, 0, 0), TW1(0, 0, 1));
      h4_a1m(S1X(1, 0, 1), TW1(0, 1, 0)); h4_a0m(S1X(1, 1, 0), TW1(0, 1, 1));
      h4_a1m(S1X(1, 1, 1), TW1(1, 0, 0)); h4_b0m(S2X(0, 0), TW1(1, 0, 1));
      h4_b0m(S2X(0, 1), TW1(1, 1, 0));
      sb();
      CP(false, f2[1][1], re[1][1], im[1][1], t1c[1], t1s[1], 1)
      UNP(0, 0) UNP(0, 1)
      sb();
      h4_b0p(S2X(1, 0), FLT(0, 0, 0)); h4_b0p(S2X(1, 1), FLT(0, 0, 1));
      sb();
      CP(true, f3[0][1], ur[0][1], ui[0][1], fr_[0][1], fi_[0][1], 0) CP(true, f3[0][1], ur[0][1], ui[0][1], fr_[0][1], fi_[0][1], 1)
      UNP(1, 0) UNP(1, 1)
      sb();
      h4_a0p(S3X(0, 0), FLT(1, 0, 0)); h4_a0p(S3X(0, 1), FLT(1, 0, 1));
      sb();
      CP(true, f3[1][1], ur[1][1], ui[1][1], fr_[1][1], fi_[1][1], 0) CP(true, f3[1][1], ur[1][1], ui[1][1], fr_[1][1], fi_[1][1], 1)
#pragma unroll
      for (int e = 0; e < 4; ++e) pin(f3[1][1][e]);
      nop2();
      h4_a0p(S3X(1, 0), TW2(0, 0, 0)); h4_a0p(S3X(1, 1), TW2(0, 0, 1));
      XW(0, 0)
      sb();
      CP(true, f4, re[0][1], im[0][1], t2c[1], t2s[1], 0) CP(true, f4, re[0][1], im[0][1], t2c[1], t2s[1], 1) XW(0, 1)
      sb();
      mfence<0>(re[1][0], im[1][0], re[1][1], im[1][1]);
      CP(true, f4, re[1][0], im[1][0], t2c[0], t2s[0], 0) CP(true, f4, re[1][0], im[1][0], t2c[0], t2s[0], 1) XW(1, 0)
      CP(true, f4, re[1][1], im[1][1], t2c[1], t2s[1], 0) CP(true, f4, re[1][1], im[1][1], t2c[1], t2s[1], 1) XW(1, 1)
      bar();
#pragma unroll
      for (int c = 0; c < 2; ++c)
#pragma unroll
        for (int nt = 0; nt < 2; ++nt)
#pragma unroll
          for (int s = 0; s < 4; ++s) a[c][nt][s] = lds4(LB, L_CX + ((c * 2 + nt) * 4 + s) * 1024);
      const int o2 = WS_T1 + (32 * wave + (lane >> 2)) * T1_BLK + pair * 128 + (lane & 3) * 16;
#define S4(c, t) mf8(im[c][t], re[c][t], c64[0], a[c][t], c64[1], a[c][t]);
#define NEQ(x, y) ((x) == (y) ? (x) ^ 1u : (x))
#define OUT(c, t) { const v4u o = {pk(re[c][t].x, im[c][t].x), pk(re[c][t].y, im[c][t].y), pk(re[c][t].z, im[c][t].z), pk(re[c][t].w, im[c][t].w)}; \
      st4c(ws, o2 + c * 64 + t * 16 * T1_BLK, v4u{NEQ(o.x, old[c][t].x), NEQ(o.y, old[c][t].y), NEQ(o.z, old[c][t].z), NEQ(o.w, old[c][t].w)}); sb(); }
      S4(0, 0) S4(0, 1) mfence<0>(re[0][0], im[0][0]); OUT(0, 0)
      S4(1, 0) mfence<0>(re[0][1], im[0][1]); OUT(0, 1)
      S4(1, 1) mfence<0>(re[1][0], im[1][0]); OUT(1, 0)
      mfence<0>(re[1][1], im[1][1]); OUT(1, 1)
    }
    STAMP(5);
    sb();
    if (!isG) { DUMP(); return; }
    if (!wall(fd.x == tag && fd.y == ~tag)) wait_flags(ws, WS_DRDY + grp * 1024 + min(lane, 48) * 8, tag);
    dma_w1(wg, wu, slice, wave, lane, lbase);
#pragma unroll
    for (int f = 0; f < WD_MID; ++f) wdn[f >> 2][f & 3] = ld4c(ws, wl16 + (f & 3) * 1024, wdb + (f >> 2) * 4096);
    fd = ld2c(ws, hro);
    sb();
    ACT_CLEAR(0, 8)
    sb();
    if (!wall(fd.x == tag && fd.y == ~tag)) wait_flags(ws, hro, tag);
    bar();
  }

  STAMP(7);
  const int blk0 = 16 * tile + 4 * (grp & 3);
  const int ab = WS_ACT + blk0 * 57344 + wave * 14 * 1024;
  v4u h[14][2];
  {
    const int hb = WS_H2 + (16 * tile + 4 * wave) * 14336;
    int tb[2][4];
#pragma unroll
    for (int u = 0; u < 8; ++u) {
      tb[u >> 2][u & 3] = (((lane >> 2) * 128 + ((lane >> 3) & 3) * 32 + (lane & 3) * 8) ^ ((u & 3) << 5)) + (u >> 2) * 57344;
      pin(tb[u >> 2][u & 3]);
    }
#define W_RD(dst, k, u) dst = ldtr2(L + tb[k >= 7][u & 3], (k % 7) * 8192 + (u >> 2) * 4096, (k % 7) * 8192 + (u >> 2) * 4096 + 2048)
#define H_LD(v, tt) hh_[v][tt] = ld4c(ws, l16 + (((v) % 14) & 3) * 1024, hb + (2 * ((v) / 14) + (tt)) * 14336 + (((v) % 14) >> 2) * 4096)
    const int abx[2] = {ab + (wave >> 1) * 57344, ab + (1 - (wave >> 1)) * 57344};
#define G_LD(e) h[(e) >> 1][(e) & 1] = ld4c(ws, l16 + (((e) >> 1) & 3) * 1024, abx[(e) & 1] + ((e) >> 3) * 4096)
    v4u wfr[2][8], hh_[28][2];
#pragma unroll
    for (int u = 0; u < 8; ++u) W_RD(wfr[0][u], 0, u);
    light_poll(ws, hb + (lane & 1) * 14336 + 13 * 1024);
    STAMP(15);
#pragma unroll
    for (int v = 0; v < LEAD; ++v) { H_LD(v, 0); H_LD(v, 1); }
    sb();
    if (!isR && lane < (CLR_MODE == 1 ? 1 : 2))
      st2c(ws, WS_CRDY + ((tile + 2 * lane) * 4 + (grp & 3)) * 256 + (ct * 4 + wave) * 8, v2u{tag, ~tag});
    sb();
#define BAD2(x0, x1) wany(max(max(max(max(x0.x, x0.y), x0.z), max(max(x0.w, x1.x), x1.y)), max(x1.z, x1.w)) == SENT)
#define RETRY2(x0, x1, o0, o1) \
      _Pragma("nounroll") for (int s_ = 0; s_ < SPIN; ++s_) { \
        const int lf_ = fresh(l16); SLOWCNT \
        x0 = ld4c(ws, lf_, o0); x1 = ld4c(ws, lf_, o1); \
        if (wall(max(max4(x0), max4(x1)) != SENT)) break; \
      }
    if (__builtin_expect(BAD2(hh_[0][0], hh_[0][1]), 0)) { RETRY2(hh_[0][0], hh_[0][1], hb, hb + 14336) }
    STAMP(8); CSTAMP(28);
    v4f acc[8][2];
    v2u fa, fb;
    const int fy = lane < 8 ? WS_ARDY + (tile * 4 + (grp & 3)) * 512 + (7 * wave + min(lane, 6)) * 8 : WS_XRDY + (blk0 + (wave >> 1) + (lane & 4 ? 2 : 0)) * 64 + (lane & 3) * 8;
    const int fx = WS_CRDY + (tile * 4 + (lane < 32 ? wave : grp & 3)) * 256 + min(lane & 31, 27) * 8;
    const int ao = WS_ACT + (16 * tile + 4 * wave) * 57344 + 2 * slice * 1024 + l16;
#define ACT_OUT(ps, tt, f) { v4u o; \
      _Pragma("unroll") for (int hh = 0; hh < 2; ++hh) { \
        const v4f av = relu4(acc[2 * f + hh][tt]) * acc[4 + 2 * f + hh][tt]; \
        o[2 * hh] = pk2(lo2(av)); o[2 * hh + 1] = pk2(hi2(av)); } \
      st4c(ws, ao + (2 * ps + tt) * 57344 + f * 1024, o); }
#define ACT_ALL(ps) { mfence<0>(acc[0][0], acc[1][0], acc[2][0], acc[3][0]); mfence<0>(acc[4][0], acc[5][0], acc[6][0], acc[7][0]); \
      ACT_OUT(ps, 0, 0) ACT_OUT(ps, 0, 1) sb(); \
      mfence<0>(acc[0][1], acc[1][1], acc[2][1], acc[3][1]); mfence<0>(acc[4][1], acc[5][1], acc[6][1], acc[7][1]); \
      ACT_OUT(ps, 1, 0) ACT_OUT(ps, 1, 1) sb(); }
#define W_RH(dst, k, u, hf) { const v2u t_ = ldtr(L + tb[k >= 7][u & 3], (k % 7) * 8192 + (u >> 2) * 4096 + hf * 2048); \
      dst[2 * hf] = t_.x; dst[2 * hf + 1] = t_.y; }
#pragma unroll
    for (int ps = 0; ps < 2; ++ps)
#pragma unroll
    for (int ks = 0; ks < 14; ++ks) {
      const int v = ps * 14 + ks, cur = v & 1, nk = (v + 1) % 14, vn = v < 27 ? v + 1 : v;
      const bool more = v < 27;
      u32 m0, m1;
      __builtin_amdgcn_s_waitcnt(0xC07F);
      sb();
#pragma unroll
      for (int i = 0; i < 16; ++i) {
        const int u = i & 7, tt = i >> 3;
        if (ks == 0) mv0(acc[u][tt], wfr[cur][u], hh_[v][tt]); else mv(acc[u][tt], wfr[cur][u], hh_[v][tt]);
        if (more) {
          if (i < 4) { W_RH(wfr[cur ^ 1][i], nk, i, 0) W_RH(wfr[cur ^ 1][i], nk, i, 1) }
          else if (i < 12) W_RH(wfr[cur ^ 1][4 + ((i - 4) >> 1)], nk, (4 + ((i - 4) >> 1)), (i & 1))
        }
        if (i == 5 && v + LEAD < 28) H_LD(v + LEAD, 0);
        if (i == 7 && v + LEAD < 28) H_LD(v + LEAD, 1);
        if (i == 9 || i == 11 || i == 13) {
          const int e = (i - 9) >> 1, f = WD_MID + (v < 14 ? v - 2 : 12 + WD_PER * (v - 14) + e);
          if (f < 56 && ((v >= 2 && v < 14 && e == 0) || (v >= 14 && e < WD_PER))) wdn[f >> 2][f & 3] = ld4c(ws, wl16 + (f & 3) * 1024, wdb + (f >> 2) * 4096);
        }
        if (v > A_V + 1 && (i & 1) && i >= 5 && ((i - 5) >> 1) < A_PER) G_LD(A_PER * (v - A_V - 2) + ((i - 5) >> 1));
        if (v == 16 && i == 15) fa = ld2c(ws, fx);
        if (v == A_V && i == 15) fb = ld2c(ws, fy);
        if (more && i == 12) { m0 = max(max(hh_[vn][0].x, hh_[vn][0].y), hh_[vn][0].z); pin(m0); }
        if (more && i == 13) { m1 = max(max(hh_[vn][0].w, hh_[vn][1].x), hh_[vn][1].y); pin(m1); }
        if (more && i == 14) { m0 = max(max(m0, hh_[vn][1].z), hh_[vn][1].w); m0 = max(m0, m1); pin(m0); }
        sb();
      }
      if (v == 20 && !wall(fa.x == tag && fa.y == ~tag)) wait_flags(ws, fx, tag);
      if (v == F_V) {
        static_assert(F_V + LEAD < 28, "the count below");
        asm volatile("s_waitcnt vmcnt(%0)" :: "i"(2 * (F_V - 13)) : "memory");
        if (lane < 2) st2c(ws, lane == 0 ? WS_ARDY + (tile * 4 + wave) * 512 + slice * 8 : isR ? WS_XRDY + (16 * grp + j - 14) * 64 + wave * 8 : OOB, v2u{tag, ~tag});
      }
      if (v == A_V + 1 && !wall(fb.x == tag && fb.y == ~tag)) wait_flags(ws, fy, tag);
      if (more && __builtin_expect(wany(m0 == SENT), 0)) {
        const int pb = 2 * ((v + 1) / 14) * 14336 + nk * 1024;
        RETRY2(hh_[vn][0], hh_[vn][1], hb + pb, hb + pb + 14336)
      }
      if (v == 13) {
        STAMP(16); CSTAMP(29);
        ACT_ALL(0)
        if (isR) x2_store(ws, x2r, 16 * grp + j - 14, tid);
        nop2();
      }
      sb();
    }
    STAMP(27);
#pragma unroll
    for (int e = A_PER * (26 - A_V); e < A_PRE; ++e) G_LD(e);
    sb();
    ACT_ALL(1)
    CSTAMP(30);
#pragma unroll
    for (int e = A_PRE; e < 28; ++e) G_LD(e);
    sb();
    STAMP(9);
    v4u a[14][2];
    v4f ac2[4][4];
#define A_LD(e) a[(e) >> 1][(e) & 1] = ld4c(ws, l16 + (((e) >> 1) & 3) * 1024, abx[(e) & 1] + 2 * 57344 + ((e) >> 3) * 4096)
    const int po = ab + 3 * 57344 + min(lane, 6) * 2048 + 1024;
    u32 probe;
#pragma unroll
    for (int ks = 0; ks < 14; ++ks) {
#pragma unroll
      for (int tt = 0; tt < 2; ++tt)
#pragma unroll
        for (int n = 0; n < 4; ++n) ac2[tt][n] = mfma(wdn[ks][n], h[ks][tt], ks ? ac2[tt][n] : splat(0.f));
      if (ks == P_AT) probe = ld1c(ws, po);
      sb();
      if (ks == B_AT2) {
        STAMP(25);
        if (wany(probe == SENT)) light_poll(ws, po);
        STAMP(26);
      }
      if (ks >= B_AT2) {
#pragma unroll
        for (int e = 4 * (ks - B_AT2); e < 4 * (ks - B_AT2 + 1); ++e) A_LD(e);
      }
      sb();
    }
    STAMP(17);
#pragma unroll
    for (int e = 4 * (14 - B_AT2); e < 4 * (14 - B_AT2) + 6; ++e) A_LD(e);
    sb();
    int rw[3], rr_ = L_B + lane * 8 + wave * 6144, oo[2];
#pragma unroll
    for (int x = 1; x < 4; ++x) { rw[x - 1] = L_B + lane * 8 + (wave ^ x) * 6144 + (x - 1) * 2048; pin(rw[x - 1]); }
    pin(rr_);
    const int blk = blk0 + (wave >> 1);
#pragma unroll
    for (int ph = 0; ph < 2; ++ph) { oo[ph] = (tok_row(blk + 2 * ph, c16) * D + ct * 64 + g * 16 + (wave & 1) * 8) * 2; pin(oo[ph]); }
#define RED_W1(ph, x, n) *(LAS v2f*)(L + rw[(x) - 1] + (ph) * 24576 + (n) * 512) = ((x) & 1) ? hi2(ac2[2 * (ph) + ((x) >> 1)][n]) : lo2(ac2[2 * (ph) + ((x) >> 1)][n]);
    const int xo = WS_X2 + blk * 14336 + c16 * 896 + ct * 128 + g * 32 + (wave & 1) * 16;
    const int go = WS_MOD + grp * MOD_REP + (70 + 2 * ct) * 128 + g * 64 + (wave & 1) * 32;
    v4u xv[2], gv[2];
    STAMP(18);
    sb();
#pragma unroll
    for (int ks = 0; ks < 14; ++ks) {
      if (__builtin_expect(BAD2(a[ks][0], a[ks][1]), 0)) { RETRY2(a[ks][0], a[ks][1], abx[0] + 2 * 57344 + ks * 1024, abx[1] + 2 * 57344 + ks * 1024) }
#pragma unroll
      for (int tt = 0; tt < 2; ++tt)
#pragma unroll
        for (int n = 0; n < 4; ++n) ac2[2 + tt][n] = mfma(wdn[ks][n], a[ks][tt], ks ? ac2[2 + tt][n] : splat(0.f));
#pragma unroll
      for (int e = 4 * (14 - B_AT2) + 6 + 3 * ks; e < 4 * (14 - B_AT2) + 9 + 3 * ks; ++e)
        if (e < 28) A_LD(e);
      if (ks == 6) { xv[0] = ld4c(ws, xo); xv[1] = ld4c(ws, xo + 2 * 14336); gv[0] = ld4c(ws, go); gv[1] = ld4c(ws, go + 16); }
      if (ks < 12) RED_W1(0, 1 + (ks >> 2), (ks & 3))
#pragma unroll
      for (int u = 0; u < 4; ++u) {
        __builtin_amdgcn_sched_group_barrier(0x8, 2, 0);
        __builtin_amdgcn_sched_group_barrier(0x20, 1, 0);
        if (u == 2) __builtin_amdgcn_sched_group_barrier(0x200, 1, 0);
      }
      sb();
    }
    STAMP(10);
#pragma unroll
    for (int e = 0; e < 12; ++e) RED_W1(1, 3 - (e >> 2), (e & 3))
#define FIN_OK() wall(gv[0].y == tag && gv[0].w == tag && gv[1].y == tag && gv[1].w == tag)
    if (__builtin_expect(!FIN_OK(), 0)) {
      _Pragma("nounroll") for (int s = 0; s < SPIN; ++s) {
        const int gf = fresh(go), xf = fresh(xo);
        gv[0] = ld4c(ws, gf); gv[1] = ld4c(ws, gf + 16); xv[0] = ld4c(ws, xf); xv[1] = ld4c(ws, xf + 2 * 14336);
        if (FIN_OK()) break;
      }
    }
    bar();
#pragma unroll
    for (int ph = 0; ph < 2; ++ph) {
      v2f r[4];
#pragma unroll
      for (int n = 0; n < 4; ++n) {
        r[n] = lo2(ac2[2 * ph][n]);
#pragma unroll
        for (int w = 0; w < 3; ++w) r[n] += *(const LAS v2f*)(L + rr_ + w * 2048 + ph * 24576 + n * 512);
      }
      v4u o;
#pragma unroll
      for (int q = 0; q < 2; ++q) {
        o[2 * q] = pk2(fma2(bun(gv[q].x), v2f{r[0][q], r[1][q]}, bun(xv[ph][2 * q])));
        o[2 * q + 1] = pk2(fma2(bun(gv[q].z), v2f{r[2][q], r[3][q]}, bun(xv[ph][2 * q + 1])));
      }
      __builtin_amdgcn_raw_buffer_store_b128(o, mkrs(out, S * D * 2), oo[ph], 0, OUT_AUX);
    }
    if (blockIdx.x == 0 && wave == 0) {
      _Pragma("nounroll") for (int s = 0; s < SPIN; ++s) {
        const v4u v = ld4c(ws, fresh(WS_MOD + (70 + (lane < 14 ? lane : 0)) * 128));
        if (wall(v.y == tag)) break;
      }
      if (lane == 0) __builtin_amdgcn_raw_buffer_store_b32(epoch + 1u, ws, WS_EPOCH, 0, SC1);
    }
  }
  STAMP(11);
  DUMP();
}

extern "C" void kernel_launch(void* const* d_in, const int* in_sizes, int n_in, void* d_out, int out_size, void* d_ws,
                              size_t ws_size, hipStream_t stream) {
  fused<<<256, 256, 0, stream>>>((const u16*)d_in[0], (const u16*)d_in[1], (const u16*)d_in[2], (const u16*)d_in[4],
                                 (const u16*)d_in[5], (const u16*)d_in[6], (const u16*)d_in[7], (const u16*)d_in[8],
                                 (u16*)d_out, (char*)d_ws);
}
```

```cpp
#include <hip/hip_runtime.h>

typedef unsigned short u16;
typedef unsigned int u32;
typedef unsigned long long u64;
typedef u32 v4u __attribute__((ext_vector_type(4)));
typedef u32 v2u __attribute__((ext_vector_type(2)));
typedef float v4f __attribute__((ext_vector_type(4)));
typedef float v2f __attribute__((ext_vector_type(2)));
typedef short v8s __attribute__((ext_vector_type(8)));
typedef short v4s __attribute__((ext_vector_type(4)));
typedef __bf16 v8b __attribute__((ext_vector_type(8)));
typedef __bf16 v2b __attribute__((ext_vector_type(2)));
#define LAS __attribute__((address_space(3)))
#define GAS __attribute__((address_space(1)))
#define DEV __device__ __forceinline__

#define C_W1_EARLY 0
#define CLR_DELAY 0
#define W_DELAY 0
#define CLR2_DELAY 0
#define PF_POS 2
#define SL1 1
#define SL2 2
#define SL4 4
#define X_AFTER 8
#define PF_AUX 2
#define W_LATE 1
#define W1_PF 1
#define DMA_MOD ""
#define LEAD 6
#define A_V 25
#define A_PER 6
#define WDV(f) ((f) - WD_MID + 2 < 14 ? (f) - WD_MID + 2 : ((f) - WD_MID - 12) / 2 + 16)
#define B_AT2 12
#define RE_PF 0
#define PF2_DELAY 0
#define C_DMA_EARLY 0
#define EVEN_PF 0
#define XBAL 1
#define W1EVEN 1
#define PRE_OUT 0
#define OUT_AUX 16
#define CLR_MODE 0
#define F_V 17
#define A_PRE 20
#define P_AT 7
#define WD_PER 3
#define A_FIRST 10
#define B_FIRST 10
#define A_AT 9
#define F_AT 4
#define CODE_BYTES 45056
#define WD_EARLY 16
#define WD_MID 24
#define B_AT 9
constexpr int S = 2048, D = 448, DF = 225, F = 1792;

struct alignas(16) Chain { u16 v[2048]; };
struct Cos448 { u16 v[448]; };
constexpr double c_cos_t(double a) {
  double a2 = a * a, t = 1.0, s = 1.0;
  for (int i = 1; i <= 10; ++i) { t = -t * a2 / ((2 * i - 1) * (2 * i)); s += t; }
  return s;
}
constexpr double c_sin_t(double a) {
  double a2 = a * a, t = a, s = a;
  for (int i = 1; i <= 10; ++i) { t = -t * a2 / ((2 * i) * (2 * i + 1)); s += t; }
  return s;
}
constexpr double c_cos448(int m) {
  if (m > 224) m = 448 - m;
  double sg = 1.0;
  if (m > 112) { m = 224 - m; sg = -1.0; }
  const double w = 6.283185307179586476925286766559 / 448.0;
  return m == 112 ? 0.0 : (m <= 56 ? sg * c_cos_t(w * m) : sg * c_sin_t(w * (112 - m)));
}
constexpr u16 c_bf16(double d) {
  const u32 u = __builtin_bit_cast(u32, (float)d);
  return (u16)((u + 0x7FFFu + ((u >> 16) & 1u)) >> 16);
}
constexpr Cos448 mk_cos() { Cos448 c{}; for (int m = 0; m < 448; ++m) c.v[m] = c_bf16(c_cos448(m)); return c; }
constexpr Cos448 CTAB = mk_cos();
constexpr u16 bcos(int m) { return CTAB.v[m % 448]; }
constexpr u16 bsin(int m) { return CTAB.v[(m % 448 + 336) % 448]; }
constexpr u16 bneg(u16 b) { return b ? (u16)(b ^ 0x8000u) : b; }
constexpr u16 bscale(u16 b, int e) { return b ? (u16)(b + e * 0x80) : b; }

constexpr Chain mk_p1(int c) {
  const int tile = c >> 2, par = c & 1, sn = (c >> 1) & 1;
  Chain ch{};
  for (int s = 0; s < 4; ++s)
    for (int l = 0; l < 64; ++l) {
      const int kp = 16 * tile + (l & 15), k = 2 * kp + par;
      if (k > 224) continue;
      for (int j = 0; j < 8; ++j) {
        const int dl = 32 * s + 8 * (l >> 4) + j;
        if (dl > 112) continue;
        u16 b = sn ? bneg(bsin(k * dl)) : bcos(k * dl);
        if (dl == 0 || dl == 112) b = bscale(b, -1);
        ch.v[(s * 64 + l) * 8 + j] = b;
      }
    }
  return ch;
}
constexpr Chain mk_p3(int c) {
  const int tile = c >> 1, sn = c & 1;
  Chain ch{};
  for (int s = 0; s < 4; ++s)
    for (int l = 0; l < 64; ++l) {
      int dl = 16 * tile + (l & 15);
      if (c == 1 && dl == 0) dl = 112;
      for (int j = 0; j < 8; ++j) {
        const int k = 2 * (32 * s + 8 * (l >> 4) + j) + 1;
        if (k < 224) ch.v[(s * 64 + l) * 8 + j] = sn ? bsin(k * dl) : bcos(k * dl);
      }
    }
  return ch;
}
constexpr Chain mk_p2(int c) {
  Chain ch{};
  for (int f = 0; f < 4; ++f)
    for (int l = 0; l < 64; ++l)
      for (int j = 0; j < 8; ++j) {
        const int comp = c < 8 ? (c & 1) : (f >> 1), s = c < 8 ? f : (f & 1);
        const int o = 16 * (c < 8 ? (c >> 1) : (c - 8)) + (l & 15), i = 16 * s + 4 * (l >> 4) + (j >> 1);
        const int m = (c < 8 ? 7 : 14) * ((i * o) % 64);
        u16 b = 0;
        if (comp == 0) b = (j & 1) ? bsin(m) : bcos(m);
        else b = (j & 1) ? bcos(m) : bneg(bsin(m));
        ch.v[(f * 64 + l) * 8 + j] = b;
      }
  return ch;
}
template <int I> constexpr Chain P1C = mk_p1(I);
template <int I> constexpr Chain P3C = mk_p3(I);
template <int I> constexpr Chain P2C = mk_p2(I);
#define L10(T, b) T<b>, T<b + 1>, T<b + 2>, T<b + 3>, T<b + 4>, T<b + 5>, T<b + 6>, T<b + 7>, T<b + 8>, T<b + 9>
#define L29(T) L10(T, 0), L10(T, 10), T<20>, T<21>, T<22>, T<23>, T<24>, T<25>, T<26>, T<27>, T<28>
constexpr int TAB_P1 = 0, TAB_P3 = 29, TAB_P2 = 43, TAB_N = 53;
static __device__ const Chain TAB[TAB_N] = {L29(P1C), L10(P3C, 0), P3C<10>, P3C<11>, P3C<12>, P3C<13>, L10(P2C, 0)};

constexpr int WS_EPOCH = 0;
constexpr int WS_RDY = 4096;
constexpr int WS_HRDY = 4096 + 8192;
constexpr int WS_DRDY = 4096 + 16384;
constexpr int WS_XRDY = 131072 + 65536;
constexpr int WS_ARDY = 131072;
constexpr int WS_CRDY = 131072 + 32768;
constexpr int WS_MOD = 32768, MOD_REP = 12288;
constexpr int T1_BLK = 226 * 64;
constexpr int WS_T1 = 1 << 18;
constexpr int WS_WT = WS_T1 + 128 * T1_BLK;
constexpr int WS_H2 = WS_T1, H2_BLK = T1_BLK;
constexpr int WS_X2 = WS_WT + 113 * 16384;
constexpr int WS_WD = WS_X2 + 128 * 14336;
constexpr int WS_ACT = WS_WD + 7 * 56 * 4096;
constexpr int WS_END = WS_ACT + 128 * 57344;
constexpr int WS_DBG = 32 << 20;

constexpr int L_W1 = 0;
constexpr int L_B = 114688;
constexpr int XP = 912;
constexpr int L_XS = L_B;
constexpr int L_ST = L_XS + 16 * XP;
constexpr int L_M1 = L_ST + 128;
constexpr int L_AF = L_M1 + 4 * 1792;
constexpr int L_M2 = L_AF + 16384;
constexpr int L_EX = L_M2 + 3 * 1792;
constexpr int L_REND = L_EX + 512;
constexpr int ZPL = 8192;
constexpr int L_CI = L_B;
constexpr int L_CX = L_CI + 2 * 8256;
constexpr int L_RED = L_CX + 16384;
constexpr int L_DBG = 163840 - 1024;
constexpr int L_SZ = 163840;
static_assert(L_REND <= L_DBG && L_RED + 2048 <= L_DBG && 2 * ZPL <= 16384, "lds");

constexpr u32 SENT = 0xFFFFFFFFu;
constexpr int SPIN = 1 << 14;
constexpr int SC1 = 16;
constexpr int OOB = 0x7FFF0000;

typedef __amdgpu_buffer_rsrc_t rsrc_t;
DEV rsrc_t mkrs(const void* p, int bytes) { return __builtin_amdgcn_make_buffer_rsrc((void*)p, 0, bytes, 0x00020000); }
DEV v4u ld4(rsrc_t r, int off, int so = 0) { return __builtin_amdgcn_raw_buffer_load_b128(r, off, so, 0); }
DEV v2u ld2(rsrc_t r, int off, int so = 0) { return __builtin_amdgcn_raw_buffer_load_b64(r, off, so, 0); }
DEV v4u ld4c(rsrc_t r, int off, int so = 0) { return __builtin_amdgcn_raw_buffer_load_b128(r, off, so, SC1); }
DEV v2u ld2c(rsrc_t r, int off, int so = 0) { return __builtin_amdgcn_raw_buffer_load_b64(r, off, so, SC1); }
DEV u32 ld1c(rsrc_t r, int off, int so = 0) { return __builtin_amdgcn_raw_buffer_load_b32(r, off, so, SC1); }
DEV void st4c(rsrc_t r, int off, v4u v) { __builtin_amdgcn_raw_buffer_store_b128(v, r, off, 0, SC1); }
DEV void st2c(rsrc_t r, int off, v2u v) { __builtin_amdgcn_raw_buffer_store_b64(v, r, off, 0, SC1); }
DEV float blo(u32 v) { return __builtin_bit_cast(float, v << 16); }
DEV float bhi(u32 v) { return __builtin_bit_cast(float, v & 0xFFFF0000u); }
DEV v2f bun(u32 v) { return v2f{blo(v), bhi(v)}; }
DEV u32 pk(float lo, float hi) { return __builtin_bit_cast(u32, __builtin_convertvector(v2f{lo, hi}, v2b)); }
DEV u32 pk2(v2f v) { return __builtin_bit_cast(u32, __builtin_convertvector(v, v2b)); }
DEV float rb(float f) { return blo(pk(f, 0.f)); }
DEV v2f lo2(v4f v) { return __builtin_shufflevector(v, v, 0, 1); }
DEV v2f hi2(v4f v) { return __builtin_shufflevector(v, v, 2, 3); }
DEV v2f fma2(v2f a, v2f b, v2f c) { return __builtin_elementwise_fma(a, b, c); }
DEV v4f fma4(v4f a, v4f b, v4f c) { return __builtin_elementwise_fma(a, b, c); }
typedef int v4i __attribute__((ext_vector_type(4)));
DEV v4f relu4(v4f v) { return __builtin_bit_cast(v4f, __builtin_elementwise_max(__builtin_bit_cast(v4i, v), v4i{0, 0, 0, 0})); }
DEV v4f splat(float f) { return v4f{f, f, f, f}; }
DEV v4f mfma(v4u a, v4u b, v4f c) {
  return __builtin_amdgcn_mfma_f32_16x16x32_bf16(__builtin_bit_cast(v8b, a), __builtin_bit_cast(v8b, b), c, 0, 0, 0);
}
DEV void mfa(v4f& c, v4u a, v4u b) { asm volatile("s_nop 1\n\tv_mfma_f32_16x16x32_bf16 %0, %1, %2, %0" : "+v"(c) : "v"(a), "v"(b)); }
DEV void mf0(v4f& c, v4u a, v4u b) { asm volatile("s_nop 1\n\tv_mfma_f32_16x16x32_bf16 %0, %1, %2, 0" : "=&v"(c) : "v"(a), "v"(b)); }
template <int N> DEV void mfence(v4f& a, v4f& b) { asm volatile("s_nop %2" : "+v"(a), "+v"(b) : "i"(N)); }
template <int N> DEV void mfence(v4f& a, v4f& b, v4f& c, v4f& d) { asm volatile("s_nop %4" : "+v"(a), "+v"(b), "+v"(c), "+v"(d) : "i"(N)); }
#define MF "v_mfma_f32_16x16x32_bf16 "
DEV void mf8(v4f& d0, v4f& d1, const v4u (&a0)[4], const v4u (&b0)[4], const v4u (&a1)[4], const v4u (&b1)[4]) {
  asm volatile("s_nop 1\n\t" MF "%0, %2, %6, 0\n\t" MF "%1, %10, %14, 0\n\t" MF "%0, %3, %7, %0\n\t" MF "%1, %11, %15, %1\n\t"
               MF "%0, %4, %8, %0\n\t" MF "%1, %12, %16, %1\n\t" MF "%0, %5, %9, %0\n\t" MF "%1, %13, %17, %1"
               : "=&v"(d0), "=&v"(d1)
               : "v"(a0[0]), "v"(a0[1]), "v"(a0[2]), "v"(a0[3]), "v"(b0[0]), "v"(b0[1]), "v"(b0[2]), "v"(b0[3]),
                 "v"(a1[0]), "v"(a1[1]), "v"(a1[2]), "v"(a1[3]), "v"(b1[0]), "v"(b1[1]), "v"(b1[2]), "v"(b1[3]));
}
#define H4_M(XA, D, X, K, C) MF D ", " XA(X, K) ", " C "\n\t"
#define H4_XA(X, K) X ", " K
#define H4_XB(X, K) K ", " X
#define H4_DEF(NAME, XA, C0, C1, CONS, N0, N1, PA, PB, PC, PD) \
DEV void NAME(v4f& d0, v4f& d1, v4u x0, v4u x1, v4u k00, v4u k01, v4u k10, v4u k11, \
              u32& o0, u32& o1, float p0, float q0, float c0, float s0, float p1, float q1, float c1, float s1) { \
  float t0, t1, t2, t3; \
  asm volatile(H4_M(XA, "%0", "%8", "%10", C0) "v_mul_f32 %4, %14, %16\n\tv_mul_f32 %5, %15, %16\n\t" \
               H4_M(XA, "%1", "%8", "%12", C1) "v_mul_f32 %6, %18, %20\n\tv_mul_f32 %7, %19, %20\n\t" \
               H4_M(XA, "%0", "%9", "%11", "%0") "v_fma_f32 %4, " N0 "%15, %17, %4\n\tv_fma_f32 %5, " N1 "%14, %17, %5\n\t" \
               H4_M(XA, "%1", "%9", "%13", "%1") "v_fma_f32 %6, " N0 "%19, %21, %6\n\tv_fma_f32 %7, " N1 "%18, %21, %7\n\t" \
               "v_cvt_pk_bf16_f32 %2, " PA ", " PB "\n\tv_cvt_pk_bf16_f32 %3, " PC ", " PD \
               : CONS(d0), CONS(d1), "=&v"(o0), "=&v"(o1), "=&v"(t0), "=&v"(t1), "=&v"(t2), "=&v"(t3) \
               : "v"(x0), "v"(x1), "v"(k00), "v"(k01), "v"(k10), "v"(k11), \
                 "v"(p0), "v"(q0), "v"(c0), "v"(s0), "v"(p1), "v"(q1), "v"(c1), "v"(s1)); \
}
H4_DEF(h4_a0m, H4_XA, "0", "0", "=&v", "", "-", "%4", "%5", "%6", "%7")
H4_DEF(h4_a1m, H4_XA, "%0", "%1", "+v", "", "-", "%4", "%5", "%6", "%7")
H4_DEF(h4_b0m, H4_XB, "0", "0", "=&v", "", "-", "%4", "%5", "%6", "%7")
H4_DEF(h4_b0p, H4_XB, "0", "0", "=&v", "-", "", "%5", "%4", "%7", "%6")
H4_DEF(h4_a0p, H4_XA, "0", "0", "=&v", "-", "", "%5", "%4", "%7", "%6")
#define M4_DEF(NAME, XA, C0, C1, CONS) \
DEV void NAME(v4f& d0, v4f& d1, v4u x0, v4u x1, v4u k00, v4u k01, v4u k10, v4u k11) { \
  asm volatile(H4_M(XA, "%0", "%2", "%4", C0) H4_M(XA, "%1", "%2", "%6", C1) H4_M(XA, "%0", "%3", "%5", "%0") MF "%1, " XA("%3", "%7") ", %1" \
               : CONS(d0), CONS(d1) : "v"(x0), "v"(x1), "v"(k00), "v"(k01), "v"(k10), "v"(k11)); \
}
M4_DEF(m4_a0, H4_XA, "0", "0", "=&v") M4_DEF(m4_a1, H4_XA, "%0", "%1", "+v")
M4_DEF(m4_b0, H4_XB, "0", "0", "=&v") M4_DEF(m4_b1, H4_XB, "%0", "%1", "+v")
DEV void mv(v4f& c, v4u a, v4u b) { asm volatile(MF "%0, %1, %2, %0" : "+v"(c) : "v"(a), "v"(b)); }
DEV void mv0(v4f& c, v4u a, v4u b) { asm volatile(MF "%0, %1, %2, 0" : "=&v"(c) : "v"(a), "v"(b)); }
DEV void nop2() { asm volatile("s_nop 1"); }
template <class T> DEV void pin(T& v) { asm volatile("" : "+v"(v)); }
DEV int fresh(int off) { asm volatile("" : "+v"(off) :: "memory"); return off; }
DEV bool wall(bool ok) { return __builtin_amdgcn_ballot_w64(!ok) == 0; }
DEV bool wany(bool b) { return __builtin_amdgcn_ballot_w64(b) != 0; }
DEV void sb() { __builtin_amdgcn_sched_barrier(0); }
DEV u32 max4(v4u v) { return max(max(v.x, v.y), max(v.z, v.w)); }
DEV void drain() { asm volatile("s_waitcnt vmcnt(0)" ::: "memory"); }
DEV void cfence() { asm volatile("" ::: "memory"); }
DEV void bar() { asm volatile("s_waitcnt lgkmcnt(0)\n\ts_barrier" ::: "memory"); }
template <int CTRL> DEV float dpp(float v) {
  return __builtin_bit_cast(float, __builtin_amdgcn_update_dpp(0, __builtin_bit_cast(int, v), CTRL, 0xF, 0xF, true));
}
DEV float sum16(float v) {
  v += dpp<0x128>(v); v += dpp<0x124>(v); v += dpp<0x122>(v); v += dpp<0x121>(v); return v;
}
DEV void sumg2(float& a, float& b) {
  float c = a, d = b;
  asm volatile("s_nop 1\n\tv_permlane16_swap_b32 %0, %1\n\tv_permlane16_swap_b32 %2, %3" : "+v"(a), "+v"(c), "+v"(b), "+v"(d));
  a += c; b += d; c = a; d = b;
  asm volatile("s_nop 1\n\tv_permlane32_swap_b32 %0, %1\n\tv_permlane32_swap_b32 %2, %3" : "+v"(a), "+v"(c), "+v"(b), "+v"(d));
  a += c; b += d;
}
DEV v4u lds4(const char LAS* L, int off) { return *(const LAS v4u*)(L + off); }
DEV v4f lds4f(const char LAS* L, int off) { return *(const LAS v4f*)(L + off); }
DEV v2u ldtr(const char LAS* L, int off) { return __builtin_bit_cast(v2u, __builtin_amdgcn_ds_read_tr16_b64_v4i16((LAS v4s*)(L + off))); }
DEV v4u ldtr2(const char LAS* L, int o0, int o1) { const v2u a = ldtr(L, o0), b = ldtr(L, o1); return v4u{a.x, a.y, b.x, b.y}; }
DEV void dma16(rsrc_t r, int voff, int soff, u32 lds_dst) {
  u32 keep;
  asm volatile("s_mov_b32 %0, m0\n\ts_mov_b32 m0, %1\n\ts_nop 0\n\tbuffer_load_dwordx4 %2, %3, %4 offen" DMA_MOD " lds\n\ts_mov_b32 m0, %0"
               : "=&s"(keep) : "s"(lds_dst), "v"(voff), "s"(r), "s"(soff) : "memory");
}
DEV void light_poll(rsrc_t ws, int off) {
  _Pragma("nounroll") for (int s = 0; s < SPIN; ++s) {
    if (wall(ld1c(ws, fresh(off)) != SENT)) break;
    __builtin_amdgcn_s_sleep(SL2);
  }
}
DEV int tok_row(int blk, int t) {
  const int n1 = 16 * (blk >> 5) + 4 * ((blk >> 2) & 3) + (t & 3), n2 = 16 * ((blk >> 4) & 1) + 4 * (blk & 3) + (t >> 2);
  return n2 + 32 * n1;
}
template <bool SWAP> DEV v4u cmul_pack(v4f a, v4f b, v4f c, v4f s) {
  const v4f re = fma4(b, s, a * c), im = fma4(b, c, -(a * s));
  return SWAP ? v4u{pk(im.x, re.x), pk(im.y, re.y), pk(im.z, re.z), pk(im.w, re.w)}
              : v4u{pk(re.x, im.x), pk(re.y, im.y), pk(re.z, im.z), pk(re.w, im.w)};
}

template <bool PLUS> DEV void cmul2(u32& o0, u32& o1, v2f p, v2f q, v2f c, v2f s) {
  const v2f re = fma2(PLUS ? -q : q, s, p * c), im = fma2(PLUS ? p : -p, s, q * c);
  o0 = PLUS ? pk(im.x, re.x) : pk(re.x, im.x); o1 = PLUS ? pk(im.y, re.y) : pk(re.y, im.y);
}

#define STAMP(i)
#define CSTAMP(i)
#define SLOWCNT
#define DUMP()

DEV void dma_w1(const u16* wg, const u16* wu, int slice, int wave, int lane, u32 lbase) {
  const rsrc_t rg = mkrs(wg, D * F * 2), ru = mkrs(wu, D * F * 2);
  const int r = lane >> 3, ch = ((((lane & 7) >> 1) ^ (lane >> 4)) & 3) * 32 + (lane & 1) * 16 + slice * 128;
  const int vp = r * (F * 2) + ch, vn = (7 - r) * (F * 2) + ch;
#pragma unroll
  for (int n = 0; n < 4; ++n) {
    const int ks = wave + 4 * n;
    if (n == 3 && wave >= 2) break;
    const int t16 = (ks >> 1) * 16, a = ks & 1;
#pragma unroll
    for (int q = 0; q < 4; ++q) {
      const int dl0 = t16 + 8 * (q & 1);
      const bool pos = (q >> 1) == a;
      const int d0 = q < 2 ? (a ? 224 - dl0 - 7 : dl0) : (a ? 224 + dl0 : 448 - dl0 - 7);
      int vo = pos ? vp : vn, so = d0 * (F * 2);
      if (n == 0 && q == 2) {
        vo += so; so = 0;
        if (wave < 2 && lane < 8) vo = (wave ? 336 : 112) * (F * 2) + ch;
      }
      dma16(rg, vo, so, lbase + L_W1 + ks * 8192 + q * 1024);
      dma16(ru, vo, so, lbase + L_W1 + ks * 8192 + 4096 + q * 1024);
    }
  }
}
struct ModLoad { u32 cv[7]; v4u mw[7]; };
DEV void mod_load(ModLoad& m, const u16* cin, const u16* wm, int q, int tid) {
  const rsrc_t cr = mkrs(cin, q >= 0 ? D * 2 : 0), mr = mkrs(wm, q >= 0 ? D * 6 * D * 2 : 0);
#pragma unroll
  for (int i = 0; i < 7; ++i) m.cv[i] = __builtin_amdgcn_raw_buffer_load_b16(cr, (tid >> 2) * 2 + i * 128, 0, 0);
#pragma unroll
  for (int i = 0; i < 7; ++i) m.mw[i] = __builtin_amdgcn_raw_buffer_load_b128(mr, ((tid >> 2) * (6 * D) + q * 32 + (tid & 3) * 8) * 2, i * 64 * 6 * D * 2, PF_AUX);
}
DEV void mod_part(ModLoad& m, char LAS* L, int tid) {
  float sl[7];
#pragma unroll
  for (int i = 0; i < 7; ++i) { const float cc = blo(m.cv[i]); sl[i] = rb(cc * __builtin_amdgcn_rcpf(1.f + __expf(-cc))); pin(sl[i]); }
  v2f a[4];
#pragma unroll
  for (int e = 0; e < 4; ++e) a[e] = v2f{0.f, 0.f};
#pragma unroll
  for (int i = 0; i < 7; ++i)
#pragma unroll
    for (int e = 0; e < 4; ++e) a[e] = fma2(bun(m.mw[i][e]), v2f{sl[i], sl[i]}, a[e]);
#pragma unroll
  for (int e = 0; e < 4; ++e) {
    a[e].x += dpp<0x128>(a[e].x); a[e].y += dpp<0x128>(a[e].y);
    a[e].x += dpp<0x124>(a[e].x); a[e].y += dpp<0x124>(a[e].y);
  }
  if ((tid & 15) < 4) {
    *(LAS v4f*)(L + L_RED + ((tid >> 4) * 32 + (tid & 15) * 8) * 4) = v4f{a[0].x, a[0].y, a[1].x, a[1].y};
    *(LAS v4f*)(L + L_RED + ((tid >> 4) * 32 + (tid & 15) * 8 + 4) * 4) = v4f{a[2].x, a[2].y, a[3].x, a[3].y};
  }
}
DEV void mod_publish(char LAS* L, rsrc_t ws, int q, int lane, u32 tag) {
  v4f s = {0.f, 0.f, 0.f, 0.f};
#pragma unroll
  for (int r = 0; r < 16; ++r) s += lds4f(L, L_RED + (r * 32 + (lane & 7) * 4) * 4);
  const float one = (q / 14 == 1 || q / 14 == 4) ? 1.f : 0.f;
  st4c(ws, WS_MOD + (lane >> 3) * MOD_REP + q * 128 + (lane & 7) * 16,
       v4u{pk(rb(s.x) + one, rb(s.y) + one), tag, pk(rb(s.z) + one, rb(s.w) + one), tag});
}
DEV u32 ld1(rsrc_t r, int off) { return __builtin_amdgcn_raw_buffer_load_b32(r, off, 0, 0); }
DEV u32 ld1n(rsrc_t r, int off) { return __builtin_amdgcn_raw_buffer_load_b32(r, off, 0, PF_AUX); }
DEV u32 mktag(u32 epoch) { return epoch * 0x9E3779B1u + 0x7F4A7C15u; }

DEV void x2_read(const char LAS* L, v4u (&r)[4], int tid) {
#pragma unroll
  for (int m = 0; m < 4; ++m) { const int ch = min(tid + 256 * m, 895), tk = ch / 56; r[m] = lds4(L, L_XS + tk * XP + (ch - tk * 56) * 16); }
}
DEV void x2_store(rsrc_t ws, const v4u (&r)[4], int blk, int tid) {
#pragma unroll
  for (int m = 0; m < 4; ++m) st4c(ws, tid + 256 * m < 896 ? WS_X2 + blk * 14336 + (tid + 256 * m) * 16 : OOB, r[m]);
}
DEV bool flags_ok(v4u rv, u32 tag) { return rv.x == tag && rv.y == ~tag && rv.z == tag && rv.w == ~tag; }
DEV void wait_flags(rsrc_t ws, int off, u32 tag) {
  _Pragma("nounroll") for (int s = 0; s < SPIN; ++s) {
    const v2u rv = ld2c(ws, fresh(off));
    if (wall(rv.x == tag && rv.y == ~tag)) break;
    __builtin_amdgcn_s_sleep(SL1);
  }
}

__global__ void __launch_bounds__(256, 1)
fused(const u16* __restrict__ xin, const u16* __restrict__ wr, const u16* __restrict__ wi, const u16* __restrict__ wg,
      const u16* __restrict__ wu, const u16* __restrict__ wd, const u16* __restrict__ cin, const u16* __restrict__ wm,
      u16* __restrict__ out, char* wsp) {
  __shared__ __attribute__((aligned(16))) char smem[L_SZ];
  char LAS* L = (char LAS*)smem;
  const u32 lbase = (u32)(size_t)L;
  const int tid = threadIdx.x, lane = tid & 63, wave = __builtin_amdgcn_readfirstlane(tid >> 6);
  const int g = lane >> 4, c16 = lane & 15, l16 = lane * 16;
  const int grp = blockIdx.x & 7, j = blockIdx.x >> 3;
  u64 pc;
  asm volatile("s_getpc_b64 %0" : "=s"(pc));
  STAMP(0); CSTAMP(31);
  asm volatile("" :: "s"(xin), "s"(wr), "s"(wi), "s"(wg), "s"(wu), "s"(wd), "s"(cin), "s"(wm), "s"(out), "s"(wsp));
  const rsrc_t ws = mkrs(wsp, WS_END);
  const rsrc_t tab = mkrs(TAB, TAB_N * 4096);
  const rsrc_t code = mkrs((const void*)(pc & ~127ull), CODE_BYTES);
  const bool isR = j >= 14 && j < 30;
  const bool isG = j < 28;
  const int gk = j / 7, ct = j - gk * 7;
  const int tile = 4 * (grp >> 2) + gk, slice = 7 * (grp & 3) + ct;
  const int wdb = WS_WD + (ct * 56 + wave * 14) * 4096;
  const v4u sv = {SENT, SENT, SENT, SENT};
  int lb = L_B + l16;
  pin(lb);
  const char LAS* LB = L + lb - L_B;
  const int pfo = ((j * 4 + wave) * 14 + lane) * 128, pfc = ((j * 4 + wave) * 4 + lane) * 128;
  u32 pfa = ld1(tab, lane < 14 ? pfo : OOB), pfb = ld1(code, lane < 4 ? pfc : OOB);
  v4u x2r[4];
  const int wl16 = (lane ^ ((wave & 1) << 1)) * 16;
  v4u wdn[14][4];
  u32 epoch, tag;

#define ACT_CLEAR(lo, hi) _Pragma("unroll") for (int m = lo; m < hi; ++m) \
      st4c(ws, isG ? WS_ACT + (16 * (tile + 2 * (m >> 2)) + 4 * (grp & 3) + 2) * 57344 + ct * 16384 + (m & 3) * 4096 + tid * 16 : OOB, sv);
  if (isR) {
    const int blk = 16 * grp + (j - 14);
    epoch = ld1c(ws, WS_EPOCH);
    const int t0 = wave == 0 ? 0 : 2 * wave - 1;
    v4u cf[8][4];
#define CF_LOAD(q) { const int chn = TAB_P1 + ((wave == 0 && q >= 4) ? 28 : 4 * t0 + q), vo = (q < 5 || wave != 0) ? l16 : OOB; \
      _Pragma("unroll") for (int s = 0; s < 4; ++s) cf[q][s] = ld4(tab, vo + s * 1024, chn * 4096); sb(); }
#pragma unroll
    for (int q = 0; q < X_AFTER; ++q) CF_LOAD(q)
    v2u xv[7];
    {
      const rsrc_t xr = mkrs(xin, S * D * 2);
      const int off = tok_row(blk, 4 * wave + g) * (D * 2) + c16 * 8;
#pragma unroll
      for (int m = 0; m < 7; ++m) xv[m] = ld2(xr, off + m * 128);
    }
    typedef u32 v3u __attribute__((ext_vector_type(3)));
    v2u fa_[2], fb_[2]; v3u fc_[2], fd_[2];
    {
      const rsrc_t rr = mkrs(wr, S * DF * 2), ri = mkrs(wi, S * DF * 2);
      const int fo_ = 16 * wave + c16 < 57 ? 2 * g * 450 + (16 * wave + c16) * 8 : OOB;
#pragma unroll
      for (int hh = 0; hh < 2; ++hh) {
        const int so = blk * 7200 + hh * 3600;
        fa_[hh] = ld2(rr, fo_, so); fb_[hh] = ld2(ri, fo_, so);
        fc_[hh] = __builtin_amdgcn_raw_buffer_load_b96(rr, fo_ + 448, so, 0); fd_[hh] = __builtin_amdgcn_raw_buffer_load_b96(ri, fo_ + 448, so, 0);
      }
    }
    sb();
#pragma unroll
    for (int q = X_AFTER; q < 8; ++q) CF_LOAD(q)
    STAMP(23);
    {
      v2f f[14], s2 = {0.f, 0.f};
#pragma unroll
      for (int m = 0; m < 7; ++m) {
        f[2 * m] = bun(xv[m].x); f[2 * m + 1] = bun(xv[m].y);
        s2 += f[2 * m] + f[2 * m + 1];
        *(LAS v2u*)(L + L_XS + (4 * wave + g) * XP + c16 * 8 + m * 128) = xv[m];
      }
      STAMP(21);
      sb();
      {
        const int q = 16 * wave + c16, o = WS_WT + q * 16384 + blk * 128 + g * 16;
#pragma unroll
        for (int hh = 0; hh < 2; ++hh) {
          const v2u a = fa_[hh], b = fb_[hh]; const v3u c = fc_[hh], d = fd_[hh];
          st4c(ws, q < 57 ? o + hh * 64 : OOB, v4u{__builtin_amdgcn_perm(b.x, a.x, 0x05040100u), __builtin_amdgcn_perm(b.y, a.y, 0x05040100u),
                                                   __builtin_amdgcn_perm(d.x, c.x, 0x07060302u), __builtin_amdgcn_perm(d.y, c.y, 0x07060302u)});
          st4c(ws, q < 56 ? o + hh * 64 + 57 * 16384 : OOB,
               v4u{__builtin_amdgcn_perm(b.x, a.x, 0x07060302u), __builtin_amdgcn_perm(b.y, a.y, 0x07060302u),
                   __builtin_amdgcn_perm(d.y, c.y, 0x05040100u), __builtin_amdgcn_perm(d.z, c.z, 0x05040100u)});
        }
      }
      sb();
      const float mean = sum16(s2.x + s2.y) * (1.f / D);
      v2f q2 = {0.f, 0.f};
#pragma unroll
      for (int m = 0; m < 14; ++m) { const v2f d = f[m] - mean; q2 = fma2(d, d, q2); }
      const float rstd = __builtin_amdgcn_rsqf(sum16(q2.x + q2.y) * (1.f / D) + 1e-5f);
      if (c16 == 0) *(LAS v2f*)(L + L_ST + (4 * wave + g) * 8) = v2f{mean, rstd};
    }
    pin(pfa); pin(pfb);
    epoch = __builtin_amdgcn_readfirstlane(epoch);
    tag = mktag(epoch);
    STAMP(22);
    bar();
    v4f n[4][2];
    const int d0 = 8 * (4 * wave + g);
    const v2f st = *(const LAS v2f*)(L + L_ST + c16 * 8);
    {
      int xr_ = L_XS + c16 * XP + d0 * 2, xb_ = L_XS + c16 * XP + (216 - d0) * 2, x0_ = d0 == 0 ? xr_ : xb_ + 464;
      pin(xr_); pin(xb_); pin(x0_);
      const v4u a = lds4(L + xr_, 0), e = lds4(L + xr_, 448), c = lds4(L + xb_, 0), b = lds4(L + xb_, 448);
      const u32 c0 = *(const LAS u16*)(L + xb_ + 16), b0 = *(const LAS u16*)(L + x0_);
#pragma unroll
      for (int i = 0; i < 8; ++i) {
        n[0][i >> 2][i & 3] = (i & 1) ? bhi(a[i >> 1]) : blo(a[i >> 1]);
        n[3][i >> 2][i & 3] = (i & 1) ? bhi(e[i >> 1]) : blo(e[i >> 1]);
        n[1][i >> 2][i & 3] = i == 0 ? blo(b0) : (i & 1) ? bhi(b[(8 - i) >> 1]) : blo(b[(8 - i) >> 1]);
        n[2][i >> 2][i & 3] = i == 0 ? blo(c0) : (i & 1) ? bhi(c[(8 - i) >> 1]) : blo(c[(8 - i) >> 1]);
      }
      const float nm = -st.x * st.y;
#pragma unroll
      for (int m = 0; m < 4; ++m) {
#pragma unroll
        for (int h = 0; h < 2; ++h) { n[m][h] = fma4(n[m][h], splat(st.y), splat(nm)); pin(n[m][h]); }
      }
    }
    sb();
    STAMP(1);
    int mb = L_M1 + d0 * 4;
    pin(mb);
    v4u xs[4];
    int to[4];
    to[0] = WS_T1 + blk * T1_BLK + (16 * t0 + c16) * 64 + g * 16;
    to[1] = wave != 0 ? to[0] + 1024 : c16 < 2 ? to[0] + 112 * 64 : OOB;
    to[2] = to[0] + 114 * 64;
    to[3] = wave != 0 ? to[0] + 1024 + 114 * 64 : OOB;
#pragma unroll
    for (int q = 0; q < 4; ++q) pin(to[q]);

    {
      const int kind = tid >= 112, d = 4 * (tid - 112 * kind);
      int wb = L_M1 + (kind ? 0 : 2 * 1792) + d * 4, wrv = L_M1 + (kind ? 0 : 2 * 1792) + 1792 + (445 - d) * 4;
      int wr0 = d == 0 ? wrv + 12 - 1792 : wrv + 12;
      const int mo = tid < 224 ? WS_MOD + grp * MOD_REP + tid * 16 : OOB;
      pin(wb); pin(wrv); pin(wr0);
      const int ro = WS_RDY + grp * 1024 + min(tid >> 1, 112) * 8;
      v4u mv;
      _Pragma("nounroll") for (int s = 0; s < SPIN; ++s) {
        mv = ld4c(ws, fresh(mo));
        const v2u rv = ld2c(ws, fresh(ro));
        if (wall((tid >= 224 || (mv.y == tag && mv.w == tag)) && rv.x == tag && rv.y == ~tag)) break;
        __builtin_amdgcn_s_sleep(SL1);
      }
      STAMP(2);
      if (tid < 224) {
        const v4f v = {blo(mv.x), bhi(mv.x), blo(mv.z), bhi(mv.z)};
        *(LAS v4f*)(L + wb) = v;
        *(LAS float*)(L + wr0) = v.x;
        *(LAS float*)(L + wrv + 8) = v.y;
        *(LAS float*)(L + wrv + 4) = v.z;
        *(LAS float*)(L + wrv) = v.w;
      }
    }
    bar();
    {
      v4u fr[4];
#pragma unroll
      for (int hh = 0; hh < 2; ++hh) {
        v4f h[4];
#pragma unroll
        for (int m = 0; m < 4; ++m) {
          const int o = ((m == 1 || m == 2) ? 1792 : 0) + (m >= 2 ? 896 : 0) + hh * 16;
          h[m] = fma4(n[m][hh], lds4f(L + mb, o), lds4f(L + mb, 2 * 1792 + o));
        }
        const v4f p = h[0] + h[1], q = h[2] + h[3], u = h[0] - h[1], v = h[3] - h[2];
        const v4f t[4] = {p + q, p - q, u + v, u - v};
#pragma unroll
        for (int m = 0; m < 4; ++m) { fr[m][2 * hh] = pk2(lo2(t[m])); fr[m][2 * hh + 1] = pk2(hi2(t[m])); }
      }
#pragma unroll
      for (int m = 0; m < 4; ++m) *(LAS v4u*)(LB + wave * 1024 + L_AF + m * 4096) = fr[m];
    }
    bar();
    {
      v4u a[4][4];
#pragma unroll
      for (int p = 0; p < 2; ++p)
#pragma unroll
        for (int s = 0; s < 4; ++s) { a[p][s] = lds4(LB, L_AF + (p * 4 + s) * 1024); a[2 + p][s] = lds4(LB, L_AF + ((2 + p) * 4 + s) * 1024); }
      v4f re[4], im[4];
#pragma unroll
      for (int q = 0; q < 5; ++q) {
        if (q < 4) mf8(re[q], im[q], a[q >> 1], cf[4 * (q & 1) + (q >> 1)], a[2 + (q >> 1)], cf[4 * (q & 1) + 2 + (q >> 1)]);
        if (q > 0) {
          if (q < 4) mfence<0>(re[q - 1], im[q - 1]); else mfence<7>(re[3], im[3]);
          xs[q - 1] = v4u{pk(re[q - 1].x, im[q - 1].x), pk(re[q - 1].y, im[q - 1].y), pk(re[q - 1].z, im[q - 1].z), pk(re[q - 1].w, im[q - 1].w)};
          st4c(ws, to[q - 1], xs[q - 1]);
          sb();
        }
      }
    }
    STAMP(3);
    sb();
    sb();
    if (isG) dma_w1(wg, wu, slice, wave, lane, lbase);
#pragma unroll
    for (int q = 0; q < 4; ++q) {
      const int vo = (q < 2 || wave != 0) ? l16 : OOB;
#pragma unroll
      for (int s = 0; s < 4; ++s) cf[2 * q + 1][s] = ld4(tab, vo + s * 1024, (TAB_P3 + 2 * t0 + q) * 4096);
    }
    sb();
    STAMP(20);
    v4f xo[2][4];
    int xa = L_XS + c16 * XP + (16 * t0 + 4 * g) * 2, xd = L_XS + c16 * XP + (192 - 16 * t0 - 4 * g) * 2;
    int xs1 = tid < 16 ? L_XS + c16 * XP + 224 : xd + 512, xs3 = tid < 16 ? L_XS + c16 * XP + 672 : xa + 448;
    pin(xa); pin(xd); pin(xs1); pin(xs3);
#define XOFF(t, m, r) ((t) == 0 && (r) == 0 && (m) == 1 ? xs1 : (t) == 0 && (r) == 0 && (m) == 3 ? xs3 : \
    (m) == 0 ? xa + 32 * (t) + 2 * (r) : (m) == 3 ? xa + 448 + 32 * (t) + 2 * (r) : (m) == 1 ? xd + 512 - 32 * (t) - 2 * (r) : xd + 64 - 32 * (t) - 2 * (r))
    {
      v2u w[2][4]; u32 e[2][2], e3;
#pragma unroll
      for (int t = 0; t < 2; ++t) {
        w[t][0] = *(const LAS v2u*)(L + xa + 32 * t); w[t][3] = *(const LAS v2u*)(L + xa + 448 + 32 * t);
        w[t][1] = *(const LAS v2u*)(L + xd + 504 - 32 * t); w[t][2] = *(const LAS v2u*)(L + xd + 56 - 32 * t);
        e[t][0] = *(const LAS u16*)(L + XOFF(t, 1, 0)); e[t][1] = *(const LAS u16*)(L + XOFF(t, 2, 0));
      }
      e3 = *(const LAS u16*)(L + xs3);
      sb();
#pragma unroll
      for (int t = 0; t < 2; ++t) {
        xo[t][0] = v4f{blo(w[t][0].x), bhi(w[t][0].x), blo(w[t][0].y), bhi(w[t][0].y)};
        xo[t][3] = v4f{t == 0 ? blo(e3) : blo(w[t][3].x), bhi(w[t][3].x), blo(w[t][3].y), bhi(w[t][3].y)};
        xo[t][1] = v4f{blo(e[t][0]), bhi(w[t][1].y), blo(w[t][1].y), bhi(w[t][1].x)};
        xo[t][2] = v4f{blo(e[t][1]), bhi(w[t][2].y), blo(w[t][2].y), bhi(w[t][2].x)};
#pragma unroll
        for (int m = 0; m < 4; ++m) { xo[t][m] -= st.x; pin(xo[t][m]); }
      }
    }
    int m0_[2], m1_[2], md_[2];
#pragma unroll
    for (int r = 0; r < 2; ++r) {
      const int q = r == 0 || tid < 80 ? tid + 256 * r : tid;
      const int kind = (q * 586) >> 16, d = 4 * (q - 112 * kind);
      const int reg = (d >= 112) + (d >= 224) + (d >= 336), cc = ((reg + 1) >> 1) * 224, m1 = (0x1320 >> (4 * reg)) & 3;
      const int d1 = abs(d + 1 - cc), d0_ = abs(d - cc), edge = d == 112 * reg && reg > 0;
      m1_[r] = L_M2 + kind * 1792 + (((d1 >> 4) << 6) | (m1 << 4) | (d1 & 15)) * 4;
      m0_[r] = L_M2 + kind * 1792 + (edge ? reg << 4 : ((d0_ >> 4) << 6) | (m1 << 4) | (d0_ & 15)) * 4;
      md_[r] = (reg & 1) ? -4 : 4;
      pin(m0_[r]); pin(m1_[r]); pin(md_[r]);
    }
    {
      v4u mv[2];
      _Pragma("nounroll") for (int s = 0; s < SPIN; ++s) {
        mv[0] = ld4c(ws, fresh(WS_MOD + grp * MOD_REP + (224 + tid) * 16));
        mv[1] = ld4c(ws, fresh(WS_MOD + grp * MOD_REP + (224 + (tid < 80 ? 256 + tid : tid)) * 16));
        const v2u rv = ld2c(ws, fresh(WS_DRDY + grp * 1024 + min(lane, 48) * 8));
        if (wall(mv[0].y == tag && mv[0].w == tag && mv[1].y == tag && mv[1].w == tag && rv.x == tag && rv.y == ~tag)) break;
        __builtin_amdgcn_s_sleep(SL4);
      }
      STAMP(12);
#pragma unroll
      for (int f = 0; f < WD_EARLY; ++f) wdn[f >> 2][f & 3] = ld4c(ws, (isG ? wl16 : OOB) + (f & 3) * 1024, wdb + (f >> 2) * 4096);
      sb();
#pragma unroll
      for (int r = 0; r < 2; ++r) {
        const float sc = m0_[r] < L_M2 + 1792 ? 2.f / (float)(S * D) : 1.f;
        *(LAS float*)(L + m0_[r]) = blo(mv[r].x) * sc;
        *(LAS float*)(L + m1_[r]) = bhi(mv[r].x) * sc;
        *(LAS float*)(L + m1_[r] + md_[r]) = blo(mv[r].z) * sc;
        *(LAS float*)(L + m1_[r] + 2 * md_[r]) = bhi(mv[r].z) * sc;
      }
    }
    bar();
    *(LAS v4u*)(L + L_AF + (tid >> 6) * 4096 + 3072 + l16) = v4u{0u, 0u, 0u, 0u};
    STAMP(4);
    int zr = L_AF + lane * 8, ro = L_M2 + (t0 * 64 + 4 * g) * 4, ro1 = wave == 0 ? ro : ro + 256, ex = L_EX + c16 * 8;
    pin(zr); pin(ro); pin(ro1); pin(ex);
    {
      int zl[4];
#pragma unroll
      for (int m = 0; m < 4; ++m) {
        const int kq = (m >> 1) * 128 + (to[m] == OOB ? 127 : wave == 0 && m == 1 ? 112 + c16 : 16 * (t0 + (m & 1)) + c16);
        zl[m] = L_AF + ((kq & ~31) + ((kq & 4) << 2) + ((kq & 24) >> 1) + (kq & 3)) * 32 + g * 8;
        pin(zl[m]);
        if (to[m] == OOB) xs[m] = sv;
      }
      bar();
      {
        const int src = (blk * 5 + wave * 13) & 63;
        const int po = __builtin_amdgcn_readlane(to[(0)], 0) + (src & 15) * 64 + (src >> 4) * 16;
        const u32 old = __builtin_amdgcn_ds_bpermute(src * 4, xs[0].x);
        _Pragma("nounroll") for (int s = 0; s < SPIN; ++s) {
          if (wall(ld1c(ws, fresh(po)) != old)) break;
          __builtin_amdgcn_s_sleep(SL2);
        }
      }
      STAMP(13);
      v4u z[4];
      _Pragma("nounroll") for (int s = 0; s < SPIN; ++s) {
        u32 mn = 1;
#pragma unroll
        for (int m = 0; m < 4; ++m) z[m] = ld4c(ws, fresh(to[m]));
#pragma unroll
        for (int m = 0; m < 4; ++m) { const v4u d = z[m] ^ xs[m]; mn = min(min(mn, d.x), min(min(d.y, d.z), d.w)); }
        if (wall(mn != 0)) break;
      }
      STAMP(14);
#pragma unroll
      for (int m = 0; m < 4; ++m) st4c(ws, to[m], sv);
      sb();
#pragma unroll
      for (int m = 0; m < 4; ++m) {
        char LAS* p = L + zl[m];
        *(LAS v2u*)(p) = v2u{__builtin_amdgcn_perm(z[m].y, z[m].x, 0x05040100u), __builtin_amdgcn_perm(z[m].w, z[m].z, 0x05040100u)};
        *(LAS v2u*)(p + ZPL) = v2u{__builtin_amdgcn_perm(z[m].y, z[m].x, 0x07060302u), __builtin_amdgcn_perm(z[m].w, z[m].z, 0x07060302u)};
      }
    }
    bar();
    STAMP(5);
    {
      v4u zf[4][4];
#pragma unroll
      for (int ty = 0; ty < 4; ++ty)
#pragma unroll
        for (int s = 0; s < 4; ++s) zf[ty][s] = ldtr2(L + zr, ty * 4096 + s * 1024, ty * 4096 + s * 1024 + 512);
      v4f acc[2][4], x2[2][4], gav[2][4];
      v2f s1 = {0.f, 0.f}, s2 = {0.f, 0.f};
#define P3M(t, ty) mf8(acc[t][ty], acc[t][ty + 1], cf[4 * t + ty], zf[ty], cf[4 * t + ty + 1], zf[ty + 1]);
#define P3X(t, m, yv) { x2[t][m] = fma4(gav[t][m], yv, xo[t][m]); \
      if (t == 0 || wave != 0) { s1 += lo2(x2[t][m]) + hi2(x2[t][m]); \
        s2 = fma2(lo2(x2[t][m]), lo2(x2[t][m]), s2); s2 = fma2(hi2(x2[t][m]), hi2(x2[t][m]), s2); } }
      P3M(1, 0) P3M(1, 2)
#pragma unroll
      for (int m = 0; m < 4; ++m) { gav[0][m] = lds4f(L + ro, m * 64); gav[1][m] = lds4f(L + ro1, m * 64); }
      sb();
      P3M(0, 0) P3M(0, 2)
      mfence<0>(acc[1][0], acc[1][1], acc[1][2], acc[1][3]);
      const v4f A1 = acc[1][0] + acc[1][1], B1 = acc[1][0] - acc[1][1], P1 = acc[1][3] - acc[1][2], Q1 = acc[1][2] + acc[1][3];
      P3X(1, 0, A1 - P1) P3X(1, 1, A1 + P1) P3X(1, 2, B1 - Q1) P3X(1, 3, B1 + Q1)
      sb();
      drain();
      if (lane == 0) st2c(ws, WS_HRDY + blk * 32 + wave * 8, v2u{tag, ~tag});
      mfence<0>(acc[0][0], acc[0][1], acc[0][2], acc[0][3]);
      {
        const v4f A = acc[0][0] + acc[0][1], B = acc[0][0] - acc[0][1], P = acc[0][3] - acc[0][2], Q = acc[0][2] + acc[0][3];
        v4f y[4] = {A - P, A + P, B - Q, B + Q};
        const bool sp = tid < 16;
        y[0].x = sp ? A.x : y[0].x; y[2].x = sp ? B.x : y[2].x;
        y[1].x = sp ? acc[1][0].x - acc[0][3].x : y[1].x; y[3].x = sp ? acc[1][0].x + acc[0][3].x : y[3].x;
        P3X(0, 0, y[0]) P3X(0, 1, y[1]) P3X(0, 2, y[2]) P3X(0, 3, y[3])
      }
      float sm = s1.x + s1.y, sq = s2.x + s2.y;
      sumg2(sm, sq);
      if (g == 0) *(LAS v2f*)(L + ex + wave * 128) = v2f{sm, sq};
      v4f sbv[2][4], cbv[2][4];
#pragma unroll
      for (int m = 0; m < 4; ++m) {
        sbv[0][m] = lds4f(L + ro, 1792 + m * 64); cbv[0][m] = lds4f(L + ro, 2 * 1792 + m * 64);
        sbv[1][m] = lds4f(L + ro1, 1792 + m * 64); cbv[1][m] = lds4f(L + ro1, 2 * 1792 + m * 64);
      }
      bar();
      v2f e = *(const LAS v2f*)(L + ex);
#pragma unroll
      for (int w = 1; w < 4; ++w) e += *(const LAS v2f*)(L + ex + w * 128);
      const float mc = e.x * (1.f / D), rstd = __builtin_amdgcn_rsqf(fmaxf(e.y * (1.f / D) - mc * mc, 0.f) + 1e-5f), nb = -mc * rstd;
      const int ho = WS_H2 + blk * H2_BLK + 2 * t0 * 1024 + l16, ho1 = wave == 0 ? OOB : ho + 2048;
#pragma unroll
      for (int t = 0; t < 2; ++t)
#pragma unroll
        for (int a = 0; a < 2; ++a) {
          v4u o;
#pragma unroll
          for (int hh = 0; hh < 2; ++hh) {
            const int m = 2 * a + hh;
            const v4f hv = fma4(fma4(x2[t][m], splat(rstd), splat(nb)), cbv[t][m], sbv[t][m]);
            o[2 * hh] = pk2(lo2(hv)); o[2 * hh + 1] = pk2(hi2(hv));
          }
          st4c(ws, (t ? ho1 : ho) + a * 1024, o);
        }
      STAMP(6);
      sb();
#pragma unroll
      for (int f = WD_EARLY; f < WD_MID; ++f) wdn[f >> 2][f & 3] = ld4c(ws, (isG ? wl16 : OOB) + (f & 3) * 1024, wdb + (f >> 2) * 4096);
      sb();
#pragma unroll
      for (int t = 0; t < 2; ++t)
        if (t == 0 || wave != 0)
#pragma unroll
          for (int m = 0; m < 4; ++m)
#pragma unroll
            for (int r = 0; r < 4; r += 2) {
              const u32 p = pk(x2[t][m][r] + st.x, x2[t][m][r + 1] + st.x);
              *(LAS u16*)(L + XOFF(t, m, r)) = (u16)p;
              *(LAS u16*)(L + XOFF(t, m, r + 1)) = (u16)(p >> 16);
            }
      sb();
    }
    bar();
    x2_read(L, x2r, tid);
    bar();
    if (!isG) {
      x2_store(ws, x2r, blk, tid);
      drain();
      if (lane == 0) st2c(ws, WS_XRDY + blk * 64 + wave * 8, v2u{tag, ~tag});
      DUMP(); return;
    }
  } else {
    const int sj = j >= 30 ? j - 30 : j + 2;
    const int idx = sj * 8 + grp;
    const bool isC = j < 14 || idx == 0;
    const int pair = j < 14 ? grp * 14 + j : 112;
    const int lc = grp + 8 * (sj >> 1);
    const int qu = sj < 4 && lc < 14 ? 2 * lc + (sj & 1) : -1;
    const int t1o = WS_T1 + (32 * wave + (lane >> 3)) * T1_BLK + pair * 128 + (lane & 7) * 16;
    ModLoad ml;
    epoch = ld1c(ws, WS_EPOCH);
    if (qu >= 0) mod_load(ml, cin, wm, qu, tid);
#pragma unroll
    for (int q = 0; q < 4; ++q) st4c(ws, isC ? t1o + 8 * q * T1_BLK : OOB, sv);
    u32 pf0, pf1, pf2;
    {
      const rsrc_t mr = mkrs(wm, D * 6 * D * 2), xr = mkrs(xin, S * D * 2), fr_ = mkrs((wave & 1) ? wi : wr, S * DF * 2);
      const int row = (sj * 4 + wave) * 7 + (lane >> 1), l7 = lane / 7;
      pf0 = __builtin_amdgcn_raw_buffer_load_b32(mr, lane < 14 && (grp < 6 || !(lane & 1)) ? row * (6 * D * 2) + (grp + 8 * (lane & 1)) * 128 : OOB, 0, PF_AUX);
      pf1 = __builtin_amdgcn_raw_buffer_load_b32(xr, lane < (!XBAL ? 28 : sj < 4 ? 0 : sj < 12 ? 28 : 56) ? tok_row(16 * grp + (lane < 28 ? sj : sj - 12), 4 * wave + (l7 & 3)) * (D * 2) + (lane - 7 * l7) * 128 : OOB, 0, PF_AUX);
    }
    sb();
    epoch = __builtin_amdgcn_readfirstlane(epoch);
    tag = mktag(epoch);
    if (qu >= 0) mod_part(ml, L, tid);
    asm volatile("s_waitcnt vmcnt(%0)" :: "i"(W_LATE ? 2 : 3) : "memory");
    { const rsrc_t fr_ = mkrs((wave & 1) ? wi : wr, S * DF * 2);
      pf2 = __builtin_amdgcn_raw_buffer_load_b32(fr_, (lane & 31) < 29 && lane < (!XBAL ? 32 : sj < 4 ? 0 : sj < 12 ? 32 : 64) ? (((16 * grp + (lane < 32 ? sj : sj - 12)) * 7200) & ~127) + ((wave >> 1) * 29 + (lane & 31)) * 128 : OOB, 0, PF_AUX);
    }
    bar();
    if (wave == 0 && qu >= 0) mod_publish(L, ws, qu, lane, tag);
    if (wave == 1 && lane < 8) st2c(ws, isC ? WS_RDY + lane * 1024 + pair * 8 : OOB, v2u{tag, ~tag});
    STAMP(1);
    pin(pf0); pin(pf1); pin(pf2); pin(pfa); pin(pfb);
    const int qn = sj >= 4 && sj < 10 && grp < 7 ? 28 + grp * 6 + (sj - 4) : !isC && idx < 15 ? 69 + idx : -1;
    const int tw = sj >= 10 ? (sj - 10) * 8 + grp : sj == 4 && grp == 7 ? 48 : -1;
    v2u dw[2][8];
    if (qn >= 0) mod_load(ml, cin, wm, qn, tid);
    if (tw >= 0) {
      const rsrc_t dr = mkrs(wd, F * D * 2);
      const int u0 = tw * 4 + wave, uc = u0 / 28, kap = 2 * (u0 - uc * 28);
#pragma unroll
      for (int e = 0; e < 16; ++e)
        dw[e >> 3][e & 7] = __builtin_amdgcn_raw_buffer_load_b64(dr, (4 * g * D + 4 * c16) * 2 + (e & 3) * D * 2, ((32 * kap + 16 * (e >> 2)) * D + 64 * uc) * 2, PF_AUX);
    }
    sb();
    u32 pw[4];
    {
      const rsrc_t rg = mkrs(wg, D * F * 2), ru = mkrs(wu, D * F * 2);
      const int r7 = lane / 7, r0 = W1EVEN ? (j * 4 + wave) * 8 : sj < 4 ? (sj * 4 + wave) * 16 : 256 + ((sj - 4) * 4 + wave) * 4;
      const int po = (r0 + r7) * (F * 2) + ((grp & 3) * 7 + lane - r7 * 7) * 128;
      const int o0 = lane < (W1EVEN ? (j < 14 ? 56 : 0) : sj < 4 ? 56 : 28) ? po : OOB, o1 = !W1EVEN && lane < 56 && sj < 4 ? po + 8 * (F * 2) : OOB;
      pw[0] = ld1(rg, o0); pw[1] = ld1(ru, o0); pw[2] = ld1(rg, o1); pw[3] = ld1(ru, o1);
    }
    sb();
    STAMP(20);
    v4f t1c[2], t1s[2], t2c[2], t2s[2];
#pragma unroll
    for (int t = 0; t < 2; ++t) {
#pragma unroll
      for (int r = 0; r < 4; ++r) {
        const float a1 = (float)((16 * t + 4 * g + r) * (16 * wave + c16)) * (1.f / S);
        const float a2 = (float)((16 * t + c16) * (16 * wave + 4 * g + r)) * (1.f / S);
        t1c[t][r] = __builtin_amdgcn_cosf(a1); t1s[t][r] = __builtin_amdgcn_sinf(a1);
        t2c[t][r] = __builtin_amdgcn_cosf(a2); t2s[t][r] = __builtin_amdgcn_sinf(a2);
      }
      pin(t1c[t]); pin(t1s[t]); pin(t2c[t]); pin(t2s[t]);
    }
    if (tw >= 0) {
#pragma unroll
      for (int tt = 0; tt < 8; ++tt) {
        v4u o;
#pragma unroll
        for (int m = 0; m < 4; ++m)
          o[m] = __builtin_amdgcn_perm(dw[tt >> 2][2 * m + 1][(tt & 3) >> 1], dw[tt >> 2][2 * m][(tt & 3) >> 1], (tt & 1) ? 0x07060302u : 0x05040100u);
        st4c(ws, WS_WD + (tw * 4 + wave) * 8192 + tt * 1024 + l16, o);
      }
    }
    if (qn >= 0) {
      bar();
      mod_part(ml, L, tid);
      bar();
      if (wave == 0) mod_publish(L, ws, qn, lane, tag);
    }
    pin(pw[0]); pin(pw[1]); pin(pw[2]); pin(pw[3]);
    STAMP(21);
    if (!isC) { DUMP(); return; }
    v4u c64[2][4], c32[2][2][2];
#pragma unroll
    for (int cp = 0; cp < 2; ++cp)
#pragma unroll
      for (int s = 0; s < 4; ++s) c64[cp][s] = ld4(tab, l16 + s * 1024, (TAB_P2 + wave * 2 + cp) * 4096);
#pragma unroll
    for (int t = 0; t < 2; ++t)
#pragma unroll
      for (int f = 0; f < 4; ++f) c32[t][f >> 1][f & 1] = ld4(tab, l16 + f * 1024, (TAB_P2 + 8 + t) * 4096);
    sb();
    if (tw >= 0) {
      asm volatile("s_waitcnt vmcnt(16)" ::: "memory");
      bar();
      if (tid < 8) st2c(ws, WS_DRDY + tid * 1024 + tw * 8, v2u{tag, ~tag});
    }
    STAMP(2);
    int wfo = WS_WT + pair * 16384 + (16 * g + wave) * 128 + c16 * 8;
    pin(wfo);
    v2u wf[2][4];
    {
      int wl = L_CI + ((lane >> 2) & 1) * 8256 + wave * 2048 + ((lane >> 3) * 4 + (lane & 3)) * 16, lo = t1o;
      pin(wl); pin(lo);
      light_poll(ws, WS_T1 + (32 * wave + (pair & 31)) * T1_BLK + pair * 128);
      STAMP(13);
      v4u z[4];
      _Pragma("nounroll") for (int s = 0; s < SPIN; ++s) {
        lo = fresh(lo);
#pragma unroll
        for (int q = 0; q < 4; ++q) z[q] = ld4c(ws, lo, 8 * q * T1_BLK);
        if (wall(max(max(max4(z[0]), max4(z[1])), max(max4(z[2]), max4(z[3]))) != SENT)) break;
      }
      STAMP(14);
#pragma unroll
      for (int q = 0; q < 4; ++q) *(LAS v4u*)(L + wl + q * 512) = z[q];
    }
    bar();
#pragma unroll
    for (int t = 0; t < 2; ++t)
#pragma unroll
      for (int r = 0; r < 4; ++r) wf[t][r] = ld2c(ws, wfo + r * 512, t * 8192);
    v2u fd = ld2c(ws, WS_DRDY + grp * 1024 + min(lane, 48) * 8);
    sb();
    STAMP(4);
    {
      v4u a[2][2][4];
#pragma unroll
      for (int c = 0; c < 2; ++c)
#pragma unroll
        for (int mt = 0; mt < 2; ++mt)
#pragma unroll
          for (int s = 0; s < 4; ++s) a[c][mt][s] = lds4(LB, L_CI + c * 8256 + (s * 2 + mt) * 1024);
      v4u old[2][2];
#pragma unroll
      for (int c = 0; c < 2; ++c)
#pragma unroll
        for (int mt = 0; mt < 2; ++mt) old[c][mt] = lds4(LB + wave * 2048, L_CI + c * 8256 + mt * 1024);
      v4f re[2][2], im[2][2], ur[2][2], ui[2][2], fr_[2][2], fi_[2][2];
      u32 f2[2][2][4], f3[2][2][4], f4[4];
      char LAS* xw = L + lb + wave * 1024 - L_B;
#define V4(x) v4u{x[0], x[1], x[2], x[3]}
#define S1K(h) c64[0][2 * h], c64[0][2 * h + 1], c64[1][2 * h], c64[1][2 * h + 1]
#define S1X(c, t, h) re[c][t], im[c][t], a[c][t][2 * h], a[c][t][2 * h + 1], S1K(h)
#define S2X(c, t) ur[c][t], ui[c][t], V4(f2[c][0]), V4(f2[c][1]), c32[t][0][0], c32[t][0][1], c32[t][1][0], c32[t][1][1]
#define S3X(c, t) im[c][t], re[c][t], V4(f3[c][0]), V4(f3[c][1]), c32[t][0][0], c32[t][0][1], c32[t][1][0], c32[t][1][1]
#define CM(o, P, Q, C, S_, h) o[2 * h], o[2 * h + 1], P[2 * h], Q[2 * h], C[2 * h], S_[2 * h], P[2 * h + 1], Q[2 * h + 1], C[2 * h + 1], S_[2 * h + 1]
#define TW1(c, t, h) CM(f2[c][t], re[c][t], im[c][t], t1c[t], t1s[t], h)
#define FLT(c, t, h) CM(f3[c][t], ur[c][t], ui[c][t], fr_[c][t], fi_[c][t], h)
#define TW2(c, t, h) CM(f4, re[c][t], im[c][t], t2c[t], t2s[t], h)
#define HV(x, h) (h ? hi2(x) : lo2(x))
#define CP(PL, o, P, Q, C, S_, h) cmul2<PL>(o[2 * h], o[2 * h + 1], HV(P, h), HV(Q, h), HV(C, h), HV(S_, h));
#define XW(c, t) *(LAS v4u*)(xw + L_CX + (c * 2 + t) * 4096) = V4(f4);
#define UNP(c, t) { _Pragma("unroll") for (int r = 0; r < 4; ++r) { fr_[c][t][r] = blo(wf[t][r][c]); fi_[c][t][r] = bhi(wf[t][r][c]); } \
      pin(fr_[c][t]); pin(fi_[c][t]); }
      nop2();
      m4_a0(S1X(0, 0, 0)); m4_a1(S1X(0, 0, 1)); m4_a0(S1X(0, 1, 0)); mfence<3>(re[0][0], im[0][0]);
      h4_a1m(S1X(0, 1, 1), TW1(0, 0, 0)); h4_a0m(S1X(1, 0, 0), TW1(0, 0, 1));
      h4_a1m(S1X(1, 0, 1), TW1(0, 1, 0)); h4_a0m(S1X(1, 1, 0), TW1(0, 1, 1));
      h4_a1m(S1X(1, 1, 1), TW1(1, 0, 0)); h4_b0m(S2X(0, 0), TW1(1, 0, 1));
      h4_b0m(S2X(0, 1), TW1(1, 1, 0));
      sb();
      CP(false, f2[1][1], re[1][1], im[1][1], t1c[1], t1s[1], 1)
      UNP(0, 0) UNP(0, 1)
      sb();
      h4_b0p(S2X(1, 0), FLT(0, 0, 0)); h4_b0p(S2X(1, 1), FLT(0, 0, 1));
      sb();
      CP(true, f3[0][1], ur[0][1], ui[0][1], fr_[0][1], fi_[0][1], 0) CP(true, f3[0][1], ur[0][1], ui[0][1], fr_[0][1], fi_[0][1], 1)
      UNP(1, 0) UNP(1, 1)
      sb();
      h4_a0p(S3X(0, 0), FLT(1, 0, 0)); h4_a0p(S3X(0, 1), FLT(1, 0, 1));
      sb();
      CP(true, f3[1][1], ur[1][1], ui[1][1], fr_[1][1], fi_[1][1], 0) CP(true, f3[1][1], ur[1][1], ui[1][1], fr_[1][1], fi_[1][1], 1)
#pragma unroll
      for (int e = 0; e < 4; ++e) pin(f3[1][1][e]);
      nop2();
      h4_a0p(S3X(1, 0), TW2(0, 0, 0)); h4_a0p(S3X(1, 1), TW2(0, 0, 1));
      XW(0, 0)
      sb();
      CP(true, f4, re[0][1], im[0][1], t2c[1], t2s[1], 0) CP(true, f4, re[0][1], im[0][1], t2c[1], t2s[1], 1) XW(0, 1)
      sb();
      mfence<0>(re[1][0], im[1][0], re[1][1], im[1][1]);
      CP(true, f4, re[1][0], im[1][0], t2c[0], t2s[0], 0) CP(true, f4, re[1][0], im[1][0], t2c[0], t2s[0], 1) XW(1, 0)
      CP(true, f4, re[1][1], im[1][1], t2c[1], t2s[1], 0) CP(true, f4, re[1][1], im[1][1], t2c[1], t2s[1], 1) XW(1, 1)
      bar();
#pragma unroll
      for (int c = 0; c < 2; ++c)
#pragma unroll
        for (int nt = 0; nt < 2; ++nt)
#pragma unroll
          for (int s = 0; s < 4; ++s) a[c][nt][s] = lds4(LB, L_CX + ((c * 2 + nt) * 4 + s) * 1024);
      const int o2 = WS_T1 + (32 * wave + (lane >> 2)) * T1_BLK + pair * 128 + (lane & 3) * 16;
#define S4(c, t) mf8(im[c][t], re[c][t], c64[0], a[c][t], c64[1], a[c][t]);
#define NEQ(x, y) ((x) == (y) ? (x) ^ 1u : (x))
#define OUT(c, t) { const v4u o = {pk(re[c][t].x, im[c][t].x), pk(re[c][t].y, im[c][t].y), pk(re[c][t].z, im[c][t].z), pk(re[c][t].w, im[c][t].w)}; \
      st4c(ws, o2 + c * 64 + t * 16 * T1_BLK, v4u{NEQ(o.x, old[c][t].x), NEQ(o.y, old[c][t].y), NEQ(o.z, old[c][t].z), NEQ(o.w, old[c][t].w)}); sb(); }
      S4(0, 0) S4(0, 1) mfence<0>(re[0][0], im[0][0]); OUT(0, 0)
      S4(1, 0) mfence<0>(re[0][1], im[0][1]); OUT(0, 1)
      S4(1, 1) mfence<0>(re[1][0], im[1][0]); OUT(1, 0)
      mfence<0>(re[1][1], im[1][1]); OUT(1, 1)
    }
    STAMP(5);
    sb();
    if (!isG) { DUMP(); return; }
    if (!wall(fd.x == tag && fd.y == ~tag)) wait_flags(ws, WS_DRDY + grp * 1024 + min(lane, 48) * 8, tag);
    dma_w1(wg, wu, slice, wave, lane, lbase);
#pragma unroll
    for (int f = 0; f < WD_MID; ++f) wdn[f >> 2][f & 3] = ld4c(ws, wl16 + (f & 3) * 1024, wdb + (f >> 2) * 4096);
    sb();
    ACT_CLEAR(0, 8)
    sb();
    asm volatile("s_waitcnt vmcnt(%0)" :: "i"(WD_MID + 8) : "memory");
    bar();
  }

  STAMP(7);
  const int blk0 = 16 * tile + 4 * (grp & 3);
  const int ab = WS_ACT + blk0 * 57344 + wave * 14 * 1024;
  v4u h[14][2];
  {
    const int hb = WS_H2 + (16 * tile + 4 * wave) * H2_BLK, hro = WS_HRDY + (16 * tile + 4 * wave) * 32 + (lane & 7) * 8;
    int tb[2][4];
#pragma unroll
    for (int u = 0; u < 8; ++u) {
      tb[u >> 2][u & 3] = (((lane >> 2) * 128 + ((lane >> 3) & 3) * 32 + (lane & 3) * 8) ^ ((u & 3) << 5)) + (u >> 2) * 57344;
      pin(tb[u >> 2][u & 3]);
    }
#define W_RD(dst, k, u) dst = ldtr2(L + tb[k >= 7][u & 3], (k % 7) * 8192 + (u >> 2) * 4096, (k % 7) * 8192 + (u >> 2) * 4096 + 2048)
#define H_LD(v, tt) hh_[v][tt] = ld4c(ws, l16 + (((v) % 14) & 3) * 1024, hb + (2 * ((v) / 14) + (tt)) * H2_BLK + (((v) % 14) >> 2) * 4096)
    const int abx[2] = {ab + (wave >> 1) * 57344, ab + (1 - (wave >> 1)) * 57344};
#define G_LD(e) h[(e) >> 1][(e) & 1] = ld4c(ws, l16 + (((e) >> 1) & 3) * 1024, abx[(e) & 1] + ((e) >> 3) * 4096)
    v4u wfr[2][8], hh_[28][2];
#pragma unroll
    for (int u = 0; u < 8; ++u) W_RD(wfr[0][u], 0, u);
    wait_flags(ws, hro, tag);
    light_poll(ws, hb + (lane & 1) * H2_BLK + 13 * 1024);
    STAMP(15);
#pragma unroll
    for (int v = 0; v < LEAD; ++v) { H_LD(v, 0); H_LD(v, 1); }
    sb();
    if (!isR && lane < (CLR_MODE == 1 ? 1 : 2))
      st2c(ws, WS_CRDY + ((tile + 2 * lane) * 4 + (grp & 3)) * 256 + (ct * 4 + wave) * 8, v2u{tag, ~tag});
    sb();
#define BAD2(x0, x1) wany(max(max(max(max(x0.x, x0.y), x0.z), max(max(x0.w, x1.x), x1.y)), max(x1.z, x1.w)) == SENT)
#define RETRY2(x0, x1, o0, o1) \
      _Pragma("nounroll") for (int s_ = 0; s_ < SPIN; ++s_) { \
        const int lf_ = fresh(l16); SLOWCNT \
        x0 = ld4c(ws, lf_, o0); x1 = ld4c(ws, lf_, o1); \
        if (wall(max(max4(x0), max4(x1)) != SENT)) break; \
      }
    if (__builtin_expect(BAD2(hh_[0][0], hh_[0][1]), 0)) { RETRY2(hh_[0][0], hh_[0][1], hb, hb + H2_BLK) }
    STAMP(8); CSTAMP(28);
    v4f acc[8][2];
    v2u fa, fb;
    const int fy = lane < 8 ? WS_ARDY + (tile * 4 + (grp & 3)) * 512 + (7 * wave + min(lane, 6)) * 8 : WS_XRDY + (blk0 + (wave >> 1) + (lane & 4 ? 2 : 0)) * 64 + (lane & 3) * 8;
    const int fx = WS_CRDY + (tile * 4 + (lane < 32 ? wave : grp & 3)) * 256 + min(lane & 31, 27) * 8;
    const int ao = WS_ACT + (16 * tile + 4 * wave) * 57344 + 2 * slice * 1024 + l16;
#define ACT_OUT(ps, tt, f) { v4u o; \
      _Pragma("unroll") for (int hh = 0; hh < 2; ++hh) { \
        const v4f av = relu4(acc[2 * f + hh][tt]) * acc[4 + 2 * f + hh][tt]; \
        o[2 * hh] = pk2(lo2(av)); o[2 * hh + 1] = pk2(hi2(av)); } \
      st4c(ws, ao + (2 * ps + tt) * 57344 + f * 1024, o); }
#define ACT_ALL(ps) { mfence<0>(acc[0][0], acc[1][0], acc[2][0], acc[3][0]); mfence<0>(acc[4][0], acc[5][0], acc[6][0], acc[7][0]); \
      ACT_OUT(ps, 0, 0) ACT_OUT(ps, 0, 1) sb(); \
      mfence<0>(acc[0][1], acc[1][1], acc[2][1], acc[3][1]); mfence<0>(acc[4][1], acc[5][1], acc[6][1], acc[7][1]); \
      ACT_OUT(ps, 1, 0) ACT_OUT(ps, 1, 1) sb(); }
#define W_RH(dst, k, u, hf) { const v2u t_ = ldtr(L + tb[k >= 7][u & 3], (k % 7) * 8192 + (u >> 2) * 4096 + hf * 2048); \
      dst[2 * hf] = t_.x; dst[2 * hf + 1] = t_.y; }
#pragma unroll
    for (int ps = 0; ps < 2; ++ps)
#pragma unroll
    for (int ks = 0; ks < 14; ++ks) {
      const int v = ps * 14 + ks, cur = v & 1, nk = (v + 1) % 14, vn = v < 27 ? v + 1 : v;
      const bool more = v < 27;
      u32 m0, m1;
      __builtin_amdgcn_s_waitcnt(0xC07F);
      sb();
#pragma unroll
      for (int i = 0; i < 16; ++i) {
        const int u = i & 7, tt = i >> 3;
        if (ks == 0) mv0(acc[u][tt], wfr[cur][u], hh_[v][tt]); else mv(acc[u][tt], wfr[cur][u], hh_[v][tt]);
        if (more) {
          if (i < 4) { W_RH(wfr[cur ^ 1][i], nk, i, 0) W_RH(wfr[cur ^ 1][i], nk, i, 1) }
          else if (i < 12) W_RH(wfr[cur ^ 1][4 + ((i - 4) >> 1)], nk, (4 + ((i - 4) >> 1)), (i & 1))
        }
        if (i == 5 && v + LEAD < 28) H_LD(v + LEAD, 0);
        if (i == 7 && v + LEAD < 28) H_LD(v + LEAD, 1);
        if (i == 9 || i == 11 || i == 13) {
          const int e = (i - 9) >> 1, f = WD_MID + (v < 14 ? v - 2 : 12 + WD_PER * (v - 14) + e);
          if (f < 56 && ((v >= 2 && v < 14 && e == 0) || (v >= 14 && e < WD_PER))) wdn[f >> 2][f & 3] = ld4c(ws, wl16 + (f & 3) * 1024, wdb + (f >> 2) * 4096);
        }
        if (v > A_V + 1 && (i & 1) && i >= 5 && ((i - 5) >> 1) < A_PER) G_LD(A_PER * (v - A_V - 2) + ((i - 5) >> 1));
        if (v == 10 - LEAD && i == 15) fa = ld2c(ws, hro + 64);
        if (v == 16 && i == 15) fa = ld2c(ws, fx);
        if (v == A_V && i == 15) fb = ld2c(ws, fy);
        if (more && i == 12) { m0 = max(max(hh_[vn][0].x, hh_[vn][0].y), hh_[vn][0].z); pin(m0); }
        if (more && i == 13) { m1 = max(max(hh_[vn][0].w, hh_[vn][1].x), hh_[vn][1].y); pin(m1); }
        if (more && i == 14) { m0 = max(max(m0, hh_[vn][1].z), hh_[vn][1].w); m0 = max(m0, m1); pin(m0); }
        sb();
      }
      if (v == 13 - LEAD && !wall(fa.x == tag && fa.y == ~tag)) wait_flags(ws, hro + 64, tag);
      if (v == 20 && !wall(fa.x == tag && fa.y == ~tag)) wait_flags(ws, fx, tag);
      if (v == F_V) {
        static_assert(F_V + LEAD < 28, "the count below");
        asm volatile("s_waitcnt vmcnt(%0)" :: "i"(2 * (F_V - 13)) : "memory");
        if (lane < 2) st2c(ws, lane == 0 ? WS_ARDY + (tile * 4 + wave) * 512 + slice * 8 : isR ? WS_XRDY + (16 * grp + j - 14) * 64 + wave * 8 : OOB, v2u{tag, ~tag});
      }
      if (v == A_V + 1 && !wall(fb.x == tag && fb.y == ~tag)) wait_flags(ws, fy, tag);
      if (more && __builtin_expect(wany(m0 == SENT), 0)) {
        const int pb = 2 * ((v + 1) / 14) * H2_BLK + nk * 1024;
        RETRY2(hh_[vn][0], hh_[vn][1], hb + pb, hb + pb + H2_BLK)
      }
      if (v == 13) {
        STAMP(16); CSTAMP(29);
        ACT_ALL(0)
        if (isR) x2_store(ws, x2r, 16 * grp + j - 14, tid);
        nop2();
      }
      sb();
    }
    STAMP(27);
#pragma unroll
    for (int e = A_PER * (26 - A_V); e < A_PRE; ++e) G_LD(e);
    sb();
    ACT_ALL(1)
    CSTAMP(30);
#pragma unroll
    for (int e = A_PRE; e < 28; ++e) G_LD(e);
    sb();
    STAMP(9);
    v4u a[14][2];
    v4f ac2[4][4];
#define A_LD(e) a[(e) >> 1][(e) & 1] = ld4c(ws, l16 + (((e) >> 1) & 3) * 1024, abx[(e) & 1] + 2 * 57344 + ((e) >> 3) * 4096)
    const int po = ab + 3 * 57344 + min(lane, 6) * 2048 + 1024;
    u32 probe;
#pragma unroll
    for (int ks = 0; ks < 14; ++ks) {
#pragma unroll
      for (int tt = 0; tt < 2; ++tt)
#pragma unroll
        for (int n = 0; n < 4; ++n) ac2[tt][n] = mfma(wdn[ks][n], h[ks][tt], ks ? ac2[tt][n] : splat(0.f));
      if (ks == P_AT) probe = ld1c(ws, po);
      sb();
      if (ks == B_AT2) {
        STAMP(25);
        if (wany(probe == SENT)) light_poll(ws, po);
        STAMP(26);
      }
      if (ks >= B_AT2) {
#pragma unroll
        for (int e = 4 * (ks - B_AT2); e < 4 * (ks - B_AT2 + 1); ++e) A_LD(e);
      }
      sb();
    }
    STAMP(17);
#pragma unroll
    for (int e = 4 * (14 - B_AT2); e < 4 * (14 - B_AT2) + 6; ++e) A_LD(e);
    sb();
    int rw[3], rr_ = L_B + lane * 8 + wave * 6144, oo[2];
#pragma unroll
    for (int x = 1; x < 4; ++x) { rw[x - 1] = L_B + lane * 8 + (wave ^ x) * 6144 + (x - 1) * 2048; pin(rw[x - 1]); }
    pin(rr_);
    const int blk = blk0 + (wave >> 1);
#pragma unroll
    for (int ph = 0; ph < 2; ++ph) { oo[ph] = (tok_row(blk + 2 * ph, c16) * D + ct * 64 + g * 16 + (wave & 1) * 8) * 2; pin(oo[ph]); }
#define RED_W1(ph, x, n) *(LAS v2f*)(L + rw[(x) - 1] + (ph) * 24576 + (n) * 512) = ((x) & 1) ? hi2(ac2[2 * (ph) + ((x) >> 1)][n]) : lo2(ac2[2 * (ph) + ((x) >> 1)][n]);
    const int xo = WS_X2 + blk * 14336 + c16 * 896 + ct * 128 + g * 32 + (wave & 1) * 16;
    const int go = WS_MOD + grp * MOD_REP + (70 + 2 * ct) * 128 + g * 64 + (wave & 1) * 32;
    v4u xv[2], gv[2];
    STAMP(18);
    sb();
#pragma unroll
    for (int ks = 0; ks < 14; ++ks) {
      if (__builtin_expect(BAD2(a[ks][0], a[ks][1]), 0)) { RETRY2(a[ks][0], a[ks][1], abx[0] + 2 * 57344 + ks * 1024, abx[1] + 2 * 57344 + ks * 1024) }
#pragma unroll
      for (int tt = 0; tt < 2; ++tt)
#pragma unroll
        for (int n = 0; n < 4; ++n) ac2[2 + tt][n] = mfma(wdn[ks][n], a[ks][tt], ks ? ac2[2 + tt][n] : splat(0.f));
#pragma unroll
      for (int e = 4 * (14 - B_AT2) + 6 + 3 * ks; e < 4 * (14 - B_AT2) + 9 + 3 * ks; ++e)
        if (e < 28) A_LD(e);
      if (ks == 6) { xv[0] = ld4c(ws, xo); xv[1] = ld4c(ws, xo + 2 * 14336); gv[0] = ld4c(ws, go); gv[1] = ld4c(ws, go + 16); }
      if (ks < 12) RED_W1(0, 1 + (ks >> 2), (ks & 3))
#pragma unroll
      for (int u = 0; u < 4; ++u) {
        __builtin_amdgcn_sched_group_barrier(0x8, 2, 0);
        __builtin_amdgcn_sched_group_barrier(0x20, 1, 0);
        if (u == 2) __builtin_amdgcn_sched_group_barrier(0x200, 1, 0);
      }
      sb();
    }
    STAMP(10);
#pragma unroll
    for (int e = 0; e < 12; ++e) RED_W1(1, 3 - (e >> 2), (e & 3))
#define FIN_OK() wall(gv[0].y == tag && gv[0].w == tag && gv[1].y == tag && gv[1].w == tag)
    if (__builtin_expect(!FIN_OK(), 0)) {
      _Pragma("nounroll") for (int s = 0; s < SPIN; ++s) {
        const int gf = fresh(go), xf = fresh(xo);
        gv[0] = ld4c(ws, gf); gv[1] = ld4c(ws, gf + 16); xv[0] = ld4c(ws, xf); xv[1] = ld4c(ws, xf + 2 * 14336);
        if (FIN_OK()) break;
      }
    }
    bar();
#pragma unroll
    for (int ph = 0; ph < 2; ++ph) {
      v2f r[4];
#pragma unroll
      for (int n = 0; n < 4; ++n) {
        r[n] = lo2(ac2[2 * ph][n]);
#pragma unroll
        for (int w = 0; w < 3; ++w) r[n] += *(const LAS v2f*)(L + rr_ + w * 2048 + ph * 24576 + n * 512);
      }
      v4u o;
#pragma unroll
      for (int q = 0; q < 2; ++q) {
        o[2 * q] = pk2(fma2(bun(gv[q].x), v2f{r[0][q], r[1][q]}, bun(xv[ph][2 * q])));
        o[2 * q + 1] = pk2(fma2(bun(gv[q].z), v2f{r[2][q], r[3][q]}, bun(xv[ph][2 * q + 1])));
      }
      __builtin_amdgcn_raw_buffer_store_b128(o, mkrs(out, S * D * 2), oo[ph], 0, OUT_AUX);
    }
    if (blockIdx.x == 0 && wave == 0) {
      _Pragma("nounroll") for (int s = 0; s < SPIN; ++s) {
        const v4u v = ld4c(ws, fresh(WS_MOD + (70 + (lane < 14 ? lane : 0)) * 128));
        if (wall(v.y == tag)) break;
      }
      if (lane == 0) __builtin_amdgcn_raw_buffer_store_b32(epoch + 1u, ws, WS_EPOCH, 0, SC1);
    }
  }
  STAMP(11);
  DUMP();
}

extern "C" void kernel_launch(void* const* d_in, const int* in_sizes, int n_in, void* d_out, int out_size, void* d_ws,
                              size_t ws_size, hipStream_t stream) {
  fused<<<256, 256, 0, stream>>>((const u16*)d_in[0], (const u16*)d_in[1], (const u16*)d_in[2], (const u16*)d_in[4],
                                 (const u16*)d_in[5], (const u16*)d_in[6], (const u16*)d_in[7], (const u16*)d_in[8],
                                 (u16*)d_out, (char*)d_ws);
}
```

```cpp
#define WD_EARLY 20
#define WD_MID 20
#include <hip/hip_runtime.h>

typedef unsigned short u16;
typedef unsigned int u32;
typedef unsigned long long u64;
typedef u32 v4u __attribute__((ext_vector_type(4)));
typedef u32 v2u __attribute__((ext_vector_type(2)));
typedef float v4f __attribute__((ext_vector_type(4)));
typedef float v2f __attribute__((ext_vector_type(2)));
typedef short v8s __attribute__((ext_vector_type(8)));
typedef short v4s __attribute__((ext_vector_type(4)));
typedef __bf16 v8b __attribute__((ext_vector_type(8)));
typedef __bf16 v2b __attribute__((ext_vector_type(2)));
#define LAS __attribute__((address_space(3)))
#define GAS __attribute__((address_space(1)))
#define DEV __device__ __forceinline__

#define C_W1_EARLY 0
#define CLR_DELAY 0
#define W_DELAY 0
#define CLR2_DELAY 0
#define PF_POS 2
#define SL1 1
#define SL2 2
#define SL4 4
#define X_AFTER 8
#define PF_AUX 2
#define W_LATE 1
#define W1_PF 1
#define DMA_MOD ""
#define LEAD 6
#define A_V 22
#define A_PER 4
#define WDV(f) ((f) - WD_MID + 2 < 14 ? (f) - WD_MID + 2 : ((f) - WD_MID - 12) / 2 + 16)
#define B_AT2 12
#define RE_PF 0
#define PF2_DELAY 0
#define C_DMA_EARLY 0
#define EVEN_PF 0
#define XBAL 1
#define W1EVEN 1
#define PRE_OUT 0
#define OUT_AUX 16
#define CLR_MODE 0
#define P_AT 7
#define WD_PER 3
#define A_FIRST 10
#define B_FIRST 10
#define A_AT 9
#define F_AT 4
#define CODE_BYTES 45056
#define B_AT 9
constexpr int S = 2048, D = 448, DF = 225, F = 1792;

struct alignas(16) Chain { u16 v[2048]; };
struct Cos448 { u16 v[448]; };
constexpr double c_cos_t(double a) {
  double a2 = a * a, t = 1.0, s = 1.0;
  for (int i = 1; i <= 10; ++i) { t = -t * a2 / ((2 * i - 1) * (2 * i)); s += t; }
  return s;
}
constexpr double c_sin_t(double a) {
  double a2 = a * a, t = a, s = a;
  for (int i = 1; i <= 10; ++i) { t = -t * a2 / ((2 * i) * (2 * i + 1)); s += t; }
  return s;
}
constexpr double c_cos448(int m) {
  if (m > 224) m = 448 - m;
  double sg = 1.0;
  if (m > 112) { m = 224 - m; sg = -1.0; }
  const double w = 6.283185307179586476925286766559 / 448.0;
  return m == 112 ? 0.0 : (m <= 56 ? sg * c_cos_t(w * m) : sg * c_sin_t(w * (112 - m)));
}
constexpr u16 c_bf16(double d) {
  const u32 u = __builtin_bit_cast(u32, (float)d);
  return (u16)((u + 0x7FFFu + ((u >> 16) & 1u)) >> 16);
}
constexpr Cos448 mk_cos() { Cos448 c{}; for (int m = 0; m < 448; ++m) c.v[m] = c_bf16(c_cos448(m)); return c; }
constexpr Cos448 CTAB = mk_cos();
constexpr u16 bcos(int m) { return CTAB.v[m % 448]; }
constexpr u16 bsin(int m) { return CTAB.v[(m % 448 + 336) % 448]; }
constexpr u16 bneg(u16 b) { return b ? (u16)(b ^ 0x8000u) : b; }
constexpr u16 bscale(u16 b, int e) { return b ? (u16)(b + e * 0x80) : b; }

constexpr Chain mk_p1(int c) {
  const int tile = c >> 2, par = c & 1, sn = (c >> 1) & 1;
  Chain ch{};
  for (int s = 0; s < 4; ++s)
    for (int l = 0; l < 64; ++l) {
      const int kp = 16 * tile + (l & 15), k = 2 * kp + par;
      if (k > 224) continue;
      for (int j = 0; j < 8; ++j) {
        const int dl = 32 * s + 8 * (l >> 4) + j;
        if (dl > 112) continue;
        u16 b = sn ? bneg(bsin(k * dl)) : bcos(k * dl);
        if (dl == 0 || dl == 112) b = bscale(b, -1);
        ch.v[(s * 64 + l) * 8 + j] = b;
      }
    }
  return ch;
}
constexpr Chain mk_p3(int c) {
  const int tile = c >> 1, sn = c & 1;
  Chain ch{};
  for (int s = 0; s < 4; ++s)
    for (int l = 0; l < 64; ++l) {
      int dl = 16 * tile + (l & 15);
      if (c == 1 && dl == 0) dl = 112;
      for (int j = 0; j < 8; ++j) {
        const int k = 2 * (32 * s + 8 * (l >> 4) + j) + 1;
        if (k < 224) ch.v[(s * 64 + l) * 8 + j] = sn ? bsin(k * dl) : bcos(k * dl);
      }
    }
  return ch;
}
constexpr Chain mk_p2(int c) {
  Chain ch{};
  for (int f = 0; f < 4; ++f)
    for (int l = 0; l < 64; ++l)
      for (int j = 0; j < 8; ++j) {
        const int comp = c < 8 ? (c & 1) : (f >> 1), s = c < 8 ? f : (f & 1);
        const int o = 16 * (c < 8 ? (c >> 1) : (c - 8)) + (l & 15), i = 16 * s + 4 * (l >> 4) + (j >> 1);
        const int m = (c < 8 ? 7 : 14) * ((i * o) % 64);
        u16 b = 0;
        if (comp == 0) b = (j & 1) ? bsin(m) : bcos(m);
        else b = (j & 1) ? bcos(m) : bneg(bsin(m));
        ch.v[(f * 64 + l) * 8 + j] = b;
      }
  return ch;
}
template <int I> constexpr Chain P1C = mk_p1(I);
template <int I> constexpr Chain P3C = mk_p3(I);
template <int I> constexpr Chain P2C = mk_p2(I);
#define L10(T, b) T<b>, T<b + 1>, T<b + 2>, T<b + 3>, T<b + 4>, T<b + 5>, T<b + 6>, T<b + 7>, T<b + 8>, T<b + 9>
#define L29(T) L10(T, 0), L10(T, 10), T<20>, T<21>, T<22>, T<23>, T<24>, T<25>, T<26>, T<27>, T<28>
constexpr int TAB_P1 = 0, TAB_P3 = 29, TAB_P2 = 43, TAB_N = 53;
static __device__ const Chain TAB[TAB_N] = {L29(P1C), L10(P3C, 0), P3C<10>, P3C<11>, P3C<12>, P3C<13>, L10(P2C, 0)};

constexpr int WS_EPOCH = 0;
constexpr int WS_RDY = 4096;
constexpr int WS_HRDY = 4096 + 8192;
constexpr int WS_DRDY = 4096 + 16384;
constexpr int WS_XRDY = 131072 + 65536;
constexpr int WS_ARDY = 131072;
constexpr int WS_CRDY = 131072 + 32768;
constexpr int WS_MOD = 32768, MOD_REP = 12288;
constexpr int T1_BLK = 226 * 64;
constexpr int WS_T1 = 1 << 18;
constexpr int WS_WT = WS_T1 + 128 * T1_BLK;
constexpr int WS_H2 = WS_WT + 113 * 16384;
constexpr int WS_X2 = WS_H2 + 128 * 14336;
constexpr int WS_WD = WS_X2 + 128 * 14336;
constexpr int WS_ACT = WS_WD + 7 * 56 * 4096;
constexpr int WS_END = WS_ACT + 128 * 57344;
constexpr int WS_DBG = 32 << 20;

constexpr int L_W1 = 0;
constexpr int L_B = 114688;
constexpr int XP = 912;
constexpr int L_XS = L_B;
constexpr int L_ST = L_XS + 16 * XP;
constexpr int L_M1 = L_ST + 128;
constexpr int L_AF = L_M1 + 4 * 1792;
constexpr int L_M2 = L_AF + 16384;
constexpr int L_EX = L_M2 + 3 * 1792;
constexpr int L_REND = L_EX + 512;
constexpr int ZPL = 8192;
constexpr int L_CI = L_B;
constexpr int L_CX = L_CI + 2 * 8256;
constexpr int L_RED = L_CX + 16384;
constexpr int L_DBG = 163840 - 1024;
constexpr int L_SZ = 163840;
static_assert(L_REND <= L_DBG && L_RED + 2048 <= L_DBG && 2 * ZPL <= 16384, "lds");

constexpr u32 SENT = 0xFFFFFFFFu;
constexpr int SPIN = 1 << 14;
constexpr int SC1 = 16;
constexpr int OOB = 0x7FFF0000;

typedef __amdgpu_buffer_rsrc_t rsrc_t;
DEV rsrc_t mkrs(const void* p, int bytes) { return __builtin_amdgcn_make_buffer_rsrc((void*)p, 0, bytes, 0x00020000); }
DEV v4u ld4(rsrc_t r, int off, int so = 0) { return __builtin_amdgcn_raw_buffer_load_b128(r, off, so, 0); }
DEV v2u ld2(rsrc_t r, int off, int so = 0) { return __builtin_amdgcn_raw_buffer_load_b64(r, off, so, 0); }
DEV v4u ld4c(rsrc_t r, int off, int so = 0) { return __builtin_amdgcn_raw_buffer_load_b128(r, off, so, SC1); }
DEV v2u ld2c(rsrc_t r, int off, int so = 0) { return __builtin_amdgcn_raw_buffer_load_b64(r, off, so, SC1); }
DEV u32 ld1c(rsrc_t r, int off, int so = 0) { return __builtin_amdgcn_raw_buffer_load_b32(r, off, so, SC1); }
DEV void st4c(rsrc_t r, int off, v4u v) { __builtin_amdgcn_raw_buffer_store_b128(v, r, off, 0, SC1); }
DEV void st2c(rsrc_t r, int off, v2u v) { __builtin_amdgcn_raw_buffer_store_b64(v, r, off, 0, SC1); }
DEV float blo(u32 v) { return __builtin_bit_cast(float, v << 16); }
DEV float bhi(u32 v) { return __builtin_bit_cast(float, v & 0xFFFF0000u); }
DEV v2f bun(u32 v) { return v2f{blo(v), bhi(v)}; }
DEV u32 pk(float lo, float hi) { return __builtin_bit_cast(u32, __builtin_convertvector(v2f{lo, hi}, v2b)); }
DEV u32 pk2(v2f v) { return __builtin_bit_cast(u32, __builtin_convertvector(v, v2b)); }
DEV float rb(float f) { return blo(pk(f, 0.f)); }
DEV v2f lo2(v4f v) { return __builtin_shufflevector(v, v, 0, 1); }
DEV v2f hi2(v4f v) { return __builtin_shufflevector(v, v, 2, 3); }
DEV v2f fma2(v2f a, v2f b, v2f c) { return __builtin_elementwise_fma(a, b, c); }
DEV v4f fma4(v4f a, v4f b, v4f c) { return __builtin_elementwise_fma(a, b, c); }
typedef int v4i __attribute__((ext_vector_type(4)));
DEV v4f relu4(v4f v) { return __builtin_bit_cast(v4f, __builtin_elementwise_max(__builtin_bit_cast(v4i, v), v4i{0, 0, 0, 0})); }
DEV v4f splat(float f) { return v4f{f, f, f, f}; }
DEV v4f mfma(v4u a, v4u b, v4f c) {
  return __builtin_amdgcn_mfma_f32_16x16x32_bf16(__builtin_bit_cast(v8b, a), __builtin_bit_cast(v8b, b), c, 0, 0, 0);
}
DEV void mfa(v4f& c, v4u a, v4u b) { asm volatile("s_nop 1\n\tv_mfma_f32_16x16x32_bf16 %0, %1, %2, %0" : "+v"(c) : "v"(a), "v"(b)); }
DEV void mf0(v4f& c, v4u a, v4u b) { asm volatile("s_nop 1\n\tv_mfma_f32_16x16x32_bf16 %0, %1, %2, 0" : "=&v"(c) : "v"(a), "v"(b)); }
template <int N> DEV void mfence(v4f& a, v4f& b) { asm volatile("s_nop %2" : "+v"(a), "+v"(b) : "i"(N)); }
template <int N> DEV void mfence(v4f& a, v4f& b, v4f& c, v4f& d) { asm volatile("s_nop %4" : "+v"(a), "+v"(b), "+v"(c), "+v"(d) : "i"(N)); }
#define MF "v_mfma_f32_16x16x32_bf16 "
DEV void mf8(v4f& d0, v4f& d1, const v4u (&a0)[4], const v4u (&b0)[4], const v4u (&a1)[4], const v4u (&b1)[4]) {
  asm volatile("s_nop 1\n\t" MF "%0, %2, %6, 0\n\t" MF "%1, %10, %14, 0\n\t" MF "%0, %3, %7, %0\n\t" MF "%1, %11, %15, %1\n\t"
               MF "%0, %4, %8, %0\n\t" MF "%1, %12, %16, %1\n\t" MF "%0, %5, %9, %0\n\t" MF "%1, %13, %17, %1"
               : "=&v"(d0), "=&v"(d1)
               : "v"(a0[0]), "v"(a0[1]), "v"(a0[2]), "v"(a0[3]), "v"(b0[0]), "v"(b0[1]), "v"(b0[2]), "v"(b0[3]),
                 "v"(a1[0]), "v"(a1[1]), "v"(a1[2]), "v"(a1[3]), "v"(b1[0]), "v"(b1[1]), "v"(b1[2]), "v"(b1[3]));
}
#define H4_M(XA, D, X, K, C) MF D ", " XA(X, K) ", " C "\n\t"
#define H4_XA(X, K) X ", " K
#define H4_XB(X, K) K ", " X
#define H4_DEF(NAME, XA, C0, C1, CONS, N0, N1, PA, PB, PC, PD) \
DEV void NAME(v4f& d0, v4f& d1, v4u x0, v4u x1, v4u k00, v4u k01, v4u k10, v4u k11, \
              u32& o0, u32& o1, float p0, float q0, float c0, float s0, float p1, float q1, float c1, float s1) { \
  float t0, t1, t2, t3; \
  asm volatile(H4_M(XA, "%0", "%8", "%10", C0) "v_mul_f32 %4, %14, %16\n\tv_mul_f32 %5, %15, %16\n\t" \
               H4_M(XA, "%1", "%8", "%12", C1) "v_mul_f32 %6, %18, %20\n\tv_mul_f32 %7, %19, %20\n\t" \
               H4_M(XA, "%0", "%9", "%11", "%0") "v_fma_f32 %4, " N0 "%15, %17, %4\n\tv_fma_f32 %5, " N1 "%14, %17, %5\n\t" \
               H4_M(XA, "%1", "%9", "%13", "%1") "v_fma_f32 %6, " N0 "%19, %21, %6\n\tv_fma_f32 %7, " N1 "%18, %21, %7\n\t" \
               "v_cvt_pk_bf16_f32 %2, " PA ", " PB "\n\tv_cvt_pk_bf16_f32 %3, " PC ", " PD \
               : CONS(d0), CONS(d1), "=&v"(o0), "=&v"(o1), "=&v"(t0), "=&v"(t1), "=&v"(t2), "=&v"(t3) \
               : "v"(x0), "v"(x1), "v"(k00), "v"(k01), "v"(k10), "v"(k11), \
                 "v"(p0), "v"(q0), "v"(c0), "v"(s0), "v"(p1), "v"(q1), "v"(c1), "v"(s1)); \
}
H4_DEF(h4_a0m, H4_XA, "0", "0", "=&v", "", "-", "%4", "%5", "%6", "%7")
H4_DEF(h4_a1m, H4_XA, "%0", "%1", "+v", "", "-", "%4", "%5", "%6", "%7")
H4_DEF(h4_b0m, H4_XB, "0", "0", "=&v", "", "-", "%4", "%5", "%6", "%7")
H4_DEF(h4_b0p, H4_XB, "0", "0", "=&v", "-", "", "%5", "%4", "%7", "%6")
H4_DEF(h4_a0p, H4_XA, "0", "0", "=&v", "-", "", "%5", "%4", "%7", "%6")
#define M4_DEF(NAME, XA, C0, C1, CONS) \
DEV void NAME(v4f& d0, v4f& d1, v4u x0, v4u x1, v4u k00, v4u k01, v4u k10, v4u k11) { \
  asm volatile(H4_M(XA, "%0", "%2", "%4", C0) H4_M(XA, "%1", "%2", "%6", C1) H4_M(XA, "%0", "%3", "%5", "%0") MF "%1, " XA("%3", "%7") ", %1" \
               : CONS(d0), CONS(d1) : "v"(x0), "v"(x1), "v"(k00), "v"(k01), "v"(k10), "v"(k11)); \
}
M4_DEF(m4_a0, H4_XA, "0", "0", "=&v") M4_DEF(m4_a1, H4_XA, "%0", "%1", "+v")
M4_DEF(m4_b0, H4_XB, "0", "0", "=&v") M4_DEF(m4_b1, H4_XB, "%0", "%1", "+v")
DEV void mv(v4f& c, v4u a, v4u b) { asm volatile(MF "%0, %1, %2, %0" : "+v"(c) : "v"(a), "v"(b)); }
DEV void mv0(v4f& c, v4u a, v4u b) { asm volatile(MF "%0, %1, %2, 0" : "=&v"(c) : "v"(a), "v"(b)); }
DEV void nop2() { asm volatile("s_nop 1"); }
template <class T> DEV void pin(T& v) { asm volatile("" : "+v"(v)); }
DEV int fresh(int off) { asm volatile("" : "+v"(off) :: "memory"); return off; }
DEV bool wall(bool ok) { return __builtin_amdgcn_ballot_w64(!ok) == 0; }
DEV bool wany(bool b) { return __builtin_amdgcn_ballot_w64(b) != 0; }
DEV void sb() { __builtin_amdgcn_sched_barrier(0); }
DEV u32 max4(v4u v) { return max(max(v.x, v.y), max(v.z, v.w)); }
DEV void drain() { asm volatile("s_waitcnt vmcnt(0)" ::: "memory"); }
DEV void cfence() { asm volatile("" ::: "memory"); }
DEV void bar() { asm volatile("s_waitcnt lgkmcnt(0)\n\ts_barrier" ::: "memory"); }
template <int CTRL> DEV float dpp(float v) {
  return __builtin_bit_cast(float, __builtin_amdgcn_update_dpp(0, __builtin_bit_cast(int, v), CTRL, 0xF, 0xF, true));
}
DEV float sum16(float v) {
  v += dpp<0x128>(v); v += dpp<0x124>(v); v += dpp<0x122>(v); v += dpp<0x121>(v); return v;
}
DEV void sumg2(float& a, float& b) {
  float c = a, d = b;
  asm volatile("s_nop 1\n\tv_permlane16_swap_b32 %0, %1\n\tv_permlane16_swap_b32 %2, %3" : "+v"(a), "+v"(c), "+v"(b), "+v"(d));
  a += c; b += d; c = a; d = b;
  asm volatile("s_nop 1\n\tv_permlane32_swap_b32 %0, %1\n\tv_permlane32_swap_b32 %2, %3" : "+v"(a), "+v"(c), "+v"(b), "+v"(d));
  a += c; b += d;
}
DEV v4u lds4(const char LAS* L, int off) { return *(const LAS v4u*)(L + off); }
DEV v4f lds4f(const char LAS* L, int off) { return *(const LAS v4f*)(L + off); }
DEV v2u ldtr(const char LAS* L, int off) { return __builtin_bit_cast(v2u, __builtin_amdgcn_ds_read_tr16_b64_v4i16((LAS v4s*)(L + off))); }
DEV v4u ldtr2(const char LAS* L, int o0, int o1) { const v2u a = ldtr(L, o0), b = ldtr(L, o1); return v4u{a.x, a.y, b.x, b.y}; }
DEV void dma16(rsrc_t r, int voff, int soff, u32 lds_dst) {
  u32 keep;
  asm volatile("s_mov_b32 %0, m0\n\ts_mov_b32 m0, %1\n\ts_nop 0\n\tbuffer_load_dwordx4 %2, %3, %4 offen" DMA_MOD " lds\n\ts_mov_b32 m0, %0"
               : "=&s"(keep) : "s"(lds_dst), "v"(voff), "s"(r), "s"(soff) : "memory");
}
DEV void light_poll(rsrc_t ws, int off) {
  _Pragma("nounroll") for (int s = 0; s < SPIN; ++s) {
    if (wall(ld1c(ws, fresh(off)) != SENT)) break;
    __builtin_amdgcn_s_sleep(SL2);
  }
}
DEV int tok_row(int blk, int t) {
  const int n1 = 16 * (blk >> 5) + 4 * ((blk >> 2) & 3) + (t & 3), n2 = 16 * ((blk >> 4) & 1) + 4 * (blk & 3) + (t >> 2);
  return n2 + 32 * n1;
}
template <bool SWAP> DEV v4u cmul_pack(v4f a, v4f b, v4f c, v4f s) {
  const v4f re = fma4(b, s, a * c), im = fma4(b, c, -(a * s));
  return SWAP ? v4u{pk(im.x, re.x), pk(im.y, re.y), pk(im.z, re.z), pk(im.w, re.w)}
              : v4u{pk(re.x, im.x), pk(re.y, im.y), pk(re.z, im.z), pk(re.w, im.w)};
}

template <bool PLUS> DEV void cmul2(u32& o0, u32& o1, v2f p, v2f q, v2f c, v2f s) {
  const v2f re = fma2(PLUS ? -q : q, s, p * c), im = fma2(PLUS ? p : -p, s, q * c);
  o0 = PLUS ? pk(im.x, re.x) : pk(re.x, im.x); o1 = PLUS ? pk(im.y, re.y) : pk(re.y, im.y);
}

#define STAMP(i)
#define CSTAMP(i)
#define SLOWCNT
#define DUMP()

DEV void dma_w1(const u16* wg, const u16* wu, int slice, int wave, int lane, u32 lbase) {
  const rsrc_t rg = mkrs(wg, D * F * 2), ru = mkrs(wu, D * F * 2);
  const int r = lane >> 3, ch = ((((lane & 7) >> 1) ^ (lane >> 4)) & 3) * 32 + (lane & 1) * 16 + slice * 128;
  const int vp = r * (F * 2) + ch, vn = (7 - r) * (F * 2) + ch;
#pragma unroll
  for (int n = 0; n < 4; ++n) {
    const int ks = wave + 4 * n;
    if (n == 3 && wave >= 2) break;
    const int t16 = (ks >> 1) * 16, a = ks & 1;
#pragma unroll
    for (int q = 0; q < 4; ++q) {
      const int dl0 = t16 + 8 * (q & 1);
      const bool pos = (q >> 1) == a;
      const int d0 = q < 2 ? (a ? 224 - dl0 - 7 : dl0) : (a ? 224 + dl0 : 448 - dl0 - 7);
      int vo = pos ? vp : vn, so = d0 * (F * 2);
      if (n == 0 && q == 2) {
        vo += so; so = 0;
        if (wave < 2 && lane < 8) vo = (wave ? 336 : 112) * (F * 2) + ch;
      }
      dma16(rg, vo, so, lbase + L_W1 + ks * 8192 + q * 1024);
      dma16(ru, vo, so, lbase + L_W1 + ks * 8192 + 4096 + q * 1024);
    }
  }
}
struct ModLoad { u32 cv[7]; v4u mw[7]; };
DEV void mod_load(ModLoad& m, const u16* cin, const u16* wm, int q, int tid) {
  const rsrc_t cr = mkrs(cin, q >= 0 ? D * 2 : 0), mr = mkrs(wm, q >= 0 ? D * 6 * D * 2 : 0);
#pragma unroll
  for (int i = 0; i < 7; ++i) m.cv[i] = __builtin_amdgcn_raw_buffer_load_b16(cr, (tid >> 2) * 2 + i * 128, 0, 0);
#pragma unroll
  for (int i = 0; i < 7; ++i) m.mw[i] = __builtin_amdgcn_raw_buffer_load_b128(mr, ((tid >> 2) * (6 * D) + q * 32 + (tid & 3) * 8) * 2, i * 64 * 6 * D * 2, PF_AUX);
}
DEV void mod_part(ModLoad& m, char LAS* L, int tid) {
  float sl[7];
#pragma unroll
  for (int i = 0; i < 7; ++i) { const float cc = blo(m.cv[i]); sl[i] = rb(cc * __builtin_amdgcn_rcpf(1.f + __expf(-cc))); pin(sl[i]); }
  v2f a[4];
#pragma unroll
  for (int e = 0; e < 4; ++e) a[e] = v2f{0.f, 0.f};
#pragma unroll
  for (int i = 0; i < 7; ++i)
#pragma unroll
    for (int e = 0; e < 4; ++e) a[e] = fma2(bun(m.mw[i][e]), v2f{sl[i], sl[i]}, a[e]);
#pragma unroll
  for (int e = 0; e < 4; ++e) {
    a[e].x += dpp<0x128>(a[e].x); a[e].y += dpp<0x128>(a[e].y);
    a[e].x += dpp<0x124>(a[e].x); a[e].y += dpp<0x124>(a[e].y);
  }
  if ((tid & 15) < 4) {
    *(LAS v4f*)(L + L_RED + ((tid >> 4) * 32 + (tid & 15) * 8) * 4) = v4f{a[0].x, a[0].y, a[1].x, a[1].y};
    *(LAS v4f*)(L + L_RED + ((tid >> 4) * 32 + (tid & 15) * 8 + 4) * 4) = v4f{a[2].x, a[2].y, a[3].x, a[3].y};
  }
}
DEV void mod_publish(char LAS* L, rsrc_t ws, int q, int lane, u32 tag) {
  v4f s = {0.f, 0.f, 0.f, 0.f};
#pragma unroll
  for (int r = 0; r < 16; ++r) s += lds4f(L, L_RED + (r * 32 + (lane & 7) * 4) * 4);
  const float one = (q / 14 == 1 || q / 14 == 4) ? 1.f : 0.f;
  st4c(ws, WS_MOD + (lane >> 3) * MOD_REP + q * 128 + (lane & 7) * 16,
       v4u{pk(rb(s.x) + one, rb(s.y) + one), tag, pk(rb(s.z) + one, rb(s.w) + one), tag});
}
DEV u32 ld1(rsrc_t r, int off) { return __builtin_amdgcn_raw_buffer_load_b32(r, off, 0, 0); }
DEV u32 ld1n(rsrc_t r, int off) { return __builtin_amdgcn_raw_buffer_load_b32(r, off, 0, PF_AUX); }
DEV u32 mktag(u32 epoch) { return epoch * 0x9E3779B1u + 0x7F4A7C15u; }

DEV void x2_store(rsrc_t ws, const v4u (&r)[4], int blk, int wave, int l16) {
  const int o = WS_X2 + blk * 14336 + (wave == 0 ? 0 : 4 * wave - 2) * 1024 + l16;
#pragma unroll
  for (int m = 0; m < 4; ++m) st4c(ws, m < 2 || wave != 0 ? o + m * 1024 : OOB, r[m]);
}
DEV bool flags_ok(v4u rv, u32 tag) { return rv.x == tag && rv.y == ~tag && rv.z == tag && rv.w == ~tag; }
DEV void wait_flags(rsrc_t ws, int off, u32 tag) {
  _Pragma("nounroll") for (int s = 0; s < SPIN; ++s) {
    const v2u rv = ld2c(ws, fresh(off));
    if (wall(rv.x == tag && rv.y == ~tag)) break;
    __builtin_amdgcn_s_sleep(SL1);
  }
}

__global__ void __launch_bounds__(256, 1)
fused(const u16* __restrict__ xin, const u16* __restrict__ wr, const u16* __restrict__ wi, const u16* __restrict__ wg,
      const u16* __restrict__ wu, const u16* __restrict__ wd, const u16* __restrict__ cin, const u16* __restrict__ wm,
      u16* __restrict__ out, char* wsp) {
  __shared__ __attribute__((aligned(16))) char smem[L_SZ];
  char LAS* L = (char LAS*)smem;
  const u32 lbase = (u32)(size_t)L;
  const int tid = threadIdx.x, lane = tid & 63, wave = __builtin_amdgcn_readfirstlane(tid >> 6);
  const int g = lane >> 4, c16 = lane & 15, l16 = lane * 16;
  const int grp = blockIdx.x & 7, j = blockIdx.x >> 3;
  u64 pc;
  asm volatile("s_getpc_b64 %0" : "=s"(pc));
  STAMP(0); CSTAMP(31);
  asm volatile("" :: "s"(xin), "s"(wr), "s"(wi), "s"(wg), "s"(wu), "s"(wd), "s"(cin), "s"(wm), "s"(out), "s"(wsp));
  const rsrc_t ws = mkrs(wsp, WS_END);
  const rsrc_t tab = mkrs(TAB, TAB_N * 4096);
  const rsrc_t code = mkrs((const void*)(pc & ~127ull), CODE_BYTES);
  const bool isR = j >= 14 && j < 30;
  const bool isG = j < 28;
  const int gk = j / 7, ct = j - gk * 7;
  const int tile = 4 * (grp >> 2) + gk, slice = 7 * (grp & 3) + ct;
  const int wdb = WS_WD + (ct * 56 + wave * 14) * 4096;
  const v4u sv = {SENT, SENT, SENT, SENT};
  int lb = L_B + l16;
  pin(lb);
  const char LAS* LB = L + lb - L_B;
  const int pfo = ((j * 4 + wave) * 14 + lane) * 128, pfc = ((j * 4 + wave) * 4 + lane) * 128;
  u32 pfa = ld1(tab, lane < 14 ? pfo : OOB), pfb = ld1(code, lane < 4 ? pfc : OOB);
  const int hro = WS_HRDY + (16 * tile + 4 * wave) * 32 + c16 * 8;
  v4u x2r[4];
  const int wl16 = (lane ^ ((wave & 1) << 1)) * 16;
  v4u wdn[14][4];
  u32 epoch, tag;

#define ACT_CLEAR(lo, hi) _Pragma("unroll") for (int m = lo; m < hi; ++m) \
      st4c(ws, isG ? WS_ACT + (16 * (tile + 2 * (m >> 3)) + 4 * (grp & 3)) * 57344 + ct * 32768 + (m & 7) * 4096 + tid * 16 : OOB, sv);
  if (isR) {
    const int blk = 16 * grp + (j - 14);
    epoch = ld1c(ws, WS_EPOCH);
    const int t0 = wave == 0 ? 0 : 2 * wave - 1;
    v4u cf[8][4];
#define CF_LOAD(q) { const int chn = TAB_P1 + ((wave == 0 && q >= 4) ? 28 : 4 * t0 + q), vo = (q < 5 || wave != 0) ? l16 : OOB; \
      _Pragma("unroll") for (int s = 0; s < 4; ++s) cf[q][s] = ld4(tab, vo + s * 1024, chn * 4096); sb(); }
#pragma unroll
    for (int q = 0; q < X_AFTER; ++q) CF_LOAD(q)
    v2u xv[7];
    {
      const rsrc_t xr = mkrs(xin, S * D * 2);
      const int off = tok_row(blk, 4 * wave + g) * (D * 2) + c16 * 8;
#pragma unroll
      for (int m = 0; m < 7; ++m) xv[m] = ld2(xr, off + m * 128);
    }
    typedef u32 v3u __attribute__((ext_vector_type(3)));
    v2u fa_[2], fb_[2]; v3u fc_[2], fd_[2];
    {
      const rsrc_t rr = mkrs(wr, S * DF * 2), ri = mkrs(wi, S * DF * 2);
      const int fo_ = 16 * wave + c16 < 57 ? 2 * g * 450 + (16 * wave + c16) * 8 : OOB;
#pragma unroll
      for (int hh = 0; hh < 2; ++hh) {
        const int so = blk * 7200 + hh * 3600;
        fa_[hh] = ld2(rr, fo_, so); fb_[hh] = ld2(ri, fo_, so);
        fc_[hh] = __builtin_amdgcn_raw_buffer_load_b96(rr, fo_ + 448, so, 0); fd_[hh] = __builtin_amdgcn_raw_buffer_load_b96(ri, fo_ + 448, so, 0);
      }
    }
    sb();
#pragma unroll
    for (int q = X_AFTER; q < 8; ++q) CF_LOAD(q)
    STAMP(23);
    {
      v2f f[14], s2 = {0.f, 0.f};
#pragma unroll
      for (int m = 0; m < 7; ++m) {
        f[2 * m] = bun(xv[m].x); f[2 * m + 1] = bun(xv[m].y);
        s2 += f[2 * m] + f[2 * m + 1];
        *(LAS v2u*)(L + L_XS + (4 * wave + g) * XP + c16 * 8 + m * 128) = xv[m];
      }
      STAMP(21);
      sb();
      {
        const int q = 16 * wave + c16, o = WS_WT + q * 16384 + blk * 128 + g * 16;
#pragma unroll
        for (int hh = 0; hh < 2; ++hh) {
          const v2u a = fa_[hh], b = fb_[hh]; const v3u c = fc_[hh], d = fd_[hh];
          st4c(ws, q < 57 ? o + hh * 64 : OOB, v4u{__builtin_amdgcn_perm(b.x, a.x, 0x05040100u), __builtin_amdgcn_perm(b.y, a.y, 0x05040100u),
                                                   __builtin_amdgcn_perm(d.x, c.x, 0x07060302u), __builtin_amdgcn_perm(d.y, c.y, 0x07060302u)});
          st4c(ws, q < 56 ? o + hh * 64 + 57 * 16384 : OOB,
               v4u{__builtin_amdgcn_perm(b.x, a.x, 0x07060302u), __builtin_amdgcn_perm(b.y, a.y, 0x07060302u),
                   __builtin_amdgcn_perm(d.y, c.y, 0x05040100u), __builtin_amdgcn_perm(d.z, c.z, 0x05040100u)});
        }
      }
      sb();
      const float mean = sum16(s2.x + s2.y) * (1.f / D);
      v2f q2 = {0.f, 0.f};
#pragma unroll
      for (int m = 0; m < 14; ++m) { const v2f d = f[m] - mean; q2 = fma2(d, d, q2); }
      const float rstd = __builtin_amdgcn_rsqf(sum16(q2.x + q2.y) * (1.f / D) + 1e-5f);
      if (c16 == 0) *(LAS v2f*)(L + L_ST + (4 * wave + g) * 8) = v2f{mean, rstd};
    }
    pin(pfa); pin(pfb);
    epoch = __builtin_amdgcn_readfirstlane(epoch);
    tag = mktag(epoch);
    STAMP(22);
    bar();
    v4f n[4][2];
    const int d0 = 8 * (4 * wave + g);
    const v2f st = *(const LAS v2f*)(L + L_ST + c16 * 8);
    {
      int xr_ = L_XS + c16 * XP + d0 * 2, xb_ = L_XS + c16 * XP + (216 - d0) * 2, x0_ = d0 == 0 ? xr_ : xb_ + 464;
      pin(xr_); pin(xb_); pin(x0_);
      const v4u a = lds4(L + xr_, 0), e = lds4(L + xr_, 448), c = lds4(L + xb_, 0), b = lds4(L + xb_, 448);
      const u32 c0 = *(const LAS u16*)(L + xb_ + 16), b0 = *(const LAS u16*)(L + x0_);
#pragma unroll
      for (int i = 0; i < 8; ++i) {
        n[0][i >> 2][i & 3] = (i & 1) ? bhi(a[i >> 1]) : blo(a[i >> 1]);
        n[3][i >> 2][i & 3] = (i & 1) ? bhi(e[i >> 1]) : blo(e[i >> 1]);
        n[1][i >> 2][i & 3] = i == 0 ? blo(b0) : (i & 1) ? bhi(b[(8 - i) >> 1]) : blo(b[(8 - i) >> 1]);
        n[2][i >> 2][i & 3] = i == 0 ? blo(c0) : (i & 1) ? bhi(c[(8 - i) >> 1]) : blo(c[(8 - i) >> 1]);
      }
      const float nm = -st.x * st.y;
#pragma unroll
      for (int m = 0; m < 4; ++m) {
#pragma unroll
        for (int h = 0; h < 2; ++h) { n[m][h] = fma4(n[m][h], splat(st.y), splat(nm)); pin(n[m][h]); }
      }
    }
    sb();
    STAMP(1);
    int mb = L_M1 + d0 * 4;
    pin(mb);
    v4u xs[4];
    int to[4];
    to[0] = WS_T1 + blk * T1_BLK + (16 * t0 + c16) * 64 + g * 16;
    to[1] = wave != 0 ? to[0] + 1024 : c16 < 2 ? to[0] + 112 * 64 : OOB;
    to[2] = to[0] + 114 * 64;
    to[3] = wave != 0 ? to[0] + 1024 + 114 * 64 : OOB;
#pragma unroll
    for (int q = 0; q < 4; ++q) pin(to[q]);

    {
      const int kind = tid >= 112, d = 4 * (tid - 112 * kind);
      int wb = L_M1 + (kind ? 0 : 2 * 1792) + d * 4, wrv = L_M1 + (kind ? 0 : 2 * 1792) + 1792 + (445 - d) * 4;
      int wr0 = d == 0 ? wrv + 12 - 1792 : wrv + 12;
      const int mo = tid < 224 ? WS_MOD + grp * MOD_REP + tid * 16 : OOB;
      pin(wb); pin(wrv); pin(wr0);
      const int ro = WS_RDY + grp * 1024 + min(tid >> 1, 112) * 8;
      v4u mv;
      _Pragma("nounroll") for (int s = 0; s < SPIN; ++s) {
        mv = ld4c(ws, fresh(mo));
        const v2u rv = ld2c(ws, fresh(ro));
        if (wall((tid >= 224 || (mv.y == tag && mv.w == tag)) && rv.x == tag && rv.y == ~tag)) break;
        __builtin_amdgcn_s_sleep(SL1);
      }
      STAMP(2);
      if (tid < 224) {
        const v4f v = {blo(mv.x), bhi(mv.x), blo(mv.z), bhi(mv.z)};
        *(LAS v4f*)(L + wb) = v;
        *(LAS float*)(L + wr0) = v.x;
        *(LAS float*)(L + wrv + 8) = v.y;
        *(LAS float*)(L + wrv + 4) = v.z;
        *(LAS float*)(L + wrv) = v.w;
      }
    }
    bar();
    {
      v4u fr[4];
#pragma unroll
      for (int hh = 0; hh < 2; ++hh) {
        v4f h[4];
#pragma unroll
        for (int m = 0; m < 4; ++m) {
          const int o = ((m == 1 || m == 2) ? 1792 : 0) + (m >= 2 ? 896 : 0) + hh * 16;
          h[m] = fma4(n[m][hh], lds4f(L + mb, o), lds4f(L + mb, 2 * 1792 + o));
        }
        const v4f p = h[0] + h[1], q = h[2] + h[3], u = h[0] - h[1], v = h[3] - h[2];
        const v4f t[4] = {p + q, p - q, u + v, u - v};
#pragma unroll
        for (int m = 0; m < 4; ++m) { fr[m][2 * hh] = pk2(lo2(t[m])); fr[m][2 * hh + 1] = pk2(hi2(t[m])); }
      }
#pragma unroll
      for (int m = 0; m < 4; ++m) *(LAS v4u*)(LB + wave * 1024 + L_AF + m * 4096) = fr[m];
    }
    bar();
    {
      v4u a[4][4];
#pragma unroll
      for (int p = 0; p < 2; ++p)
#pragma unroll
        for (int s = 0; s < 4; ++s) { a[p][s] = lds4(LB, L_AF + (p * 4 + s) * 1024); a[2 + p][s] = lds4(LB, L_AF + ((2 + p) * 4 + s) * 1024); }
      v4f re[4], im[4];
#pragma unroll
      for (int q = 0; q < 5; ++q) {
        if (q < 4) mf8(re[q], im[q], a[q >> 1], cf[4 * (q & 1) + (q >> 1)], a[2 + (q >> 1)], cf[4 * (q & 1) + 2 + (q >> 1)]);
        if (q > 0) {
          if (q < 4) mfence<0>(re[q - 1], im[q - 1]); else mfence<7>(re[3], im[3]);
          xs[q - 1] = v4u{pk(re[q - 1].x, im[q - 1].x), pk(re[q - 1].y, im[q - 1].y), pk(re[q - 1].z, im[q - 1].z), pk(re[q - 1].w, im[q - 1].w)};
          st4c(ws, to[q - 1], xs[q - 1]);
          sb();
        }
      }
    }
    STAMP(3);
    sb();
    sb();
    if (isG) dma_w1(wg, wu, slice, wave, lane, lbase);
#pragma unroll
    for (int q = 0; q < 4; ++q) {
      const int vo = (q < 2 || wave != 0) ? l16 : OOB;
#pragma unroll
      for (int s = 0; s < 4; ++s) cf[2 * q + 1][s] = ld4(tab, vo + s * 1024, (TAB_P3 + 2 * t0 + q) * 4096);
    }
#pragma unroll
    for (int m = 0; m < 4; ++m) {
      st4c(ws, tid + 256 * m < 896 ? WS_H2 + blk * 14336 + 4096 * m + tid * 16 : OOB, sv);
    }
    sb();
    STAMP(20);
    v4f xo[2][4];
    int xa = L_XS + c16 * XP + (16 * t0 + 4 * g) * 2, xd = L_XS + c16 * XP + (192 - 16 * t0 - 4 * g) * 2;
    int xs1 = tid < 16 ? L_XS + c16 * XP + 224 : xd + 512, xs3 = tid < 16 ? L_XS + c16 * XP + 672 : xa + 448;
    pin(xa); pin(xd); pin(xs1); pin(xs3);
#define XOFF(t, m, r) ((t) == 0 && (r) == 0 && (m) == 1 ? xs1 : (t) == 0 && (r) == 0 && (m) == 3 ? xs3 : \
    (m) == 0 ? xa + 32 * (t) + 2 * (r) : (m) == 3 ? xa + 448 + 32 * (t) + 2 * (r) : (m) == 1 ? xd + 512 - 32 * (t) - 2 * (r) : xd + 64 - 32 * (t) - 2 * (r))
    {
      v2u w[2][4]; u32 e[2][2], e3;
#pragma unroll
      for (int t = 0; t < 2; ++t) {
        w[t][0] = *(const LAS v2u*)(L + xa + 32 * t); w[t][3] = *(const LAS v2u*)(L + xa + 448 + 32 * t);
        w[t][1] = *(const LAS v2u*)(L + xd + 504 - 32 * t); w[t][2] = *(const LAS v2u*)(L + xd + 56 - 32 * t);
        e[t][0] = *(const LAS u16*)(L + XOFF(t, 1, 0)); e[t][1] = *(const LAS u16*)(L + XOFF(t, 2, 0));
      }
      e3 = *(const LAS u16*)(L + xs3);
      sb();
#pragma unroll
      for (int t = 0; t < 2; ++t) {
        xo[t][0] = v4f{blo(w[t][0].x), bhi(w[t][0].x), blo(w[t][0].y), bhi(w[t][0].y)};
        xo[t][3] = v4f{t == 0 ? blo(e3) : blo(w[t][3].x), bhi(w[t][3].x), blo(w[t][3].y), bhi(w[t][3].y)};
        xo[t][1] = v4f{blo(e[t][0]), bhi(w[t][1].y), blo(w[t][1].y), bhi(w[t][1].x)};
        xo[t][2] = v4f{blo(e[t][1]), bhi(w[t][2].y), blo(w[t][2].y), bhi(w[t][2].x)};
#pragma unroll
        for (int m = 0; m < 4; ++m) { xo[t][m] -= st.x; pin(xo[t][m]); }
      }
    }
    int m0_[2], m1_[2], md_[2];
#pragma unroll
    for (int r = 0; r < 2; ++r) {
      const int q = r == 0 || tid < 80 ? tid + 256 * r : tid;
      const int kind = (q * 586) >> 16, d = 4 * (q - 112 * kind);
      const int reg = (d >= 112) + (d >= 224) + (d >= 336), cc = ((reg + 1) >> 1) * 224, m1 = (0x1320 >> (4 * reg)) & 3;
      const int d1 = abs(d + 1 - cc), d0_ = abs(d - cc), edge = d == 112 * reg && reg > 0;
      m1_[r] = L_M2 + kind * 1792 + (((d1 >> 4) << 6) | (m1 << 4) | (d1 & 15)) * 4;
      m0_[r] = L_M2 + kind * 1792 + (edge ? reg << 4 : ((d0_ >> 4) << 6) | (m1 << 4) | (d0_ & 15)) * 4;
      md_[r] = (reg & 1) ? -4 : 4;
      pin(m0_[r]); pin(m1_[r]); pin(md_[r]);
    }
    {
      v4u mv[2];
      _Pragma("nounroll") for (int s = 0; s < SPIN; ++s) {
        mv[0] = ld4c(ws, fresh(WS_MOD + grp * MOD_REP + (224 + tid) * 16));
        mv[1] = ld4c(ws, fresh(WS_MOD + grp * MOD_REP + (224 + (tid < 80 ? 256 + tid : tid)) * 16));
        const v2u rv = ld2c(ws, fresh(WS_DRDY + grp * 1024 + min(lane, 48) * 8));
        if (wall(mv[0].y == tag && mv[0].w == tag && mv[1].y == tag && mv[1].w == tag && rv.x == tag && rv.y == ~tag)) break;
        __builtin_amdgcn_s_sleep(SL4);
      }
      if (lane < (CLR_MODE == 1 ? 2 : 1)) st2c(ws, lane == 0 ? WS_HRDY + blk * 32 + wave * 8 : isG ? WS_CRDY + (tile * 4 + (grp & 3)) * 256 + (ct * 4 + wave) * 8 : OOB, v2u{tag, ~tag});
      STAMP(12);
#pragma unroll
      for (int f = 0; f < WD_EARLY; ++f) wdn[f >> 2][f & 3] = ld4c(ws, (isG ? wl16 : OOB) + (f & 3) * 1024, wdb + (f >> 2) * 4096);
      sb();
#pragma unroll
      for (int r = 0; r < 2; ++r) {
        const float sc = m0_[r] < L_M2 + 1792 ? 2.f / (float)(S * D) : 1.f;
        *(LAS float*)(L + m0_[r]) = blo(mv[r].x) * sc;
        *(LAS float*)(L + m1_[r]) = bhi(mv[r].x) * sc;
        *(LAS float*)(L + m1_[r] + md_[r]) = blo(mv[r].z) * sc;
        *(LAS float*)(L + m1_[r] + 2 * md_[r]) = bhi(mv[r].z) * sc;
      }
    }
    bar();
    *(LAS v4u*)(L + L_AF + (tid >> 6) * 4096 + 3072 + l16) = v4u{0u, 0u, 0u, 0u};
    STAMP(4);
    int zr = L_AF + lane * 8, ro = L_M2 + (t0 * 64 + 4 * g) * 4, ro1 = wave == 0 ? ro : ro + 256, ex = L_EX + c16 * 8;
    pin(zr); pin(ro); pin(ro1); pin(ex);
    {
      int zl[4];
#pragma unroll
      for (int m = 0; m < 4; ++m) {
        const int kq = (m >> 1) * 128 + (to[m] == OOB ? 127 : wave == 0 && m == 1 ? 112 + c16 : 16 * (t0 + (m & 1)) + c16);
        zl[m] = L_AF + ((kq & ~31) + ((kq & 4) << 2) + ((kq & 24) >> 1) + (kq & 3)) * 32 + g * 8;
        pin(zl[m]);
        if (to[m] == OOB) xs[m] = sv;
      }
      bar();
      if (isG) wait_flags(ws, hro, tag);
      {
        const int src = (blk * 5 + wave * 13) & 63;
        const int po = __builtin_amdgcn_readlane(to[(0)], 0) + (src & 15) * 64 + (src >> 4) * 16;
        const u32 old = __builtin_amdgcn_ds_bpermute(src * 4, xs[0].x);
        _Pragma("nounroll") for (int s = 0; s < SPIN; ++s) {
          if (wall(ld1c(ws, fresh(po)) != old)) break;
          __builtin_amdgcn_s_sleep(SL2);
        }
      }
      STAMP(13);
      v4u z[4];
      _Pragma("nounroll") for (int s = 0; s < SPIN; ++s) {
        u32 mn = 1;
#pragma unroll
        for (int m = 0; m < 4; ++m) z[m] = ld4c(ws, fresh(to[m]));
#pragma unroll
        for (int m = 0; m < 4; ++m) { const v4u d = z[m] ^ xs[m]; mn = min(min(mn, d.x), min(min(d.y, d.z), d.w)); }
        if (wall(mn != 0)) break;
      }
      STAMP(14);
#pragma unroll
      for (int m = 0; m < 4; ++m) {
        char LAS* p = L + zl[m];
        *(LAS v2u*)(p) = v2u{__builtin_amdgcn_perm(z[m].y, z[m].x, 0x05040100u), __builtin_amdgcn_perm(z[m].w, z[m].z, 0x05040100u)};
        *(LAS v2u*)(p + ZPL) = v2u{__builtin_amdgcn_perm(z[m].y, z[m].x, 0x07060302u), __builtin_amdgcn_perm(z[m].w, z[m].z, 0x07060302u)};
      }
    }
    bar();
    STAMP(5);
    {
      v4u zf[4][4];
#pragma unroll
      for (int ty = 0; ty < 4; ++ty)
#pragma unroll
        for (int s = 0; s < 4; ++s) zf[ty][s] = ldtr2(L + zr, ty * 4096 + s * 1024, ty * 4096 + s * 1024 + 512);
      v4f acc[2][4], x2[2][4], gav[2][4];
      v2f s1 = {0.f, 0.f}, s2 = {0.f, 0.f};
#define P3M(t, ty) mf8(acc[t][ty], acc[t][ty + 1], cf[4 * t + ty], zf[ty], cf[4 * t + ty + 1], zf[ty + 1]);
#define P3X(t, m, yv) { x2[t][m] = fma4(gav[t][m], yv, xo[t][m]); \
      if (t == 0 || wave != 0) { s1 += lo2(x2[t][m]) + hi2(x2[t][m]); \
        s2 = fma2(lo2(x2[t][m]), lo2(x2[t][m]), s2); s2 = fma2(hi2(x2[t][m]), hi2(x2[t][m]), s2); } }
      P3M(1, 0) P3M(1, 2)
#pragma unroll
      for (int m = 0; m < 4; ++m) { gav[0][m] = lds4f(L + ro, m * 64); gav[1][m] = lds4f(L + ro1, m * 64); }
      sb();
      P3M(0, 0) P3M(0, 2)
      mfence<0>(acc[1][0], acc[1][1], acc[1][2], acc[1][3]);
      const v4f A1 = acc[1][0] + acc[1][1], B1 = acc[1][0] - acc[1][1], P1 = acc[1][3] - acc[1][2], Q1 = acc[1][2] + acc[1][3];
      P3X(1, 0, A1 - P1) P3X(1, 1, A1 + P1) P3X(1, 2, B1 - Q1) P3X(1, 3, B1 + Q1)
      sb();
      mfence<0>(acc[0][0], acc[0][1], acc[0][2], acc[0][3]);
      {
        const v4f A = acc[0][0] + acc[0][1], B = acc[0][0] - acc[0][1], P = acc[0][3] - acc[0][2], Q = acc[0][2] + acc[0][3];
        v4f y[4] = {A - P, A + P, B - Q, B + Q};
        const bool sp = tid < 16;
        y[0].x = sp ? A.x : y[0].x; y[2].x = sp ? B.x : y[2].x;
        y[1].x = sp ? acc[1][0].x - acc[0][3].x : y[1].x; y[3].x = sp ? acc[1][0].x + acc[0][3].x : y[3].x;
        P3X(0, 0, y[0]) P3X(0, 1, y[1]) P3X(0, 2, y[2]) P3X(0, 3, y[3])
      }
      float sm = s1.x + s1.y, sq = s2.x + s2.y;
      sumg2(sm, sq);
      if (g == 0) *(LAS v2f*)(L + ex + wave * 128) = v2f{sm, sq};
      v4f sbv[2][4], cbv[2][4];
#pragma unroll
      for (int m = 0; m < 4; ++m) {
        sbv[0][m] = lds4f(L + ro, 1792 + m * 64); cbv[0][m] = lds4f(L + ro, 2 * 1792 + m * 64);
        sbv[1][m] = lds4f(L + ro1, 1792 + m * 64); cbv[1][m] = lds4f(L + ro1, 2 * 1792 + m * 64);
      }
      bar();
      v2f e = *(const LAS v2f*)(L + ex);
#pragma unroll
      for (int w = 1; w < 4; ++w) e += *(const LAS v2f*)(L + ex + w * 128);
      const float mc = e.x * (1.f / D), rstd = __builtin_amdgcn_rsqf(fmaxf(e.y * (1.f / D) - mc * mc, 0.f) + 1e-5f), nb = -mc * rstd;
      const int ho = WS_H2 + blk * 14336 + 2 * t0 * 1024 + l16, ho1 = wave == 0 ? OOB : ho + 2048;
#pragma unroll
      for (int t = 0; t < 2; ++t)
#pragma unroll
        for (int a = 0; a < 2; ++a) {
          v4u o;
#pragma unroll
          for (int hh = 0; hh < 2; ++hh) {
            const int m = 2 * a + hh;
            const v4f hv = fma4(fma4(x2[t][m], splat(rstd), splat(nb)), cbv[t][m], sbv[t][m]);
            o[2 * hh] = pk2(lo2(hv)); o[2 * hh + 1] = pk2(hi2(hv));
          }
          st4c(ws, (t ? ho1 : ho) + a * 1024, o);
        }
      STAMP(6);
      sb();
#pragma unroll
      for (int f = WD_EARLY; f < WD_MID; ++f) wdn[f >> 2][f & 3] = ld4c(ws, (isG ? wl16 : OOB) + (f & 3) * 1024, wdb + (f >> 2) * 4096);
      sb();
#pragma unroll
      for (int e = 0; e < 8; ++e) {
        x2r[e >> 1][2 * (e & 1)] = pk2(lo2(x2[e >> 2][e & 3]) + st.x); x2r[e >> 1][2 * (e & 1) + 1] = pk2(hi2(x2[e >> 2][e & 3]) + st.x);
      }
#pragma unroll
      for (int m = 0; m < 4; ++m) pin(x2r[m]);
    }
    bar();
    if (!isG) {
      x2_store(ws, x2r, blk, wave, l16);
      drain();
      if (lane == 0) st2c(ws, WS_XRDY + blk * 64 + wave * 8, v2u{tag, ~tag});
      DUMP(); return;
    }
  } else {
    const int sj = j >= 30 ? j - 30 : j + 2;
    const int idx = sj * 8 + grp;
    const bool isC = j < 14 || idx == 0;
    const int pair = j < 14 ? grp * 14 + j : 112;
    const int lc = grp + 8 * (sj >> 1);
    const int qu = sj < 4 && lc < 14 ? 2 * lc + (sj & 1) : -1;
    const int t1o = WS_T1 + (32 * wave + (lane >> 3)) * T1_BLK + pair * 128 + (lane & 7) * 16;
    ModLoad ml;
    epoch = ld1c(ws, WS_EPOCH);
    if (qu >= 0) mod_load(ml, cin, wm, qu, tid);
#pragma unroll
    for (int q = 0; q < 4; ++q) st4c(ws, isC ? t1o + 8 * q * T1_BLK : OOB, sv);
    u32 pf0, pf1, pf2;
    {
      const rsrc_t mr = mkrs(wm, D * 6 * D * 2), xr = mkrs(xin, S * D * 2), fr_ = mkrs((wave & 1) ? wi : wr, S * DF * 2);
      const int row = (sj * 4 + wave) * 7 + (lane >> 1), l7 = lane / 7;
      pf0 = __builtin_amdgcn_raw_buffer_load_b32(mr, lane < 14 && (grp < 6 || !(lane & 1)) ? row * (6 * D * 2) + (grp + 8 * (lane & 1)) * 128 : OOB, 0, PF_AUX);
      pf1 = __builtin_amdgcn_raw_buffer_load_b32(xr, lane < (!XBAL ? 28 : sj < 4 ? 0 : sj < 12 ? 28 : 56) ? tok_row(16 * grp + (lane < 28 ? sj : sj - 12), 4 * wave + (l7 & 3)) * (D * 2) + (lane - 7 * l7) * 128 : OOB, 0, PF_AUX);
    }
    sb();
    epoch = __builtin_amdgcn_readfirstlane(epoch);
    tag = mktag(epoch);
    if (qu >= 0) mod_part(ml, L, tid);
    asm volatile("s_waitcnt vmcnt(%0)" :: "i"(W_LATE ? 2 : 3) : "memory");
    { const rsrc_t fr_ = mkrs((wave & 1) ? wi : wr, S * DF * 2);
      pf2 = __builtin_amdgcn_raw_buffer_load_b32(fr_, (lane & 31) < 29 && lane < (!XBAL ? 32 : sj < 4 ? 0 : sj < 12 ? 32 : 64) ? (((16 * grp + (lane < 32 ? sj : sj - 12)) * 7200) & ~127) + ((wave >> 1) * 29 + (lane & 31)) * 128 : OOB, 0, PF_AUX);
    }
    bar();
    if (wave == 0 && qu >= 0) mod_publish(L, ws, qu, lane, tag);
    if (wave == 1 && lane < 8) st2c(ws, isC ? WS_RDY + lane * 1024 + pair * 8 : OOB, v2u{tag, ~tag});
    STAMP(1);
    pin(pf0); pin(pf1); pin(pf2); pin(pfa); pin(pfb);
    const int qn = sj >= 4 && sj < 10 && grp < 7 ? 28 + grp * 6 + (sj - 4) : !isC && idx < 15 ? 69 + idx : -1;
    const int tw = sj >= 10 ? (sj - 10) * 8 + grp : sj == 4 && grp == 7 ? 48 : -1;
    v2u dw[2][8];
    if (qn >= 0) mod_load(ml, cin, wm, qn, tid);
    if (tw >= 0) {
      const rsrc_t dr = mkrs(wd, F * D * 2);
      const int u0 = tw * 4 + wave, uc = u0 / 28, kap = 2 * (u0 - uc * 28);
#pragma unroll
      for (int e = 0; e < 16; ++e)
        dw[e >> 3][e & 7] = __builtin_amdgcn_raw_buffer_load_b64(dr, (4 * g * D + 4 * c16) * 2 + (e & 3) * D * 2, ((32 * kap + 16 * (e >> 2)) * D + 64 * uc) * 2, PF_AUX);
    }
    sb();
    u32 pw[4];
    {
      const rsrc_t rg = mkrs(wg, D * F * 2), ru = mkrs(wu, D * F * 2);
      const int r7 = lane / 7, r0 = W1EVEN ? (j * 4 + wave) * 8 : sj < 4 ? (sj * 4 + wave) * 16 : 256 + ((sj - 4) * 4 + wave) * 4;
      const int po = (r0 + r7) * (F * 2) + ((grp & 3) * 7 + lane - r7 * 7) * 128;
      const int o0 = lane < (W1EVEN ? (j < 14 ? 56 : 0) : sj < 4 ? 56 : 28) ? po : OOB, o1 = !W1EVEN && lane < 56 && sj < 4 ? po + 8 * (F * 2) : OOB;
      pw[0] = ld1(rg, o0); pw[1] = ld1(ru, o0); pw[2] = ld1(rg, o1); pw[3] = ld1(ru, o1);
    }
    sb();
    STAMP(20);
    v4f t1c[2], t1s[2], t2c[2], t2s[2];
#pragma unroll
    for (int t = 0; t < 2; ++t) {
#pragma unroll
      for (int r = 0; r < 4; ++r) {
        const float a1 = (float)((16 * t + 4 * g + r) * (16 * wave + c16)) * (1.f / S);
        const float a2 = (float)((16 * t + c16) * (16 * wave + 4 * g + r)) * (1.f / S);
        t1c[t][r] = __builtin_amdgcn_cosf(a1); t1s[t][r] = __builtin_amdgcn_sinf(a1);
        t2c[t][r] = __builtin_amdgcn_cosf(a2); t2s[t][r] = __builtin_amdgcn_sinf(a2);
      }
      pin(t1c[t]); pin(t1s[t]); pin(t2c[t]); pin(t2s[t]);
    }
    if (tw >= 0) {
#pragma unroll
      for (int tt = 0; tt < 8; ++tt) {
        v4u o;
#pragma unroll
        for (int m = 0; m < 4; ++m)
          o[m] = __builtin_amdgcn_perm(dw[tt >> 2][2 * m + 1][(tt & 3) >> 1], dw[tt >> 2][2 * m][(tt & 3) >> 1], (tt & 1) ? 0x07060302u : 0x05040100u);
        st4c(ws, WS_WD + (tw * 4 + wave) * 8192 + tt * 1024 + l16, o);
      }
    }
    if (qn >= 0) {
      bar();
      mod_part(ml, L, tid);
      bar();
      if (wave == 0) mod_publish(L, ws, qn, lane, tag);
    }
    pin(pw[0]); pin(pw[1]); pin(pw[2]); pin(pw[3]);
    STAMP(21);
    if (!isC) { DUMP(); return; }
    v4u c64[2][4], c32[2][2][2];
#pragma unroll
    for (int cp = 0; cp < 2; ++cp)
#pragma unroll
      for (int s = 0; s < 4; ++s) c64[cp][s] = ld4(tab, l16 + s * 1024, (TAB_P2 + wave * 2 + cp) * 4096);
#pragma unroll
    for (int t = 0; t < 2; ++t)
#pragma unroll
      for (int f = 0; f < 4; ++f) c32[t][f >> 1][f & 1] = ld4(tab, l16 + f * 1024, (TAB_P2 + 8 + t) * 4096);
    sb();
    if (tw >= 0) {
      asm volatile("s_waitcnt vmcnt(16)" ::: "memory");
      bar();
      if (tid < 8) st2c(ws, WS_DRDY + tid * 1024 + tw * 8, v2u{tag, ~tag});
    }
    STAMP(2);
    int wfo = WS_WT + pair * 16384 + (16 * g + wave) * 128 + c16 * 8;
    pin(wfo);
    v2u wf[2][4];
    {
      int wl = L_CI + ((lane >> 2) & 1) * 8256 + wave * 2048 + ((lane >> 3) * 4 + (lane & 3)) * 16, lo = t1o;
      pin(wl); pin(lo);
      light_poll(ws, WS_T1 + (32 * wave + (pair & 31)) * T1_BLK + pair * 128);
      STAMP(13);
      v4u z[4];
      _Pragma("nounroll") for (int s = 0; s < SPIN; ++s) {
        lo = fresh(lo);
#pragma unroll
        for (int q = 0; q < 4; ++q) z[q] = ld4c(ws, lo, 8 * q * T1_BLK);
        if (wall(max(max(max4(z[0]), max4(z[1])), max(max4(z[2]), max4(z[3]))) != SENT)) break;
      }
      STAMP(14);
#pragma unroll
      for (int q = 0; q < 4; ++q) *(LAS v4u*)(L + wl + q * 512) = z[q];
    }
    bar();
#pragma unroll
    for (int t = 0; t < 2; ++t)
#pragma unroll
      for (int r = 0; r < 4; ++r) wf[t][r] = ld2c(ws, wfo + r * 512, t * 8192);
    v2u fd = ld2c(ws, WS_DRDY + grp * 1024 + min(lane, 48) * 8);
    sb();
    STAMP(4);
    {
      v4u a[2][2][4];
#pragma unroll
      for (int c = 0; c < 2; ++c)
#pragma unroll
        for (int mt = 0; mt < 2; ++mt)
#pragma unroll
          for (int s = 0; s < 4; ++s) a[c][mt][s] = lds4(LB, L_CI + c * 8256 + (s * 2 + mt) * 1024);
      v4u old[2][2];
#pragma unroll
      for (int c = 0; c < 2; ++c)
#pragma unroll
        for (int mt = 0; mt < 2; ++mt) old[c][mt] = lds4(LB + wave * 2048, L_CI + c * 8256 + mt * 1024);
      v4f re[2][2], im[2][2], ur[2][2], ui[2][2], fr_[2][2], fi_[2][2];
      u32 f2[2][2][4], f3[2][2][4], f4[4];
      char LAS* xw = L + lb + wave * 1024 - L_B;
#define V4(x) v4u{x[0], x[1], x[2], x[3]}
#define S1K(h) c64[0][2 * h], c64[0][2 * h + 1], c64[1][2 * h], c64[1][2 * h + 1]
#define S1X(c, t, h) re[c][t], im[c][t], a[c][t][2 * h], a[c][t][2 * h + 1], S1K(h)
#define S2X(c, t) ur[c][t], ui[c][t], V4(f2[c][0]), V4(f2[c][1]), c32[t][0][0], c32[t][0][1], c32[t][1][0], c32[t][1][1]
#define S3X(c, t) im[c][t], re[c][t], V4(f3[c][0]), V4(f3[c][1]), c32[t][0][0], c32[t][0][1], c32[t][1][0], c32[t][1][1]
#define CM(o, P, Q, C, S_, h) o[2 * h], o[2 * h + 1], P[2 * h], Q[2 * h], C[2 * h], S_[2 * h], P[2 * h + 1], Q[2 * h + 1], C[2 * h + 1], S_[2 * h + 1]
#define TW1(c, t, h) CM(f2[c][t], re[c][t], im[c][t], t1c[t], t1s[t], h)
#define FLT(c, t, h) CM(f3[c][t], ur[c][t], ui[c][t], fr_[c][t], fi_[c][t], h)
#define TW2(c, t, h) CM(f4, re[c][t], im[c][t], t2c[t], t2s[t], h)
#define HV(x, h) (h ? hi2(x) : lo2(x))
#define CP(PL, o, P, Q, C, S_, h) cmul2<PL>(o[2 * h], o[2 * h + 1], HV(P, h), HV(Q, h), HV(C, h), HV(S_, h));
#define XW(c, t) *(LAS v4u*)(xw + L_CX + (c * 2 + t) * 4096) = V4(f4);
#define UNP(c, t) { _Pragma("unroll") for (int r = 0; r < 4; ++r) { fr_[c][t][r] = blo(wf[t][r][c]); fi_[c][t][r] = bhi(wf[t][r][c]); } \
      pin(fr_[c][t]); pin(fi_[c][t]); }
      nop2();
      m4_a0(S1X(0, 0, 0)); m4_a1(S1X(0, 0, 1)); m4_a0(S1X(0, 1, 0)); mfence<3>(re[0][0], im[0][0]);
      h4_a1m(S1X(0, 1, 1), TW1(0, 0, 0)); h4_a0m(S1X(1, 0, 0), TW1(0, 0, 1));
      h4_a1m(S1X(1, 0, 1), TW1(0, 1, 0)); h4_a0m(S1X(1, 1, 0), TW1(0, 1, 1));
      h4_a1m(S1X(1, 1, 1), TW1(1, 0, 0)); h4_b0m(S2X(0, 0), TW1(1, 0, 1));
      h4_b0m(S2X(0, 1), TW1(1, 1, 0));
      sb();
      CP(false, f2[1][1], re[1][1], im[1][1], t1c[1], t1s[1], 1)
      UNP(0, 0) UNP(0, 1)
      sb();
      h4_b0p(S2X(1, 0), FLT(0, 0, 0)); h4_b0p(S2X(1, 1), FLT(0, 0, 1));
      sb();
      CP(true, f3[0][1], ur[0][1], ui[0][1], fr_[0][1], fi_[0][1], 0) CP(true, f3[0][1], ur[0][1], ui[0][1], fr_[0][1], fi_[0][1], 1)
      UNP(1, 0) UNP(1, 1)
      sb();
      h4_a0p(S3X(0, 0), FLT(1, 0, 0)); h4_a0p(S3X(0, 1), FLT(1, 0, 1));
      sb();
      CP(true, f3[1][1], ur[1][1], ui[1][1], fr_[1][1], fi_[1][1], 0) CP(true, f3[1][1], ur[1][1], ui[1][1], fr_[1][1], fi_[1][1], 1)
#pragma unroll
      for (int e = 0; e < 4; ++e) pin(f3[1][1][e]);
      nop2();
      h4_a0p(S3X(1, 0), TW2(0, 0, 0)); h4_a0p(S3X(1, 1), TW2(0, 0, 1));
      XW(0, 0)
      sb();
      CP(true, f4, re[0][1], im[0][1], t2c[1], t2s[1], 0) CP(true, f4, re[0][1], im[0][1], t2c[1], t2s[1], 1) XW(0, 1)
      sb();
      mfence<0>(re[1][0], im[1][0], re[1][1], im[1][1]);
      CP(true, f4, re[1][0], im[1][0], t2c[0], t2s[0], 0) CP(true, f4, re[1][0], im[1][0], t2c[0], t2s[0], 1) XW(1, 0)
      CP(true, f4, re[1][1], im[1][1], t2c[1], t2s[1], 0) CP(true, f4, re[1][1], im[1][1], t2c[1], t2s[1], 1) XW(1, 1)
      bar();
#pragma unroll
      for (int c = 0; c < 2; ++c)
#pragma unroll
        for (int nt = 0; nt < 2; ++nt)
#pragma unroll
          for (int s = 0; s < 4; ++s) a[c][nt][s] = lds4(LB, L_CX + ((c * 2 + nt) * 4 + s) * 1024);
      const int o2 = WS_T1 + (32 * wave + (lane >> 2)) * T1_BLK + pair * 128 + (lane & 3) * 16;
#define S4(c, t) mf8(im[c][t], re[c][t], c64[0], a[c][t], c64[1], a[c][t]);
#define NEQ(x, y) ((x) == (y) ? (x) ^ 1u : (x))
#define OUT(c, t) { const v4u o = {pk(re[c][t].x, im[c][t].x), pk(re[c][t].y, im[c][t].y), pk(re[c][t].z, im[c][t].z), pk(re[c][t].w, im[c][t].w)}; \
      st4c(ws, o2 + c * 64 + t * 16 * T1_BLK, v4u{NEQ(o.x, old[c][t].x), NEQ(o.y, old[c][t].y), NEQ(o.z, old[c][t].z), NEQ(o.w, old[c][t].w)}); sb(); }
      S4(0, 0) S4(0, 1) mfence<0>(re[0][0], im[0][0]); OUT(0, 0)
      S4(1, 0) mfence<0>(re[0][1], im[0][1]); OUT(0, 1)
      S4(1, 1) mfence<0>(re[1][0], im[1][0]); OUT(1, 0)
      mfence<0>(re[1][1], im[1][1]); OUT(1, 1)
    }
    STAMP(5);
    sb();
    if (!isG) { DUMP(); return; }
    if (!wall(fd.x == tag && fd.y == ~tag)) wait_flags(ws, WS_DRDY + grp * 1024 + min(lane, 48) * 8, tag);
    dma_w1(wg, wu, slice, wave, lane, lbase);
#pragma unroll
    for (int f = 0; f < WD_MID; ++f) wdn[f >> 2][f & 3] = ld4c(ws, wl16 + (f & 3) * 1024, wdb + (f >> 2) * 4096);
    fd = ld2c(ws, hro);
    sb();
    ACT_CLEAR(0, 16)
    sb();
    if (!wall(fd.x == tag && fd.y == ~tag)) wait_flags(ws, hro, tag);
    bar();
  }

  STAMP(7);
  const int blk0 = 16 * tile + 4 * (grp & 3);
  const int ab = WS_ACT + blk0 * 57344 + wave * 14 * 1024;
  v4u h[14][2];
  {
    const int hb = WS_H2 + (16 * tile + 4 * wave) * 14336;
    int tb[2][4];
#pragma unroll
    for (int u = 0; u < 8; ++u) {
      tb[u >> 2][u & 3] = (((lane >> 2) * 128 + ((lane >> 3) & 3) * 32 + (lane & 3) * 8) ^ ((u & 3) << 5)) + (u >> 2) * 57344;
      pin(tb[u >> 2][u & 3]);
    }
#define W_RD(dst, k, u) dst = ldtr2(L + tb[k >= 7][u & 3], (k % 7) * 8192 + (u >> 2) * 4096, (k % 7) * 8192 + (u >> 2) * 4096 + 2048)
#define H_LD(v, tt) hh_[v][tt] = ld4c(ws, l16 + (((v) % 14) & 3) * 1024, hb + (2 * ((v) / 14) + (tt)) * 14336 + (((v) % 14) >> 2) * 4096)
    const int abx[2] = {ab + (wave >> 1) * 57344, ab + (1 - (wave >> 1)) * 57344};
#define G_LD(e) h[(e) >> 1][(e) & 1] = ld4c(ws, l16 + (((e) >> 1) & 3) * 1024, abx[(e) & 1] + ((e) >> 3) * 4096)
    v4u wfr[2][8], hh_[28][2];
#pragma unroll
    for (int u = 0; u < 8; ++u) W_RD(wfr[0][u], 0, u);
    light_poll(ws, hb + (lane & 1) * 14336 + 13 * 1024);
    STAMP(15);
#pragma unroll
    for (int v = 0; v < LEAD; ++v) { H_LD(v, 0); H_LD(v, 1); }
    sb();
    if (!isR && lane < (CLR_MODE == 1 ? 1 : 2))
      st2c(ws, WS_CRDY + ((tile + 2 * lane) * 4 + (grp & 3)) * 256 + (ct * 4 + wave) * 8, v2u{tag, ~tag});
    sb();
#define BAD2(x0, x1) wany(max(max(max(max(x0.x, x0.y), x0.z), max(max(x0.w, x1.x), x1.y)), max(x1.z, x1.w)) == SENT)
#define RETRY2(x0, x1, o0, o1) \
      _Pragma("nounroll") for (int s_ = 0; s_ < SPIN; ++s_) { \
        const int lf_ = fresh(l16); SLOWCNT \
        x0 = ld4c(ws, lf_, o0); x1 = ld4c(ws, lf_, o1); \
        if (wall(max(max4(x0), max4(x1)) != SENT)) break; \
      }
    if (__builtin_expect(BAD2(hh_[0][0], hh_[0][1]), 0)) { RETRY2(hh_[0][0], hh_[0][1], hb, hb + 14336) }
    STAMP(8); CSTAMP(28);
    v4f acc[8][2];
    v2u fa;
    const int fx = WS_CRDY + (tile * 4 + (lane < 32 ? wave : grp & 3)) * 256 + min(lane & 31, 27) * 8;
    const int ao = WS_ACT + (16 * tile + 4 * wave) * 57344 + 2 * slice * 1024 + l16;
#define ACT_OUT(ps, tt, f) { v4u o; \
      _Pragma("unroll") for (int hh = 0; hh < 2; ++hh) { \
        const v4f av = relu4(acc[2 * f + hh][tt]) * acc[4 + 2 * f + hh][tt]; \
        o[2 * hh] = pk2(lo2(av)); o[2 * hh + 1] = pk2(hi2(av)); } \
      st4c(ws, ao + (2 * ps + tt) * 57344 + f * 1024, o); }
#define ACT_ALL(ps) { mfence<0>(acc[0][0], acc[1][0], acc[2][0], acc[3][0]); mfence<0>(acc[4][0], acc[5][0], acc[6][0], acc[7][0]); \
      ACT_OUT(ps, 0, 0) ACT_OUT(ps, 0, 1) sb(); \
      mfence<0>(acc[0][1], acc[1][1], acc[2][1], acc[3][1]); mfence<0>(acc[4][1], acc[5][1], acc[6][1], acc[7][1]); \
      ACT_OUT(ps, 1, 0) ACT_OUT(ps, 1, 1) sb(); }
#define W_RH(dst, k, u, hf) { const v2u t_ = ldtr(L + tb[k >= 7][u & 3], (k % 7) * 8192 + (u >> 2) * 4096 + hf * 2048); \
      dst[2 * hf] = t_.x; dst[2 * hf + 1] = t_.y; }
#pragma unroll
    for (int ps = 0; ps < 2; ++ps)
#pragma unroll
    for (int ks = 0; ks < 14; ++ks) {
      const int v = ps * 14 + ks, cur = v & 1, nk = (v + 1) % 14, vn = v < 27 ? v + 1 : v;
      const bool more = v < 27;
      u32 m0, m1;
      __builtin_amdgcn_s_waitcnt(0xC07F);
      sb();
#pragma unroll
      for (int i = 0; i < 16; ++i) {
        const int u = i & 7, tt = i >> 3;
        if (ks == 0) mv0(acc[u][tt], wfr[cur][u], hh_[v][tt]); else mv(acc[u][tt], wfr[cur][u], hh_[v][tt]);
        if (more) {
          if (i < 4) { W_RH(wfr[cur ^ 1][i], nk, i, 0) W_RH(wfr[cur ^ 1][i], nk, i, 1) }
          else if (i < 12) W_RH(wfr[cur ^ 1][4 + ((i - 4) >> 1)], nk, (4 + ((i - 4) >> 1)), (i & 1))
        }
        if (i == 5 && v + LEAD < 28) H_LD(v + LEAD, 0);
        if (i == 7 && v + LEAD < 28) H_LD(v + LEAD, 1);
        if (i == 9 || i == 11 || i == 13) {
          const int e = (i - 9) >> 1, f = WD_MID + (v < 14 ? v - 2 : 12 + WD_PER * (v - 14) + e);
          if (f < 56 && ((v >= 2 && v < 14 && e == 0) || (v >= 14 && e < WD_PER))) wdn[f >> 2][f & 3] = ld4c(ws, wl16 + (f & 3) * 1024, wdb + (f >> 2) * 4096);
        }
        if (v > A_V && (i & 1) && i >= 5 && ((i - 5) >> 1) < A_PER) G_LD(A_PER * (v - A_V - 1) + ((i - 5) >> 1));
        if (v == 8 && i == 13) fa = ld2c(ws, fx);
        if (more && i == 12) { m0 = max(max(hh_[vn][0].x, hh_[vn][0].y), hh_[vn][0].z); pin(m0); }
        if (more && i == 13) { m1 = max(max(hh_[vn][0].w, hh_[vn][1].x), hh_[vn][1].y); pin(m1); }
        if (more && i == 14) { m0 = max(max(m0, hh_[vn][1].z), hh_[vn][1].w); m0 = max(m0, m1); pin(m0); }
        sb();
      }
      if (v == 12 && !wall(fa.x == tag && fa.y == ~tag)) wait_flags(ws, fx, tag);
      if (v == 19 && isR) {
        static_assert(19 + LEAD < 28, "the count below");
        asm volatile("s_waitcnt vmcnt(12)" ::: "memory");
        if (lane == 0) st2c(ws, WS_XRDY + (16 * grp + j - 14) * 64 + wave * 8, v2u{tag, ~tag});
      }
      if (more && __builtin_expect(wany(m0 == SENT), 0)) {
        const int pb = 2 * ((v + 1) / 14) * 14336 + nk * 1024;
        RETRY2(hh_[vn][0], hh_[vn][1], hb + pb, hb + pb + 14336)
      }
      if (v == 13) {
        STAMP(16); CSTAMP(29);
        ACT_ALL(0)
        if (isR) x2_store(ws, x2r, 16 * grp + j - 14, wave, l16);
        nop2();
      }
      sb();
    }
    STAMP(27);
    ACT_ALL(1)
    CSTAMP(30);
#pragma unroll
    for (int e = A_PER * (27 - A_V); e < 28; ++e) G_LD(e);
    sb();
    STAMP(9);
    v4u a[14][2];
    v4f ac2[4][4];
#define A_LD(e) a[(e) >> 1][(e) & 1] = ld4c(ws, l16 + (((e) >> 1) & 3) * 1024, abx[(e) & 1] + 2 * 57344 + ((e) >> 3) * 4096)
    const int po = ab + 3 * 57344 + min(lane, 6) * 2048 + 1024;
    u32 probe;
#pragma unroll
    for (int ks = 0; ks < 14; ++ks) {
      if (__builtin_expect(BAD2(h[ks][0], h[ks][1]), 0)) { RETRY2(h[ks][0], h[ks][1], abx[0] + ks * 1024, abx[1] + ks * 1024) }
#pragma unroll
      for (int tt = 0; tt < 2; ++tt)
#pragma unroll
        for (int n = 0; n < 4; ++n) ac2[tt][n] = mfma(wdn[ks][n], h[ks][tt], ks ? ac2[tt][n] : splat(0.f));
      if (ks == P_AT) probe = ld1c(ws, po);
      sb();
      if (ks == B_AT2) {
        STAMP(25);
        if (wany(probe == SENT)) light_poll(ws, po);
        STAMP(26);
      }
      if (ks >= B_AT2) {
#pragma unroll
        for (int e = 4 * (ks - B_AT2); e < 4 * (ks - B_AT2 + 1); ++e) A_LD(e);
      }
      sb();
    }
    STAMP(17);
#pragma unroll
    for (int e = 4 * (14 - B_AT2); e < 4 * (14 - B_AT2) + 6; ++e) A_LD(e);
    sb();
    int rw[3], rr_ = L_B + lane * 8 + wave * 6144, oo[2];
#pragma unroll
    for (int x = 1; x < 4; ++x) { rw[x - 1] = L_B + lane * 8 + (wave ^ x) * 6144 + (x - 1) * 2048; pin(rw[x - 1]); }
    pin(rr_);
    const int blk = blk0 + (wave >> 1);
#pragma unroll
    for (int ph = 0; ph < 2; ++ph) { oo[ph] = (tok_row(blk + 2 * ph, c16) * D + ct * 64 + g * 16 + (wave & 1) * 8) * 2; pin(oo[ph]); }
#define RED_W1(ph, x, n) *(LAS v2f*)(L + rw[(x) - 1] + (ph) * 24576 + (n) * 512) = ((x) & 1) ? hi2(ac2[2 * (ph) + ((x) >> 1)][n]) : lo2(ac2[2 * (ph) + ((x) >> 1)][n]);
    const int n0 = ct * 64 + g * 16 + (wave & 1) * 8, reg = (n0 >= 112) + (n0 >= 224) + (n0 >= 336), xm = (0x1320 >> (4 * reg)) & 3;
    const int dz = abs(n0 - ((reg + 1) >> 1) * 224), db = (reg & 1) ? dz - 8 : dz;
#define XSLOT(m, dl) ((((dl) >> 4) * 2 + ((m) >> 1)) * 1024 + (((dl) >> 2) & 3) * 256 + ((m) & 1) * 8)
    int xo = WS_X2 + blk * 14336 + c16 * 16 + XSLOT(xm, db);
    int xe = WS_X2 + blk * 14336 + c16 * 16 + (n0 == 112 * reg && reg > 0 ? XSLOT(reg, 0) : XSLOT(xm, dz));
    pin(xo); pin(xe);
    const int xf = WS_XRDY + (blk + (lane & 4 ? 2 : 0)) * 64 + (lane & 3) * 8;
    v2u xp[2][2]; u32 x1[2];
    const int go = WS_MOD + grp * MOD_REP + (70 + 2 * ct) * 128 + g * 64 + (wave & 1) * 32;
    v4u xv[2], gv[2];
    STAMP(18);
    sb();
#pragma unroll
    for (int ks = 0; ks < 14; ++ks) {
      if (__builtin_expect(BAD2(a[ks][0], a[ks][1]), 0)) { RETRY2(a[ks][0], a[ks][1], abx[0] + 2 * 57344 + ks * 1024, abx[1] + 2 * 57344 + ks * 1024) }
#pragma unroll
      for (int tt = 0; tt < 2; ++tt)
#pragma unroll
        for (int n = 0; n < 4; ++n) ac2[2 + tt][n] = mfma(wdn[ks][n], a[ks][tt], ks ? ac2[2 + tt][n] : splat(0.f));
#pragma unroll
      for (int e = 4 * (14 - B_AT2) + 6 + 3 * ks; e < 4 * (14 - B_AT2) + 9 + 3 * ks; ++e)
        if (e < 28) A_LD(e);
      if (ks == 2) fa = ld2c(ws, xf);
      if (ks == 7) {
        if (!wall(fa.x == tag && fa.y == ~tag)) wait_flags(ws, xf, tag);
#pragma unroll
        for (int ph = 0; ph < 2; ++ph) {
          xp[ph][0] = ld2c(ws, xo, ph * 2 * 14336); xp[ph][1] = ld2c(ws, xo + 256, ph * 2 * 14336);
          x1[ph] = __builtin_amdgcn_raw_buffer_load_b16(ws, xe, ph * 2 * 14336, SC1);
        }
        gv[0] = ld4c(ws, go); gv[1] = ld4c(ws, go + 16);
      }
      if (ks < 12) RED_W1(0, 1 + (ks >> 2), (ks & 3))
#pragma unroll
      for (int u = 0; u < 4; ++u) {
        __builtin_amdgcn_sched_group_barrier(0x8, 2, 0);
        __builtin_amdgcn_sched_group_barrier(0x20, 1, 0);
        if (u == 2) __builtin_amdgcn_sched_group_barrier(0x200, 1, 0);
      }
      sb();
    }
    STAMP(10);
#pragma unroll
    for (int e = 0; e < 12; ++e) RED_W1(1, 3 - (e >> 2), (e & 3))
#define FIN_OK() wall(gv[0].y == tag && gv[0].w == tag && gv[1].y == tag && gv[1].w == tag)
    if (__builtin_expect(!FIN_OK(), 0)) {
      _Pragma("nounroll") for (int s = 0; s < SPIN; ++s) {
        const int gf = fresh(go);
        gv[0] = ld4c(ws, gf); gv[1] = ld4c(ws, gf + 16);
        if (FIN_OK()) break;
      }
    }
#pragma unroll
    for (int ph = 0; ph < 2; ++ph) {
      const v2u p = xp[ph][0], q = xp[ph][1];
      const bool dn = reg & 1;
      xv[ph].x = __builtin_amdgcn_perm(dn ? q.y : p.x, x1[ph], 0x07060100u);
      xv[ph].y = dn ? __builtin_amdgcn_perm(q.x, q.y, 0x07060100u) : p.y;
      xv[ph].z = dn ? __builtin_amdgcn_perm(p.y, q.x, 0x07060100u) : q.x;
      xv[ph].w = dn ? __builtin_amdgcn_perm(p.x, p.y, 0x07060100u) : q.y;
      pin(xv[ph]);
    }
    bar();
#pragma unroll
    for (int ph = 0; ph < 2; ++ph) {
      v2f r[4];
#pragma unroll
      for (int n = 0; n < 4; ++n) {
        r[n] = lo2(ac2[2 * ph][n]);
#pragma unroll
        for (int w = 0; w < 3; ++w) r[n] += *(const LAS v2f*)(L + rr_ + w * 2048 + ph * 24576 + n * 512);
      }
      v4u o;
#pragma unroll
      for (int q = 0; q < 2; ++q) {
        o[2 * q] = pk2(fma2(bun(gv[q].x), v2f{r[0][q], r[1][q]}, bun(xv[ph][2 * q])));
        o[2 * q + 1] = pk2(fma2(bun(gv[q].z), v2f{r[2][q], r[3][q]}, bun(xv[ph][2 * q + 1])));
      }
      __builtin_amdgcn_raw_buffer_store_b128(o, mkrs(out, S * D * 2), oo[ph], 0, OUT_AUX);
    }
    if (blockIdx.x == 0 && wave == 0) {
      _Pragma("nounroll") for (int s = 0; s < SPIN; ++s) {
        const v4u v = ld4c(ws, fresh(WS_MOD + (70 + (lane < 14 ? lane : 0)) * 128));
        if (wall(v.y == tag)) break;
      }
      if (lane == 0) __builtin_amdgcn_raw_buffer_store_b32(epoch + 1u, ws, WS_EPOCH, 0, SC1);
    }
  }
  STAMP(11);
  DUMP();
}

extern "C" void kernel_launch(void* const* d_in, const int* in_sizes, int n_in, void* d_out, int out_size, void* d_ws,
                              size_t ws_size, hipStream_t stream) {
  fused<<<256, 256, 0, stream>>>((const u16*)d_in[0], (const u16*)d_in[1], (const u16*)d_in[2], (const u16*)d_in[4],
                                 (const u16*)d_in[5], (const u16*)d_in[6], (const u16*)d_in[7], (const u16*)d_in[8],
                                 (u16*)d_out, (char*)d_ws);
}
```

```cpp
#define A_V 21
#define A_PER 4
#include <hip/hip_runtime.h>

typedef unsigned short u16;
typedef unsigned int u32;
typedef unsigned long long u64;
typedef u32 v4u __attribute__((ext_vector_type(4)));
typedef u32 v2u __attribute__((ext_vector_type(2)));
typedef float v4f __attribute__((ext_vector_type(4)));
typedef float v2f __attribute__((ext_vector_type(2)));
typedef short v8s __attribute__((ext_vector_type(8)));
typedef short v4s __attribute__((ext_vector_type(4)));
typedef __bf16 v8b __attribute__((ext_vector_type(8)));
typedef __bf16 v2b __attribute__((ext_vector_type(2)));
#define LAS __attribute__((address_space(3)))
#define GAS __attribute__((address_space(1)))
#define DEV __device__ __forceinline__

#define C_W1_EARLY 0
#define CLR_DELAY 0
#define W_DELAY 0
#define CLR2_DELAY 0
#define PF_POS 2
#define SL1 1
#define SL2 2
#define SL4 4
#define X_AFTER 8
#define PF_AUX 2
#define W_LATE 1
#define W1_PF 1
#define DMA_MOD ""
#define LEAD 6
#define WDV(f) ((f) - WD_MID + 2 < 14 ? (f) - WD_MID + 2 : ((f) - WD_MID - 12) / 2 + 16)
#define B_AT2 12
#define RE_PF 0
#define PF2_DELAY 0
#define C_DMA_EARLY 0
#define EVEN_PF 0
#define XBAL 1
#define W1EVEN 1
#define PRE_OUT 0
#define OUT_AUX 16
#define CLR_MODE 0
#define P_AT 7
#define WD_PER 3
#define A_FIRST 10
#define B_FIRST 10
#define A_AT 9
#define F_AT 4
#define CODE_BYTES 45056
#define WD_EARLY 20
#define WD_MID 20
#define B_AT 9
constexpr int S = 2048, D = 448, DF = 225, F = 1792;

struct alignas(16) Chain { u16 v[2048]; };
struct Cos448 { u16 v[448]; };
constexpr double c_cos_t(double a) {
  double a2 = a * a, t = 1.0, s = 1.0;
  for (int i = 1; i <= 10; ++i) { t = -t * a2 / ((2 * i - 1) * (2 * i)); s += t; }
  return s;
}
constexpr double c_sin_t(double a) {
  double a2 = a * a, t = a, s = a;
  for (int i = 1; i <= 10; ++i) { t = -t * a2 / ((2 * i) * (2 * i + 1)); s += t; }
  return s;
}
constexpr double c_cos448(int m) {
  if (m > 224) m = 448 - m;
  double sg = 1.0;
  if (m > 112) { m = 224 - m; sg = -1.0; }
  const double w = 6.283185307179586476925286766559 / 448.0;
  return m == 112 ? 0.0 : (m <= 56 ? sg * c_cos_t(w * m) : sg * c_sin_t(w * (112 - m)));
}
constexpr u16 c_bf16(double d) {
  const u32 u = __builtin_bit_cast(u32, (float)d);
  return (u16)((u + 0x7FFFu + ((u >> 16) & 1u)) >> 16);
}
constexpr Cos448 mk_cos() { Cos448 c{}; for (int m = 0; m < 448; ++m) c.v[m] = c_bf16(c_cos448(m)); return c; }
constexpr Cos448 CTAB = mk_cos();
constexpr u16 bcos(int m) { return CTAB.v[m % 448]; }
constexpr u16 bsin(int m) { return CTAB.v[(m % 448 + 336) % 448]; }
constexpr u16 bneg(u16 b) { return b ? (u16)(b ^ 0x8000u) : b; }
constexpr u16 bscale(u16 b, int e) { return b ? (u16)(b + e * 0x80) : b; }

constexpr Chain mk_p1(int c) {
  const int tile = c >> 2, par = c & 1, sn = (c >> 1) & 1;
  Chain ch{};
  for (int s = 0; s < 4; ++s)
    for (int l = 0; l < 64; ++l) {
      const int kp = 16 * tile + (l & 15), k = 2 * kp + par;
      if (k > 224) continue;
      for (int j = 0; j < 8; ++j) {
        const int dl = 32 * s + 8 * (l >> 4) + j;
        if (dl > 112) continue;
        u16 b = sn ? bneg(bsin(k * dl)) : bcos(k * dl);
        if (dl == 0 || dl == 112) b = bscale(b, -1);
        ch.v[(s * 64 + l) * 8 + j] = b;
      }
    }
  return ch;
}
constexpr Chain mk_p3(int c) {
  const int tile = c >> 1, sn = c & 1;
  Chain ch{};
  for (int s = 0; s < 4; ++s)
    for (int l = 0; l < 64; ++l) {
      int dl = 16 * tile + (l & 15);
      if (c == 1 && dl == 0) dl = 112;
      for (int j = 0; j < 8; ++j) {
        const int k = 2 * (32 * s + 8 * (l >> 4) + j) + 1;
        if (k < 224) ch.v[(s * 64 + l) * 8 + j] = sn ? bsin(k * dl) : bcos(k * dl);
      }
    }
  return ch;
}
constexpr Chain mk_p2(int c) {
  Chain ch{};
  for (int f = 0; f < 4; ++f)
    for (int l = 0; l < 64; ++l)
      for (int j = 0; j < 8; ++j) {
        const int comp = c < 8 ? (c & 1) : (f >> 1), s = c < 8 ? f : (f & 1);
        const int o = 16 * (c < 8 ? (c >> 1) : (c - 8)) + (l & 15), i = 16 * s + 4 * (l >> 4) + (j >> 1);
        const int m = (c < 8 ? 7 : 14) * ((i * o) % 64);
        u16 b = 0;
        if (comp == 0) b = (j & 1) ? bsin(m) : bcos(m);
        else b = (j & 1) ? bcos(m) : bneg(bsin(m));
        ch.v[(f * 64 + l) * 8 + j] = b;
      }
  return ch;
}
template <int I> constexpr Chain P1C = mk_p1(I);
template <int I> constexpr Chain P3C = mk_p3(I);
template <int I> constexpr Chain P2C = mk_p2(I);
#define L10(T, b) T<b>, T<b + 1>, T<b + 2>, T<b + 3>, T<b + 4>, T<b + 5>, T<b + 6>, T<b + 7>, T<b + 8>, T<b + 9>
#define L29(T) L10(T, 0), L10(T, 10), T<20>, T<21>, T<22>, T<23>, T<24>, T<25>, T<26>, T<27>, T<28>
constexpr int TAB_P1 = 0, TAB_P3 = 29, TAB_P2 = 43, TAB_N = 53;
static __device__ const Chain TAB[TAB_N] = {L29(P1C), L10(P3C, 0), P3C<10>, P3C<11>, P3C<12>, P3C<13>, L10(P2C, 0)};

constexpr int WS_EPOCH = 0;
constexpr int WS_RDY = 4096;
constexpr int WS_HRDY = 4096 + 8192;
constexpr int WS_DRDY = 4096 + 16384;
constexpr int WS_XRDY = 131072 + 65536;
constexpr int WS_ARDY = 131072;
constexpr int WS_CRDY = 131072 + 32768;
constexpr int WS_MOD = 32768, MOD_REP = 12288;
constexpr int T1_BLK = 226 * 64;
constexpr int WS_T1 = 1 << 18;
constexpr int WS_WT = WS_T1 + 128 * T1_BLK;
constexpr int WS_H2 = WS_WT + 113 * 16384;
constexpr int WS_X2 = WS_H2 + 128 * 14336;
constexpr int WS_WD = WS_X2 + 128 * 14336;
constexpr int WS_ACT = WS_WD + 7 * 56 * 4096;
constexpr int WS_END = WS_ACT + 128 * 57344;
constexpr int WS_DBG = 32 << 20;

constexpr int L_W1 = 0;
constexpr int L_B = 114688;
constexpr int XP = 912;
constexpr int L_XS = L_B;
constexpr int L_ST = L_XS + 16 * XP;
constexpr int L_M1 = L_ST + 128;
constexpr int L_AF = L_M1 + 4 * 1792;
constexpr int L_M2 = L_AF + 16384;
constexpr int L_EX = L_M2 + 3 * 1792;
constexpr int L_REND = L_EX + 512;
constexpr int ZPL = 8192;
constexpr int L_CI = L_B;
constexpr int L_CX = L_CI + 2 * 8256;
constexpr int L_RED = L_CX + 16384;
constexpr int L_DBG = 163840 - 1024;
constexpr int L_SZ = 163840;
static_assert(L_REND <= L_DBG && L_RED + 2048 <= L_DBG && 2 * ZPL <= 16384, "lds");

constexpr u32 SENT = 0xFFFFFFFFu;
constexpr int SPIN = 1 << 14;
constexpr int SC1 = 16;
constexpr int OOB = 0x7FFF0000;

typedef __amdgpu_buffer_rsrc_t rsrc_t;
DEV rsrc_t mkrs(const void* p, int bytes) { return __builtin_amdgcn_make_buffer_rsrc((void*)p, 0, bytes, 0x00020000); }
DEV v4u ld4(rsrc_t r, int off, int so = 0) { return __builtin_amdgcn_raw_buffer_load_b128(r, off, so, 0); }
DEV v2u ld2(rsrc_t r, int off, int so = 0) { return __builtin_amdgcn_raw_buffer_load_b64(r, off, so, 0); }
DEV v4u ld4c(rsrc_t r, int off, int so = 0) { return __builtin_amdgcn_raw_buffer_load_b128(r, off, so, SC1); }
DEV v2u ld2c(rsrc_t r, int off, int so = 0) { return __builtin_amdgcn_raw_buffer_load_b64(r, off, so, SC1); }
DEV u32 ld1c(rsrc_t r, int off, int so = 0) { return __builtin_amdgcn_raw_buffer_load_b32(r, off, so, SC1); }
DEV void st4c(rsrc_t r, int off, v4u v) { __builtin_amdgcn_raw_buffer_store_b128(v, r, off, 0, SC1); }
DEV void st2c(rsrc_t r, int off, v2u v) { __builtin_amdgcn_raw_buffer_store_b64(v, r, off, 0, SC1); }
DEV float blo(u32 v) { return __builtin_bit_cast(float, v << 16); }
DEV float bhi(u32 v) { return __builtin_bit_cast(float, v & 0xFFFF0000u); }
DEV v2f bun(u32 v) { return v2f{blo(v), bhi(v)}; }
DEV u32 pk(float lo, float hi) { return __builtin_bit_cast(u32, __builtin_convertvector(v2f{lo, hi}, v2b)); }
DEV u32 pk2(v2f v) { return __builtin_bit_cast(u32, __builtin_convertvector(v, v2b)); }
DEV float rb(float f) { return blo(pk(f, 0.f)); }
DEV v2f lo2(v4f v) { return __builtin_shufflevector(v, v, 0, 1); }
DEV v2f hi2(v4f v) { return __builtin_shufflevector(v, v, 2, 3); }
DEV v2f fma2(v2f a, v2f b, v2f c) { return __builtin_elementwise_fma(a, b, c); }
DEV v4f fma4(v4f a, v4f b, v4f c) { return __builtin_elementwise_fma(a, b, c); }
typedef int v4i __attribute__((ext_vector_type(4)));
DEV v4f relu4(v4f v) { return __builtin_bit_cast(v4f, __builtin_elementwise_max(__builtin_bit_cast(v4i, v), v4i{0, 0, 0, 0})); }
DEV v4f splat(float f) { return v4f{f, f, f, f}; }
DEV v4f mfma(v4u a, v4u b, v4f c) {
  return __builtin_amdgcn_mfma_f32_16x16x32_bf16(__builtin_bit_cast(v8b, a), __builtin_bit_cast(v8b, b), c, 0, 0, 0);
}
DEV void mfa(v4f& c, v4u a, v4u b) { asm volatile("s_nop 1\n\tv_mfma_f32_16x16x32_bf16 %0, %1, %2, %0" : "+v"(c) : "v"(a), "v"(b)); }
DEV void mf0(v4f& c, v4u a, v4u b) { asm volatile("s_nop 1\n\tv_mfma_f32_16x16x32_bf16 %0, %1, %2, 0" : "=&v"(c) : "v"(a), "v"(b)); }
template <int N> DEV void mfence(v4f& a, v4f& b) { asm volatile("s_nop %2" : "+v"(a), "+v"(b) : "i"(N)); }
template <int N> DEV void mfence(v4f& a, v4f& b, v4f& c, v4f& d) { asm volatile("s_nop %4" : "+v"(a), "+v"(b), "+v"(c), "+v"(d) : "i"(N)); }
#define MF "v_mfma_f32_16x16x32_bf16 "
DEV void mf8(v4f& d0, v4f& d1, const v4u (&a0)[4], const v4u (&b0)[4], const v4u (&a1)[4], const v4u (&b1)[4]) {
  asm volatile("s_nop 1\n\t" MF "%0, %2, %6, 0\n\t" MF "%1, %10, %14, 0\n\t" MF "%0, %3, %7, %0\n\t" MF "%1, %11, %15, %1\n\t"
               MF "%0, %4, %8, %0\n\t" MF "%1, %12, %16, %1\n\t" MF "%0, %5, %9, %0\n\t" MF "%1, %13, %17, %1"
               : "=&v"(d0), "=&v"(d1)
               : "v"(a0[0]), "v"(a0[1]), "v"(a0[2]), "v"(a0[3]), "v"(b0[0]), "v"(b0[1]), "v"(b0[2]), "v"(b0[3]),
                 "v"(a1[0]), "v"(a1[1]), "v"(a1[2]), "v"(a1[3]), "v"(b1[0]), "v"(b1[1]), "v"(b1[2]), "v"(b1[3]));
}
#define H4_M(XA, D, X, K, C) MF D ", " XA(X, K) ", " C "\n\t"
#define H4_XA(X, K) X ", " K
#define H4_XB(X, K) K ", " X
#define H4_DEF(NAME, XA, C0, C1, CONS, N0, N1, PA, PB, PC, PD) \
DEV void NAME(v4f& d0, v4f& d1, v4u x0, v4u x1, v4u k00, v4u k01, v4u k10, v4u k11, \
              u32& o0, u32& o1, float p0, float q0, float c0, float s0, float p1, float q1, float c1, float s1) { \
  float t0, t1, t2, t3; \
  asm volatile(H4_M(XA, "%0", "%8", "%10", C0) "v_mul_f32 %4, %14, %16\n\tv_mul_f32 %5, %15, %16\n\t" \
               H4_M(XA, "%1", "%8", "%12", C1) "v_mul_f32 %6, %18, %20\n\tv_mul_f32 %7, %19, %20\n\t" \
               H4_M(XA, "%0", "%9", "%11", "%0") "v_fma_f32 %4, " N0 "%15, %17, %4\n\tv_fma_f32 %5, " N1 "%14, %17, %5\n\t" \
               H4_M(XA, "%1", "%9", "%13", "%1") "v_fma_f32 %6, " N0 "%19, %21, %6\n\tv_fma_f32 %7, " N1 "%18, %21, %7\n\t" \
               "v_cvt_pk_bf16_f32 %2, " PA ", " PB "\n\tv_cvt_pk_bf16_f32 %3, " PC ", " PD \
               : CONS(d0), CONS(d1), "=&v"(o0), "=&v"(o1), "=&v"(t0), "=&v"(t1), "=&v"(t2), "=&v"(t3) \
               : "v"(x0), "v"(x1), "v"(k00), "v"(k01), "v"(k10), "v"(k11), \
                 "v"(p0), "v"(q0), "v"(c0), "v"(s0), "v"(p1), "v"(q1), "v"(c1), "v"(s1)); \
}
H4_DEF(h4_a0m, H4_XA, "0", "0", "=&v", "", "-", "%4", "%5", "%6", "%7")
H4_DEF(h4_a1m, H4_XA, "%0", "%1", "+v", "", "-", "%4", "%5", "%6", "%7")
H4_DEF(h4_b0m, H4_XB, "0", "0", "=&v", "", "-", "%4", "%5", "%6", "%7")
H4_DEF(h4_b0p, H4_XB, "0", "0", "=&v", "-", "", "%5", "%4", "%7", "%6")
H4_DEF(h4_a0p, H4_XA, "0", "0", "=&v", "-", "", "%5", "%4", "%7", "%6")
#define M4_DEF(NAME, XA, C0, C1, CONS) \
DEV void NAME(v4f& d0, v4f& d1, v4u x0, v4u x1, v4u k00, v4u k01, v4u k10, v4u k11) { \
  asm volatile(H4_M(XA, "%0", "%2", "%4", C0) H4_M(XA, "%1", "%2", "%6", C1) H4_M(XA, "%0", "%3", "%5", "%0") MF "%1, " XA("%3", "%7") ", %1" \
               : CONS(d0), CONS(d1) : "v"(x0), "v"(x1), "v"(k00), "v"(k01), "v"(k10), "v"(k11)); \
}
M4_DEF(m4_a0, H4_XA, "0", "0", "=&v") M4_DEF(m4_a1, H4_XA, "%0", "%1", "+v")
M4_DEF(m4_b0, H4_XB, "0", "0", "=&v") M4_DEF(m4_b1, H4_XB, "%0", "%1", "+v")
DEV void mv(v4f& c, v4u a, v4u b) { asm volatile(MF "%0, %1, %2, %0" : "+v"(c) : "v"(a), "v"(b)); }
DEV void mv0(v4f& c, v4u a, v4u b) { asm volatile(MF "%0, %1, %2, 0" : "=&v"(c) : "v"(a), "v"(b)); }
DEV void nop2() { asm volatile("s_nop 1"); }
template <class T> DEV void pin(T& v) { asm volatile("" : "+v"(v)); }
DEV int fresh(int off) { asm volatile("" : "+v"(off) :: "memory"); return off; }
DEV bool wall(bool ok) { return __builtin_amdgcn_ballot_w64(!ok) == 0; }
DEV bool wany(bool b) { return __builtin_amdgcn_ballot_w64(b) != 0; }
DEV void sb() { __builtin_amdgcn_sched_barrier(0); }
DEV u32 max4(v4u v) { return max(max(v.x, v.y), max(v.z, v.w)); }
DEV void drain() { asm volatile("s_waitcnt vmcnt(0)" ::: "memory"); }
DEV void cfence() { asm volatile("" ::: "memory"); }
DEV void bar() { asm volatile("s_waitcnt lgkmcnt(0)\n\ts_barrier" ::: "memory"); }
template <int CTRL> DEV float dpp(float v) {
  return __builtin_bit_cast(float, __builtin_amdgcn_update_dpp(0, __builtin_bit_cast(int, v), CTRL, 0xF, 0xF, true));
}
DEV float sum16(float v) {
  v += dpp<0x128>(v); v += dpp<0x124>(v); v += dpp<0x122>(v); v += dpp<0x121>(v); return v;
}
DEV void sumg2(float& a, float& b) {
  float c = a, d = b;
  asm volatile("s_nop 1\n\tv_permlane16_swap_b32 %0, %1\n\tv_permlane16_swap_b32 %2, %3" : "+v"(a), "+v"(c), "+v"(b), "+v"(d));
  a += c; b += d; c = a; d = b;
  asm volatile("s_nop 1\n\tv_permlane32_swap_b32 %0, %1\n\tv_permlane32_swap_b32 %2, %3" : "+v"(a), "+v"(c), "+v"(b), "+v"(d));
  a += c; b += d;
}
DEV v4u lds4(const char LAS* L, int off) { return *(const LAS v4u*)(L + off); }
DEV v4f lds4f(const char LAS* L, int off) { return *(const LAS v4f*)(L + off); }
DEV v2u ldtr(const char LAS* L, int off) { return __builtin_bit_cast(v2u, __builtin_amdgcn_ds_read_tr16_b64_v4i16((LAS v4s*)(L + off))); }
DEV v4u ldtr2(const char LAS* L, int o0, int o1) { const v2u a = ldtr(L, o0), b = ldtr(L, o1); return v4u{a.x, a.y, b.x, b.y}; }
DEV void dma16(rsrc_t r, int voff, int soff, u32 lds_dst) {
  u32 keep;
  asm volatile("s_mov_b32 %0, m0\n\ts_mov_b32 m0, %1\n\ts_nop 0\n\tbuffer_load_dwordx4 %2, %3, %4 offen" DMA_MOD " lds\n\ts_mov_b32 m0, %0"
               : "=&s"(keep) : "s"(lds_dst), "v"(voff), "s"(r), "s"(soff) : "memory");
}
DEV void light_poll(rsrc_t ws, int off) {
  _Pragma("nounroll") for (int s = 0; s < SPIN; ++s) {
    if (wall(ld1c(ws, fresh(off)) != SENT)) break;
    __builtin_amdgcn_s_sleep(SL2);
  }
}
DEV int tok_row(int blk, int t) {
  const int n1 = 16 * (blk >> 5) + 4 * ((blk >> 2) & 3) + (t & 3), n2 = 16 * ((blk >> 4) & 1) + 4 * (blk & 3) + (t >> 2);
  return n2 + 32 * n1;
}
template <bool SWAP> DEV v4u cmul_pack(v4f a, v4f b, v4f c, v4f s) {
  const v4f re = fma4(b, s, a * c), im = fma4(b, c, -(a * s));
  return SWAP ? v4u{pk(im.x, re.x), pk(im.y, re.y), pk(im.z, re.z), pk(im.w, re.w)}
              : v4u{pk(re.x, im.x), pk(re.y, im.y), pk(re.z, im.z), pk(re.w, im.w)};
}

template <bool PLUS> DEV void cmul2(u32& o0, u32& o1, v2f p, v2f q, v2f c, v2f s) {
  const v2f re = fma2(PLUS ? -q : q, s, p * c), im = fma2(PLUS ? p : -p, s, q * c);
  o0 = PLUS ? pk(im.x, re.x) : pk(re.x, im.x); o1 = PLUS ? pk(im.y, re.y) : pk(re.y, im.y);
}

#define STAMP(i)
#define CSTAMP(i)
#define SLOWCNT
#define DUMP()

DEV void dma_w1(const u16* wg, const u16* wu, int slice, int wave, int lane, u32 lbase) {
  const rsrc_t rg = mkrs(wg, D * F * 2), ru = mkrs(wu, D * F * 2);
  const int r = lane >> 3, ch = ((((lane & 7) >> 1) ^ (lane >> 4)) & 3) * 32 + (lane & 1) * 16 + slice * 128;
  const int vp = r * (F * 2) + ch, vn = (7 - r) * (F * 2) + ch;
#pragma unroll
  for (int n = 0; n < 4; ++n) {
    const int ks = wave + 4 * n;
    if (n == 3 && wave >= 2) break;
    const int t16 = (ks >> 1) * 16, a = ks & 1;
#pragma unroll
    for (int q = 0; q < 4; ++q) {
      const int dl0 = t16 + 8 * (q & 1);
      const bool pos = (q >> 1) == a;
      const int d0 = q < 2 ? (a ? 224 - dl0 - 7 : dl0) : (a ? 224 + dl0 : 448 - dl0 - 7);
      int vo = pos ? vp : vn, so = d0 * (F * 2);
      if (n == 0 && q == 2) {
        vo += so; so = 0;
        if (wave < 2 && lane < 8) vo = (wave ? 336 : 112) * (F * 2) + ch;
      }
      dma16(rg, vo, so, lbase + L_W1 + ks * 8192 + q * 1024);
      dma16(ru, vo, so, lbase + L_W1 + ks * 8192 + 4096 + q * 1024);
    }
  }
}
struct ModLoad { u32 cv[7]; v4u mw[7]; };
DEV void mod_load(ModLoad& m, const u16* cin, const u16* wm, int q, int tid) {
  const rsrc_t cr = mkrs(cin, q >= 0 ? D * 2 : 0), mr = mkrs(wm, q >= 0 ? D * 6 * D * 2 : 0);
#pragma unroll
  for (int i = 0; i < 7; ++i) m.cv[i] = __builtin_amdgcn_raw_buffer_load_b16(cr, (tid >> 2) * 2 + i * 128, 0, 0);
#pragma unroll
  for (int i = 0; i < 7; ++i) m.mw[i] = __builtin_amdgcn_raw_buffer_load_b128(mr, ((tid >> 2) * (6 * D) + q * 32 + (tid & 3) * 8) * 2, i * 64 * 6 * D * 2, PF_AUX);
}
DEV void mod_part(ModLoad& m, char LAS* L, int tid) {
  float sl[7];
#pragma unroll
  for (int i = 0; i < 7; ++i) { const float cc = blo(m.cv[i]); sl[i] = rb(cc * __builtin_amdgcn_rcpf(1.f + __expf(-cc))); pin(sl[i]); }
  v2f a[4];
#pragma unroll
  for (int e = 0; e < 4; ++e) a[e] = v2f{0.f, 0.f};
#pragma unroll
  for (int i = 0; i < 7; ++i)
#pragma unroll
    for (int e = 0; e < 4; ++e) a[e] = fma2(bun(m.mw[i][e]), v2f{sl[i], sl[i]}, a[e]);
#pragma unroll
  for (int e = 0; e < 4; ++e) {
    a[e].x += dpp<0x128>(a[e].x); a[e].y += dpp<0x128>(a[e].y);
    a[e].x += dpp<0x124>(a[e].x); a[e].y += dpp<0x124>(a[e].y);
  }
  if ((tid & 15) < 4) {
    *(LAS v4f*)(L + L_RED + ((tid >> 4) * 32 + (tid & 15) * 8) * 4) = v4f{a[0].x, a[0].y, a[1].x, a[1].y};
    *(LAS v4f*)(L + L_RED + ((tid >> 4) * 32 + (tid & 15) * 8 + 4) * 4) = v4f{a[2].x, a[2].y, a[3].x, a[3].y};
  }
}
DEV void mod_publish(char LAS* L, rsrc_t ws, int q, int lane, u32 tag) {
  v4f s = {0.f, 0.f, 0.f, 0.f};
#pragma unroll
  for (int r = 0; r < 16; ++r) s += lds4f(L, L_RED + (r * 32 + (lane & 7) * 4) * 4);
  const float one = (q / 14 == 1 || q / 14 == 4) ? 1.f : 0.f;
  st4c(ws, WS_MOD + (lane >> 3) * MOD_REP + q * 128 + (lane & 7) * 16,
       v4u{pk(rb(s.x) + one, rb(s.y) + one), tag, pk(rb(s.z) + one, rb(s.w) + one), tag});
}
DEV u32 ld1(rsrc_t r, int off) { return __builtin_amdgcn_raw_buffer_load_b32(r, off, 0, 0); }
DEV u32 ld1n(rsrc_t r, int off) { return __builtin_amdgcn_raw_buffer_load_b32(r, off, 0, PF_AUX); }
DEV u32 mktag(u32 epoch) { return epoch * 0x9E3779B1u + 0x7F4A7C15u; }

DEV void x2_store(rsrc_t ws, const v4u (&r)[4], int blk, int wave, int l16) {
  const int o = WS_X2 + blk * 14336 + (wave == 0 ? 0 : 4 * wave - 2) * 1024 + l16;
#pragma unroll
  for (int m = 0; m < 4; ++m) st4c(ws, m < 2 || wave != 0 ? o + m * 1024 : OOB, r[m]);
}
DEV bool flags_ok(v4u rv, u32 tag) { return rv.x == tag && rv.y == ~tag && rv.z == tag && rv.w == ~tag; }
DEV void wait_flags(rsrc_t ws, int off, u32 tag) {
  _Pragma("nounroll") for (int s = 0; s < SPIN; ++s) {
    const v2u rv = ld2c(ws, fresh(off));
    if (wall(rv.x == tag && rv.y == ~tag)) break;
    __builtin_amdgcn_s_sleep(SL1);
  }
}

__global__ void __launch_bounds__(256, 1)
fused(const u16* __restrict__ xin, const u16* __restrict__ wr, const u16* __restrict__ wi, const u16* __restrict__ wg,
      const u16* __restrict__ wu, const u16* __restrict__ wd, const u16* __restrict__ cin, const u16* __restrict__ wm,
      u16* __restrict__ out, char* wsp) {
  __shared__ __attribute__((aligned(16))) char smem[L_SZ];
  char LAS* L = (char LAS*)smem;
  const u32 lbase = (u32)(size_t)L;
  const int tid = threadIdx.x, lane = tid & 63, wave = __builtin_amdgcn_readfirstlane(tid >> 6);
  const int g = lane >> 4, c16 = lane & 15, l16 = lane * 16;
  const int grp = blockIdx.x & 7, j = blockIdx.x >> 3;
  u64 pc;
  asm volatile("s_getpc_b64 %0" : "=s"(pc));
  STAMP(0); CSTAMP(31);
  asm volatile("" :: "s"(xin), "s"(wr), "s"(wi), "s"(wg), "s"(wu), "s"(wd), "s"(cin), "s"(wm), "s"(out), "s"(wsp));
  const rsrc_t ws = mkrs(wsp, WS_END);
  const rsrc_t tab = mkrs(TAB, TAB_N * 4096);
  const rsrc_t code = mkrs((const void*)(pc & ~127ull), CODE_BYTES);
  const bool isR = j >= 14 && j < 30;
  const bool isG = j < 28;
  const int gk = j / 7, ct = j - gk * 7;
  const int tile = 4 * (grp >> 2) + gk, slice = 7 * (grp & 3) + ct;
  const int wdb = WS_WD + (ct * 56 + wave * 14) * 4096;
  const v4u sv = {SENT, SENT, SENT, SENT};
  int lb = L_B + l16;
  pin(lb);
  const char LAS* LB = L + lb - L_B;
  const int pfo = ((j * 4 + wave) * 14 + lane) * 128, pfc = ((j * 4 + wave) * 4 + lane) * 128;
  u32 pfa = ld1(tab, lane < 14 ? pfo : OOB), pfb = ld1(code, lane < 4 ? pfc : OOB);
  const int hro = WS_HRDY + (16 * tile + 4 * wave) * 32 + c16 * 8;
  v4u x2r[4];
  const int wl16 = (lane ^ ((wave & 1) << 1)) * 16;
  v4u wdn[14][4];
  u32 epoch, tag;

#define ACT_CLEAR(lo, hi) _Pragma("unroll") for (int m = lo; m < hi; ++m) \
      st4c(ws, isG ? WS_ACT + (16 * (tile + 2 * (m >> 3)) + 4 * (grp & 3)) * 57344 + ct * 32768 + (m & 7) * 4096 + tid * 16 : OOB, sv);
  if (isR) {
    const int blk = 16 * grp + (j - 14);
    epoch = ld1c(ws, WS_EPOCH);
    const int t0 = wave == 0 ? 0 : 2 * wave - 1;
    v4u cf[8][4];
#define CF_LOAD(q) { const int chn = TAB_P1 + ((wave == 0 && q >= 4) ? 28 : 4 * t0 + q), vo = (q < 5 || wave != 0) ? l16 : OOB; \
      _Pragma("unroll") for (int s = 0; s < 4; ++s) cf[q][s] = ld4(tab, vo + s * 1024, chn * 4096); sb(); }
#pragma unroll
    for (int q = 0; q < X_AFTER; ++q) CF_LOAD(q)
    v2u xv[7];
    {
      const rsrc_t xr = mkrs(xin, S * D * 2);
      const int off = tok_row(blk, 4 * wave + g) * (D * 2) + c16 * 8;
#pragma unroll
      for (int m = 0; m < 7; ++m) xv[m] = ld2(xr, off + m * 128);
    }
    typedef u32 v3u __attribute__((ext_vector_type(3)));
    v2u fa_[2], fb_[2]; v3u fc_[2], fd_[2];
    {
      const rsrc_t rr = mkrs(wr, S * DF * 2), ri = mkrs(wi, S * DF * 2);
      const int fo_ = 16 * wave + c16 < 57 ? 2 * g * 450 + (16 * wave + c16) * 8 : OOB;
#pragma unroll
      for (int hh = 0; hh < 2; ++hh) {
        const int so = blk * 7200 + hh * 3600;
        fa_[hh] = ld2(rr, fo_, so); fb_[hh] = ld2(ri, fo_, so);
        fc_[hh] = __builtin_amdgcn_raw_buffer_load_b96(rr, fo_ + 448, so, 0); fd_[hh] = __builtin_amdgcn_raw_buffer_load_b96(ri, fo_ + 448, so, 0);
      }
    }
    sb();
#pragma unroll
    for (int q = X_AFTER; q < 8; ++q) CF_LOAD(q)
    STAMP(23);
    {
      v2f f[14], s2 = {0.f, 0.f};
#pragma unroll
      for (int m = 0; m < 7; ++m) {
        f[2 * m] = bun(xv[m].x); f[2 * m + 1] = bun(xv[m].y);
        s2 += f[2 * m] + f[2 * m + 1];
        *(LAS v2u*)(L + L_XS + (4 * wave + g) * XP + c16 * 8 + m * 128) = xv[m];
      }
      STAMP(21);
      sb();
      {
        const int q = 16 * wave + c16, o = WS_WT + q * 16384 + blk * 128 + g * 16;
#pragma unroll
        for (int hh = 0; hh < 2; ++hh) {
          const v2u a = fa_[hh], b = fb_[hh]; const v3u c = fc_[hh], d = fd_[hh];
          st4c(ws, q < 57 ? o + hh * 64 : OOB, v4u{__builtin_amdgcn_perm(b.x, a.x, 0x05040100u), __builtin_amdgcn_perm(b.y, a.y, 0x05040100u),
                                                   __builtin_amdgcn_perm(d.x, c.x, 0x07060302u), __builtin_amdgcn_perm(d.y, c.y, 0x07060302u)});
          st4c(ws, q < 56 ? o + hh * 64 + 57 * 16384 : OOB,
               v4u{__builtin_amdgcn_perm(b.x, a.x, 0x07060302u), __builtin_amdgcn_perm(b.y, a.y, 0x07060302u),
                   __builtin_amdgcn_perm(d.y, c.y, 0x05040100u), __builtin_amdgcn_perm(d.z, c.z, 0x05040100u)});
        }
      }
      sb();
      const float mean = sum16(s2.x + s2.y) * (1.f / D);
      v2f q2 = {0.f, 0.f};
#pragma unroll
      for (int m = 0; m < 14; ++m) { const v2f d = f[m] - mean; q2 = fma2(d, d, q2); }
      const float rstd = __builtin_amdgcn_rsqf(sum16(q2.x + q2.y) * (1.f / D) + 1e-5f);
      if (c16 == 0) *(LAS v2f*)(L + L_ST + (4 * wave + g) * 8) = v2f{mean, rstd};
    }
    pin(pfa); pin(pfb);
    epoch = __builtin_amdgcn_readfirstlane(epoch);
    tag = mktag(epoch);
    STAMP(22);
    bar();
    v4f n[4][2];
    const int d0 = 8 * (4 * wave + g);
    const v2f st = *(const LAS v2f*)(L + L_ST + c16 * 8);
    {
      int xr_ = L_XS + c16 * XP + d0 * 2, xb_ = L_XS + c16 * XP + (216 - d0) * 2, x0_ = d0 == 0 ? xr_ : xb_ + 464;
      pin(xr_); pin(xb_); pin(x0_);
      const v4u a = lds4(L + xr_, 0), e = lds4(L + xr_, 448), c = lds4(L + xb_, 0), b = lds4(L + xb_, 448);
      const u32 c0 = *(const LAS u16*)(L + xb_ + 16), b0 = *(const LAS u16*)(L + x0_);
#pragma unroll
      for (int i = 0; i < 8; ++i) {
        n[0][i >> 2][i & 3] = (i & 1) ? bhi(a[i >> 1]) : blo(a[i >> 1]);
        n[3][i >> 2][i & 3] = (i & 1) ? bhi(e[i >> 1]) : blo(e[i >> 1]);
        n[1][i >> 2][i & 3] = i == 0 ? blo(b0) : (i & 1) ? bhi(b[(8 - i) >> 1]) : blo(b[(8 - i) >> 1]);
        n[2][i >> 2][i & 3] = i == 0 ? blo(c0) : (i & 1) ? bhi(c[(8 - i) >> 1]) : blo(c[(8 - i) >> 1]);
      }
      const float nm = -st.x * st.y;
#pragma unroll
      for (int m = 0; m < 4; ++m) {
#pragma unroll
        for (int h = 0; h < 2; ++h) { n[m][h] = fma4(n[m][h], splat(st.y), splat(nm)); pin(n[m][h]); }
      }
    }
    sb();
    STAMP(1);
    int mb = L_M1 + d0 * 4;
    pin(mb);
    v4u xs[4];
    int to[4];
    to[0] = WS_T1 + blk * T1_BLK + (16 * t0 + c16) * 64 + g * 16;
    to[1] = wave != 0 ? to[0] + 1024 : c16 < 2 ? to[0] + 112 * 64 : OOB;
    to[2] = to[0] + 114 * 64;
    to[3] = wave != 0 ? to[0] + 1024 + 114 * 64 : OOB;
#pragma unroll
    for (int q = 0; q < 4; ++q) pin(to[q]);

    {
      const int kind = tid >= 112, d = 4 * (tid - 112 * kind);
      int wb = L_M1 + (kind ? 0 : 2 * 1792) + d * 4, wrv = L_M1 + (kind ? 0 : 2 * 1792) + 1792 + (445 - d) * 4;
      int wr0 = d == 0 ? wrv + 12 - 1792 : wrv + 12;
      const int mo = tid < 224 ? WS_MOD + grp * MOD_REP + tid * 16 : OOB;
      pin(wb); pin(wrv); pin(wr0);
      const int ro = WS_RDY + grp * 1024 + min(tid >> 1, 112) * 8;
      v4u mv;
      _Pragma("nounroll") for (int s = 0; s < SPIN; ++s) {
        mv = ld4c(ws, fresh(mo));
        const v2u rv = ld2c(ws, fresh(ro));
        if (wall((tid >= 224 || (mv.y == tag && mv.w == tag)) && rv.x == tag && rv.y == ~tag)) break;
        __builtin_amdgcn_s_sleep(SL1);
      }
      STAMP(2);
      if (tid < 224) {
        const v4f v = {blo(mv.x), bhi(mv.x), blo(mv.z), bhi(mv.z)};
        *(LAS v4f*)(L + wb) = v;
        *(LAS float*)(L + wr0) = v.x;
        *(LAS float*)(L + wrv + 8) = v.y;
        *(LAS float*)(L + wrv + 4) = v.z;
        *(LAS float*)(L + wrv) = v.w;
      }
    }
    bar();
    {
      v4u fr[4];
#pragma unroll
      for (int hh = 0; hh < 2; ++hh) {
        v4f h[4];
#pragma unroll
        for (int m = 0; m < 4; ++m) {
          const int o = ((m == 1 || m == 2) ? 1792 : 0) + (m >= 2 ? 896 : 0) + hh * 16;
          h[m] = fma4(n[m][hh], lds4f(L + mb, o), lds4f(L + mb, 2 * 1792 + o));
        }
        const v4f p = h[0] + h[1], q = h[2] + h[3], u = h[0] - h[1], v = h[3] - h[2];
        const v4f t[4] = {p + q, p - q, u + v, u - v};
#pragma unroll
        for (int m = 0; m < 4; ++m) { fr[m][2 * hh] = pk2(lo2(t[m])); fr[m][2 * hh + 1] = pk2(hi2(t[m])); }
      }
#pragma unroll
      for (int m = 0; m < 4; ++m) *(LAS v4u*)(LB + wave * 1024 + L_AF + m * 4096) = fr[m];
    }
    bar();
    {
      v4u a[4][4];
#pragma unroll
      for (int p = 0; p < 2; ++p)
#pragma unroll
        for (int s = 0; s < 4; ++s) { a[p][s] = lds4(LB, L_AF + (p * 4 + s) * 1024); a[2 + p][s] = lds4(LB, L_AF + ((2 + p) * 4 + s) * 1024); }
      v4f re[4], im[4];
#pragma unroll
      for (int q = 0; q < 5; ++q) {
        if (q < 4) mf8(re[q], im[q], a[q >> 1], cf[4 * (q & 1) + (q >> 1)], a[2 + (q >> 1)], cf[4 * (q & 1) + 2 + (q >> 1)]);
        if (q > 0) {
          if (q < 4) mfence<0>(re[q - 1], im[q - 1]); else mfence<7>(re[3], im[3]);
          xs[q - 1] = v4u{pk(re[q - 1].x, im[q - 1].x), pk(re[q - 1].y, im[q - 1].y), pk(re[q - 1].z, im[q - 1].z), pk(re[q - 1].w, im[q - 1].w)};
          st4c(ws, to[q - 1], xs[q - 1]);
          sb();
        }
      }
    }
    STAMP(3);
    sb();
    sb();
    if (isG) dma_w1(wg, wu, slice, wave, lane, lbase);
#pragma unroll
    for (int q = 0; q < 4; ++q) {
      const int vo = (q < 2 || wave != 0) ? l16 : OOB;
#pragma unroll
      for (int s = 0; s < 4; ++s) cf[2 * q + 1][s] = ld4(tab, vo + s * 1024, (TAB_P3 + 2 * t0 + q) * 4096);
    }
#pragma unroll
    for (int m = 0; m < 4; ++m) {
      st4c(ws, tid + 256 * m < 896 ? WS_H2 + blk * 14336 + 4096 * m + tid * 16 : OOB, sv);
    }
    sb();
    STAMP(20);
    v4f xo[2][4];
    int xa = L_XS + c16 * XP + (16 * t0 + 4 * g) * 2, xd = L_XS + c16 * XP + (192 - 16 * t0 - 4 * g) * 2;
    int xs1 = tid < 16 ? L_XS + c16 * XP + 224 : xd + 512, xs3 = tid < 16 ? L_XS + c16 * XP + 672 : xa + 448;
    pin(xa); pin(xd); pin(xs1); pin(xs3);
#define XOFF(t, m, r) ((t) == 0 && (r) == 0 && (m) == 1 ? xs1 : (t) == 0 && (r) == 0 && (m) == 3 ? xs3 : \
    (m) == 0 ? xa + 32 * (t) + 2 * (r) : (m) == 3 ? xa + 448 + 32 * (t) + 2 * (r) : (m) == 1 ? xd + 512 - 32 * (t) - 2 * (r) : xd + 64 - 32 * (t) - 2 * (r))
    {
      v2u w[2][4]; u32 e[2][2], e3;
#pragma unroll
      for (int t = 0; t < 2; ++t) {
        w[t][0] = *(const LAS v2u*)(L + xa + 32 * t); w[t][3] = *(const LAS v2u*)(L + xa + 448 + 32 * t);
        w[t][1] = *(const LAS v2u*)(L + xd + 504 - 32 * t); w[t][2] = *(const LAS v2u*)(L + xd + 56 - 32 * t);
        e[t][0] = *(const LAS u16*)(L + XOFF(t, 1, 0)); e[t][1] = *(const LAS u16*)(L + XOFF(t, 2, 0));
      }
      e3 = *(const LAS u16*)(L + xs3);
      sb();
#pragma unroll
      for (int t = 0; t < 2; ++t) {
        xo[t][0] = v4f{blo(w[t][0].x), bhi(w[t][0].x), blo(w[t][0].y), bhi(w[t][0].y)};
        xo[t][3] = v4f{t == 0 ? blo(e3) : blo(w[t][3].x), bhi(w[t][3].x), blo(w[t][3].y), bhi(w[t][3].y)};
        xo[t][1] = v4f{blo(e[t][0]), bhi(w[t][1].y), blo(w[t][1].y), bhi(w[t][1].x)};
        xo[t][2] = v4f{blo(e[t][1]), bhi(w[t][2].y), blo(w[t][2].y), bhi(w[t][2].x)};
#pragma unroll
        for (int m = 0; m < 4; ++m) { xo[t][m] -= st.x; pin(xo[t][m]); }
      }
    }
    int m0_[2], m1_[2], md_[2];
#pragma unroll
    for (int r = 0; r < 2; ++r) {
      const int q = r == 0 || tid < 80 ? tid + 256 * r : tid;
      const int kind = (q * 586) >> 16, d = 4 * (q - 112 * kind);
      const int reg = (d >= 112) + (d >= 224) + (d >= 336), cc = ((reg + 1) >> 1) * 224, m1 = (0x1320 >> (4 * reg)) & 3;
      const int d1 = abs(d + 1 - cc), d0_ = abs(d - cc), edge = d == 112 * reg && reg > 0;
      m1_[r] = L_M2 + kind * 1792 + (((d1 >> 4) << 6) | (m1 << 4) | (d1 & 15)) * 4;
      m0_[r] = L_M2 + kind * 1792 + (edge ? reg << 4 : ((d0_ >> 4) << 6) | (m1 << 4) | (d0_ & 15)) * 4;
      md_[r] = (reg & 1) ? -4 : 4;
      pin(m0_[r]); pin(m1_[r]); pin(md_[r]);
    }
    {
      v4u mv[2];
      _Pragma("nounroll") for (int s = 0; s < SPIN; ++s) {
        mv[0] = ld4c(ws, fresh(WS_MOD + grp * MOD_REP + (224 + tid) * 16));
        mv[1] = ld4c(ws, fresh(WS_MOD + grp * MOD_REP + (224 + (tid < 80 ? 256 + tid : tid)) * 16));
        const v2u rv = ld2c(ws, fresh(WS_DRDY + grp * 1024 + min(lane, 48) * 8));
        if (wall(mv[0].y == tag && mv[0].w == tag && mv[1].y == tag && mv[1].w == tag && rv.x == tag && rv.y == ~tag)) break;
        __builtin_amdgcn_s_sleep(SL4);
      }
      if (lane < (CLR_MODE == 1 ? 2 : 1)) st2c(ws, lane == 0 ? WS_HRDY + blk * 32 + wave * 8 : isG ? WS_CRDY + (tile * 4 + (grp & 3)) * 256 + (ct * 4 + wave) * 8 : OOB, v2u{tag, ~tag});
      STAMP(12);
#pragma unroll
      for (int f = 0; f < WD_EARLY; ++f) wdn[f >> 2][f & 3] = ld4c(ws, (isG ? wl16 : OOB) + (f & 3) * 1024, wdb + (f >> 2) * 4096);
      sb();
#pragma unroll
      for (int r = 0; r < 2; ++r) {
        const float sc = m0_[r] < L_M2 + 1792 ? 2.f / (float)(S * D) : 1.f;
        *(LAS float*)(L + m0_[r]) = blo(mv[r].x) * sc;
        *(LAS float*)(L + m1_[r]) = bhi(mv[r].x) * sc;
        *(LAS float*)(L + m1_[r] + md_[r]) = blo(mv[r].z) * sc;
        *(LAS float*)(L + m1_[r] + 2 * md_[r]) = bhi(mv[r].z) * sc;
      }
    }
    bar();
    *(LAS v4u*)(L + L_AF + (tid >> 6) * 4096 + 3072 + l16) = v4u{0u, 0u, 0u, 0u};
    STAMP(4);
    int zr = L_AF + lane * 8, ro = L_M2 + (t0 * 64 + 4 * g) * 4, ro1 = wave == 0 ? ro : ro + 256, ex = L_EX + c16 * 8;
    pin(zr); pin(ro); pin(ro1); pin(ex);
    {
      int zl[4];
#pragma unroll
      for (int m = 0; m < 4; ++m) {
        const int kq = (m >> 1) * 128 + (to[m] == OOB ? 127 : wave == 0 && m == 1 ? 112 + c16 : 16 * (t0 + (m & 1)) + c16);
        zl[m] = L_AF + ((kq & ~31) + ((kq & 4) << 2) + ((kq & 24) >> 1) + (kq & 3)) * 32 + g * 8;
        pin(zl[m]);
        if (to[m] == OOB) xs[m] = sv;
      }
      bar();
      if (isG) wait_flags(ws, hro, tag);
      {
        const int src = (blk * 5 + wave * 13) & 63;
        const int po = __builtin_amdgcn_readlane(to[(0)], 0) + (src & 15) * 64 + (src >> 4) * 16;
        const u32 old = __builtin_amdgcn_ds_bpermute(src * 4, xs[0].x);
        _Pragma("nounroll") for (int s = 0; s < SPIN; ++s) {
          if (wall(ld1c(ws, fresh(po)) != old)) break;
          __builtin_amdgcn_s_sleep(SL2);
        }
      }
      STAMP(13);
      v4u z[4];
      _Pragma("nounroll") for (int s = 0; s < SPIN; ++s) {
        u32 mn = 1;
#pragma unroll
        for (int m = 0; m < 4; ++m) z[m] = ld4c(ws, fresh(to[m]));
#pragma unroll
        for (int m = 0; m < 4; ++m) { const v4u d = z[m] ^ xs[m]; mn = min(min(mn, d.x), min(min(d.y, d.z), d.w)); }
        if (wall(mn != 0)) break;
      }
      STAMP(14);
#pragma unroll
      for (int m = 0; m < 4; ++m) {
        char LAS* p = L + zl[m];
        *(LAS v2u*)(p) = v2u{__builtin_amdgcn_perm(z[m].y, z[m].x, 0x05040100u), __builtin_amdgcn_perm(z[m].w, z[m].z, 0x05040100u)};
        *(LAS v2u*)(p + ZPL) = v2u{__builtin_amdgcn_perm(z[m].y, z[m].x, 0x07060302u), __builtin_amdgcn_perm(z[m].w, z[m].z, 0x07060302u)};
      }
    }
    bar();
    STAMP(5);
    {
      v4u zf[4][4];
#pragma unroll
      for (int ty = 0; ty < 4; ++ty)
#pragma unroll
        for (int s = 0; s < 4; ++s) zf[ty][s] = ldtr2(L + zr, ty * 4096 + s * 1024, ty * 4096 + s * 1024 + 512);
      v4f acc[2][4], x2[2][4], gav[2][4];
      v2f s1 = {0.f, 0.f}, s2 = {0.f, 0.f};
#define P3M(t, ty) mf8(acc[t][ty], acc[t][ty + 1], cf[4 * t + ty], zf[ty], cf[4 * t + ty + 1], zf[ty + 1]);
#define P3X(t, m, yv) { x2[t][m] = fma4(gav[t][m], yv, xo[t][m]); \
      if (t == 0 || wave != 0) { s1 += lo2(x2[t][m]) + hi2(x2[t][m]); \
        s2 = fma2(lo2(x2[t][m]), lo2(x2[t][m]), s2); s2 = fma2(hi2(x2[t][m]), hi2(x2[t][m]), s2); } }
      P3M(1, 0) P3M(1, 2)
#pragma unroll
      for (int m = 0; m < 4; ++m) { gav[0][m] = lds4f(L + ro, m * 64); gav[1][m] = lds4f(L + ro1, m * 64); }
      sb();
      P3M(0, 0) P3M(0, 2)
      mfence<0>(acc[1][0], acc[1][1], acc[1][2], acc[1][3]);
      const v4f A1 = acc[1][0] + acc[1][1], B1 = acc[1][0] - acc[1][1], P1 = acc[1][3] - acc[1][2], Q1 = acc[1][2] + acc[1][3];
      P3X(1, 0, A1 - P1) P3X(1, 1, A1 + P1) P3X(1, 2, B1 - Q1) P3X(1, 3, B1 + Q1)
      sb();
      mfence<0>(acc[0][0], acc[0][1], acc[0][2], acc[0][3]);
      {
        const v4f A = acc[0][0] + acc[0][1], B = acc[0][0] - acc[0][1], P = acc[0][3] - acc[0][2], Q = acc[0][2] + acc[0][3];
        v4f y[4] = {A - P, A + P, B - Q, B + Q};
        const bool sp = tid < 16;
        y[0].x = sp ? A.x : y[0].x; y[2].x = sp ? B.x : y[2].x;
        y[1].x = sp ? acc[1][0].x - acc[0][3].x : y[1].x; y[3].x = sp ? acc[1][0].x + acc[0][3].x : y[3].x;
        P3X(0, 0, y[0]) P3X(0, 1, y[1]) P3X(0, 2, y[2]) P3X(0, 3, y[3])
      }
      float sm = s1.x + s1.y, sq = s2.x + s2.y;
      sumg2(sm, sq);
      if (g == 0) *(LAS v2f*)(L + ex + wave * 128) = v2f{sm, sq};
      v4f sbv[2][4], cbv[2][4];
#pragma unroll
      for (int m = 0; m < 4; ++m) {
        sbv[0][m] = lds4f(L + ro, 1792 + m * 64); cbv[0][m] = lds4f(L + ro, 2 * 1792 + m * 64);
        sbv[1][m] = lds4f(L + ro1, 1792 + m * 64); cbv[1][m] = lds4f(L + ro1, 2 * 1792 + m * 64);
      }
      bar();
      v2f e = *(const LAS v2f*)(L + ex);
#pragma unroll
      for (int w = 1; w < 4; ++w) e += *(const LAS v2f*)(L + ex + w * 128);
      const float mc = e.x * (1.f / D), rstd = __builtin_amdgcn_rsqf(fmaxf(e.y * (1.f / D) - mc * mc, 0.f) + 1e-5f), nb = -mc * rstd;
      const int ho = WS_H2 + blk * 14336 + 2 * t0 * 1024 + l16, ho1 = wave == 0 ? OOB : ho + 2048;
#pragma unroll
      for (int t = 0; t < 2; ++t)
#pragma unroll
        for (int a = 0; a < 2; ++a) {
          v4u o;
#pragma unroll
          for (int hh = 0; hh < 2; ++hh) {
            const int m = 2 * a + hh;
            const v4f hv = fma4(fma4(x2[t][m], splat(rstd), splat(nb)), cbv[t][m], sbv[t][m]);
            o[2 * hh] = pk2(lo2(hv)); o[2 * hh + 1] = pk2(hi2(hv));
          }
          st4c(ws, (t ? ho1 : ho) + a * 1024, o);
        }
      STAMP(6);
      sb();
#pragma unroll
      for (int f = WD_EARLY; f < WD_MID; ++f) wdn[f >> 2][f & 3] = ld4c(ws, (isG ? wl16 : OOB) + (f & 3) * 1024, wdb + (f >> 2) * 4096);
      sb();
#pragma unroll
      for (int e = 0; e < 8; ++e) {
        x2r[e >> 1][2 * (e & 1)] = pk2(lo2(x2[e >> 2][e & 3]) + st.x); x2r[e >> 1][2 * (e & 1) + 1] = pk2(hi2(x2[e >> 2][e & 3]) + st.x);
      }
#pragma unroll
      for (int m = 0; m < 4; ++m) pin(x2r[m]);
    }
    bar();
    if (!isG) {
      x2_store(ws, x2r, blk, wave, l16);
      if (blockIdx.x == 224 && tid == 0) __builtin_amdgcn_raw_buffer_store_b32(epoch + 1u, ws, WS_EPOCH, 0, SC1);
      drain();
      if (lane == 0) st2c(ws, WS_XRDY + blk * 64 + wave * 8, v2u{tag, ~tag});
      DUMP(); return;
    }
  } else {
    const int sj = j >= 30 ? j - 30 : j + 2;
    const int idx = sj * 8 + grp;
    const bool isC = j < 14 || idx == 0;
    const int pair = j < 14 ? grp * 14 + j : 112;
    const int lc = grp + 8 * (sj >> 1);
    const int qu = sj < 4 && lc < 14 ? 2 * lc + (sj & 1) : -1;
    const int t1o = WS_T1 + (32 * wave + (lane >> 3)) * T1_BLK + pair * 128 + (lane & 7) * 16;
    ModLoad ml;
    epoch = ld1c(ws, WS_EPOCH);
    if (qu >= 0) mod_load(ml, cin, wm, qu, tid);
#pragma unroll
    for (int q = 0; q < 4; ++q) st4c(ws, isC ? t1o + 8 * q * T1_BLK : OOB, sv);
    u32 pf0, pf1, pf2;
    {
      const rsrc_t mr = mkrs(wm, D * 6 * D * 2), xr = mkrs(xin, S * D * 2), fr_ = mkrs((wave & 1) ? wi : wr, S * DF * 2);
      const int row = (sj * 4 + wave) * 7 + (lane >> 1), l7 = lane / 7;
      pf0 = __builtin_amdgcn_raw_buffer_load_b32(mr, lane < 14 && (grp < 6 || !(lane & 1)) ? row * (6 * D * 2) + (grp + 8 * (lane & 1)) * 128 : OOB, 0, PF_AUX);
      pf1 = __builtin_amdgcn_raw_buffer_load_b32(xr, lane < (!XBAL ? 28 : sj < 4 ? 0 : sj < 12 ? 28 : 56) ? tok_row(16 * grp + (lane < 28 ? sj : sj - 12), 4 * wave + (l7 & 3)) * (D * 2) + (lane - 7 * l7) * 128 : OOB, 0, PF_AUX);
    }
    sb();
    epoch = __builtin_amdgcn_readfirstlane(epoch);
    tag = mktag(epoch);
    if (qu >= 0) mod_part(ml, L, tid);
    asm volatile("s_waitcnt vmcnt(%0)" :: "i"(W_LATE ? 2 : 3) : "memory");
    { const rsrc_t fr_ = mkrs((wave & 1) ? wi : wr, S * DF * 2);
      pf2 = __builtin_amdgcn_raw_buffer_load_b32(fr_, (lane & 31) < 29 && lane < (!XBAL ? 32 : sj < 4 ? 0 : sj < 12 ? 32 : 64) ? (((16 * grp + (lane < 32 ? sj : sj - 12)) * 7200) & ~127) + ((wave >> 1) * 29 + (lane & 31)) * 128 : OOB, 0, PF_AUX);
    }
    bar();
    if (wave == 0 && qu >= 0) mod_publish(L, ws, qu, lane, tag);
    if (wave == 1 && lane < 8) st2c(ws, isC ? WS_RDY + lane * 1024 + pair * 8 : OOB, v2u{tag, ~tag});
    STAMP(1);
    pin(pf0); pin(pf1); pin(pf2); pin(pfa); pin(pfb);
    const int qn = sj >= 4 && sj < 10 && grp < 7 ? 28 + grp * 6 + (sj - 4) : !isC && idx < 15 ? 69 + idx : -1;
    const int tw = sj >= 10 ? (sj - 10) * 8 + grp : sj == 4 && grp == 7 ? 48 : -1;
    v2u dw[2][8];
    if (qn >= 0) mod_load(ml, cin, wm, qn, tid);
    if (tw >= 0) {
      const rsrc_t dr = mkrs(wd, F * D * 2);
      const int u0 = tw * 4 + wave, uc = u0 / 28, kap = 2 * (u0 - uc * 28);
#pragma unroll
      for (int e = 0; e < 16; ++e)
        dw[e >> 3][e & 7] = __builtin_amdgcn_raw_buffer_load_b64(dr, (4 * g * D + 4 * c16) * 2 + (e & 3) * D * 2, ((32 * kap + 16 * (e >> 2)) * D + 64 * uc) * 2, PF_AUX);
    }
    sb();
    u32 pw[4];
    {
      const rsrc_t rg = mkrs(wg, D * F * 2), ru = mkrs(wu, D * F * 2);
      const int r7 = lane / 7, r0 = W1EVEN ? (j * 4 + wave) * 8 : sj < 4 ? (sj * 4 + wave) * 16 : 256 + ((sj - 4) * 4 + wave) * 4;
      const int po = (r0 + r7) * (F * 2) + ((grp & 3) * 7 + lane - r7 * 7) * 128;
      const int o0 = lane < (W1EVEN ? (j < 14 ? 56 : 0) : sj < 4 ? 56 : 28) ? po : OOB, o1 = !W1EVEN && lane < 56 && sj < 4 ? po + 8 * (F * 2) : OOB;
      pw[0] = ld1(rg, o0); pw[1] = ld1(ru, o0); pw[2] = ld1(rg, o1); pw[3] = ld1(ru, o1);
    }
    sb();
    STAMP(20);
    v4f t1c[2], t1s[2], t2c[2], t2s[2];
#pragma unroll
    for (int t = 0; t < 2; ++t) {
#pragma unroll
      for (int r = 0; r < 4; ++r) {
        const float a1 = (float)((16 * t + 4 * g + r) * (16 * wave + c16)) * (1.f / S);
        const float a2 = (float)((16 * t + c16) * (16 * wave + 4 * g + r)) * (1.f / S);
        t1c[t][r] = __builtin_amdgcn_cosf(a1); t1s[t][r] = __builtin_amdgcn_sinf(a1);
        t2c[t][r] = __builtin_amdgcn_cosf(a2); t2s[t][r] = __builtin_amdgcn_sinf(a2);
      }
      pin(t1c[t]); pin(t1s[t]); pin(t2c[t]); pin(t2s[t]);
    }
    if (tw >= 0) {
#pragma unroll
      for (int tt = 0; tt < 8; ++tt) {
        v4u o;
#pragma unroll
        for (int m = 0; m < 4; ++m)
          o[m] = __builtin_amdgcn_perm(dw[tt >> 2][2 * m + 1][(tt & 3) >> 1], dw[tt >> 2][2 * m][(tt & 3) >> 1], (tt & 1) ? 0x07060302u : 0x05040100u);
        st4c(ws, WS_WD + (tw * 4 + wave) * 8192 + tt * 1024 + l16, o);
      }
    }
    if (qn >= 0) {
      bar();
      mod_part(ml, L, tid);
      bar();
      if (wave == 0) mod_publish(L, ws, qn, lane, tag);
    }
    pin(pw[0]); pin(pw[1]); pin(pw[2]); pin(pw[3]);
    STAMP(21);
    if (!isC) { DUMP(); return; }
    v4u c64[2][4], c32[2][2][2];
#pragma unroll
    for (int cp = 0; cp < 2; ++cp)
#pragma unroll
      for (int s = 0; s < 4; ++s) c64[cp][s] = ld4(tab, l16 + s * 1024, (TAB_P2 + wave * 2 + cp) * 4096);
#pragma unroll
    for (int t = 0; t < 2; ++t)
#pragma unroll
      for (int f = 0; f < 4; ++f) c32[t][f >> 1][f & 1] = ld4(tab, l16 + f * 1024, (TAB_P2 + 8 + t) * 4096);
    sb();
    if (tw >= 0) {
      asm volatile("s_waitcnt vmcnt(16)" ::: "memory");
      bar();
      if (tid < 8) st2c(ws, WS_DRDY + tid * 1024 + tw * 8, v2u{tag, ~tag});
    }
    STAMP(2);
    int wfo = WS_WT + pair * 16384 + (16 * g + wave) * 128 + c16 * 8;
    pin(wfo);
    v2u wf[2][4];
    {
      int wl = L_CI + ((lane >> 2) & 1) * 8256 + wave * 2048 + ((lane >> 3) * 4 + (lane & 3)) * 16, lo = t1o;
      pin(wl); pin(lo);
      light_poll(ws, WS_T1 + (32 * wave + (pair & 31)) * T1_BLK + pair * 128);
      STAMP(13);
      v4u z[4];
      _Pragma("nounroll") for (int s = 0; s < SPIN; ++s) {
        lo = fresh(lo);
#pragma unroll
        for (int q = 0; q < 4; ++q) z[q] = ld4c(ws, lo, 8 * q * T1_BLK);
        if (wall(max(max(max4(z[0]), max4(z[1])), max(max4(z[2]), max4(z[3]))) != SENT)) break;
      }
      STAMP(14);
#pragma unroll
      for (int q = 0; q < 4; ++q) *(LAS v4u*)(L + wl + q * 512) = z[q];
    }
    bar();
#pragma unroll
    for (int t = 0; t < 2; ++t)
#pragma unroll
      for (int r = 0; r < 4; ++r) wf[t][r] = ld2c(ws, wfo + r * 512, t * 8192);
    v2u fd = ld2c(ws, WS_DRDY + grp * 1024 + min(lane, 48) * 8);
    sb();
    STAMP(4);
    {
      v4u a[2][2][4];
#pragma unroll
      for (int c = 0; c < 2; ++c)
#pragma unroll
        for (int mt = 0; mt < 2; ++mt)
#pragma unroll
          for (int s = 0; s < 4; ++s) a[c][mt][s] = lds4(LB, L_CI + c * 8256 + (s * 2 + mt) * 1024);
      v4u old[2][2];
#pragma unroll
      for (int c = 0; c < 2; ++c)
#pragma unroll
        for (int mt = 0; mt < 2; ++mt) old[c][mt] = lds4(LB + wave * 2048, L_CI + c * 8256 + mt * 1024);
      v4f re[2][2], im[2][2], ur[2][2], ui[2][2], fr_[2][2], fi_[2][2];
      u32 f2[2][2][4], f3[2][2][4], f4[4];
      char LAS* xw = L + lb + wave * 1024 - L_B;
#define V4(x) v4u{x[0], x[1], x[2], x[3]}
#define S1K(h) c64[0][2 * h], c64[0][2 * h + 1], c64[1][2 * h], c64[1][2 * h + 1]
#define S1X(c, t, h) re[c][t], im[c][t], a[c][t][2 * h], a[c][t][2 * h + 1], S1K(h)
#define S2X(c, t) ur[c][t], ui[c][t], V4(f2[c][0]), V4(f2[c][1]), c32[t][0][0], c32[t][0][1], c32[t][1][0], c32[t][1][1]
#define S3X(c, t) im[c][t], re[c][t], V4(f3[c][0]), V4(f3[c][1]), c32[t][0][0], c32[t][0][1], c32[t][1][0], c32[t][1][1]
#define CM(o, P, Q, C, S_, h) o[2 * h], o[2 * h + 1], P[2 * h], Q[2 * h], C[2 * h], S_[2 * h], P[2 * h + 1], Q[2 * h + 1], C[2 * h + 1], S_[2 * h + 1]
#define TW1(c, t, h) CM(f2[c][t], re[c][t], im[c][t], t1c[t], t1s[t], h)
#define FLT(c, t, h) CM(f3[c][t], ur[c][t], ui[c][t], fr_[c][t], fi_[c][t], h)
#define TW2(c, t, h) CM(f4, re[c][t], im[c][t], t2c[t], t2s[t], h)
#define HV(x, h) (h ? hi2(x) : lo2(x))
#define CP(PL, o, P, Q, C, S_, h) cmul2<PL>(o[2 * h], o[2 * h + 1], HV(P, h), HV(Q, h), HV(C, h), HV(S_, h));
#define XW(c, t) *(LAS v4u*)(xw + L_CX + (c * 2 + t) * 4096) = V4(f4);
#define UNP(c, t) { _Pragma("unroll") for (int r = 0; r < 4; ++r) { fr_[c][t][r] = blo(wf[t][r][c]); fi_[c][t][r] = bhi(wf[t][r][c]); } \
      pin(fr_[c][t]); pin(fi_[c][t]); }
      nop2();
      m4_a0(S1X(0, 0, 0)); m4_a1(S1X(0, 0, 1)); m4_a0(S1X(0, 1, 0)); mfence<3>(re[0][0], im[0][0]);
      h4_a1m(S1X(0, 1, 1), TW1(0, 0, 0)); h4_a0m(S1X(1, 0, 0), TW1(0, 0, 1));
      h4_a1m(S1X(1, 0, 1), TW1(0, 1, 0)); h4_a0m(S1X(1, 1, 0), TW1(0, 1, 1));
      h4_a1m(S1X(1, 1, 1), TW1(1, 0, 0)); h4_b0m(S2X(0, 0), TW1(1, 0, 1));
      h4_b0m(S2X(0, 1), TW1(1, 1, 0));
      sb();
      CP(false, f2[1][1], re[1][1], im[1][1], t1c[1], t1s[1], 1)
      UNP(0, 0) UNP(0, 1)
      sb();
      h4_b0p(S2X(1, 0), FLT(0, 0, 0)); h4_b0p(S2X(1, 1), FLT(0, 0, 1));
      sb();
      CP(true, f3[0][1], ur[0][1], ui[0][1], fr_[0][1], fi_[0][1], 0) CP(true, f3[0][1], ur[0][1], ui[0][1], fr_[0][1], fi_[0][1], 1)
      UNP(1, 0) UNP(1, 1)
      sb();
      h4_a0p(S3X(0, 0), FLT(1, 0, 0)); h4_a0p(S3X(0, 1), FLT(1, 0, 1));
      sb();
      CP(true, f3[1][1], ur[1][1], ui[1][1], fr_[1][1], fi_[1][1], 0) CP(true, f3[1][1], ur[1][1], ui[1][1], fr_[1][1], fi_[1][1], 1)
#pragma unroll
      for (int e = 0; e < 4; ++e) pin(f3[1][1][e]);
      nop2();
      h4_a0p(S3X(1, 0), TW2(0, 0, 0)); h4_a0p(S3X(1, 1), TW2(0, 0, 1));
      XW(0, 0)
      sb();
      CP(true, f4, re[0][1], im[0][1], t2c[1], t2s[1], 0) CP(true, f4, re[0][1], im[0][1], t2c[1], t2s[1], 1) XW(0, 1)
      sb();
      mfence<0>(re[1][0], im[1][0], re[1][1], im[1][1]);
      CP(true, f4, re[1][0], im[1][0], t2c[0], t2s[0], 0) CP(true, f4, re[1][0], im[1][0], t2c[0], t2s[0], 1) XW(1, 0)
      CP(true, f4, re[1][1], im[1][1], t2c[1], t2s[1], 0) CP(true, f4, re[1][1], im[1][1], t2c[1], t2s[1], 1) XW(1, 1)
      bar();
#pragma unroll
      for (int c = 0; c < 2; ++c)
#pragma unroll
        for (int nt = 0; nt < 2; ++nt)
#pragma unroll
          for (int s = 0; s < 4; ++s) a[c][nt][s] = lds4(LB, L_CX + ((c * 2 + nt) * 4 + s) * 1024);
      const int o2 = WS_T1 + (32 * wave + (lane >> 2)) * T1_BLK + pair * 128 + (lane & 3) * 16;
#define S4(c, t) mf8(im[c][t], re[c][t], c64[0], a[c][t], c64[1], a[c][t]);
#define NEQ(x, y) ((x) == (y) ? (x) ^ 1u : (x))
#define OUT(c, t) { const v4u o = {pk(re[c][t].x, im[c][t].x), pk(re[c][t].y, im[c][t].y), pk(re[c][t].z, im[c][t].z), pk(re[c][t].w, im[c][t].w)}; \
      st4c(ws, o2 + c * 64 + t * 16 * T1_BLK, v4u{NEQ(o.x, old[c][t].x), NEQ(o.y, old[c][t].y), NEQ(o.z, old[c][t].z), NEQ(o.w, old[c][t].w)}); sb(); }
      S4(0, 0) S4(0, 1) mfence<0>(re[0][0], im[0][0]); OUT(0, 0)
      S4(1, 0) mfence<0>(re[0][1], im[0][1]); OUT(0, 1)
      S4(1, 1) mfence<0>(re[1][0], im[1][0]); OUT(1, 0)
      mfence<0>(re[1][1], im[1][1]); OUT(1, 1)
    }
    STAMP(5);
    sb();
    if (!isG) { DUMP(); return; }
    if (!wall(fd.x == tag && fd.y == ~tag)) wait_flags(ws, WS_DRDY + grp * 1024 + min(lane, 48) * 8, tag);
    dma_w1(wg, wu, slice, wave, lane, lbase);
#pragma unroll
    for (int f = 0; f < WD_MID; ++f) wdn[f >> 2][f & 3] = ld4c(ws, wl16 + (f & 3) * 1024, wdb + (f >> 2) * 4096);
    fd = ld2c(ws, hro);
    sb();
    ACT_CLEAR(0, 16)
    sb();
    if (!wall(fd.x == tag && fd.y == ~tag)) wait_flags(ws, hro, tag);
    bar();
  }

  STAMP(7);
  const int blk0 = 16 * tile + 4 * (grp & 3);
  const int ab = WS_ACT + blk0 * 57344 + wave * 14 * 1024;
  v4u h[14][2];
  {
    const int hb = WS_H2 + (16 * tile + 4 * wave) * 14336;
    int tb[2][4];
#pragma unroll
    for (int u = 0; u < 8; ++u) {
      tb[u >> 2][u & 3] = (((lane >> 2) * 128 + ((lane >> 3) & 3) * 32 + (lane & 3) * 8) ^ ((u & 3) << 5)) + (u >> 2) * 57344;
      pin(tb[u >> 2][u & 3]);
    }
#define W_RD(dst, k, u) dst = ldtr2(L + tb[k >= 7][u & 3], (k % 7) * 8192 + (u >> 2) * 4096, (k % 7) * 8192 + (u >> 2) * 4096 + 2048)
#define H_LD(v, tt) hh_[v][tt] = ld4c(ws, l16 + (((v) % 14) & 3) * 1024, hb + (2 * ((v) / 14) + (tt)) * 14336 + (((v) % 14) >> 2) * 4096)
    const int abx[2] = {ab + (wave >> 1) * 57344, ab + (1 - (wave >> 1)) * 57344};
#define G_LD(e) h[(e) >> 1][(e) & 1] = ld4c(ws, l16 + (((e) >> 1) & 3) * 1024, abx[(e) & 1] + ((e) >> 3) * 4096)
    v4u wfr[2][8], hh_[28][2];
#pragma unroll
    for (int u = 0; u < 8; ++u) W_RD(wfr[0][u], 0, u);
    light_poll(ws, hb + (lane & 1) * 14336 + 13 * 1024);
    STAMP(15);
#pragma unroll
    for (int v = 0; v < LEAD; ++v) { H_LD(v, 0); H_LD(v, 1); }
    sb();
    if (!isR && lane < (CLR_MODE == 1 ? 1 : 2))
      st2c(ws, WS_CRDY + ((tile + 2 * lane) * 4 + (grp & 3)) * 256 + (ct * 4 + wave) * 8, v2u{tag, ~tag});
    sb();
#define BAD2(x0, x1) wany(max(max(max(max(x0.x, x0.y), x0.z), max(max(x0.w, x1.x), x1.y)), max(x1.z, x1.w)) == SENT)
#define RETRY2(x0, x1, o0, o1) \
      _Pragma("nounroll") for (int s_ = 0; s_ < SPIN; ++s_) { \
        const int lf_ = fresh(l16); SLOWCNT \
        x0 = ld4c(ws, lf_, o0); x1 = ld4c(ws, lf_, o1); \
        if (wall(max(max4(x0), max4(x1)) != SENT)) break; \
      }
    if (__builtin_expect(BAD2(hh_[0][0], hh_[0][1]), 0)) { RETRY2(hh_[0][0], hh_[0][1], hb, hb + 14336) }
    STAMP(8); CSTAMP(28);
    v4f acc[8][2];
    v2u fa;
    const int fx = WS_CRDY + (tile * 4 + (lane < 32 ? wave : grp & 3)) * 256 + min(lane & 31, 27) * 8;
    const int ao = WS_ACT + (16 * tile + 4 * wave) * 57344 + 2 * slice * 1024 + l16;
#define ACT_OUT(ps, tt, f) { v4u o; \
      _Pragma("unroll") for (int hh = 0; hh < 2; ++hh) { \
        const v4f av = relu4(acc[2 * f + hh][tt]) * acc[4 + 2 * f + hh][tt]; \
        o[2 * hh] = pk2(lo2(av)); o[2 * hh + 1] = pk2(hi2(av)); } \
      st4c(ws, ao + (2 * ps + tt) * 57344 + f * 1024, o); }
#define ACT_ALL(ps) { mfence<0>(acc[0][0], acc[1][0], acc[2][0], acc[3][0]); mfence<0>(acc[4][0], acc[5][0], acc[6][0], acc[7][0]); \
      ACT_OUT(ps, 0, 0) ACT_OUT(ps, 0, 1) sb(); \
      mfence<0>(acc[0][1], acc[1][1], acc[2][1], acc[3][1]); mfence<0>(acc[4][1], acc[5][1], acc[6][1], acc[7][1]); \
      ACT_OUT(ps, 1, 0) ACT_OUT(ps, 1, 1) sb(); }
#define W_RH(dst, k, u, hf) { const v2u t_ = ldtr(L + tb[k >= 7][u & 3], (k % 7) * 8192 + (u >> 2) * 4096 + hf * 2048); \
      dst[2 * hf] = t_.x; dst[2 * hf + 1] = t_.y; }
#pragma unroll
    for (int ps = 0; ps < 2; ++ps)
#pragma unroll
    for (int ks = 0; ks < 14; ++ks) {
      const int v = ps * 14 + ks, cur = v & 1, nk = (v + 1) % 14, vn = v < 27 ? v + 1 : v;
      const bool more = v < 27;
      u32 m0, m1;
      __builtin_amdgcn_s_waitcnt(0xC07F);
      sb();
#pragma unroll
      for (int i = 0; i < 16; ++i) {
        const int u = i & 7, tt = i >> 3;
        if (ks == 0) mv0(acc[u][tt], wfr[cur][u], hh_[v][tt]); else mv(acc[u][tt], wfr[cur][u], hh_[v][tt]);
        if (more) {
          if (i < 4) { W_RH(wfr[cur ^ 1][i], nk, i, 0) W_RH(wfr[cur ^ 1][i], nk, i, 1) }
          else if (i < 12) W_RH(wfr[cur ^ 1][4 + ((i - 4) >> 1)], nk, (4 + ((i - 4) >> 1)), (i & 1))
        }
        if (i == 5 && v + LEAD < 28) H_LD(v + LEAD, 0);
        if (i == 7 && v + LEAD < 28) H_LD(v + LEAD, 1);
        if (i == 9 || i == 11 || i == 13) {
          const int e = (i - 9) >> 1, f = WD_MID + (v < 14 ? v - 2 : 12 + WD_PER * (v - 14) + e);
          if (f < 56 && ((v >= 2 && v < 14 && e == 0) || (v >= 14 && e < WD_PER))) wdn[f >> 2][f & 3] = ld4c(ws, wl16 + (f & 3) * 1024, wdb + (f >> 2) * 4096);
        }
        if (v > A_V && (i & 1) && i >= 5 && ((i - 5) >> 1) < A_PER) G_LD(A_PER * (v - A_V - 1) + ((i - 5) >> 1));
        if (v == 8 && i == 13) fa = ld2c(ws, fx);
        if (more && i == 12) { m0 = max(max(hh_[vn][0].x, hh_[vn][0].y), hh_[vn][0].z); pin(m0); }
        if (more && i == 13) { m1 = max(max(hh_[vn][0].w, hh_[vn][1].x), hh_[vn][1].y); pin(m1); }
        if (more && i == 14) { m0 = max(max(m0, hh_[vn][1].z), hh_[vn][1].w); m0 = max(m0, m1); pin(m0); }
        sb();
      }
      if (v == 12 && !wall(fa.x == tag && fa.y == ~tag)) wait_flags(ws, fx, tag);
      if (v == 19 && isR) {
        static_assert(19 + LEAD < 28, "the count below");
        asm volatile("s_waitcnt vmcnt(12)" ::: "memory");
        if (lane == 0) st2c(ws, WS_XRDY + (16 * grp + j - 14) * 64 + wave * 8, v2u{tag, ~tag});
      }
      if (more && __builtin_expect(wany(m0 == SENT), 0)) {
        const int pb = 2 * ((v + 1) / 14) * 14336 + nk * 1024;
        RETRY2(hh_[vn][0], hh_[vn][1], hb + pb, hb + pb + 14336)
      }
      if (v == 13) {
        STAMP(16); CSTAMP(29);
        ACT_ALL(0)
        if (isR) x2_store(ws, x2r, 16 * grp + j - 14, wave, l16);
        nop2();
      }
      sb();
    }
    STAMP(27);
    ACT_ALL(1)
    CSTAMP(30);
#pragma unroll
    for (int e = A_PER * (27 - A_V); e < 28; ++e) G_LD(e);
    sb();
    STAMP(9);
    v4u a[14][2];
    v4f ac2[4][4];
#define A_LD(e) a[(e) >> 1][(e) & 1] = ld4c(ws, l16 + (((e) >> 1) & 3) * 1024, abx[(e) & 1] + 2 * 57344 + ((e) >> 3) * 4096)
    const int po = ab + 3 * 57344 + min(lane, 6) * 2048 + 1024;
#define KN (ks < 13 ? ks + 1 : ks)
    u32 probe;
#pragma unroll
    for (int ks = 0; ks < 14; ++ks) {
      if (ks == 0 && __builtin_expect(BAD2(h[0][0], h[0][1]), 0)) { RETRY2(h[0][0], h[0][1], abx[0], abx[1]) }
#pragma unroll
      for (int tt = 0; tt < 2; ++tt)
#pragma unroll
        for (int n = 0; n < 4; ++n) ac2[tt][n] = mfma(wdn[ks][n], h[ks][tt], ks ? ac2[tt][n] : splat(0.f));
      const bool bad = ks < 13 && BAD2(h[KN][0], h[KN][1]);
      if (ks == P_AT) probe = ld1c(ws, po);
#pragma unroll
      for (int u = 0; u < 8; ++u) { __builtin_amdgcn_sched_group_barrier(0x8, 1, 0); if (u >= 2) __builtin_amdgcn_sched_group_barrier(0x2, 1, 0); }
      sb();
      if (__builtin_expect(bad, 0)) { RETRY2(h[KN][0], h[KN][1], abx[0] + KN * 1024, abx[1] + KN * 1024) }
      if (ks == B_AT2) {
        STAMP(25);
        if (wany(probe == SENT)) light_poll(ws, po);
        STAMP(26);
      }
      if (ks >= B_AT2) {
#pragma unroll
        for (int e = 4 * (ks - B_AT2); e < 4 * (ks - B_AT2 + 1); ++e) A_LD(e);
      }
      sb();
    }
    STAMP(17);
#pragma unroll
    for (int e = 4 * (14 - B_AT2); e < 4 * (14 - B_AT2) + 6; ++e) A_LD(e);
    sb();
    int rw[3], rr_ = L_B + lane * 8 + wave * 6144, oo[2];
#pragma unroll
    for (int x = 1; x < 4; ++x) { rw[x - 1] = L_B + lane * 8 + (wave ^ x) * 6144 + (x - 1) * 2048; pin(rw[x - 1]); }
    pin(rr_);
    const int blk = blk0 + (wave >> 1);
#pragma unroll
    for (int ph = 0; ph < 2; ++ph) { oo[ph] = (tok_row(blk + 2 * ph, c16) * D + ct * 64 + g * 16 + (wave & 1) * 8) * 2; pin(oo[ph]); }
#define RED_W1(ph, x, n) *(LAS v2f*)(L + rw[(x) - 1] + (ph) * 24576 + (n) * 512) = ((x) & 1) ? hi2(ac2[2 * (ph) + ((x) >> 1)][n]) : lo2(ac2[2 * (ph) + ((x) >> 1)][n]);
    const int n0 = ct * 64 + g * 16 + (wave & 1) * 8, reg = (n0 >= 112) + (n0 >= 224) + (n0 >= 336), xm = (0x1320 >> (4 * reg)) & 3;
    const int dz = abs(n0 - ((reg + 1) >> 1) * 224), db = (reg & 1) ? dz - 8 : dz;
#define XSLOT(m, dl) ((((dl) >> 4) * 2 + ((m) >> 1)) * 1024 + (((dl) >> 2) & 3) * 256 + ((m) & 1) * 8)
    int xo = WS_X2 + blk * 14336 + c16 * 16 + XSLOT(xm, db);
    int xe = WS_X2 + blk * 14336 + c16 * 16 + (n0 == 112 * reg && reg > 0 ? XSLOT(reg, 0) : XSLOT(xm, dz));
    pin(xo); pin(xe);
    const int xf = WS_XRDY + (blk + (lane & 4 ? 2 : 0)) * 64 + (lane & 3) * 8;
    v2u xp[2][2]; u32 x1[2];
    const int go = WS_MOD + grp * MOD_REP + (70 + 2 * ct) * 128 + g * 64 + (wave & 1) * 32;
    v4u xv[2], gv[2];
    STAMP(18);
    sb();
#pragma unroll
    for (int ks = 0; ks < 14; ++ks) {
      if (ks == 0 && __builtin_expect(BAD2(a[0][0], a[0][1]), 0)) { RETRY2(a[0][0], a[0][1], abx[0] + 2 * 57344, abx[1] + 2 * 57344) }
#pragma unroll
      for (int tt = 0; tt < 2; ++tt)
#pragma unroll
        for (int n = 0; n < 4; ++n) ac2[2 + tt][n] = mfma(wdn[ks][n], a[ks][tt], ks ? ac2[2 + tt][n] : splat(0.f));
#pragma unroll
      for (int e = 4 * (14 - B_AT2) + 6 + 3 * ks; e < 4 * (14 - B_AT2) + 9 + 3 * ks; ++e)
        if (e < 28) A_LD(e);
      const bool bad = ks < 13 && BAD2(a[KN][0], a[KN][1]);
      if (ks < 12) RED_W1(0, 1 + (ks >> 2), (ks & 3))
#pragma unroll
      for (int u = 0; u < 4; ++u) {
        __builtin_amdgcn_sched_group_barrier(0x8, 2, 0);
        __builtin_amdgcn_sched_group_barrier(0x20, 1, 0);
        if (u == 2) __builtin_amdgcn_sched_group_barrier(0x200, 1, 0);
        if (u >= 1) __builtin_amdgcn_sched_group_barrier(0x2, 3, 0);
      }
      sb();
      if (__builtin_expect(bad, 0)) { RETRY2(a[KN][0], a[KN][1], abx[0] + 2 * 57344 + KN * 1024, abx[1] + 2 * 57344 + KN * 1024) }
      if (ks == 2) fa = ld2c(ws, xf);
      if (ks == 7) {
        if (!wall(fa.x == tag && fa.y == ~tag)) wait_flags(ws, xf, tag);
#pragma unroll
        for (int ph = 0; ph < 2; ++ph) {
          xp[ph][0] = ld2c(ws, xo, ph * 2 * 14336); xp[ph][1] = ld2c(ws, xo + 256, ph * 2 * 14336);
          x1[ph] = __builtin_amdgcn_raw_buffer_load_b16(ws, xe, ph * 2 * 14336, SC1);
        }
        gv[0] = ld4c(ws, go); gv[1] = ld4c(ws, go + 16);
      }
      sb();
    }
    STAMP(10);
#pragma unroll
    for (int e = 0; e < 12; ++e) RED_W1(1, 3 - (e >> 2), (e & 3))
#define FIN_OK() wall(gv[0].y == tag && gv[0].w == tag && gv[1].y == tag && gv[1].w == tag)
    if (__builtin_expect(!FIN_OK(), 0)) {
      _Pragma("nounroll") for (int s = 0; s < SPIN; ++s) {
        const int gf = fresh(go);
        gv[0] = ld4c(ws, gf); gv[1] = ld4c(ws, gf + 16);
        if (FIN_OK()) break;
      }
    }
#pragma unroll
    for (int ph = 0; ph < 2; ++ph) {
      const v2u p = xp[ph][0], q = xp[ph][1];
      const bool dn = reg & 1;
      xv[ph].x = __builtin_amdgcn_perm(dn ? q.y : p.x, x1[ph], 0x07060100u);
      xv[ph].y = dn ? __builtin_amdgcn_perm(q.x, q.y, 0x07060100u) : p.y;
      xv[ph].z = dn ? __builtin_amdgcn_perm(p.y, q.x, 0x07060100u) : q.x;
      xv[ph].w = dn ? __builtin_amdgcn_perm(p.x, p.y, 0x07060100u) : q.y;
      pin(xv[ph]);
    }
    bar();
#pragma unroll
    for (int ph = 0; ph < 2; ++ph) {
      v2f r[4];
#pragma unroll
      for (int n = 0; n < 4; ++n) {
        r[n] = lo2(ac2[2 * ph][n]);
#pragma unroll
        for (int w = 0; w < 3; ++w) r[n] += *(const LAS v2f*)(L + rr_ + w * 2048 + ph * 24576 + n * 512);
      }
      v4u o;
#pragma unroll
      for (int q = 0; q < 2; ++q) {
        o[2 * q] = pk2(fma2(bun(gv[q].x), v2f{r[0][q], r[1][q]}, bun(xv[ph][2 * q])));
        o[2 * q + 1] = pk2(fma2(bun(gv[q].z), v2f{r[2][q], r[3][q]}, bun(xv[ph][2 * q + 1])));
      }
      __builtin_amdgcn_raw_buffer_store_b128(o, mkrs(out, S * D * 2), oo[ph], 0, OUT_AUX);
    }
  }
  STAMP(11);
  DUMP();
}

extern "C" void kernel_launch(void* const* d_in, const int* in_sizes, int n_in, void* d_out, int out_size, void* d_ws,
                              size_t ws_size, hipStream_t stream) {
  fused<<<256, 256, 0, stream>>>((const u16*)d_in[0], (const u16*)d_in[1], (const u16*)d_in[2], (const u16*)d_in[4],
                                 (const u16*)d_in[5], (const u16*)d_in[6], (const u16*)d_in[7], (const u16*)d_in[8],
                                 (u16*)d_out, (char*)d_ws);
}
```

```cpp
#define W_LATE 0
#include <hip/hip_runtime.h>

typedef unsigned short u16;
typedef unsigned int u32;
typedef unsigned long long u64;
typedef u32 v4u __attribute__((ext_vector_type(4)));
typedef u32 v2u __attribute__((ext_vector_type(2)));
typedef float v4f __attribute__((ext_vector_type(4)));
typedef float v2f __attribute__((ext_vector_type(2)));
typedef short v8s __attribute__((ext_vector_type(8)));
typedef short v4s __attribute__((ext_vector_type(4)));
typedef __bf16 v8b __attribute__((ext_vector_type(8)));
typedef __bf16 v2b __attribute__((ext_vector_type(2)));
#define LAS __attribute__((address_space(3)))
#define GAS __attribute__((address_space(1)))
#define DEV __device__ __forceinline__

#define C_W1_EARLY 0
#define CLR_DELAY 0
#define W_DELAY 0
#define CLR2_DELAY 0
#define PF_POS 2
#define SL1 1
#define SL2 2
#define SL4 4
#define X_AFTER 8
#define PF_AUX 2
#define W1_PF 1
#define DMA_MOD ""
#define LEAD 6
#define A_V 21
#define A_PER 4
#define WDV(f) ((f) - WD_MID + 2 < 14 ? (f) - WD_MID + 2 : ((f) - WD_MID - 12) / 2 + 16)
#define B_AT2 12
#define RE_PF 0
#define PF2_DELAY 0
#define C_DMA_EARLY 0
#define EVEN_PF 0
#define XBAL 1
#define W1EVEN 1
#define PRE_OUT 0
#define OUT_AUX 16
#define CLR_MODE 0
#define MK 0
#define MKD1 0
#define MKD2 0
#define MKD3 0
#define P_AT 7
#define WD_PER 3
#define A_FIRST 10
#define B_FIRST 10
#define A_AT 9
#define F_AT 4
#define CODE_BYTES 45056
#define WD_EARLY 20
#define WD_MID 20
#define B_AT 9
constexpr int S = 2048, D = 448, DF = 225, F = 1792;

struct alignas(16) Chain { u16 v[2048]; };
struct Cos448 { u16 v[448]; };
constexpr double c_cos_t(double a) {
  double a2 = a * a, t = 1.0, s = 1.0;
  for (int i = 1; i <= 10; ++i) { t = -t * a2 / ((2 * i - 1) * (2 * i)); s += t; }
  return s;
}
constexpr double c_sin_t(double a) {
  double a2 = a * a, t = a, s = a;
  for (int i = 1; i <= 10; ++i) { t = -t * a2 / ((2 * i) * (2 * i + 1)); s += t; }
  return s;
}
constexpr double c_cos448(int m) {
  if (m > 224) m = 448 - m;
  double sg = 1.0;
  if (m > 112) { m = 224 - m; sg = -1.0; }
  const double w = 6.283185307179586476925286766559 / 448.0;
  return m == 112 ? 0.0 : (m <= 56 ? sg * c_cos_t(w * m) : sg * c_sin_t(w * (112 - m)));
}
constexpr u16 c_bf16(double d) {
  const u32 u = __builtin_bit_cast(u32, (float)d);
  return (u16)((u + 0x7FFFu + ((u >> 16) & 1u)) >> 16);
}
constexpr Cos448 mk_cos() { Cos448 c{}; for (int m = 0; m < 448; ++m) c.v[m] = c_bf16(c_cos448(m)); return c; }
constexpr Cos448 CTAB = mk_cos();
constexpr u16 bcos(int m) { return CTAB.v[m % 448]; }
constexpr u16 bsin(int m) { return CTAB.v[(m % 448 + 336) % 448]; }
constexpr u16 bneg(u16 b) { return b ? (u16)(b ^ 0x8000u) : b; }
constexpr u16 bscale(u16 b, int e) { return b ? (u16)(b + e * 0x80) : b; }

constexpr Chain mk_p1(int c) {
  const int tile = c >> 2, par = c & 1, sn = (c >> 1) & 1;
  Chain ch{};
  for (int s = 0; s < 4; ++s)
    for (int l = 0; l < 64; ++l) {
      const int kp = 16 * tile + (l & 15), k = 2 * kp + par;
      if (k > 224) continue;
      for (int j = 0; j < 8; ++j) {
        const int dl = 32 * s + 8 * (l >> 4) + j;
        if (dl > 112) continue;
        u16 b = sn ? bneg(bsin(k * dl)) : bcos(k * dl);
        if (dl == 0 || dl == 112) b = bscale(b, -1);
        ch.v[(s * 64 + l) * 8 + j] = b;
      }
    }
  return ch;
}
constexpr Chain mk_p3(int c) {
  const int tile = c >> 1, sn = c & 1;
  Chain ch{};
  for (int s = 0; s < 4; ++s)
    for (int l = 0; l < 64; ++l) {
      int dl = 16 * tile + (l & 15);
      if (c == 1 && dl == 0) dl = 112;
      for (int j = 0; j < 8; ++j) {
        const int k = 2 * (32 * s + 8 * (l >> 4) + j) + 1;
        if (k < 224) ch.v[(s * 64 + l) * 8 + j] = sn ? bsin(k * dl) : bcos(k * dl);
      }
    }
  return ch;
}
constexpr Chain mk_p2(int c) {
  Chain ch{};
  for (int f = 0; f < 4; ++f)
    for (int l = 0; l < 64; ++l)
      for (int j = 0; j < 8; ++j) {
        const int comp = c < 8 ? (c & 1) : (f >> 1), s = c < 8 ? f : (f & 1);
        const int o = 16 * (c < 8 ? (c >> 1) : (c - 8)) + (l & 15), i = 16 * s + 4 * (l >> 4) + (j >> 1);
        const int m = (c < 8 ? 7 : 14) * ((i * o) % 64);
        u16 b = 0;
        if (comp == 0) b = (j & 1) ? bsin(m) : bcos(m);
        else b = (j & 1) ? bcos(m) : bneg(bsin(m));
        ch.v[(f * 64 + l) * 8 + j] = b;
      }
  return ch;
}
template <int I> constexpr Chain P1C = mk_p1(I);
template <int I> constexpr Chain P3C = mk_p3(I);
template <int I> constexpr Chain P2C = mk_p2(I);
#define L10(T, b) T<b>, T<b + 1>, T<b + 2>, T<b + 3>, T<b + 4>, T<b + 5>, T<b + 6>, T<b + 7>, T<b + 8>, T<b + 9>
#define L29(T) L10(T, 0), L10(T, 10), T<20>, T<21>, T<22>, T<23>, T<24>, T<25>, T<26>, T<27>, T<28>
constexpr int TAB_P1 = 0, TAB_P3 = 29, TAB_P2 = 43, TAB_N = 53;
static __device__ const Chain TAB[TAB_N] = {L29(P1C), L10(P3C, 0), P3C<10>, P3C<11>, P3C<12>, P3C<13>, L10(P2C, 0)};

constexpr int WS_EPOCH = 0;
constexpr int WS_RDY = 4096;
constexpr int WS_HRDY = 4096 + 8192;
constexpr int WS_DRDY = 4096 + 16384;
constexpr int WS_XRDY = 131072 + 65536;
constexpr int WS_ARDY = 131072;
constexpr int WS_CRDY = 131072 + 32768;
constexpr int WS_MK1 = 212992, WS_MK2 = 221184, WS_MK3 = 229376;
constexpr int WS_MOD = 32768, MOD_REP = 12288;
constexpr int T1_BLK = 226 * 64;
constexpr int WS_T1 = 1 << 18;
constexpr int WS_WT = WS_T1 + 128 * T1_BLK;
constexpr int WS_H2 = WS_WT + 113 * 16384;
constexpr int WS_X2 = WS_H2 + 128 * 14336;
constexpr int WS_WD = WS_X2 + 128 * 14336;
constexpr int WS_ACT = WS_WD + 7 * 56 * 4096;
constexpr int WS_END = WS_ACT + 128 * 57344;
constexpr int WS_DBG = 32 << 20;

constexpr int L_W1 = 0;
constexpr int L_B = 114688;
constexpr int XP = 912;
constexpr int L_XS = L_B;
constexpr int L_ST = L_XS + 16 * XP;
constexpr int L_M1 = L_ST + 128;
constexpr int L_AF = L_M1 + 4 * 1792;
constexpr int L_M2 = L_AF + 16384;
constexpr int L_EX = L_M2 + 3 * 1792;
constexpr int L_REND = L_EX + 512;
constexpr int ZPL = 8192;
constexpr int L_CI = L_B;
constexpr int L_CX = L_CI + 2 * 8256;
constexpr int L_RED = L_CX + 16384;
constexpr int L_DBG = 163840 - 1024;
constexpr int L_SZ = 163840;
static_assert(L_REND <= L_DBG && L_RED + 2048 <= L_DBG && 2 * ZPL <= 16384, "lds");

constexpr u32 SENT = 0xFFFFFFFFu;
constexpr int SPIN = 1 << 14;
constexpr int SC1 = 16;
constexpr int OOB = 0x7FFF0000;

typedef __amdgpu_buffer_rsrc_t rsrc_t;
DEV rsrc_t mkrs(const void* p, int bytes) { return __builtin_amdgcn_make_buffer_rsrc((void*)p, 0, bytes, 0x00020000); }
DEV v4u ld4(rsrc_t r, int off, int so = 0) { return __builtin_amdgcn_raw_buffer_load_b128(r, off, so, 0); }
DEV v2u ld2(rsrc_t r, int off, int so = 0) { return __builtin_amdgcn_raw_buffer_load_b64(r, off, so, 0); }
DEV v4u ld4c(rsrc_t r, int off, int so = 0) { return __builtin_amdgcn_raw_buffer_load_b128(r, off, so, SC1); }
DEV v2u ld2c(rsrc_t r, int off, int so = 0) { return __builtin_amdgcn_raw_buffer_load_b64(r, off, so, SC1); }
DEV u32 ld1c(rsrc_t r, int off, int so = 0) { return __builtin_amdgcn_raw_buffer_load_b32(r, off, so, SC1); }
DEV void st4c(rsrc_t r, int off, v4u v) { __builtin_amdgcn_raw_buffer_store_b128(v, r, off, 0, SC1); }
DEV void st2c(rsrc_t r, int off, v2u v) { __builtin_amdgcn_raw_buffer_store_b64(v, r, off, 0, SC1); }
DEV float blo(u32 v) { return __builtin_bit_cast(float, v << 16); }
DEV float bhi(u32 v) { return __builtin_bit_cast(float, v & 0xFFFF0000u); }
DEV v2f bun(u32 v) { return v2f{blo(v), bhi(v)}; }
DEV u32 pk(float lo, float hi) { return __builtin_bit_cast(u32, __builtin_convertvector(v2f{lo, hi}, v2b)); }
DEV u32 pk2(v2f v) { return __builtin_bit_cast(u32, __builtin_convertvector(v, v2b)); }
DEV float rb(float f) { return blo(pk(f, 0.f)); }
DEV v2f lo2(v4f v) { return __builtin_shufflevector(v, v, 0, 1); }
DEV v2f hi2(v4f v) { return __builtin_shufflevector(v, v, 2, 3); }
DEV v2f fma2(v2f a, v2f b, v2f c) { return __builtin_elementwise_fma(a, b, c); }
DEV v4f fma4(v4f a, v4f b, v4f c) { return __builtin_elementwise_fma(a, b, c); }
typedef int v4i __attribute__((ext_vector_type(4)));
DEV v4f relu4(v4f v) { return __builtin_bit_cast(v4f, __builtin_elementwise_max(__builtin_bit_cast(v4i, v), v4i{0, 0, 0, 0})); }
DEV v4f splat(float f) { return v4f{f, f, f, f}; }
DEV v4f mfma(v4u a, v4u b, v4f c) {
  return __builtin_amdgcn_mfma_f32_16x16x32_bf16(__builtin_bit_cast(v8b, a), __builtin_bit_cast(v8b, b), c, 0, 0, 0);
}
DEV void mfa(v4f& c, v4u a, v4u b) { asm volatile("s_nop 1\n\tv_mfma_f32_16x16x32_bf16 %0, %1, %2, %0" : "+v"(c) : "v"(a), "v"(b)); }
DEV void mf0(v4f& c, v4u a, v4u b) { asm volatile("s_nop 1\n\tv_mfma_f32_16x16x32_bf16 %0, %1, %2, 0" : "=&v"(c) : "v"(a), "v"(b)); }
template <int N> DEV void mfence(v4f& a, v4f& b) { asm volatile("s_nop %2" : "+v"(a), "+v"(b) : "i"(N)); }
template <int N> DEV void mfence(v4f& a, v4f& b, v4f& c, v4f& d) { asm volatile("s_nop %4" : "+v"(a), "+v"(b), "+v"(c), "+v"(d) : "i"(N)); }
#define MF "v_mfma_f32_16x16x32_bf16 "
DEV void mf8(v4f& d0, v4f& d1, const v4u (&a0)[4], const v4u (&b0)[4], const v4u (&a1)[4], const v4u (&b1)[4]) {
  asm volatile("s_nop 1\n\t" MF "%0, %2, %6, 0\n\t" MF "%1, %10, %14, 0\n\t" MF "%0, %3, %7, %0\n\t" MF "%1, %11, %15, %1\n\t"
               MF "%0, %4, %8, %0\n\t" MF "%1, %12, %16, %1\n\t" MF "%0, %5, %9, %0\n\t" MF "%1, %13, %17, %1"
               : "=&v"(d0), "=&v"(d1)
               : "v"(a0[0]), "v"(a0[1]), "v"(a0[2]), "v"(a0[3]), "v"(b0[0]), "v"(b0[1]), "v"(b0[2]), "v"(b0[3]),
                 "v"(a1[0]), "v"(a1[1]), "v"(a1[2]), "v"(a1[3]), "v"(b1[0]), "v"(b1[1]), "v"(b1[2]), "v"(b1[3]));
}
#define H4_M(XA, D, X, K, C) MF D ", " XA(X, K) ", " C "\n\t"
#define H4_XA(X, K) X ", " K
#define H4_XB(X, K) K ", " X
#define H4_DEF(NAME, XA, C0, C1, CONS, N0, N1, PA, PB, PC, PD) \
DEV void NAME(v4f& d0, v4f& d1, v4u x0, v4u x1, v4u k00, v4u k01, v4u k10, v4u k11, \
              u32& o0, u32& o1, float p0, float q0, float c0, float s0, float p1, float q1, float c1, float s1) { \
  float t0, t1, t2, t3; \
  asm volatile(H4_M(XA, "%0", "%8", "%10", C0) "v_mul_f32 %4, %14, %16\n\tv_mul_f32 %5, %15, %16\n\t" \
               H4_M(XA, "%1", "%8", "%12", C1) "v_mul_f32 %6, %18, %20\n\tv_mul_f32 %7, %19, %20\n\t" \
               H4_M(XA, "%0", "%9", "%11", "%0") "v_fma_f32 %4, " N0 "%15, %17, %4\n\tv_fma_f32 %5, " N1 "%14, %17, %5\n\t" \
               H4_M(XA, "%1", "%9", "%13", "%1") "v_fma_f32 %6, " N0 "%19, %21, %6\n\tv_fma_f32 %7, " N1 "%18, %21, %7\n\t" \
               "v_cvt_pk_bf16_f32 %2, " PA ", " PB "\n\tv_cvt_pk_bf16_f32 %3, " PC ", " PD \
               : CONS(d0), CONS(d1), "=&v"(o0), "=&v"(o1), "=&v"(t0), "=&v"(t1), "=&v"(t2), "=&v"(t3) \
               : "v"(x0), "v"(x1), "v"(k00), "v"(k01), "v"(k10), "v"(k11), \
                 "v"(p0), "v"(q0), "v"(c0), "v"(s0), "v"(p1), "v"(q1), "v"(c1), "v"(s1)); \
}
H4_DEF(h4_a0m, H4_XA, "0", "0", "=&v", "", "-", "%4", "%5", "%6", "%7")
H4_DEF(h4_a1m, H4_XA, "%0", "%1", "+v", "", "-", "%4", "%5", "%6", "%7")
H4_DEF(h4_b0m, H4_XB, "0", "0", "=&v", "", "-", "%4", "%5", "%6", "%7")
H4_DEF(h4_b0p, H4_XB, "0", "0", "=&v", "-", "", "%5", "%4", "%7", "%6")
H4_DEF(h4_a0p, H4_XA, "0", "0", "=&v", "-", "", "%5", "%4", "%7", "%6")
#define M4_DEF(NAME, XA, C0, C1, CONS) \
DEV void NAME(v4f& d0, v4f& d1, v4u x0, v4u x1, v4u k00, v4u k01, v4u k10, v4u k11) { \
  asm volatile(H4_M(XA, "%0", "%2", "%4", C0) H4_M(XA, "%1", "%2", "%6", C1) H4_M(XA, "%0", "%3", "%5", "%0") MF "%1, " XA("%3", "%7") ", %1" \
               : CONS(d0), CONS(d1) : "v"(x0), "v"(x1), "v"(k00), "v"(k01), "v"(k10), "v"(k11)); \
}
M4_DEF(m4_a0, H4_XA, "0", "0", "=&v") M4_DEF(m4_a1, H4_XA, "%0", "%1", "+v")
M4_DEF(m4_b0, H4_XB, "0", "0", "=&v") M4_DEF(m4_b1, H4_XB, "%0", "%1", "+v")
DEV void mv(v4f& c, v4u a, v4u b) { asm volatile(MF "%0, %1, %2, %0" : "+v"(c) : "v"(a), "v"(b)); }
DEV void mv0(v4f& c, v4u a, v4u b) { asm volatile(MF "%0, %1, %2, 0" : "=&v"(c) : "v"(a), "v"(b)); }
DEV void nop2() { asm volatile("s_nop 1"); }
template <class T> DEV void pin(T& v) { asm volatile("" : "+v"(v)); }
DEV int fresh(int off) { asm volatile("" : "+v"(off) :: "memory"); return off; }
DEV bool wall(bool ok) { return __builtin_amdgcn_ballot_w64(!ok) == 0; }
DEV bool wany(bool b) { return __builtin_amdgcn_ballot_w64(b) != 0; }
DEV void sb() { __builtin_amdgcn_sched_barrier(0); }
DEV u32 max4(v4u v) { return max(max(v.x, v.y), max(v.z, v.w)); }
DEV void drain() { asm volatile("s_waitcnt vmcnt(0)" ::: "memory"); }
DEV void cfence() { asm volatile("" ::: "memory"); }
DEV void bar() { asm volatile("s_waitcnt lgkmcnt(0)\n\ts_barrier" ::: "memory"); }
template <int CTRL> DEV float dpp(float v) {
  return __builtin_bit_cast(float, __builtin_amdgcn_update_dpp(0, __builtin_bit_cast(int, v), CTRL, 0xF, 0xF, true));
}
DEV float sum16(float v) {
  v += dpp<0x128>(v); v += dpp<0x124>(v); v += dpp<0x122>(v); v += dpp<0x121>(v); return v;
}
DEV void sumg2(float& a, float& b) {
  float c = a, d = b;
  asm volatile("s_nop 1\n\tv_permlane16_swap_b32 %0, %1\n\tv_permlane16_swap_b32 %2, %3" : "+v"(a), "+v"(c), "+v"(b), "+v"(d));
  a += c; b += d; c = a; d = b;
  asm volatile("s_nop 1\n\tv_permlane32_swap_b32 %0, %1\n\tv_permlane32_swap_b32 %2, %3" : "+v"(a), "+v"(c), "+v"(b), "+v"(d));
  a += c; b += d;
}
DEV v4u lds4(const char LAS* L, int off) { return *(const LAS v4u*)(L + off); }
DEV v4f lds4f(const char LAS* L, int off) { return *(const LAS v4f*)(L + off); }
DEV v2u ldtr(const char LAS* L, int off) { return __builtin_bit_cast(v2u, __builtin_amdgcn_ds_read_tr16_b64_v4i16((LAS v4s*)(L + off))); }
DEV v4u ldtr2(const char LAS* L, int o0, int o1) { const v2u a = ldtr(L, o0), b = ldtr(L, o1); return v4u{a.x, a.y, b.x, b.y}; }
DEV void dma16(rsrc_t r, int voff, int soff, u32 lds_dst) {
  u32 keep;
  asm volatile("s_mov_b32 %0, m0\n\ts_mov_b32 m0, %1\n\ts_nop 0\n\tbuffer_load_dwordx4 %2, %3, %4 offen" DMA_MOD " lds\n\ts_mov_b32 m0, %0"
               : "=&s"(keep) : "s"(lds_dst), "v"(voff), "s"(r), "s"(soff) : "memory");
}
DEV void light_poll(rsrc_t ws, int off) {
  _Pragma("nounroll") for (int s = 0; s < SPIN; ++s) {
    if (wall(ld1c(ws, fresh(off)) != SENT)) break;
    __builtin_amdgcn_s_sleep(SL2);
  }
}
DEV int tok_row(int blk, int t) {
  const int n1 = 16 * (blk >> 5) + 4 * ((blk >> 2) & 3) + (t & 3), n2 = 16 * ((blk >> 4) & 1) + 4 * (blk & 3) + (t >> 2);
  return n2 + 32 * n1;
}
template <bool SWAP> DEV v4u cmul_pack(v4f a, v4f b, v4f c, v4f s) {
  const v4f re = fma4(b, s, a * c), im = fma4(b, c, -(a * s));
  return SWAP ? v4u{pk(im.x, re.x), pk(im.y, re.y), pk(im.z, re.z), pk(im.w, re.w)}
              : v4u{pk(re.x, im.x), pk(re.y, im.y), pk(re.z, im.z), pk(re.w, im.w)};
}

template <bool PLUS> DEV void cmul2(u32& o0, u32& o1, v2f p, v2f q, v2f c, v2f s) {
  const v2f re = fma2(PLUS ? -q : q, s, p * c), im = fma2(PLUS ? p : -p, s, q * c);
  o0 = PLUS ? pk(im.x, re.x) : pk(re.x, im.x); o1 = PLUS ? pk(im.y, re.y) : pk(re.y, im.y);
}

#define STAMP(i)
#define CSTAMP(i)
#define SLOWCNT
#define DUMP()

DEV void dma_w1(const u16* wg, const u16* wu, int slice, int wave, int lane, u32 lbase) {
  const rsrc_t rg = mkrs(wg, D * F * 2), ru = mkrs(wu, D * F * 2);
  const int r = lane >> 3, ch = ((((lane & 7) >> 1) ^ (lane >> 4)) & 3) * 32 + (lane & 1) * 16 + slice * 128;
  const int vp = r * (F * 2) + ch, vn = (7 - r) * (F * 2) + ch;
#pragma unroll
  for (int n = 0; n < 4; ++n) {
    const int ks = wave + 4 * n;
    if (n == 3 && wave >= 2) break;
    const int t16 = (ks >> 1) * 16, a = ks & 1;
#pragma unroll
    for (int q = 0; q < 4; ++q) {
      const int dl0 = t16 + 8 * (q & 1);
      const bool pos = (q >> 1) == a;
      const int d0 = q < 2 ? (a ? 224 - dl0 - 7 : dl0) : (a ? 224 + dl0 : 448 - dl0 - 7);
      int vo = pos ? vp : vn, so = d0 * (F * 2);
      if (n == 0 && q == 2) {
        vo += so; so = 0;
        if (wave < 2 && lane < 8) vo = (wave ? 336 : 112) * (F * 2) + ch;
      }
      dma16(rg, vo, so, lbase + L_W1 + ks * 8192 + q * 1024);
      dma16(ru, vo, so, lbase + L_W1 + ks * 8192 + 4096 + q * 1024);
    }
  }
}
struct ModLoad { u32 cv[7]; v4u mw[7]; };
DEV void mod_load(ModLoad& m, const u16* cin, const u16* wm, int q, int tid) {
  const rsrc_t cr = mkrs(cin, q >= 0 ? D * 2 : 0), mr = mkrs(wm, q >= 0 ? D * 6 * D * 2 : 0);
#pragma unroll
  for (int i = 0; i < 7; ++i) m.cv[i] = __builtin_amdgcn_raw_buffer_load_b16(cr, (tid >> 2) * 2 + i * 128, 0, 0);
#pragma unroll
  for (int i = 0; i < 7; ++i) m.mw[i] = __builtin_amdgcn_raw_buffer_load_b128(mr, ((tid >> 2) * (6 * D) + q * 32 + (tid & 3) * 8) * 2, i * 64 * 6 * D * 2, PF_AUX);
}
DEV void mod_part(ModLoad& m, char LAS* L, int tid) {
  float sl[7];
#pragma unroll
  for (int i = 0; i < 7; ++i) { const float cc = blo(m.cv[i]); sl[i] = rb(cc * __builtin_amdgcn_rcpf(1.f + __expf(-cc))); pin(sl[i]); }
  v2f a[4];
#pragma unroll
  for (int e = 0; e < 4; ++e) a[e] = v2f{0.f, 0.f};
#pragma unroll
  for (int i = 0; i < 7; ++i)
#pragma unroll
    for (int e = 0; e < 4; ++e) a[e] = fma2(bun(m.mw[i][e]), v2f{sl[i], sl[i]}, a[e]);
#pragma unroll
  for (int e = 0; e < 4; ++e) {
    a[e].x += dpp<0x128>(a[e].x); a[e].y += dpp<0x128>(a[e].y);
    a[e].x += dpp<0x124>(a[e].x); a[e].y += dpp<0x124>(a[e].y);
  }
  if ((tid & 15) < 4) {
    *(LAS v4f*)(L + L_RED + ((tid >> 4) * 32 + (tid & 15) * 8) * 4) = v4f{a[0].x, a[0].y, a[1].x, a[1].y};
    *(LAS v4f*)(L + L_RED + ((tid >> 4) * 32 + (tid & 15) * 8 + 4) * 4) = v4f{a[2].x, a[2].y, a[3].x, a[3].y};
  }
}
DEV void mod_publish(char LAS* L, rsrc_t ws, int q, int lane, u32 tag) {
  v4f s = {0.f, 0.f, 0.f, 0.f};
#pragma unroll
  for (int r = 0; r < 16; ++r) s += lds4f(L, L_RED + (r * 32 + (lane & 7) * 4) * 4);
  const float one = (q / 14 == 1 || q / 14 == 4) ? 1.f : 0.f;
  st4c(ws, WS_MOD + (lane >> 3) * MOD_REP + q * 128 + (lane & 7) * 16,
       v4u{pk(rb(s.x) + one, rb(s.y) + one), tag, pk(rb(s.z) + one, rb(s.w) + one), tag});
}
DEV u32 ld1(rsrc_t r, int off) { return __builtin_amdgcn_raw_buffer_load_b32(r, off, 0, 0); }
DEV u32 ld1n(rsrc_t r, int off) { return __builtin_amdgcn_raw_buffer_load_b32(r, off, 0, PF_AUX); }
DEV u32 mktag(u32 epoch) { return epoch * 0x9E3779B1u + 0x7F4A7C15u; }

DEV void x2_store(rsrc_t ws, const v4u (&r)[4], int blk, int wave, int l16) {
  const int o = WS_X2 + blk * 14336 + (wave == 0 ? 0 : 4 * wave - 2) * 1024 + l16;
#pragma unroll
  for (int m = 0; m < 4; ++m) st4c(ws, m < 2 || wave != 0 ? o + m * 1024 : OOB, r[m]);
}
DEV bool flags_ok(v4u rv, u32 tag) { return rv.x == tag && rv.y == ~tag && rv.z == tag && rv.w == ~tag; }
DEV void wait_flags(rsrc_t ws, int off, u32 tag) {
  _Pragma("nounroll") for (int s = 0; s < SPIN; ++s) {
    const v2u rv = ld2c(ws, fresh(off));
    if (wall(rv.x == tag && rv.y == ~tag)) break;
    __builtin_amdgcn_s_sleep(SL1);
  }
}

__global__ void __launch_bounds__(256, 1)
fused(const u16* __restrict__ xin, const u16* __restrict__ wr, const u16* __restrict__ wi, const u16* __restrict__ wg,
      const u16* __restrict__ wu, const u16* __restrict__ wd, const u16* __restrict__ cin, const u16* __restrict__ wm,
      u16* __restrict__ out, char* wsp) {
  __shared__ __attribute__((aligned(16))) char smem[L_SZ];
  char LAS* L = (char LAS*)smem;
  const u32 lbase = (u32)(size_t)L;
  const int tid = threadIdx.x, lane = tid & 63, wave = __builtin_amdgcn_readfirstlane(tid >> 6);
  const int g = lane >> 4, c16 = lane & 15, l16 = lane * 16;
  const int grp = blockIdx.x & 7, j = blockIdx.x >> 3;
  u64 pc;
  asm volatile("s_getpc_b64 %0" : "=s"(pc));
  STAMP(0); CSTAMP(31);
  asm volatile("" :: "s"(xin), "s"(wr), "s"(wi), "s"(wg), "s"(wu), "s"(wd), "s"(cin), "s"(wm), "s"(out), "s"(wsp));
  const rsrc_t ws = mkrs(wsp, WS_END);
  const rsrc_t tab = mkrs(TAB, TAB_N * 4096);
  const rsrc_t code = mkrs((const void*)(pc & ~127ull), CODE_BYTES);
  const bool isR = j >= 14 && j < 30;
  const bool isG = j < 28;
  const int gk = j / 7, ct = j - gk * 7;
  const int tile = 4 * (grp >> 2) + gk, slice = 7 * (grp & 3) + ct;
  const int wdb = WS_WD + (ct * 56 + wave * 14) * 4096;
  const v4u sv = {SENT, SENT, SENT, SENT};
  int lb = L_B + l16;
  pin(lb);
  const char LAS* LB = L + lb - L_B;
  const int pfo = ((j * 4 + wave) * 14 + lane) * 128, pfc = ((j * 4 + wave) * 4 + lane) * 128;
  u32 pfa = ld1(tab, lane < 14 ? pfo : OOB), pfb = ld1(code, lane < 4 ? pfc : OOB);
  const int hro = WS_HRDY + (16 * tile + 4 * wave) * 32 + c16 * 8;
  v4u x2r[4];
  const int wl16 = (lane ^ ((wave & 1) << 1)) * 16;
  v4u wdn[14][4];
  u32 epoch, tag;

#define ACT_CLEAR(lo, hi) _Pragma("unroll") for (int m = lo; m < hi; ++m) \
      st4c(ws, isG ? WS_ACT + (16 * (tile + 2 * (m >> 3)) + 4 * (grp & 3)) * 57344 + ct * 32768 + (m & 7) * 4096 + tid * 16 : OOB, sv);
  if (isR) {
    const int blk = 16 * grp + (j - 14);
    epoch = ld1c(ws, WS_EPOCH);
    const int t0 = wave == 0 ? 0 : 2 * wave - 1;
    v4u cf[8][4];
#define CF_LOAD(q) { const int chn = TAB_P1 + ((wave == 0 && q >= 4) ? 28 : 4 * t0 + q), vo = (q < 5 || wave != 0) ? l16 : OOB; \
      _Pragma("unroll") for (int s = 0; s < 4; ++s) cf[q][s] = ld4(tab, vo + s * 1024, chn * 4096); sb(); }
#pragma unroll
    for (int q = 0; q < X_AFTER; ++q) CF_LOAD(q)
    v2u xv[7];
    {
      const rsrc_t xr = mkrs(xin, S * D * 2);
      const int off = tok_row(blk, 4 * wave + g) * (D * 2) + c16 * 8;
#pragma unroll
      for (int m = 0; m < 7; ++m) xv[m] = ld2(xr, off + m * 128);
    }
    typedef u32 v3u __attribute__((ext_vector_type(3)));
    v2u fa_[2], fb_[2]; v3u fc_[2], fd_[2];
    {
      const rsrc_t rr = mkrs(wr, S * DF * 2), ri = mkrs(wi, S * DF * 2);
      const int fo_ = 16 * wave + c16 < 57 ? 2 * g * 450 + (16 * wave + c16) * 8 : OOB;
#pragma unroll
      for (int hh = 0; hh < 2; ++hh) {
        const int so = blk * 7200 + hh * 3600;
        fa_[hh] = ld2(rr, fo_, so); fb_[hh] = ld2(ri, fo_, so);
        fc_[hh] = __builtin_amdgcn_raw_buffer_load_b96(rr, fo_ + 448, so, 0); fd_[hh] = __builtin_amdgcn_raw_buffer_load_b96(ri, fo_ + 448, so, 0);
      }
    }
    sb();
#pragma unroll
    for (int q = X_AFTER; q < 8; ++q) CF_LOAD(q)
    STAMP(23);
    v2u rdy;
    {
      v2f f[14], s2 = {0.f, 0.f};
#pragma unroll
      for (int m = 0; m < 7; ++m) {
        f[2 * m] = bun(xv[m].x); f[2 * m + 1] = bun(xv[m].y);
        s2 += f[2 * m] + f[2 * m + 1];
        *(LAS v2u*)(L + L_XS + (4 * wave + g) * XP + c16 * 8 + m * 128) = xv[m];
      }
      STAMP(21);
      sb();
      {
        const int q = 16 * wave + c16, o = WS_WT + q * 16384 + blk * 128 + g * 16;
#pragma unroll
        for (int hh = 0; hh < 2; ++hh) {
          const v2u a = fa_[hh], b = fb_[hh]; const v3u c = fc_[hh], d = fd_[hh];
          st4c(ws, q < 57 ? o + hh * 64 : OOB, v4u{__builtin_amdgcn_perm(b.x, a.x, 0x05040100u), __builtin_amdgcn_perm(b.y, a.y, 0x05040100u),
                                                   __builtin_amdgcn_perm(d.x, c.x, 0x07060302u), __builtin_amdgcn_perm(d.y, c.y, 0x07060302u)});
          st4c(ws, q < 56 ? o + hh * 64 + 57 * 16384 : OOB,
               v4u{__builtin_amdgcn_perm(b.x, a.x, 0x07060302u), __builtin_amdgcn_perm(b.y, a.y, 0x07060302u),
                   __builtin_amdgcn_perm(d.y, c.y, 0x05040100u), __builtin_amdgcn_perm(d.z, c.z, 0x05040100u)});
        }
      }
      rdy = ld2c(ws, WS_RDY + grp * 1024 + min(tid >> 1, 112) * 8);
      sb();
      const float mean = sum16(s2.x + s2.y) * (1.f / D);
      v2f q2 = {0.f, 0.f};
#pragma unroll
      for (int m = 0; m < 14; ++m) { const v2f d = f[m] - mean; q2 = fma2(d, d, q2); }
      const float rstd = __builtin_amdgcn_rsqf(sum16(q2.x + q2.y) * (1.f / D) + 1e-5f);
      if (c16 == 0) *(LAS v2f*)(L + L_ST + (4 * wave + g) * 8) = v2f{mean, rstd};
    }
    pin(pfa); pin(pfb);
    epoch = __builtin_amdgcn_readfirstlane(epoch);
    tag = mktag(epoch);
    STAMP(22);
    bar();
    v4f n[4][2];
    const int d0 = 8 * (4 * wave + g);
    const v2f st = *(const LAS v2f*)(L + L_ST + c16 * 8);
    {
      int xr_ = L_XS + c16 * XP + d0 * 2, xb_ = L_XS + c16 * XP + (216 - d0) * 2, x0_ = d0 == 0 ? xr_ : xb_ + 464;
      pin(xr_); pin(xb_); pin(x0_);
      const v4u a = lds4(L + xr_, 0), e = lds4(L + xr_, 448), c = lds4(L + xb_, 0), b = lds4(L + xb_, 448);
      const u32 c0 = *(const LAS u16*)(L + xb_ + 16), b0 = *(const LAS u16*)(L + x0_);
#pragma unroll
      for (int i = 0; i < 8; ++i) {
        n[0][i >> 2][i & 3] = (i & 1) ? bhi(a[i >> 1]) : blo(a[i >> 1]);
        n[3][i >> 2][i & 3] = (i & 1) ? bhi(e[i >> 1]) : blo(e[i >> 1]);
        n[1][i >> 2][i & 3] = i == 0 ? blo(b0) : (i & 1) ? bhi(b[(8 - i) >> 1]) : blo(b[(8 - i) >> 1]);
        n[2][i >> 2][i & 3] = i == 0 ? blo(c0) : (i & 1) ? bhi(c[(8 - i) >> 1]) : blo(c[(8 - i) >> 1]);
      }
      const float nm = -st.x * st.y;
#pragma unroll
      for (int m = 0; m < 4; ++m) {
#pragma unroll
        for (int h = 0; h < 2; ++h) { n[m][h] = fma4(n[m][h], splat(st.y), splat(nm)); pin(n[m][h]); }
      }
    }
    sb();
    STAMP(1);
    int mb = L_M1 + d0 * 4;
    pin(mb);
    v4u xs[4];
    int to[4];
    to[0] = WS_T1 + blk * T1_BLK + (16 * t0 + c16) * 64 + g * 16;
    to[1] = wave != 0 ? to[0] + 1024 : c16 < 2 ? to[0] + 112 * 64 : OOB;
    to[2] = to[0] + 114 * 64;
    to[3] = wave != 0 ? to[0] + 1024 + 114 * 64 : OOB;
#pragma unroll
    for (int q = 0; q < 4; ++q) pin(to[q]);

    {
      const int kind = tid >= 112, d = 4 * (tid - 112 * kind);
      int wb = L_M1 + (kind ? 0 : 2 * 1792) + d * 4, wrv = L_M1 + (kind ? 0 : 2 * 1792) + 1792 + (445 - d) * 4;
      int wr0 = d == 0 ? wrv + 12 - 1792 : wrv + 12;
      const int mo = tid < 224 ? WS_MOD + grp * MOD_REP + tid * 16 : OOB;
      pin(wb); pin(wrv); pin(wr0);
      if (!wall(rdy.x == tag && rdy.y == ~tag)) wait_flags(ws, WS_RDY + grp * 1024 + min(tid >> 1, 112) * 8, tag);
      v4u mv;
      _Pragma("nounroll") for (int s = 0; s < SPIN; ++s) {
        mv = ld4c(ws, fresh(mo));
        if (wall(tid >= 224 || (mv.y == tag && mv.w == tag))) break;
        __builtin_amdgcn_s_sleep(SL1);
      }
      STAMP(2);
      if (tid < 224) {
        const v4f v = {blo(mv.x), bhi(mv.x), blo(mv.z), bhi(mv.z)};
        *(LAS v4f*)(L + wb) = v;
        *(LAS float*)(L + wr0) = v.x;
        *(LAS float*)(L + wrv + 8) = v.y;
        *(LAS float*)(L + wrv + 4) = v.z;
        *(LAS float*)(L + wrv) = v.w;
      }
    }
    bar();
    {
      v4u fr[4];
#pragma unroll
      for (int hh = 0; hh < 2; ++hh) {
        v4f h[4];
#pragma unroll
        for (int m = 0; m < 4; ++m) {
          const int o = ((m == 1 || m == 2) ? 1792 : 0) + (m >= 2 ? 896 : 0) + hh * 16;
          h[m] = fma4(n[m][hh], lds4f(L + mb, o), lds4f(L + mb, 2 * 1792 + o));
        }
        const v4f p = h[0] + h[1], q = h[2] + h[3], u = h[0] - h[1], v = h[3] - h[2];
        const v4f t[4] = {p + q, p - q, u + v, u - v};
#pragma unroll
        for (int m = 0; m < 4; ++m) { fr[m][2 * hh] = pk2(lo2(t[m])); fr[m][2 * hh + 1] = pk2(hi2(t[m])); }
      }
#pragma unroll
      for (int m = 0; m < 4; ++m) *(LAS v4u*)(LB + wave * 1024 + L_AF + m * 4096) = fr[m];
    }
    bar();
    {
      v4u a[4][4];
#pragma unroll
      for (int p = 0; p < 2; ++p)
#pragma unroll
        for (int s = 0; s < 4; ++s) { a[p][s] = lds4(LB, L_AF + (p * 4 + s) * 1024); a[2 + p][s] = lds4(LB, L_AF + ((2 + p) * 4 + s) * 1024); }
      v4f re[4], im[4];
#pragma unroll
      for (int q = 0; q < 5; ++q) {
        if (q < 4) mf8(re[q], im[q], a[q >> 1], cf[4 * (q & 1) + (q >> 1)], a[2 + (q >> 1)], cf[4 * (q & 1) + 2 + (q >> 1)]);
        if (q > 0) {
          if (q < 4) mfence<0>(re[q - 1], im[q - 1]); else mfence<7>(re[3], im[3]);
          xs[q - 1] = v4u{pk(re[q - 1].x, im[q - 1].x), pk(re[q - 1].y, im[q - 1].y), pk(re[q - 1].z, im[q - 1].z), pk(re[q - 1].w, im[q - 1].w)};
          st4c(ws, to[q - 1], xs[q - 1]);
          sb();
        }
      }
    }
    STAMP(3);
    sb();
    sb();
    if (isG) dma_w1(wg, wu, slice, wave, lane, lbase);
#pragma unroll
    for (int q = 0; q < 4; ++q) {
      const int vo = (q < 2 || wave != 0) ? l16 : OOB;
#pragma unroll
      for (int s = 0; s < 4; ++s) cf[2 * q + 1][s] = ld4(tab, vo + s * 1024, (TAB_P3 + 2 * t0 + q) * 4096);
    }
#pragma unroll
    for (int m = 0; m < 4; ++m) {
      st4c(ws, tid + 256 * m < 896 ? WS_H2 + blk * 14336 + 4096 * m + tid * 16 : OOB, sv);
    }
    sb();
    STAMP(20);
    v4f xo[2][4];
    int xa = L_XS + c16 * XP + (16 * t0 + 4 * g) * 2, xd = L_XS + c16 * XP + (192 - 16 * t0 - 4 * g) * 2;
    int xs1 = tid < 16 ? L_XS + c16 * XP + 224 : xd + 512, xs3 = tid < 16 ? L_XS + c16 * XP + 672 : xa + 448;
    pin(xa); pin(xd); pin(xs1); pin(xs3);
#define XOFF(t, m, r) ((t) == 0 && (r) == 0 && (m) == 1 ? xs1 : (t) == 0 && (r) == 0 && (m) == 3 ? xs3 : \
    (m) == 0 ? xa + 32 * (t) + 2 * (r) : (m) == 3 ? xa + 448 + 32 * (t) + 2 * (r) : (m) == 1 ? xd + 512 - 32 * (t) - 2 * (r) : xd + 64 - 32 * (t) - 2 * (r))
    {
      v2u w[2][4]; u32 e[2][2], e3;
#pragma unroll
      for (int t = 0; t < 2; ++t) {
        w[t][0] = *(const LAS v2u*)(L + xa + 32 * t); w[t][3] = *(const LAS v2u*)(L + xa + 448 + 32 * t);
        w[t][1] = *(const LAS v2u*)(L + xd + 504 - 32 * t); w[t][2] = *(const LAS v2u*)(L + xd + 56 - 32 * t);
        e[t][0] = *(const LAS u16*)(L + XOFF(t, 1, 0)); e[t][1] = *(const LAS u16*)(L + XOFF(t, 2, 0));
      }
      e3 = *(const LAS u16*)(L + xs3);
      sb();
#pragma unroll
      for (int t = 0; t < 2; ++t) {
        xo[t][0] = v4f{blo(w[t][0].x), bhi(w[t][0].x), blo(w[t][0].y), bhi(w[t][0].y)};
        xo[t][3] = v4f{t == 0 ? blo(e3) : blo(w[t][3].x), bhi(w[t][3].x), blo(w[t][3].y), bhi(w[t][3].y)};
        xo[t][1] = v4f{blo(e[t][0]), bhi(w[t][1].y), blo(w[t][1].y), bhi(w[t][1].x)};
        xo[t][2] = v4f{blo(e[t][1]), bhi(w[t][2].y), blo(w[t][2].y), bhi(w[t][2].x)};
#pragma unroll
        for (int m = 0; m < 4; ++m) { xo[t][m] -= st.x; pin(xo[t][m]); }
      }
    }
    int m0_[2], m1_[2], md_[2];
#pragma unroll
    for (int r = 0; r < 2; ++r) {
      const int q = r == 0 || tid < 80 ? tid + 256 * r : tid;
      const int kind = (q * 586) >> 16, d = 4 * (q - 112 * kind);
      const int reg = (d >= 112) + (d >= 224) + (d >= 336), cc = ((reg + 1) >> 1) * 224, m1 = (0x1320 >> (4 * reg)) & 3;
      const int d1 = abs(d + 1 - cc), d0_ = abs(d - cc), edge = d == 112 * reg && reg > 0;
      m1_[r] = L_M2 + kind * 1792 + (((d1 >> 4) << 6) | (m1 << 4) | (d1 & 15)) * 4;
      m0_[r] = L_M2 + kind * 1792 + (edge ? reg << 4 : ((d0_ >> 4) << 6) | (m1 << 4) | (d0_ & 15)) * 4;
      md_[r] = (reg & 1) ? -4 : 4;
      pin(m0_[r]); pin(m1_[r]); pin(md_[r]);
    }
    {
      v4u mv[2];
      _Pragma("nounroll") for (int s = 0; s < SPIN; ++s) {
        mv[0] = ld4c(ws, fresh(WS_MOD + grp * MOD_REP + (224 + tid) * 16));
        mv[1] = ld4c(ws, fresh(WS_MOD + grp * MOD_REP + (224 + (tid < 80 ? 256 + tid : tid)) * 16));
        const v2u rv = ld2c(ws, fresh(WS_DRDY + grp * 1024 + min(lane, 48) * 8));
        if (wall(mv[0].y == tag && mv[0].w == tag && mv[1].y == tag && mv[1].w == tag && rv.x == tag && rv.y == ~tag)) break;
        __builtin_amdgcn_s_sleep(SL4);
      }
      if (lane < (CLR_MODE == 1 ? 2 : 1)) st2c(ws, lane == 0 ? WS_HRDY + blk * 32 + wave * 8 : isG ? WS_CRDY + (tile * 4 + (grp & 3)) * 256 + (ct * 4 + wave) * 8 : OOB, v2u{tag, ~tag});
      STAMP(12);
#pragma unroll
      for (int f = 0; f < WD_EARLY; ++f) wdn[f >> 2][f & 3] = ld4c(ws, (isG ? wl16 : OOB) + (f & 3) * 1024, wdb + (f >> 2) * 4096);
      sb();
#pragma unroll
      for (int r = 0; r < 2; ++r) {
        const float sc = m0_[r] < L_M2 + 1792 ? 2.f / (float)(S * D) : 1.f;
        *(LAS float*)(L + m0_[r]) = blo(mv[r].x) * sc;
        *(LAS float*)(L + m1_[r]) = bhi(mv[r].x) * sc;
        *(LAS float*)(L + m1_[r] + md_[r]) = blo(mv[r].z) * sc;
        *(LAS float*)(L + m1_[r] + 2 * md_[r]) = bhi(mv[r].z) * sc;
      }
    }
    bar();
    *(LAS v4u*)(L + L_AF + (tid >> 6) * 4096 + 3072 + l16) = v4u{0u, 0u, 0u, 0u};
    STAMP(4);
    int zr = L_AF + lane * 8, ro = L_M2 + (t0 * 64 + 4 * g) * 4, ro1 = wave == 0 ? ro : ro + 256, ex = L_EX + c16 * 8;
    pin(zr); pin(ro); pin(ro1); pin(ex);
    {
      int zl[4];
#pragma unroll
      for (int m = 0; m < 4; ++m) {
        const int kq = (m >> 1) * 128 + (to[m] == OOB ? 127 : wave == 0 && m == 1 ? 112 + c16 : 16 * (t0 + (m & 1)) + c16);
        zl[m] = L_AF + ((kq & ~31) + ((kq & 4) << 2) + ((kq & 24) >> 1) + (kq & 3)) * 32 + g * 8;
        pin(zl[m]);
        if (to[m] == OOB) xs[m] = sv;
      }
      bar();
      if (isG) wait_flags(ws, hro, tag);
      {
        const int src = (blk * 5 + wave * 13) & 63;
        const int po = __builtin_amdgcn_readlane(to[(0)], 0) + (src & 15) * 64 + (src >> 4) * 16;
        const u32 old = __builtin_amdgcn_ds_bpermute(src * 4, xs[0].x);
        _Pragma("nounroll") for (int s = 0; s < SPIN; ++s) {
          if (wall(ld1c(ws, fresh(po)) != old)) break;
          __builtin_amdgcn_s_sleep(SL2);
        }
      }
      STAMP(13);
      v4u z[4];
      _Pragma("nounroll") for (int s = 0; s < SPIN; ++s) {
        u32 mn = 1;
#pragma unroll
        for (int m = 0; m < 4; ++m) z[m] = ld4c(ws, fresh(to[m]));
#pragma unroll
        for (int m = 0; m < 4; ++m) { const v4u d = z[m] ^ xs[m]; mn = min(min(mn, d.x), min(min(d.y, d.z), d.w)); }
        if (wall(mn != 0)) break;
      }
      STAMP(14);
#pragma unroll
      for (int m = 0; m < 4; ++m) {
        char LAS* p = L + zl[m];
        *(LAS v2u*)(p) = v2u{__builtin_amdgcn_perm(z[m].y, z[m].x, 0x05040100u), __builtin_amdgcn_perm(z[m].w, z[m].z, 0x05040100u)};
        *(LAS v2u*)(p + ZPL) = v2u{__builtin_amdgcn_perm(z[m].y, z[m].x, 0x07060302u), __builtin_amdgcn_perm(z[m].w, z[m].z, 0x07060302u)};
      }
    }
    bar();
    STAMP(5);
    {
      v4u zf[4][4];
#pragma unroll
      for (int ty = 0; ty < 4; ++ty)
#pragma unroll
        for (int s = 0; s < 4; ++s) zf[ty][s] = ldtr2(L + zr, ty * 4096 + s * 1024, ty * 4096 + s * 1024 + 512);
      v4f acc[2][4], x2[2][4], gav[2][4];
      v2f s1 = {0.f, 0.f}, s2 = {0.f, 0.f};
#define P3M(t, ty) mf8(acc[t][ty], acc[t][ty + 1], cf[4 * t + ty], zf[ty], cf[4 * t + ty + 1], zf[ty + 1]);
#define P3X(t, m, yv) { x2[t][m] = fma4(gav[t][m], yv, xo[t][m]); \
      if (t == 0 || wave != 0) { s1 += lo2(x2[t][m]) + hi2(x2[t][m]); \
        s2 = fma2(lo2(x2[t][m]), lo2(x2[t][m]), s2); s2 = fma2(hi2(x2[t][m]), hi2(x2[t][m]), s2); } }
      P3M(1, 0) P3M(1, 2)
#pragma unroll
      for (int m = 0; m < 4; ++m) { gav[0][m] = lds4f(L + ro, m * 64); gav[1][m] = lds4f(L + ro1, m * 64); }
      sb();
      P3M(0, 0) P3M(0, 2)
      mfence<0>(acc[1][0], acc[1][1], acc[1][2], acc[1][3]);
      const v4f A1 = acc[1][0] + acc[1][1], B1 = acc[1][0] - acc[1][1], P1 = acc[1][3] - acc[1][2], Q1 = acc[1][2] + acc[1][3];
      P3X(1, 0, A1 - P1) P3X(1, 1, A1 + P1) P3X(1, 2, B1 - Q1) P3X(1, 3, B1 + Q1)
      sb();
      mfence<0>(acc[0][0], acc[0][1], acc[0][2], acc[0][3]);
      {
        const v4f A = acc[0][0] + acc[0][1], B = acc[0][0] - acc[0][1], P = acc[0][3] - acc[0][2], Q = acc[0][2] + acc[0][3];
        v4f y[4] = {A - P, A + P, B - Q, B + Q};
        const bool sp = tid < 16;
        y[0].x = sp ? A.x : y[0].x; y[2].x = sp ? B.x : y[2].x;
        y[1].x = sp ? acc[1][0].x - acc[0][3].x : y[1].x; y[3].x = sp ? acc[1][0].x + acc[0][3].x : y[3].x;
        P3X(0, 0, y[0]) P3X(0, 1, y[1]) P3X(0, 2, y[2]) P3X(0, 3, y[3])
      }
      float sm = s1.x + s1.y, sq = s2.x + s2.y;
      sumg2(sm, sq);
      if (g == 0) *(LAS v2f*)(L + ex + wave * 128) = v2f{sm, sq};
      v4f sbv[2][4], cbv[2][4];
#pragma unroll
      for (int m = 0; m < 4; ++m) {
        sbv[0][m] = lds4f(L + ro, 1792 + m * 64); cbv[0][m] = lds4f(L + ro, 2 * 1792 + m * 64);
        sbv[1][m] = lds4f(L + ro1, 1792 + m * 64); cbv[1][m] = lds4f(L + ro1, 2 * 1792 + m * 64);
      }
      bar();
      v2f e = *(const LAS v2f*)(L + ex);
#pragma unroll
      for (int w = 1; w < 4; ++w) e += *(const LAS v2f*)(L + ex + w * 128);
      const float mc = e.x * (1.f / D), rstd = __builtin_amdgcn_rsqf(fmaxf(e.y * (1.f / D) - mc * mc, 0.f) + 1e-5f), nb = -mc * rstd;
      const int ho = WS_H2 + blk * 14336 + 2 * t0 * 1024 + l16, ho1 = wave == 0 ? OOB : ho + 2048;
#pragma unroll
      for (int t = 0; t < 2; ++t)
#pragma unroll
        for (int a = 0; a < 2; ++a) {
          v4u o;
#pragma unroll
          for (int hh = 0; hh < 2; ++hh) {
            const int m = 2 * a + hh;
            const v4f hv = fma4(fma4(x2[t][m], splat(rstd), splat(nb)), cbv[t][m], sbv[t][m]);
            o[2 * hh] = pk2(lo2(hv)); o[2 * hh + 1] = pk2(hi2(hv));
          }
          st4c(ws, (t ? ho1 : ho) + a * 1024, o);
        }
      STAMP(6);
      sb();
#pragma unroll
      for (int f = WD_EARLY; f < WD_MID; ++f) wdn[f >> 2][f & 3] = ld4c(ws, (isG ? wl16 : OOB) + (f & 3) * 1024, wdb + (f >> 2) * 4096);
      sb();
#pragma unroll
      for (int e = 0; e < 8; ++e) {
        x2r[e >> 1][2 * (e & 1)] = pk2(lo2(x2[e >> 2][e & 3]) + st.x); x2r[e >> 1][2 * (e & 1) + 1] = pk2(hi2(x2[e >> 2][e & 3]) + st.x);
      }
#pragma unroll
      for (int m = 0; m < 4; ++m) pin(x2r[m]);
    }
    bar();
    if (!isG) {
      x2_store(ws, x2r, blk, wave, l16);
      if (blockIdx.x == 224 && tid == 0) __builtin_amdgcn_raw_buffer_store_b32(epoch + 1u, ws, WS_EPOCH, 0, SC1);
      drain();
      if (lane == 0) st2c(ws, WS_XRDY + blk * 64 + wave * 8, v2u{tag, ~tag});
      DUMP(); return;
    }
  } else {
    const int sj = j >= 30 ? j - 30 : j + 2;
    const int idx = sj * 8 + grp;
    const bool isC = j < 14 || idx == 0;
    const int pair = j < 14 ? grp * 14 + j : 112;
    const int lc = grp + 8 * (sj >> 1);
    const int qu = sj < 4 && lc < 14 ? 2 * lc + (sj & 1) : -1;
    const int t1o = WS_T1 + (32 * wave + (lane >> 3)) * T1_BLK + pair * 128 + (lane & 7) * 16;
    ModLoad ml;
    epoch = ld1c(ws, WS_EPOCH);
#pragma unroll
    for (int q = 0; q < 4; ++q) st4c(ws, isC ? t1o + 8 * q * T1_BLK : OOB, sv);
    sb();
    mod_load(ml, cin, wm, qu, tid);
    u32 pf0, pf1, pf2;
    {
      const rsrc_t mr = mkrs(wm, D * 6 * D * 2), xr = mkrs(xin, S * D * 2), fr_ = mkrs((wave & 1) ? wi : wr, S * DF * 2);
      const int row = (sj * 4 + wave) * 7 + (lane >> 1), l7 = lane / 7;
      pf0 = __builtin_amdgcn_raw_buffer_load_b32(mr, lane < 14 && (grp < 6 || !(lane & 1)) ? row * (6 * D * 2) + (grp + 8 * (lane & 1)) * 128 : OOB, 0, PF_AUX);
      pf1 = __builtin_amdgcn_raw_buffer_load_b32(xr, lane < (!XBAL ? 28 : sj < 4 ? 0 : sj < 12 ? 28 : 56) ? tok_row(16 * grp + (lane < 28 ? sj : sj - 12), 4 * wave + (l7 & 3)) * (D * 2) + (lane - 7 * l7) * 128 : OOB, 0, PF_AUX);
      pf2 = __builtin_amdgcn_raw_buffer_load_b32(fr_, (lane & 31) < 29 && lane < (!XBAL ? 32 : sj < 4 ? 0 : sj < 12 ? 32 : 64) ? (((16 * grp + (lane < 32 ? sj : sj - 12)) * 7200) & ~127) + ((wave >> 1) * 29 + (lane & 31)) * 128 : OOB, 0, PF_AUX);
    }
    sb();
    asm volatile("s_waitcnt vmcnt(%0)" :: "i"(W_LATE ? 16 : 17) : "memory");
    epoch = __builtin_amdgcn_readfirstlane(epoch);
    tag = mktag(epoch);
    bar();
    if (wave == 1 && lane < 8) st2c(ws, isC ? WS_RDY + lane * 1024 + pair * 8 : OOB, v2u{tag, ~tag});
    if (qu >= 0) {
      mod_part(ml, L, tid);
      bar();
      if (wave == 0) mod_publish(L, ws, qu, lane, tag);
    }
    STAMP(1);
    pin(pf0); pin(pf1); pin(pf2); pin(pfa); pin(pfb);
    const int qn = sj >= 4 && sj < 10 && grp < 7 ? 28 + grp * 6 + (sj - 4) : !isC && idx < 15 ? 69 + idx : -1;
    const int tw = sj >= 10 ? (sj - 10) * 8 + grp : sj == 4 && grp == 7 ? 48 : -1;
    v2u dw[2][8];
    if (qn >= 0) mod_load(ml, cin, wm, qn, tid);
    if (tw >= 0) {
      const rsrc_t dr = mkrs(wd, F * D * 2);
      const int u0 = tw * 4 + wave, uc = u0 / 28, kap = 2 * (u0 - uc * 28);
#pragma unroll
      for (int e = 0; e < 16; ++e)
        dw[e >> 3][e & 7] = __builtin_amdgcn_raw_buffer_load_b64(dr, (4 * g * D + 4 * c16) * 2 + (e & 3) * D * 2, ((32 * kap + 16 * (e >> 2)) * D + 64 * uc) * 2, PF_AUX);
    }
    sb();
    u32 pw[4];
    {
      const rsrc_t rg = mkrs(wg, D * F * 2), ru = mkrs(wu, D * F * 2);
      const int r7 = lane / 7, r0 = W1EVEN ? (j * 4 + wave) * 8 : sj < 4 ? (sj * 4 + wave) * 16 : 256 + ((sj - 4) * 4 + wave) * 4;
      const int po = (r0 + r7) * (F * 2) + ((grp & 3) * 7 + lane - r7 * 7) * 128;
      const int o0 = lane < (W1EVEN ? (j < 14 ? 56 : 0) : sj < 4 ? 56 : 28) ? po : OOB, o1 = !W1EVEN && lane < 56 && sj < 4 ? po + 8 * (F * 2) : OOB;
      pw[0] = ld1(rg, o0); pw[1] = ld1(ru, o0); pw[2] = ld1(rg, o1); pw[3] = ld1(ru, o1);
    }
    sb();
    STAMP(20);
    v4f t1c[2], t1s[2], t2c[2], t2s[2];
#pragma unroll
    for (int t = 0; t < 2; ++t) {
#pragma unroll
      for (int r = 0; r < 4; ++r) {
        const float a1 = (float)((16 * t + 4 * g + r) * (16 * wave + c16)) * (1.f / S);
        const float a2 = (float)((16 * t + c16) * (16 * wave + 4 * g + r)) * (1.f / S);
        t1c[t][r] = __builtin_amdgcn_cosf(a1); t1s[t][r] = __builtin_amdgcn_sinf(a1);
        t2c[t][r] = __builtin_amdgcn_cosf(a2); t2s[t][r] = __builtin_amdgcn_sinf(a2);
      }
      pin(t1c[t]); pin(t1s[t]); pin(t2c[t]); pin(t2s[t]);
    }
    if (tw >= 0) {
#pragma unroll
      for (int tt = 0; tt < 8; ++tt) {
        v4u o;
#pragma unroll
        for (int m = 0; m < 4; ++m)
          o[m] = __builtin_amdgcn_perm(dw[tt >> 2][2 * m + 1][(tt & 3) >> 1], dw[tt >> 2][2 * m][(tt & 3) >> 1], (tt & 1) ? 0x07060302u : 0x05040100u);
        st4c(ws, WS_WD + (tw * 4 + wave) * 8192 + tt * 1024 + l16, o);
      }
    }
    if (qn >= 0) {
      bar();
      mod_part(ml, L, tid);
      bar();
      if (wave == 0) mod_publish(L, ws, qn, lane, tag);
    }
    pin(pw[0]); pin(pw[1]); pin(pw[2]); pin(pw[3]);
    STAMP(21);
    if (!isC) { DUMP(); return; }
    v4u c64[2][4], c32[2][2][2];
#pragma unroll
    for (int cp = 0; cp < 2; ++cp)
#pragma unroll
      for (int s = 0; s < 4; ++s) c64[cp][s] = ld4(tab, l16 + s * 1024, (TAB_P2 + wave * 2 + cp) * 4096);
#pragma unroll
    for (int t = 0; t < 2; ++t)
#pragma unroll
      for (int f = 0; f < 4; ++f) c32[t][f >> 1][f & 1] = ld4(tab, l16 + f * 1024, (TAB_P2 + 8 + t) * 4096);
    sb();
    if (tw >= 0) {
      asm volatile("s_waitcnt vmcnt(16)" ::: "memory");
      bar();
      if (tid < 8) st2c(ws, WS_DRDY + tid * 1024 + tw * 8, v2u{tag, ~tag});
    }
    STAMP(2);
    int wfo = WS_WT + pair * 16384 + (16 * g + wave) * 128 + c16 * 8;
    pin(wfo);
    v2u wf[2][4];
    {
      int wl = L_CI + ((lane >> 2) & 1) * 8256 + wave * 2048 + ((lane >> 3) * 4 + (lane & 3)) * 16, lo = t1o;
      pin(wl); pin(lo);
      light_poll(ws, WS_T1 + (32 * wave + (pair & 31)) * T1_BLK + pair * 128);
      STAMP(13);
      v4u z[4];
      _Pragma("nounroll") for (int s = 0; s < SPIN; ++s) {
        lo = fresh(lo);
#pragma unroll
        for (int q = 0; q < 4; ++q) z[q] = ld4c(ws, lo, 8 * q * T1_BLK);
        if (wall(max(max(max4(z[0]), max4(z[1])), max(max4(z[2]), max4(z[3]))) != SENT)) break;
      }
      STAMP(14);
#pragma unroll
      for (int q = 0; q < 4; ++q) *(LAS v4u*)(L + wl + q * 512) = z[q];
    }
    bar();
    v2u fd;
    sb();
    STAMP(4);
    {
      v4u a[2][2][4];
#pragma unroll
      for (int c = 0; c < 2; ++c)
#pragma unroll
        for (int mt = 0; mt < 2; ++mt)
#pragma unroll
          for (int s = 0; s < 4; ++s) a[c][mt][s] = lds4(LB, L_CI + c * 8256 + (s * 2 + mt) * 1024);
      v4u old[2][2];
#pragma unroll
      for (int c = 0; c < 2; ++c)
#pragma unroll
        for (int mt = 0; mt < 2; ++mt) old[c][mt] = lds4(LB + wave * 2048, L_CI + c * 8256 + mt * 1024);
      sb();
#pragma unroll
    for (int t = 0; t < 2; ++t)
#pragma unroll
      for (int r = 0; r < 4; ++r) wf[t][r] = ld2c(ws, wfo + r * 512, t * 8192);
    fd = ld2c(ws, WS_DRDY + grp * 1024 + min(lane, 48) * 8);
      sb();
      v4f re[2][2], im[2][2], ur[2][2], ui[2][2], fr_[2][2], fi_[2][2];
      u32 f2[2][2][4], f3[2][2][4], f4[4];
      char LAS* xw = L + lb + wave * 1024 - L_B;
#define V4(x) v4u{x[0], x[1], x[2], x[3]}
#define S1K(h) c64[0][2 * h], c64[0][2 * h + 1], c64[1][2 * h], c64[1][2 * h + 1]
#define S1X(c, t, h) re[c][t], im[c][t], a[c][t][2 * h], a[c][t][2 * h + 1], S1K(h)
#define S2X(c, t) ur[c][t], ui[c][t], V4(f2[c][0]), V4(f2[c][1]), c32[t][0][0], c32[t][0][1], c32[t][1][0], c32[t][1][1]
#define S3X(c, t) im[c][t], re[c][t], V4(f3[c][0]), V4(f3[c][1]), c32[t][0][0], c32[t][0][1], c32[t][1][0], c32[t][1][1]
#define CM(o, P, Q, C, S_, h) o[2 * h], o[2 * h + 1], P[2 * h], Q[2 * h], C[2 * h], S_[2 * h], P[2 * h + 1], Q[2 * h + 1], C[2 * h + 1], S_[2 * h + 1]
#define TW1(c, t, h) CM(f2[c][t], re[c][t], im[c][t], t1c[t], t1s[t], h)
#define FLT(c, t, h) CM(f3[c][t], ur[c][t], ui[c][t], fr_[c][t], fi_[c][t], h)
#define TW2(c, t, h) CM(f4, re[c][t], im[c][t], t2c[t], t2s[t], h)
#define HV(x, h) (h ? hi2(x) : lo2(x))
#define CP(PL, o, P, Q, C, S_, h) cmul2<PL>(o[2 * h], o[2 * h + 1], HV(P, h), HV(Q, h), HV(C, h), HV(S_, h));
#define XW(c, t) *(LAS v4u*)(xw + L_CX + (c * 2 + t) * 4096) = V4(f4);
#define UNP(c, t) { _Pragma("unroll") for (int r = 0; r < 4; ++r) { fr_[c][t][r] = blo(wf[t][r][c]); fi_[c][t][r] = bhi(wf[t][r][c]); } \
      pin(fr_[c][t]); pin(fi_[c][t]); }
      nop2();
      m4_a0(S1X(0, 0, 0)); m4_a1(S1X(0, 0, 1)); m4_a0(S1X(0, 1, 0)); mfence<3>(re[0][0], im[0][0]);
      h4_a1m(S1X(0, 1, 1), TW1(0, 0, 0)); h4_a0m(S1X(1, 0, 0), TW1(0, 0, 1));
      h4_a1m(S1X(1, 0, 1), TW1(0, 1, 0)); h4_a0m(S1X(1, 1, 0), TW1(0, 1, 1));
      h4_a1m(S1X(1, 1, 1), TW1(1, 0, 0)); h4_b0m(S2X(0, 0), TW1(1, 0, 1));
      h4_b0m(S2X(0, 1), TW1(1, 1, 0));
      sb();
      CP(false, f2[1][1], re[1][1], im[1][1], t1c[1], t1s[1], 1)
      UNP(0, 0) UNP(0, 1)
      sb();
      h4_b0p(S2X(1, 0), FLT(0, 0, 0)); h4_b0p(S2X(1, 1), FLT(0, 0, 1));
      sb();
      CP(true, f3[0][1], ur[0][1], ui[0][1], fr_[0][1], fi_[0][1], 0) CP(true, f3[0][1], ur[0][1], ui[0][1], fr_[0][1], fi_[0][1], 1)
      UNP(1, 0) UNP(1, 1)
      sb();
      h4_a0p(S3X(0, 0), FLT(1, 0, 0)); h4_a0p(S3X(0, 1), FLT(1, 0, 1));
      sb();
      CP(true, f3[1][1], ur[1][1], ui[1][1], fr_[1][1], fi_[1][1], 0) CP(true, f3[1][1], ur[1][1], ui[1][1], fr_[1][1], fi_[1][1], 1)
#pragma unroll
      for (int e = 0; e < 4; ++e) pin(f3[1][1][e]);
      nop2();
      h4_a0p(S3X(1, 0), TW2(0, 0, 0)); h4_a0p(S3X(1, 1), TW2(0, 0, 1));
      XW(0, 0)
      sb();
      CP(true, f4, re[0][1], im[0][1], t2c[1], t2s[1], 0) CP(true, f4, re[0][1], im[0][1], t2c[1], t2s[1], 1) XW(0, 1)
      sb();
      mfence<0>(re[1][0], im[1][0], re[1][1], im[1][1]);
      CP(true, f4, re[1][0], im[1][0], t2c[0], t2s[0], 0) CP(true, f4, re[1][0], im[1][0], t2c[0], t2s[0], 1) XW(1, 0)
      CP(true, f4, re[1][1], im[1][1], t2c[1], t2s[1], 0) CP(true, f4, re[1][1], im[1][1], t2c[1], t2s[1], 1) XW(1, 1)
      bar();
#pragma unroll
      for (int c = 0; c < 2; ++c)
#pragma unroll
        for (int nt = 0; nt < 2; ++nt)
#pragma unroll
          for (int s = 0; s < 4; ++s) a[c][nt][s] = lds4(LB, L_CX + ((c * 2 + nt) * 4 + s) * 1024);
      const int o2 = WS_T1 + (32 * wave + (lane >> 2)) * T1_BLK + pair * 128 + (lane & 3) * 16;
#define S4(c, t) mf8(im[c][t], re[c][t], c64[0], a[c][t], c64[1], a[c][t]);
#define NEQ(x, y) ((x) + ((x) == (y)))
#define OUT(c, t) { const v4u o = {pk(re[c][t].x, im[c][t].x), pk(re[c][t].y, im[c][t].y), pk(re[c][t].z, im[c][t].z), pk(re[c][t].w, im[c][t].w)}; \
      st4c(ws, o2 + c * 64 + t * 16 * T1_BLK, v4u{NEQ(o.x, old[c][t].x), NEQ(o.y, old[c][t].y), NEQ(o.z, old[c][t].z), NEQ(o.w, old[c][t].w)}); sb(); }
      S4(0, 0) S4(0, 1) mfence<0>(re[0][0], im[0][0]); OUT(0, 0)
      S4(1, 0) mfence<0>(re[0][1], im[0][1]); OUT(0, 1)
      S4(1, 1) mfence<0>(re[1][0], im[1][0]); OUT(1, 0)
      mfence<0>(re[1][1], im[1][1]); OUT(1, 1)
    }
    STAMP(5);
    sb();
    if (!isG) { DUMP(); return; }
    if (!wall(fd.x == tag && fd.y == ~tag)) wait_flags(ws, WS_DRDY + grp * 1024 + min(lane, 48) * 8, tag);
    dma_w1(wg, wu, slice, wave, lane, lbase);
#pragma unroll
    for (int f = 0; f < WD_MID; ++f) wdn[f >> 2][f & 3] = ld4c(ws, wl16 + (f & 3) * 1024, wdb + (f >> 2) * 4096);
    fd = ld2c(ws, hro);
    sb();
    ACT_CLEAR(0, 16)
    sb();
    if (!wall(fd.x == tag && fd.y == ~tag)) wait_flags(ws, hro, tag);
    bar();
  }

  STAMP(7);
  const int blk0 = 16 * tile + 4 * (grp & 3);
  const int ab = WS_ACT + blk0 * 57344 + wave * 14 * 1024;
  v4u h[14][2];
  {
    const int hb = WS_H2 + (16 * tile + 4 * wave) * 14336;
    int tb[2][4];
#pragma unroll
    for (int u = 0; u < 8; ++u) {
      tb[u >> 2][u & 3] = (((lane >> 2) * 128 + ((lane >> 3) & 3) * 32 + (lane & 3) * 8) ^ ((u & 3) << 5)) + (u >> 2) * 57344;
      pin(tb[u >> 2][u & 3]);
    }
#define W_RD(dst, k, u) dst = ldtr2(L + tb[k >= 7][u & 3], (k % 7) * 8192 + (u >> 2) * 4096, (k % 7) * 8192 + (u >> 2) * 4096 + 2048)
#define H_LD(v, tt) hh_[v][tt] = ld4c(ws, l16 + (((v) % 14) & 3) * 1024, hb + (2 * ((v) / 14) + (tt)) * 14336 + (((v) % 14) >> 2) * 4096)
    const int abx[2] = {ab + (wave >> 1) * 57344, ab + (1 - (wave >> 1)) * 57344};
#define G_LD(e) h[(e) >> 1][(e) & 1] = ld4c(ws, l16 + (((e) >> 1) & 3) * 1024, abx[(e) & 1] + ((e) >> 3) * 4096)
    v4u wfr[2][8], hh_[28][2];
#pragma unroll
    for (int u = 0; u < 8; ++u) W_RD(wfr[0][u], 0, u);
    light_poll(ws, hb + (lane & 1) * 14336 + 13 * 1024);
    STAMP(15);
#pragma unroll
    for (int v = 0; v < LEAD; ++v) { H_LD(v, 0); H_LD(v, 1); }
    sb();
    if (!isR && lane < (CLR_MODE == 1 ? 1 : 2))
      st2c(ws, WS_CRDY + ((tile + 2 * lane) * 4 + (grp & 3)) * 256 + (ct * 4 + wave) * 8, v2u{tag, ~tag});
    sb();
#define BAD2(x0, x1) wany(max(max(max(max(x0.x, x0.y), x0.z), max(max(x0.w, x1.x), x1.y)), max(x1.z, x1.w)) == SENT)
#define RETRY2(x0, x1, o0, o1) \
      _Pragma("nounroll") for (int s_ = 0; s_ < SPIN; ++s_) { \
        const int lf_ = fresh(l16); SLOWCNT \
        x0 = ld4c(ws, lf_, o0); x1 = ld4c(ws, lf_, o1); \
        if (wall(max(max4(x0), max4(x1)) != SENT)) break; \
      }
    if (__builtin_expect(BAD2(hh_[0][0], hh_[0][1]), 0)) { RETRY2(hh_[0][0], hh_[0][1], hb, hb + 14336) }
    STAMP(8); CSTAMP(28);
    v4f acc[8][2];
    v2u fa;
    const int fx = WS_CRDY + (tile * 4 + (lane < 32 ? wave : grp & 3)) * 256 + min(lane & 31, 27) * 8;
    const int ao = WS_ACT + (16 * tile + 4 * wave) * 57344 + 2 * slice * 1024 + l16;
#define ACT_OUT(ps, tt, f) { v4u o; \
      _Pragma("unroll") for (int hh = 0; hh < 2; ++hh) { \
        const v4f av = relu4(acc[2 * f + hh][tt]) * acc[4 + 2 * f + hh][tt]; \
        o[2 * hh] = pk2(lo2(av)); o[2 * hh + 1] = pk2(hi2(av)); } \
      st4c(ws, ao + (2 * ps + tt) * 57344 + f * 1024, o); }
#define ACT_ALL(ps) { mfence<0>(acc[0][0], acc[1][0], acc[2][0], acc[3][0]); mfence<0>(acc[4][0], acc[5][0], acc[6][0], acc[7][0]); \
      ACT_OUT(ps, 0, 0) ACT_OUT(ps, 0, 1) sb(); \
      mfence<0>(acc[0][1], acc[1][1], acc[2][1], acc[3][1]); mfence<0>(acc[4][1], acc[5][1], acc[6][1], acc[7][1]); \
      ACT_OUT(ps, 1, 0) ACT_OUT(ps, 1, 1) sb(); }
#define W_RH(dst, k, u, hf) { const v2u t_ = ldtr(L + tb[k >= 7][u & 3], (k % 7) * 8192 + (u >> 2) * 4096 + hf * 2048); \
      dst[2 * hf] = t_.x; dst[2 * hf + 1] = t_.y; }
#pragma unroll
    for (int ps = 0; ps < 2; ++ps)
#pragma unroll
    for (int ks = 0; ks < 14; ++ks) {
      const int v = ps * 14 + ks, cur = v & 1, nk = (v + 1) % 14, vn = v < 27 ? v + 1 : v;
      const bool more = v < 27;
      u32 m0, m1;
      __builtin_amdgcn_s_waitcnt(0xC07F);
      sb();
#pragma unroll
      for (int i = 0; i < 16; ++i) {
        const int u = i & 7, tt = i >> 3;
        if (ks == 0) mv0(acc[u][tt], wfr[cur][u], hh_[v][tt]); else mv(acc[u][tt], wfr[cur][u], hh_[v][tt]);
        if (more) {
          if (i < 4) { W_RH(wfr[cur ^ 1][i], nk, i, 0) W_RH(wfr[cur ^ 1][i], nk, i, 1) }
          else if (i < 12) W_RH(wfr[cur ^ 1][4 + ((i - 4) >> 1)], nk, (4 + ((i - 4) >> 1)), (i & 1))
        }
        if (i == 5 && v + LEAD < 28) H_LD(v + LEAD, 0);
        if (i == 7 && v + LEAD < 28) H_LD(v + LEAD, 1);
        if (i == 9 || i == 11 || i == 13) {
          const int e = (i - 9) >> 1, f = WD_MID + (v < 14 ? v - 2 : 12 + WD_PER * (v - 14) + e);
          if (f < 56 && ((v >= 2 && v < 14 && e == 0) || (v >= 14 && e < WD_PER))) wdn[f >> 2][f & 3] = ld4c(ws, wl16 + (f & 3) * 1024, wdb + (f >> 2) * 4096);
        }
        if (v > A_V && (i & 1) && i >= 5 && ((i - 5) >> 1) < A_PER) G_LD(A_PER * (v - A_V - 1) + ((i - 5) >> 1));
        if (v == 8 && i == 13) fa = ld2c(ws, fx);
        if (more && i == 12) { m0 = max(max(hh_[vn][0].x, hh_[vn][0].y), hh_[vn][0].z); pin(m0); }
        if (more && i == 13) { m1 = max(max(hh_[vn][0].w, hh_[vn][1].x), hh_[vn][1].y); pin(m1); }
        if (more && i == 14) { m0 = max(max(m0, hh_[vn][1].z), hh_[vn][1].w); m0 = max(m0, m1); pin(m0); }
        sb();
      }
      if (v == 12 && !wall(fa.x == tag && fa.y == ~tag)) wait_flags(ws, fx, tag);
      if (v == 19 && isR) {
        static_assert(19 + LEAD < 28, "the count below");
        asm volatile("s_waitcnt vmcnt(12)" ::: "memory");
        if (lane == 0) st2c(ws, WS_XRDY + (16 * grp + j - 14) * 64 + wave * 8, v2u{tag, ~tag});
      }
      if (more && __builtin_expect(wany(m0 == SENT), 0)) {
        const int pb = 2 * ((v + 1) / 14) * 14336 + nk * 1024;
        RETRY2(hh_[vn][0], hh_[vn][1], hb + pb, hb + pb + 14336)
      }
      if (v == 13) {
        STAMP(16); CSTAMP(29);
        ACT_ALL(0)
        if (isR) x2_store(ws, x2r, 16 * grp + j - 14, wave, l16);
        nop2();
      }
      sb();
    }
    STAMP(27);
    ACT_ALL(1)
    CSTAMP(30);
#pragma unroll
    for (int e = A_PER * (27 - A_V); e < 28; ++e) G_LD(e);
    sb();
    STAMP(9);
    v4u a[14][2];
    v4f ac2[4][4];
#define A_LD(e) a[(e) >> 1][(e) & 1] = ld4c(ws, l16 + (((e) >> 1) & 3) * 1024, abx[(e) & 1] + 2 * 57344 + ((e) >> 3) * 4096)
    const int po = ab + 3 * 57344 + min(lane, 6) * 2048 + 1024;
#define KN (ks < 13 ? ks + 1 : ks)
    u32 probe;
#pragma unroll
    for (int ks = 0; ks < 14; ++ks) {
      if (ks == 0 && __builtin_expect(BAD2(h[0][0], h[0][1]), 0)) { RETRY2(h[0][0], h[0][1], abx[0], abx[1]) }
#pragma unroll
      for (int tt = 0; tt < 2; ++tt)
#pragma unroll
        for (int n = 0; n < 4; ++n) ac2[tt][n] = mfma(wdn[ks][n], h[ks][tt], ks ? ac2[tt][n] : splat(0.f));
      const bool bad = ks < 13 && BAD2(h[KN][0], h[KN][1]);
      if (ks == P_AT) probe = ld1c(ws, po);
#pragma unroll
      for (int u = 0; u < 8; ++u) { __builtin_amdgcn_sched_group_barrier(0x8, 1, 0); if (u >= 2) __builtin_amdgcn_sched_group_barrier(0x2, 1, 0); }
      sb();
      if (__builtin_expect(bad, 0)) { RETRY2(h[KN][0], h[KN][1], abx[0] + KN * 1024, abx[1] + KN * 1024) }
      if (ks == B_AT2) {
        STAMP(25);
        if (wany(probe == SENT)) light_poll(ws, po);
        STAMP(26);
      }
      if (ks >= B_AT2) {
#pragma unroll
        for (int e = 4 * (ks - B_AT2); e < 4 * (ks - B_AT2 + 1); ++e) A_LD(e);
      }
      sb();
    }
    STAMP(17);
#pragma unroll
    for (int e = 4 * (14 - B_AT2); e < 4 * (14 - B_AT2) + 6; ++e) A_LD(e);
    sb();
    int rw[3], rr_ = L_B + lane * 8 + wave * 6144, oo[2];
#pragma unroll
    for (int x = 1; x < 4; ++x) { rw[x - 1] = L_B + lane * 8 + (wave ^ x) * 6144 + (x - 1) * 2048; pin(rw[x - 1]); }
    pin(rr_);
    const int blk = blk0 + (wave >> 1);
#pragma unroll
    for (int ph = 0; ph < 2; ++ph) { oo[ph] = (tok_row(blk + 2 * ph, c16) * D + ct * 64 + g * 16 + (wave & 1) * 8) * 2; pin(oo[ph]); }
#define RED_W1(ph, x, n) *(LAS v2f*)(L + rw[(x) - 1] + (ph) * 24576 + (n) * 512) = ((x) & 1) ? hi2(ac2[2 * (ph) + ((x) >> 1)][n]) : lo2(ac2[2 * (ph) + ((x) >> 1)][n]);
    const int n0 = ct * 64 + g * 16 + (wave & 1) * 8, reg = (n0 >= 112) + (n0 >= 224) + (n0 >= 336), xm = (0x1320 >> (4 * reg)) & 3;
    const int dz = abs(n0 - ((reg + 1) >> 1) * 224), db = (reg & 1) ? dz - 8 : dz;
#define XSLOT(m, dl) ((((dl) >> 4) * 2 + ((m) >> 1)) * 1024 + (((dl) >> 2) & 3) * 256 + ((m) & 1) * 8)
    int xo = WS_X2 + blk * 14336 + c16 * 16 + XSLOT(xm, db);
    int xe = WS_X2 + blk * 14336 + c16 * 16 + (n0 == 112 * reg && reg > 0 ? XSLOT(reg, 0) : XSLOT(xm, dz));
    pin(xo); pin(xe);
    const int xf = WS_XRDY + (blk + (lane & 4 ? 2 : 0)) * 64 + (lane & 3) * 8;
    v2u xp[2][2]; u32 x1[2];
    const int go = WS_MOD + grp * MOD_REP + (70 + 2 * ct) * 128 + g * 64 + (wave & 1) * 32;
    v4u xv[2], gv[2];
    STAMP(18);
    sb();
#pragma unroll
    for (int ks = 0; ks < 14; ++ks) {
      if (ks == 0 && __builtin_expect(BAD2(a[0][0], a[0][1]), 0)) { RETRY2(a[0][0], a[0][1], abx[0] + 2 * 57344, abx[1] + 2 * 57344) }
#pragma unroll
      for (int tt = 0; tt < 2; ++tt)
#pragma unroll
        for (int n = 0; n < 4; ++n) ac2[2 + tt][n] = mfma(wdn[ks][n], a[ks][tt], ks ? ac2[2 + tt][n] : splat(0.f));
#pragma unroll
      for (int e = 4 * (14 - B_AT2) + 6 + 3 * ks; e < 4 * (14 - B_AT2) + 9 + 3 * ks; ++e)
        if (e < 28) A_LD(e);
      const bool bad = ks < 13 && BAD2(a[KN][0], a[KN][1]);
      if (ks < 12) RED_W1(0, 1 + (ks >> 2), (ks & 3))
#pragma unroll
      for (int u = 0; u < 4; ++u) {
        __builtin_amdgcn_sched_group_barrier(0x8, 2, 0);
        __builtin_amdgcn_sched_group_barrier(0x20, 1, 0);
        if (u == 2) __builtin_amdgcn_sched_group_barrier(0x200, 1, 0);
        if (u >= 1) __builtin_amdgcn_sched_group_barrier(0x2, 3, 0);
      }
      sb();
      if (__builtin_expect(bad, 0)) { RETRY2(a[KN][0], a[KN][1], abx[0] + 2 * 57344 + KN * 1024, abx[1] + 2 * 57344 + KN * 1024) }
      if (ks == 2) fa = ld2c(ws, xf);
      if (ks == 7) {
        if (!wall(fa.x == tag && fa.y == ~tag)) wait_flags(ws, xf, tag);
#pragma unroll
        for (int ph = 0; ph < 2; ++ph) {
          xp[ph][0] = ld2c(ws, xo, ph * 2 * 14336); xp[ph][1] = ld2c(ws, xo + 256, ph * 2 * 14336);
          x1[ph] = __builtin_amdgcn_raw_buffer_load_b16(ws, xe, ph * 2 * 14336, SC1);
        }
        gv[0] = ld4c(ws, go); gv[1] = ld4c(ws, go + 16);
      }
      sb();
    }
    STAMP(10);
#pragma unroll
    for (int e = 0; e < 12; ++e) RED_W1(1, 3 - (e >> 2), (e & 3))
#define FIN_OK() wall(gv[0].y == tag && gv[0].w == tag && gv[1].y == tag && gv[1].w == tag)
    if (__builtin_expect(!FIN_OK(), 0)) {
      _Pragma("nounroll") for (int s = 0; s < SPIN; ++s) {
        const int gf = fresh(go);
        gv[0] = ld4c(ws, gf); gv[1] = ld4c(ws, gf + 16);
        if (FIN_OK()) break;
      }
    }
#pragma unroll
    for (int ph = 0; ph < 2; ++ph) {
      const v2u p = xp[ph][0], q = xp[ph][1];
      const bool dn = reg & 1;
      xv[ph].x = __builtin_amdgcn_perm(dn ? q.y : p.x, x1[ph], 0x07060100u);
      xv[ph].y = dn ? __builtin_amdgcn_perm(q.x, q.y, 0x07060100u) : p.y;
      xv[ph].z = dn ? __builtin_amdgcn_perm(p.y, q.x, 0x07060100u) : q.x;
      xv[ph].w = dn ? __builtin_amdgcn_perm(p.x, p.y, 0x07060100u) : q.y;
      pin(xv[ph]);
    }
    bar();
#pragma unroll
    for (int ph = 0; ph < 2; ++ph) {
      v2f r[4];
#pragma unroll
      for (int n = 0; n < 4; ++n) {
        r[n] = lo2(ac2[2 * ph][n]);
#pragma unroll
        for (int w = 0; w < 3; ++w) r[n] += *(const LAS v2f*)(L + rr_ + w * 2048 + ph * 24576 + n * 512);
      }
      v4u o;
#pragma unroll
      for (int q = 0; q < 2; ++q) {
        o[2 * q] = pk2(fma2(bun(gv[q].x), v2f{r[0][q], r[1][q]}, bun(xv[ph][2 * q])));
        o[2 * q + 1] = pk2(fma2(bun(gv[q].z), v2f{r[2][q], r[3][q]}, bun(xv[ph][2 * q + 1])));
      }
      __builtin_amdgcn_raw_buffer_store_b128(o, mkrs(out, S * D * 2), oo[ph], 0, OUT_AUX);
    }
  }
  STAMP(11);
  DUMP();
}

extern "C" void kernel_launch(void* const* d_in, const int* in_sizes, int n_in, void* d_out, int out_size, void* d_ws,
                              size_t ws_size, hipStream_t stream) {
  fused<<<256, 256, 0, stream>>>((const u16*)d_in[0], (const u16*)d_in[1], (const u16*)d_in[2], (const u16*)d_in[4],
                                 (const u16*)d_in[5], (const u16*)d_in[6], (const u16*)d_in[7], (const u16*)d_in[8],
                                 (u16*)d_out, (char*)d_ws);
}
```

```cpp
#define ALIAS 1
#define PF_AUX 0
#include <hip/hip_runtime.h>

typedef unsigned short u16;
typedef unsigned int u32;
typedef unsigned long long u64;
typedef u32 v4u __attribute__((ext_vector_type(4)));
typedef u32 v2u __attribute__((ext_vector_type(2)));
typedef float v4f __attribute__((ext_vector_type(4)));
typedef float v2f __attribute__((ext_vector_type(2)));
typedef short v8s __attribute__((ext_vector_type(8)));
typedef short v4s __attribute__((ext_vector_type(4)));
typedef __bf16 v8b __attribute__((ext_vector_type(8)));
typedef __bf16 v2b __attribute__((ext_vector_type(2)));
#define LAS __attribute__((address_space(3)))
#define GAS __attribute__((address_space(1)))
#define DEV __device__ __forceinline__

#define C_W1_EARLY 0
#define CLR_DELAY 0
#define W_DELAY 0
#define CLR2_DELAY 0
#define PF_POS 2
#define SL1 1
#define SL2 2
#define SL4 4
#define X_AFTER 8
#define W_LATE 0
#define W1_PF 1
#define DMA_MOD ""
#define LEAD 6
#define A_V 21
#define A_PER 4
#define WDV(f) ((f) - WD_MID + 2 < 14 ? (f) - WD_MID + 2 : ((f) - WD_MID - 12) / 2 + 16)
#define B_AT2 12
#define RE_PF 0
#define PF2_DELAY 0
#define C_DMA_EARLY 0
#define EVEN_PF 0
#define XBAL 1
#define W1EVEN 1
#define PRE_OUT 0
#define OUT_AUX 16
#define CLR_MODE 0
#define MK 0
#define MKD1 0
#define MKD2 0
#define MKD3 0
#define NR_S 0
#define NR_U 8
#define NR_M 8
#define NR_T 8
#define P_AT 7
#define WD_PER 3
#define A_FIRST 10
#define B_FIRST 10
#define A_AT 9
#define F_AT 4
#define CODE_BYTES 45056
#define WD_EARLY 20
#define WD_MID 20
#define B_AT 9
constexpr int S = 2048, D = 448, DF = 225, F = 1792;

struct alignas(16) Chain { u16 v[2048]; };
struct Cos448 { u16 v[448]; };
constexpr double c_cos_t(double a) {
  double a2 = a * a, t = 1.0, s = 1.0;
  for (int i = 1; i <= 10; ++i) { t = -t * a2 / ((2 * i - 1) * (2 * i)); s += t; }
  return s;
}
constexpr double c_sin_t(double a) {
  double a2 = a * a, t = a, s = a;
  for (int i = 1; i <= 10; ++i) { t = -t * a2 / ((2 * i) * (2 * i + 1)); s += t; }
  return s;
}
constexpr double c_cos448(int m) {
  if (m > 224) m = 448 - m;
  double sg = 1.0;
  if (m > 112) { m = 224 - m; sg = -1.0; }
  const double w = 6.283185307179586476925286766559 / 448.0;
  return m == 112 ? 0.0 : (m <= 56 ? sg * c_cos_t(w * m) : sg * c_sin_t(w * (112 - m)));
}
constexpr u16 c_bf16(double d) {
  const u32 u = __builtin_bit_cast(u32, (float)d);
  return (u16)((u + 0x7FFFu + ((u >> 16) & 1u)) >> 16);
}
constexpr Cos448 mk_cos() { Cos448 c{}; for (int m = 0; m < 448; ++m) c.v[m] = c_bf16(c_cos448(m)); return c; }
constexpr Cos448 CTAB = mk_cos();
constexpr u16 bcos(int m) { return CTAB.v[m % 448]; }
constexpr u16 bsin(int m) { return CTAB.v[(m % 448 + 336) % 448]; }
constexpr u16 bneg(u16 b) { return b ? (u16)(b ^ 0x8000u) : b; }
constexpr u16 bscale(u16 b, int e) { return b ? (u16)(b + e * 0x80) : b; }

constexpr Chain mk_p1(int c) {
  const int tile = c >> 2, par = c & 1, sn = (c >> 1) & 1;
  Chain ch{};
  for (int s = 0; s < 4; ++s)
    for (int l = 0; l < 64; ++l) {
      const int kp = 16 * tile + (l & 15), k = 2 * kp + par;
      if (k > 224) continue;
      for (int j = 0; j < 8; ++j) {
        const int dl = 32 * s + 8 * (l >> 4) + j;
        if (dl > 112) continue;
        u16 b = sn ? bneg(bsin(k * dl)) : bcos(k * dl);
        if (dl == 0 || dl == 112) b = bscale(b, -1);
        ch.v[(s * 64 + l) * 8 + j] = b;
      }
    }
  return ch;
}
constexpr Chain mk_p3(int c) {
  const int tile = c >> 1, sn = c & 1;
  Chain ch{};
  for (int s = 0; s < 4; ++s)
    for (int l = 0; l < 64; ++l) {
      int dl = 16 * tile + (l & 15);
      if (c == 1 && dl == 0) dl = 112;
      for (int j = 0; j < 8; ++j) {
        const int k = 2 * (32 * s + 8 * (l >> 4) + j) + 1;
        if (k < 224) ch.v[(s * 64 + l) * 8 + j] = sn ? bsin(k * dl) : bcos(k * dl);
      }
    }
  return ch;
}
constexpr Chain mk_p2(int c) {
  Chain ch{};
  for (int f = 0; f < 4; ++f)
    for (int l = 0; l < 64; ++l)
      for (int j = 0; j < 8; ++j) {
        const int comp = c < 8 ? (c & 1) : (f >> 1), s = c < 8 ? f : (f & 1);
        const int o = 16 * (c < 8 ? (c >> 1) : (c - 8)) + (l & 15), i = 16 * s + 4 * (l >> 4) + (j >> 1);
        const int m = (c < 8 ? 7 : 14) * ((i * o) % 64);
        u16 b = 0;
        if (comp == 0) b = (j & 1) ? bsin(m) : bcos(m);
        else b = (j & 1) ? bcos(m) : bneg(bsin(m));
        ch.v[(f * 64 + l) * 8 + j] = b;
      }
  return ch;
}
template <int I> constexpr Chain P1C = mk_p1(I);
template <int I> constexpr Chain P3C = mk_p3(I);
template <int I> constexpr Chain P2C = mk_p2(I);
#define L10(T, b) T<b>, T<b + 1>, T<b + 2>, T<b + 3>, T<b + 4>, T<b + 5>, T<b + 6>, T<b + 7>, T<b + 8>, T<b + 9>
#define L29(T) L10(T, 0), L10(T, 10), T<20>, T<21>, T<22>, T<23>, T<24>, T<25>, T<26>, T<27>, T<28>
constexpr int TAB_P1 = 0, TAB_P3 = 29, TAB_P2 = 43, TAB_N = 53;
static __device__ const Chain TAB[TAB_N] = {L29(P1C), L10(P3C, 0), P3C<10>, P3C<11>, P3C<12>, P3C<13>, L10(P2C, 0)};

constexpr int WS_EPOCH = 0;
constexpr int WS_RDY = 4096;
constexpr int WS_HRDY = 4096 + 8192;
constexpr int WS_DRDY = 4096 + 16384;
constexpr int WS_XRDY = 131072 + 65536;
constexpr int WS_ARDY = 131072;
constexpr int WS_CRDY = 131072 + 32768;
constexpr int WS_MK1 = 212992, WS_MK2 = 221184, WS_MK3 = 229376;
constexpr int WS_MOD = 32768, MOD_REP = 12288;
constexpr int T1_BLK = 226 * 64;
constexpr int WS_T1 = 1 << 18;
constexpr int WS_WT = WS_T1 + 128 * T1_BLK;
constexpr int WS_H2 = WS_WT + 113 * 16384, H2_BLK = 14336, WS_X2 = WS_T1, X2_BLK = T1_BLK;
constexpr int WS_WD = WS_H2 + 128 * 14336;
constexpr int WS_ACT = WS_WD + 7 * 56 * 4096;
constexpr int WS_END = WS_ACT + 128 * 57344;
constexpr int WS_DBG = 32 << 20;

constexpr int L_W1 = 0;
constexpr int L_B = 114688;
constexpr int XP = 912;
constexpr int L_XS = L_B;
constexpr int L_ST = L_XS + 16 * XP;
constexpr int L_M1 = L_ST + 128;
constexpr int L_AF = L_M1 + 4 * 1792;
constexpr int L_M2 = L_AF + 16384;
constexpr int L_EX = L_M2 + 3 * 1792;
constexpr int L_REND = L_EX + 512;
constexpr int ZPL = 8192;
constexpr int L_CI = L_B;
constexpr int L_CX = L_CI + 2 * 8256;
constexpr int L_RED = L_CX + 16384;
constexpr int L_DBG = 163840 - 1024;
constexpr int L_SZ = 163840;
static_assert(L_REND <= L_DBG && L_RED + 2048 <= L_DBG && 2 * ZPL <= 16384, "lds");

constexpr u32 SENT = 0xFFFFFFFFu;
constexpr int SPIN = 1 << 14;
constexpr int SC1 = 16;
constexpr int OOB = 0x7FFF0000;

typedef __amdgpu_buffer_rsrc_t rsrc_t;
DEV rsrc_t mkrs(const void* p, int bytes) { return __builtin_amdgcn_make_buffer_rsrc((void*)p, 0, bytes, 0x00020000); }
DEV v4u ld4(rsrc_t r, int off, int so = 0) { return __builtin_amdgcn_raw_buffer_load_b128(r, off, so, 0); }
DEV v2u ld2(rsrc_t r, int off, int so = 0) { return __builtin_amdgcn_raw_buffer_load_b64(r, off, so, 0); }
DEV v4u ld4c(rsrc_t r, int off, int so = 0) { return __builtin_amdgcn_raw_buffer_load_b128(r, off, so, SC1); }
DEV v2u ld2c(rsrc_t r, int off, int so = 0) { return __builtin_amdgcn_raw_buffer_load_b64(r, off, so, SC1); }
DEV u32 ld1c(rsrc_t r, int off, int so = 0) { return __builtin_amdgcn_raw_buffer_load_b32(r, off, so, SC1); }
DEV void st4c(rsrc_t r, int off, v4u v) { __builtin_amdgcn_raw_buffer_store_b128(v, r, off, 0, SC1); }
DEV void st2c(rsrc_t r, int off, v2u v) { __builtin_amdgcn_raw_buffer_store_b64(v, r, off, 0, SC1); }
DEV float blo(u32 v) { return __builtin_bit_cast(float, v << 16); }
DEV float bhi(u32 v) { return __builtin_bit_cast(float, v & 0xFFFF0000u); }
DEV v2f bun(u32 v) { return v2f{blo(v), bhi(v)}; }
DEV u32 pk(float lo, float hi) { return __builtin_bit_cast(u32, __builtin_convertvector(v2f{lo, hi}, v2b)); }
DEV u32 pk2(v2f v) { return __builtin_bit_cast(u32, __builtin_convertvector(v, v2b)); }
DEV float rb(float f) { return blo(pk(f, 0.f)); }
DEV v2f lo2(v4f v) { return __builtin_shufflevector(v, v, 0, 1); }
DEV v2f hi2(v4f v) { return __builtin_shufflevector(v, v, 2, 3); }
DEV v2f fma2(v2f a, v2f b, v2f c) { return __builtin_elementwise_fma(a, b, c); }
DEV v4f fma4(v4f a, v4f b, v4f c) { return __builtin_elementwise_fma(a, b, c); }
typedef int v4i __attribute__((ext_vector_type(4)));
DEV v4f relu4(v4f v) { return __builtin_bit_cast(v4f, __builtin_elementwise_max(__builtin_bit_cast(v4i, v), v4i{0, 0, 0, 0})); }
DEV v4f splat(float f) { return v4f{f, f, f, f}; }
DEV v4f mfma(v4u a, v4u b, v4f c) {
  return __builtin_amdgcn_mfma_f32_16x16x32_bf16(__builtin_bit_cast(v8b, a), __builtin_bit_cast(v8b, b), c, 0, 0, 0);
}
DEV void mfa(v4f& c, v4u a, v4u b) { asm volatile("s_nop 1\n\tv_mfma_f32_16x16x32_bf16 %0, %1, %2, %0" : "+v"(c) : "v"(a), "v"(b)); }
DEV void mf0(v4f& c, v4u a, v4u b) { asm volatile("s_nop 1\n\tv_mfma_f32_16x16x32_bf16 %0, %1, %2, 0" : "=&v"(c) : "v"(a), "v"(b)); }
template <int N> DEV void mfence(v4f& a, v4f& b) { asm volatile("s_nop %2" : "+v"(a), "+v"(b) : "i"(N)); }
template <int N> DEV void mfence(v4f& a, v4f& b, v4f& c, v4f& d) { asm volatile("s_nop %4" : "+v"(a), "+v"(b), "+v"(c), "+v"(d) : "i"(N)); }
#define MF "v_mfma_f32_16x16x32_bf16 "
DEV void mf8(v4f& d0, v4f& d1, const v4u (&a0)[4], const v4u (&b0)[4], const v4u (&a1)[4], const v4u (&b1)[4]) {
  asm volatile("s_nop 1\n\t" MF "%0, %2, %6, 0\n\t" MF "%1, %10, %14, 0\n\t" MF "%0, %3, %7, %0\n\t" MF "%1, %11, %15, %1\n\t"
               MF "%0, %4, %8, %0\n\t" MF "%1, %12, %16, %1\n\t" MF "%0, %5, %9, %0\n\t" MF "%1, %13, %17, %1"
               : "=&v"(d0), "=&v"(d1)
               : "v"(a0[0]), "v"(a0[1]), "v"(a0[2]), "v"(a0[3]), "v"(b0[0]), "v"(b0[1]), "v"(b0[2]), "v"(b0[3]),
                 "v"(a1[0]), "v"(a1[1]), "v"(a1[2]), "v"(a1[3]), "v"(b1[0]), "v"(b1[1]), "v"(b1[2]), "v"(b1[3]));
}
#define H4_M(XA, D, X, K, C) MF D ", " XA(X, K) ", " C "\n\t"
#define H4_XA(X, K) X ", " K
#define H4_XB(X, K) K ", " X
#define H4_DEF(NAME, XA, C0, C1, CONS, N0, N1, PA, PB, PC, PD) \
DEV void NAME(v4f& d0, v4f& d1, v4u x0, v4u x1, v4u k00, v4u k01, v4u k10, v4u k11, \
              u32& o0, u32& o1, float p0, float q0, float c0, float s0, float p1, float q1, float c1, float s1) { \
  float t0, t1, t2, t3; \
  asm volatile(H4_M(XA, "%0", "%8", "%10", C0) "v_mul_f32 %4, %14, %16\n\tv_mul_f32 %5, %15, %16\n\t" \
               H4_M(XA, "%1", "%8", "%12", C1) "v_mul_f32 %6, %18, %20\n\tv_mul_f32 %7, %19, %20\n\t" \
               H4_M(XA, "%0", "%9", "%11", "%0") "v_fma_f32 %4, " N0 "%15, %17, %4\n\tv_fma_f32 %5, " N1 "%14, %17, %5\n\t" \
               H4_M(XA, "%1", "%9", "%13", "%1") "v_fma_f32 %6, " N0 "%19, %21, %6\n\tv_fma_f32 %7, " N1 "%18, %21, %7\n\t" \
               "v_cvt_pk_bf16_f32 %2, " PA ", " PB "\n\tv_cvt_pk_bf16_f32 %3, " PC ", " PD \
               : CONS(d0), CONS(d1), "=&v"(o0), "=&v"(o1), "=&v"(t0), "=&v"(t1), "=&v"(t2), "=&v"(t3) \
               : "v"(x0), "v"(x1), "v"(k00), "v"(k01), "v"(k10), "v"(k11), \
                 "v"(p0), "v"(q0), "v"(c0), "v"(s0), "v"(p1), "v"(q1), "v"(c1), "v"(s1)); \
}
H4_DEF(h4_a0m, H4_XA, "0", "0", "=&v", "", "-", "%4", "%5", "%6", "%7")
H4_DEF(h4_a1m, H4_XA, "%0", "%1", "+v", "", "-", "%4", "%5", "%6", "%7")
H4_DEF(h4_b0m, H4_XB, "0", "0", "=&v", "", "-", "%4", "%5", "%6", "%7")
H4_DEF(h4_b0p, H4_XB, "0", "0", "=&v", "-", "", "%5", "%4", "%7", "%6")
H4_DEF(h4_a0p, H4_XA, "0", "0", "=&v", "-", "", "%5", "%4", "%7", "%6")
#define M4_DEF(NAME, XA, C0, C1, CONS) \
DEV void NAME(v4f& d0, v4f& d1, v4u x0, v4u x1, v4u k00, v4u k01, v4u k10, v4u k11) { \
  asm volatile(H4_M(XA, "%0", "%2", "%4", C0) H4_M(XA, "%1", "%2", "%6", C1) H4_M(XA, "%0", "%3", "%5", "%0") MF "%1, " XA("%3", "%7") ", %1" \
               : CONS(d0), CONS(d1) : "v"(x0), "v"(x1), "v"(k00), "v"(k01), "v"(k10), "v"(k11)); \
}
M4_DEF(m4_a0, H4_XA, "0", "0", "=&v") M4_DEF(m4_a1, H4_XA, "%0", "%1", "+v")
M4_DEF(m4_b0, H4_XB, "0", "0", "=&v") M4_DEF(m4_b1, H4_XB, "%0", "%1", "+v")
DEV void mv(v4f& c, v4u a, v4u b) { asm volatile(MF "%0, %1, %2, %0" : "+v"(c) : "v"(a), "v"(b)); }
DEV void mv0(v4f& c, v4u a, v4u b) { asm volatile(MF "%0, %1, %2, 0" : "=&v"(c) : "v"(a), "v"(b)); }
DEV void nop2() { asm volatile("s_nop 1"); }
template <class T> DEV void pin(T& v) { asm volatile("" : "+v"(v)); }
DEV int fresh(int off) { asm volatile("" : "+v"(off) :: "memory"); return off; }
DEV bool wall(bool ok) { return __builtin_amdgcn_ballot_w64(!ok) == 0; }
DEV bool wany(bool b) { return __builtin_amdgcn_ballot_w64(b) != 0; }
DEV void sb() { __builtin_amdgcn_sched_barrier(0); }
DEV u32 max4(v4u v) { return max(max(v.x, v.y), max(v.z, v.w)); }
DEV void drain() { asm volatile("s_waitcnt vmcnt(0)" ::: "memory"); }
DEV void cfence() { asm volatile("" ::: "memory"); }
DEV void bar() { asm volatile("s_waitcnt lgkmcnt(0)\n\ts_barrier" ::: "memory"); }
template <int CTRL> DEV float dpp(float v) {
  return __builtin_bit_cast(float, __builtin_amdgcn_update_dpp(0, __builtin_bit_cast(int, v), CTRL, 0xF, 0xF, true));
}
DEV float sum16(float v) {
  v += dpp<0x128>(v); v += dpp<0x124>(v); v += dpp<0x122>(v); v += dpp<0x121>(v); return v;
}
DEV void sumg2(float& a, float& b) {
  float c = a, d = b;
  asm volatile("s_nop 1\n\tv_permlane16_swap_b32 %0, %1\n\tv_permlane16_swap_b32 %2, %3" : "+v"(a), "+v"(c), "+v"(b), "+v"(d));
  a += c; b += d; c = a; d = b;
  asm volatile("s_nop 1\n\tv_permlane32_swap_b32 %0, %1\n\tv_permlane32_swap_b32 %2, %3" : "+v"(a), "+v"(c), "+v"(b), "+v"(d));
  a += c; b += d;
}
DEV v4u lds4(const char LAS* L, int off) { return *(const LAS v4u*)(L + off); }
DEV v4f lds4f(const char LAS* L, int off) { return *(const LAS v4f*)(L + off); }
DEV v2u ldtr(const char LAS* L, int off) { return __builtin_bit_cast(v2u, __builtin_amdgcn_ds_read_tr16_b64_v4i16((LAS v4s*)(L + off))); }
DEV v4u ldtr2(const char LAS* L, int o0, int o1) { const v2u a = ldtr(L, o0), b = ldtr(L, o1); return v4u{a.x, a.y, b.x, b.y}; }
DEV void dma16(rsrc_t r, int voff, int soff, u32 lds_dst) {
  u32 keep;
  asm volatile("s_mov_b32 %0, m0\n\ts_mov_b32 m0, %1\n\ts_nop 0\n\tbuffer_load_dwordx4 %2, %3, %4 offen" DMA_MOD " lds\n\ts_mov_b32 m0, %0"
               : "=&s"(keep) : "s"(lds_dst), "v"(voff), "s"(r), "s"(soff) : "memory");
}
DEV void light_poll(rsrc_t ws, int off) {
  _Pragma("nounroll") for (int s = 0; s < SPIN; ++s) {
    if (wall(ld1c(ws, fresh(off)) != SENT)) break;
    __builtin_amdgcn_s_sleep(SL2);
  }
}
DEV int tok_row(int blk, int t) {
  const int n1 = 16 * (blk >> 5) + 4 * ((blk >> 2) & 3) + (t & 3), n2 = 16 * ((blk >> 4) & 1) + 4 * (blk & 3) + (t >> 2);
  return n2 + 32 * n1;
}
template <bool SWAP> DEV v4u cmul_pack(v4f a, v4f b, v4f c, v4f s) {
  const v4f re = fma4(b, s, a * c), im = fma4(b, c, -(a * s));
  return SWAP ? v4u{pk(im.x, re.x), pk(im.y, re.y), pk(im.z, re.z), pk(im.w, re.w)}
              : v4u{pk(re.x, im.x), pk(re.y, im.y), pk(re.z, im.z), pk(re.w, im.w)};
}

template <bool PLUS> DEV void cmul2(u32& o0, u32& o1, v2f p, v2f q, v2f c, v2f s) {
  const v2f re = fma2(PLUS ? -q : q, s, p * c), im = fma2(PLUS ? p : -p, s, q * c);
  o0 = PLUS ? pk(im.x, re.x) : pk(re.x, im.x); o1 = PLUS ? pk(im.y, re.y) : pk(re.y, im.y);
}

#define STAMP(i)
#define CSTAMP(i)
#define SLOWCNT
#define DUMP()

DEV void dma_w1(const u16* wg, const u16* wu, int slice, int wave, int lane, u32 lbase) {
  const rsrc_t rg = mkrs(wg, D * F * 2), ru = mkrs(wu, D * F * 2);
  const int r = lane >> 3, ch = ((((lane & 7) >> 1) ^ (lane >> 4)) & 3) * 32 + (lane & 1) * 16 + slice * 128;
  const int vp = r * (F * 2) + ch, vn = (7 - r) * (F * 2) + ch;
#pragma unroll
  for (int n = 0; n < 4; ++n) {
    const int ks = wave + 4 * n;
    if (n == 3 && wave >= 2) break;
    const int t16 = (ks >> 1) * 16, a = ks & 1;
#pragma unroll
    for (int q = 0; q < 4; ++q) {
      const int dl0 = t16 + 8 * (q & 1);
      const bool pos = (q >> 1) == a;
      const int d0 = q < 2 ? (a ? 224 - dl0 - 7 : dl0) : (a ? 224 + dl0 : 448 - dl0 - 7);
      int vo = pos ? vp : vn, so = d0 * (F * 2);
      if (n == 0 && q == 2) {
        vo += so; so = 0;
        if (wave < 2 && lane < 8) vo = (wave ? 336 : 112) * (F * 2) + ch;
      }
      dma16(rg, vo, so, lbase + L_W1 + ks * 8192 + q * 1024);
      dma16(ru, vo, so, lbase + L_W1 + ks * 8192 + 4096 + q * 1024);
    }
  }
}
struct ModLoad { u32 cv[7]; v4u mw[7]; };
DEV void mod_load(ModLoad& m, const u16* cin, const u16* wm, int q, int tid) {
  const rsrc_t cr = mkrs(cin, q >= 0 ? D * 2 : 0), mr = mkrs(wm, q >= 0 ? D * 6 * D * 2 : 0);
#pragma unroll
  for (int i = 0; i < 7; ++i) m.cv[i] = __builtin_amdgcn_raw_buffer_load_b16(cr, (tid >> 2) * 2 + i * 128, 0, 0);
#pragma unroll
  for (int i = 0; i < 7; ++i) m.mw[i] = __builtin_amdgcn_raw_buffer_load_b128(mr, ((tid >> 2) * (6 * D) + q * 32 + (tid & 3) * 8) * 2, i * 64 * 6 * D * 2, PF_AUX);
}
DEV void mod_part(ModLoad& m, char LAS* L, int tid) {
  float sl[7];
#pragma unroll
  for (int i = 0; i < 7; ++i) { const float cc = blo(m.cv[i]); sl[i] = rb(cc * __builtin_amdgcn_rcpf(1.f + __expf(-cc))); pin(sl[i]); }
  v2f a[4];
#pragma unroll
  for (int e = 0; e < 4; ++e) a[e] = v2f{0.f, 0.f};
#pragma unroll
  for (int i = 0; i < 7; ++i)
#pragma unroll
    for (int e = 0; e < 4; ++e) a[e] = fma2(bun(m.mw[i][e]), v2f{sl[i], sl[i]}, a[e]);
#pragma unroll
  for (int e = 0; e < 4; ++e) {
    a[e].x += dpp<0x128>(a[e].x); a[e].y += dpp<0x128>(a[e].y);
    a[e].x += dpp<0x124>(a[e].x); a[e].y += dpp<0x124>(a[e].y);
  }
  if ((tid & 15) < 4) {
    *(LAS v4f*)(L + L_RED + ((tid >> 4) * 32 + (tid & 15) * 8) * 4) = v4f{a[0].x, a[0].y, a[1].x, a[1].y};
    *(LAS v4f*)(L + L_RED + ((tid >> 4) * 32 + (tid & 15) * 8 + 4) * 4) = v4f{a[2].x, a[2].y, a[3].x, a[3].y};
  }
}
DEV void mod_publish(char LAS* L, rsrc_t ws, int q, int lane, u32 tag) {
  v4f s = {0.f, 0.f, 0.f, 0.f};
#pragma unroll
  for (int r = 0; r < 16; ++r) s += lds4f(L, L_RED + (r * 32 + (lane & 7) * 4) * 4);
  const float one = (q / 14 == 1 || q / 14 == 4) ? 1.f : 0.f;
  st4c(ws, WS_MOD + (lane >> 3) * MOD_REP + q * 128 + (lane & 7) * 16,
       v4u{pk(rb(s.x) + one, rb(s.y) + one), tag, pk(rb(s.z) + one, rb(s.w) + one), tag});
}
DEV u32 ld1(rsrc_t r, int off) { return __builtin_amdgcn_raw_buffer_load_b32(r, off, 0, 0); }
DEV u32 ld1n(rsrc_t r, int off) { return __builtin_amdgcn_raw_buffer_load_b32(r, off, 0, PF_AUX); }
DEV u32 mktag(u32 epoch) { return epoch * 0x9E3779B1u + 0x7F4A7C15u; }

DEV void x2_store(rsrc_t ws, const v4u (&r)[4], int blk, int wave, int l16) {
  const int o = WS_X2 + blk * X2_BLK + (wave == 0 ? 0 : 4 * wave - 2) * 1024 + l16;
#pragma unroll
  for (int m = 0; m < 4; ++m) st4c(ws, m < 2 || wave != 0 ? o + m * 1024 : OOB, r[m]);
}
DEV bool flags_ok(v4u rv, u32 tag) { return rv.x == tag && rv.y == ~tag && rv.z == tag && rv.w == ~tag; }
DEV void wait_flags(rsrc_t ws, int off, u32 tag) {
  _Pragma("nounroll") for (int s = 0; s < SPIN; ++s) {
    const v2u rv = ld2c(ws, fresh(off));
    if (wall(rv.x == tag && rv.y == ~tag)) break;
    __builtin_amdgcn_s_sleep(SL1);
  }
}

__global__ void __launch_bounds__(256, 1)
fused(const u16* __restrict__ xin, const u16* __restrict__ wr, const u16* __restrict__ wi, const u16* __restrict__ wg,
      const u16* __restrict__ wu, const u16* __restrict__ wd, const u16* __restrict__ cin, const u16* __restrict__ wm,
      u16* __restrict__ out, char* wsp) {
  __shared__ __attribute__((aligned(16))) char smem[L_SZ];
  char LAS* L = (char LAS*)smem;
  const u32 lbase = (u32)(size_t)L;
  const int tid = threadIdx.x, lane = tid & 63, wave = __builtin_amdgcn_readfirstlane(tid >> 6);
  const int g = lane >> 4, c16 = lane & 15, l16 = lane * 16;
  const int grp = blockIdx.x & 7, j = blockIdx.x >> 3;
  u64 pc;
  asm volatile("s_getpc_b64 %0" : "=s"(pc));
  STAMP(0); CSTAMP(31);
  asm volatile("" :: "s"(xin), "s"(wr), "s"(wi), "s"(wg), "s"(wu), "s"(wd), "s"(cin), "s"(wm), "s"(out), "s"(wsp));
  const rsrc_t ws = mkrs(wsp, WS_END);
  const rsrc_t tab = mkrs(TAB, TAB_N * 4096);
  const rsrc_t code = mkrs((const void*)(pc & ~127ull), CODE_BYTES);
  const bool isR = j >= 14 && j < 30;
  const bool isG = j < 28;
  const int gk = j / 7, ct = j - gk * 7;
  const int tile = 4 * (grp >> 2) + gk, slice = 7 * (grp & 3) + ct;
  const int wdb = WS_WD + (ct * 56 + wave * 14) * 4096;
  const v4u sv = {SENT, SENT, SENT, SENT};
  int lb = L_B + l16;
  pin(lb);
  const char LAS* LB = L + lb - L_B;
  const int pfo = ((j * 4 + wave) * 14 + lane) * 128, pfc = ((j * 4 + wave) * 4 + lane) * 128;
  u32 pfa = ld1(tab, lane < 14 ? pfo : OOB), pfb = ld1(code, lane < 4 ? pfc : OOB);
  const int hro = WS_HRDY + (16 * tile + 4 * wave) * 32 + c16 * 8;
  v4u x2r[4];
  const int wl16 = (lane ^ ((wave & 1) << 1)) * 16;
  v4u wdn[14][4];
  u32 epoch, tag;

#define ACT_CLEAR(lo, hi) _Pragma("unroll") for (int m = lo; m < hi; ++m) \
      st4c(ws, isG ? WS_ACT + (16 * (tile + 2 * (m >> 3)) + 4 * (grp & 3)) * 57344 + ct * 32768 + (m & 7) * 4096 + tid * 16 : OOB, sv);
  if (isR) {
    const int blk = 16 * grp + (j - 14);
    epoch = ld1c(ws, WS_EPOCH);
    const int t0 = wave == 0 ? 0 : 2 * wave - 1;
    v4u cf[8][4];
#define CF_LOAD(q) { const int chn = TAB_P1 + ((wave == 0 && q >= 4) ? 28 : 4 * t0 + q), vo = (q < 5 || wave != 0) ? l16 : OOB; \
      _Pragma("unroll") for (int s = 0; s < 4; ++s) cf[q][s] = ld4(tab, vo + s * 1024, chn * 4096); sb(); }
#pragma unroll
    for (int q = 0; q < X_AFTER; ++q) CF_LOAD(q)
    v2u xv[7];
    {
      const rsrc_t xr = mkrs(xin, S * D * 2);
      const int off = tok_row(blk, 4 * wave + g) * (D * 2) + c16 * 8;
#pragma unroll
      for (int m = 0; m < 7; ++m) xv[m] = ld2(xr, off + m * 128);
    }
    typedef u32 v3u __attribute__((ext_vector_type(3)));
    v2u fa_[2], fb_[2]; v3u fc_[2], fd_[2];
    {
      const rsrc_t rr = mkrs(wr, S * DF * 2), ri = mkrs(wi, S * DF * 2);
      const int fo_ = 16 * wave + c16 < 57 ? 2 * g * 450 + (16 * wave + c16) * 8 : OOB;
#pragma unroll
      for (int hh = 0; hh < 2; ++hh) {
        const int so = blk * 7200 + hh * 3600;
        fa_[hh] = ld2(rr, fo_, so); fb_[hh] = ld2(ri, fo_, so);
        fc_[hh] = __builtin_amdgcn_raw_buffer_load_b96(rr, fo_ + 448, so, 0); fd_[hh] = __builtin_amdgcn_raw_buffer_load_b96(ri, fo_ + 448, so, 0);
      }
    }
    sb();
#pragma unroll
    for (int q = X_AFTER; q < 8; ++q) CF_LOAD(q)
    STAMP(23);
    v2u rdy;
    {
      v2f f[14], s2 = {0.f, 0.f};
#pragma unroll
      for (int m = 0; m < 7; ++m) {
        f[2 * m] = bun(xv[m].x); f[2 * m + 1] = bun(xv[m].y);
        s2 += f[2 * m] + f[2 * m + 1];
        *(LAS v2u*)(L + L_XS + (4 * wave + g) * XP + c16 * 8 + m * 128) = xv[m];
      }
      STAMP(21);
      sb();
      {
        const int q = 16 * wave + c16, o = WS_WT + q * 16384 + blk * 128 + g * 16;
#pragma unroll
        for (int hh = 0; hh < 2; ++hh) {
          const v2u a = fa_[hh], b = fb_[hh]; const v3u c = fc_[hh], d = fd_[hh];
          st4c(ws, q < 57 ? o + hh * 64 : OOB, v4u{__builtin_amdgcn_perm(b.x, a.x, 0x05040100u), __builtin_amdgcn_perm(b.y, a.y, 0x05040100u),
                                                   __builtin_amdgcn_perm(d.x, c.x, 0x07060302u), __builtin_amdgcn_perm(d.y, c.y, 0x07060302u)});
          st4c(ws, q < 56 ? o + hh * 64 + 57 * 16384 : OOB,
               v4u{__builtin_amdgcn_perm(b.x, a.x, 0x07060302u), __builtin_amdgcn_perm(b.y, a.y, 0x07060302u),
                   __builtin_amdgcn_perm(d.y, c.y, 0x05040100u), __builtin_amdgcn_perm(d.z, c.z, 0x05040100u)});
        }
      }
      rdy = ld2c(ws, WS_RDY + grp * 1024 + min(tid >> 1, 112) * 8);
      sb();
      const float mean = sum16(s2.x + s2.y) * (1.f / D);
      v2f q2 = {0.f, 0.f};
#pragma unroll
      for (int m = 0; m < 14; ++m) { const v2f d = f[m] - mean; q2 = fma2(d, d, q2); }
      const float rstd = __builtin_amdgcn_rsqf(sum16(q2.x + q2.y) * (1.f / D) + 1e-5f);
      if (c16 == 0) *(LAS v2f*)(L + L_ST + (4 * wave + g) * 8) = v2f{mean, rstd};
    }
    pin(pfa); pin(pfb);
    epoch = __builtin_amdgcn_readfirstlane(epoch);
    tag = mktag(epoch);
    STAMP(22);
    bar();
    v4f n[4][2];
    const int d0 = 8 * (4 * wave + g);
    const v2f st = *(const LAS v2f*)(L + L_ST + c16 * 8);
    {
      int xr_ = L_XS + c16 * XP + d0 * 2, xb_ = L_XS + c16 * XP + (216 - d0) * 2, x0_ = d0 == 0 ? xr_ : xb_ + 464;
      pin(xr_); pin(xb_); pin(x0_);
      const v4u a = lds4(L + xr_, 0), e = lds4(L + xr_, 448), c = lds4(L + xb_, 0), b = lds4(L + xb_, 448);
      const u32 c0 = *(const LAS u16*)(L + xb_ + 16), b0 = *(const LAS u16*)(L + x0_);
#pragma unroll
      for (int i = 0; i < 8; ++i) {
        n[0][i >> 2][i & 3] = (i & 1) ? bhi(a[i >> 1]) : blo(a[i >> 1]);
        n[3][i >> 2][i & 3] = (i & 1) ? bhi(e[i >> 1]) : blo(e[i >> 1]);
        n[1][i >> 2][i & 3] = i == 0 ? blo(b0) : (i & 1) ? bhi(b[(8 - i) >> 1]) : blo(b[(8 - i) >> 1]);
        n[2][i >> 2][i & 3] = i == 0 ? blo(c0) : (i & 1) ? bhi(c[(8 - i) >> 1]) : blo(c[(8 - i) >> 1]);
      }
      const float nm = -st.x * st.y;
#pragma unroll
      for (int m = 0; m < 4; ++m) {
#pragma unroll
        for (int h = 0; h < 2; ++h) { n[m][h] = fma4(n[m][h], splat(st.y), splat(nm)); pin(n[m][h]); }
      }
    }
    sb();
    STAMP(1);
    int mb = L_M1 + d0 * 4;
    pin(mb);
    v4u xs[4];
    int to[4];
    to[0] = WS_T1 + blk * T1_BLK + (16 * t0 + c16) * 64 + g * 16;
    to[1] = wave != 0 ? to[0] + 1024 : c16 < 2 ? to[0] + 112 * 64 : OOB;
    to[2] = to[0] + 114 * 64;
    to[3] = wave != 0 ? to[0] + 1024 + 114 * 64 : OOB;
#pragma unroll
    for (int q = 0; q < 4; ++q) pin(to[q]);

    {
      const int kind = tid >= 112, d = 4 * (tid - 112 * kind);
      int wb = L_M1 + (kind ? 0 : 2 * 1792) + d * 4, wrv = L_M1 + (kind ? 0 : 2 * 1792) + 1792 + (445 - d) * 4;
      int wr0 = d == 0 ? wrv + 12 - 1792 : wrv + 12;
      const int mo = tid < 224 ? WS_MOD + grp * MOD_REP + tid * 16 : OOB;
      pin(wb); pin(wrv); pin(wr0);
      if (!wall(rdy.x == tag && rdy.y == ~tag)) wait_flags(ws, WS_RDY + grp * 1024 + min(tid >> 1, 112) * 8, tag);
      v4u mv;
      _Pragma("nounroll") for (int s = 0; s < SPIN; ++s) {
        mv = ld4c(ws, fresh(mo));
        if (wall(tid >= 224 || (mv.y == tag && mv.w == tag))) break;
        __builtin_amdgcn_s_sleep(SL1);
      }
      STAMP(2);
      if (tid < 224) {
        const v4f v = {blo(mv.x), bhi(mv.x), blo(mv.z), bhi(mv.z)};
        *(LAS v4f*)(L + wb) = v;
        *(LAS float*)(L + wr0) = v.x;
        *(LAS float*)(L + wrv + 8) = v.y;
        *(LAS float*)(L + wrv + 4) = v.z;
        *(LAS float*)(L + wrv) = v.w;
      }
    }
    bar();
    {
      v4u fr[4];
#pragma unroll
      for (int hh = 0; hh < 2; ++hh) {
        v4f h[4];
#pragma unroll
        for (int m = 0; m < 4; ++m) {
          const int o = ((m == 1 || m == 2) ? 1792 : 0) + (m >= 2 ? 896 : 0) + hh * 16;
          h[m] = fma4(n[m][hh], lds4f(L + mb, o), lds4f(L + mb, 2 * 1792 + o));
        }
        const v4f p = h[0] + h[1], q = h[2] + h[3], u = h[0] - h[1], v = h[3] - h[2];
        const v4f t[4] = {p + q, p - q, u + v, u - v};
#pragma unroll
        for (int m = 0; m < 4; ++m) { fr[m][2 * hh] = pk2(lo2(t[m])); fr[m][2 * hh + 1] = pk2(hi2(t[m])); }
      }
#pragma unroll
      for (int m = 0; m < 4; ++m) *(LAS v4u*)(LB + wave * 1024 + L_AF + m * 4096) = fr[m];
    }
    bar();
    {
      v4u a[4][4];
#pragma unroll
      for (int p = 0; p < 2; ++p)
#pragma unroll
        for (int s = 0; s < 4; ++s) { a[p][s] = lds4(LB, L_AF + (p * 4 + s) * 1024); a[2 + p][s] = lds4(LB, L_AF + ((2 + p) * 4 + s) * 1024); }
      v4f re[4], im[4];
#pragma unroll
      for (int q = 0; q < 5; ++q) {
        if (q < 4) mf8(re[q], im[q], a[q >> 1], cf[4 * (q & 1) + (q >> 1)], a[2 + (q >> 1)], cf[4 * (q & 1) + 2 + (q >> 1)]);
        if (q > 0) {
          if (q < 4) mfence<0>(re[q - 1], im[q - 1]); else mfence<7>(re[3], im[3]);
          xs[q - 1] = v4u{pk(re[q - 1].x, im[q - 1].x), pk(re[q - 1].y, im[q - 1].y), pk(re[q - 1].z, im[q - 1].z), pk(re[q - 1].w, im[q - 1].w)};
          st4c(ws, to[q - 1], xs[q - 1]);
          sb();
        }
      }
    }
    STAMP(3);
    sb();
    sb();
    if (isG) dma_w1(wg, wu, slice, wave, lane, lbase);
#pragma unroll
    for (int q = 0; q < 4; ++q) {
      const int vo = (q < 2 || wave != 0) ? l16 : OOB;
#pragma unroll
      for (int s = 0; s < 4; ++s) cf[2 * q + 1][s] = ld4(tab, vo + s * 1024, (TAB_P3 + 2 * t0 + q) * 4096);
    }
#pragma unroll
    for (int m = 0; m < 4; ++m) {
      st4c(ws, tid + 256 * m < 896 ? WS_H2 + blk * H2_BLK + 4096 * m + tid * 16 : OOB, sv);
    }
    sb();
    STAMP(20);
    v4f xo[2][4];
    int xa = L_XS + c16 * XP + (16 * t0 + 4 * g) * 2, xd = L_XS + c16 * XP + (192 - 16 * t0 - 4 * g) * 2;
    int xs1 = tid < 16 ? L_XS + c16 * XP + 224 : xd + 512, xs3 = tid < 16 ? L_XS + c16 * XP + 672 : xa + 448;
    pin(xa); pin(xd); pin(xs1); pin(xs3);
#define XOFF(t, m, r) ((t) == 0 && (r) == 0 && (m) == 1 ? xs1 : (t) == 0 && (r) == 0 && (m) == 3 ? xs3 : \
    (m) == 0 ? xa + 32 * (t) + 2 * (r) : (m) == 3 ? xa + 448 + 32 * (t) + 2 * (r) : (m) == 1 ? xd + 512 - 32 * (t) - 2 * (r) : xd + 64 - 32 * (t) - 2 * (r))
    {
      v2u w[2][4]; u32 e[2][2], e3;
#pragma unroll
      for (int t = 0; t < 2; ++t) {
        w[t][0] = *(const LAS v2u*)(L + xa + 32 * t); w[t][3] = *(const LAS v2u*)(L + xa + 448 + 32 * t);
        w[t][1] = *(const LAS v2u*)(L + xd + 504 - 32 * t); w[t][2] = *(const LAS v2u*)(L + xd + 56 - 32 * t);
        e[t][0] = *(const LAS u16*)(L + XOFF(t, 1, 0)); e[t][1] = *(const LAS u16*)(L + XOFF(t, 2, 0));
      }
      e3 = *(const LAS u16*)(L + xs3);
      sb();
#pragma unroll
      for (int t = 0; t < 2; ++t) {
        xo[t][0] = v4f{blo(w[t][0].x), bhi(w[t][0].x), blo(w[t][0].y), bhi(w[t][0].y)};
        xo[t][3] = v4f{t == 0 ? blo(e3) : blo(w[t][3].x), bhi(w[t][3].x), blo(w[t][3].y), bhi(w[t][3].y)};
        xo[t][1] = v4f{blo(e[t][0]), bhi(w[t][1].y), blo(w[t][1].y), bhi(w[t][1].x)};
        xo[t][2] = v4f{blo(e[t][1]), bhi(w[t][2].y), blo(w[t][2].y), bhi(w[t][2].x)};
#pragma unroll
        for (int m = 0; m < 4; ++m) { xo[t][m] -= st.x; pin(xo[t][m]); }
      }
    }
    int m0_[2], m1_[2], md_[2];
#pragma unroll
    for (int r = 0; r < 2; ++r) {
      const int q = r == 0 || tid < 80 ? tid + 256 * r : tid;
      const int kind = (q * 586) >> 16, d = 4 * (q - 112 * kind);
      const int reg = (d >= 112) + (d >= 224) + (d >= 336), cc = ((reg + 1) >> 1) * 224, m1 = (0x1320 >> (4 * reg)) & 3;
      const int d1 = abs(d + 1 - cc), d0_ = abs(d - cc), edge = d == 112 * reg && reg > 0;
      m1_[r] = L_M2 + kind * 1792 + (((d1 >> 4) << 6) | (m1 << 4) | (d1 & 15)) * 4;
      m0_[r] = L_M2 + kind * 1792 + (edge ? reg << 4 : ((d0_ >> 4) << 6) | (m1 << 4) | (d0_ & 15)) * 4;
      md_[r] = (reg & 1) ? -4 : 4;
      pin(m0_[r]); pin(m1_[r]); pin(md_[r]);
    }
    {
      v4u mv[2];
      _Pragma("nounroll") for (int s = 0; s < SPIN; ++s) {
        mv[0] = ld4c(ws, fresh(WS_MOD + grp * MOD_REP + (224 + tid) * 16));
        mv[1] = ld4c(ws, fresh(WS_MOD + grp * MOD_REP + (224 + (tid < 80 ? 256 + tid : tid)) * 16));
        const v2u rv = ld2c(ws, fresh(WS_DRDY + grp * 1024 + min(lane, 48) * 8));
        if (wall(mv[0].y == tag && mv[0].w == tag && mv[1].y == tag && mv[1].w == tag && rv.x == tag && rv.y == ~tag)) break;
        __builtin_amdgcn_s_sleep(SL4);
      }
      if (lane < (CLR_MODE == 1 ? 2 : 1)) st2c(ws, lane == 0 ? WS_HRDY + blk * 32 + wave * 8 : isG ? WS_CRDY + (tile * 4 + (grp & 3)) * 256 + (ct * 4 + wave) * 8 : OOB, v2u{tag, ~tag});
      STAMP(12);
#pragma unroll
      for (int f = 0; f < WD_EARLY; ++f) wdn[f >> 2][f & 3] = ld4c(ws, (isG ? wl16 : OOB) + (f & 3) * 1024, wdb + (f >> 2) * 4096);
      sb();
#pragma unroll
      for (int r = 0; r < 2; ++r) {
        const float sc = m0_[r] < L_M2 + 1792 ? 2.f / (float)(S * D) : 1.f;
        *(LAS float*)(L + m0_[r]) = blo(mv[r].x) * sc;
        *(LAS float*)(L + m1_[r]) = bhi(mv[r].x) * sc;
        *(LAS float*)(L + m1_[r] + md_[r]) = blo(mv[r].z) * sc;
        *(LAS float*)(L + m1_[r] + 2 * md_[r]) = bhi(mv[r].z) * sc;
      }
    }
    bar();
    *(LAS v4u*)(L + L_AF + (tid >> 6) * 4096 + 3072 + l16) = v4u{0u, 0u, 0u, 0u};
    STAMP(4);
    int zr = L_AF + lane * 8, ro = L_M2 + (t0 * 64 + 4 * g) * 4, ro1 = wave == 0 ? ro : ro + 256, ex = L_EX + c16 * 8;
    pin(zr); pin(ro); pin(ro1); pin(ex);
    {
      int zl[4];
#pragma unroll
      for (int m = 0; m < 4; ++m) {
        const int kq = (m >> 1) * 128 + (to[m] == OOB ? 127 : wave == 0 && m == 1 ? 112 + c16 : 16 * (t0 + (m & 1)) + c16);
        zl[m] = L_AF + ((kq & ~31) + ((kq & 4) << 2) + ((kq & 24) >> 1) + (kq & 3)) * 32 + g * 8;
        pin(zl[m]);
        if (to[m] == OOB) xs[m] = sv;
      }
      bar();
      if (isG) wait_flags(ws, hro, tag);
      {
        const int src = (blk * 5 + wave * 13) & 63;
        const int po = __builtin_amdgcn_readlane(to[(0)], 0) + (src & 15) * 64 + (src >> 4) * 16;
        const u32 old = __builtin_amdgcn_ds_bpermute(src * 4, xs[0].x);
        _Pragma("nounroll") for (int s = 0; s < SPIN; ++s) {
          if (wall(ld1c(ws, fresh(po)) != old)) break;
          __builtin_amdgcn_s_sleep(SL2);
        }
      }
      STAMP(13);
      v4u z[4];
      _Pragma("nounroll") for (int s = 0; s < SPIN; ++s) {
        u32 mn = 1;
#pragma unroll
        for (int m = 0; m < 4; ++m) z[m] = ld4c(ws, fresh(to[m]));
#pragma unroll
        for (int m = 0; m < 4; ++m) { const v4u d = z[m] ^ xs[m]; mn = min(min(mn, d.x), min(min(d.y, d.z), d.w)); }
        if (wall(mn != 0)) break;
      }
      STAMP(14);
#pragma unroll
      for (int m = 0; m < 4; ++m) {
        char LAS* p = L + zl[m];
        *(LAS v2u*)(p) = v2u{__builtin_amdgcn_perm(z[m].y, z[m].x, 0x05040100u), __builtin_amdgcn_perm(z[m].w, z[m].z, 0x05040100u)};
        *(LAS v2u*)(p + ZPL) = v2u{__builtin_amdgcn_perm(z[m].y, z[m].x, 0x07060302u), __builtin_amdgcn_perm(z[m].w, z[m].z, 0x07060302u)};
      }
    }
    bar();
    STAMP(5);
    {
      v4u zf[4][4];
#pragma unroll
      for (int ty = 0; ty < 4; ++ty)
#pragma unroll
        for (int s = 0; s < 4; ++s) zf[ty][s] = ldtr2(L + zr, ty * 4096 + s * 1024, ty * 4096 + s * 1024 + 512);
      v4f acc[2][4], x2[2][4], gav[2][4];
      v2f s1 = {0.f, 0.f}, s2 = {0.f, 0.f};
#define P3M(t, ty) mf8(acc[t][ty], acc[t][ty + 1], cf[4 * t + ty], zf[ty], cf[4 * t + ty + 1], zf[ty + 1]);
#define P3X(t, m, yv) { x2[t][m] = fma4(gav[t][m], yv, xo[t][m]); \
      if (t == 0 || wave != 0) { s1 += lo2(x2[t][m]) + hi2(x2[t][m]); \
        s2 = fma2(lo2(x2[t][m]), lo2(x2[t][m]), s2); s2 = fma2(hi2(x2[t][m]), hi2(x2[t][m]), s2); } }
      P3M(1, 0) P3M(1, 2)
#pragma unroll
      for (int m = 0; m < 4; ++m) { gav[0][m] = lds4f(L + ro, m * 64); gav[1][m] = lds4f(L + ro1, m * 64); }
      sb();
      P3M(0, 0) P3M(0, 2)
      mfence<0>(acc[1][0], acc[1][1], acc[1][2], acc[1][3]);
      const v4f A1 = acc[1][0] + acc[1][1], B1 = acc[1][0] - acc[1][1], P1 = acc[1][3] - acc[1][2], Q1 = acc[1][2] + acc[1][3];
      P3X(1, 0, A1 - P1) P3X(1, 1, A1 + P1) P3X(1, 2, B1 - Q1) P3X(1, 3, B1 + Q1)
      sb();
      mfence<0>(acc[0][0], acc[0][1], acc[0][2], acc[0][3]);
      {
        const v4f A = acc[0][0] + acc[0][1], B = acc[0][0] - acc[0][1], P = acc[0][3] - acc[0][2], Q = acc[0][2] + acc[0][3];
        v4f y[4] = {A - P, A + P, B - Q, B + Q};
        const bool sp = tid < 16;
        y[0].x = sp ? A.x : y[0].x; y[2].x = sp ? B.x : y[2].x;
        y[1].x = sp ? acc[1][0].x - acc[0][3].x : y[1].x; y[3].x = sp ? acc[1][0].x + acc[0][3].x : y[3].x;
        P3X(0, 0, y[0]) P3X(0, 1, y[1]) P3X(0, 2, y[2]) P3X(0, 3, y[3])
      }
      float sm = s1.x + s1.y, sq = s2.x + s2.y;
      sumg2(sm, sq);
      if (g == 0) *(LAS v2f*)(L + ex + wave * 128) = v2f{sm, sq};
      v4f sbv[2][4], cbv[2][4];
#pragma unroll
      for (int m = 0; m < 4; ++m) {
        sbv[0][m] = lds4f(L + ro, 1792 + m * 64); cbv[0][m] = lds4f(L + ro, 2 * 1792 + m * 64);
        sbv[1][m] = lds4f(L + ro1, 1792 + m * 64); cbv[1][m] = lds4f(L + ro1, 2 * 1792 + m * 64);
      }
      bar();
      v2f e = *(const LAS v2f*)(L + ex);
#pragma unroll
      for (int w = 1; w < 4; ++w) e += *(const LAS v2f*)(L + ex + w * 128);
      const float mc = e.x * (1.f / D), rstd = __builtin_amdgcn_rsqf(fmaxf(e.y * (1.f / D) - mc * mc, 0.f) + 1e-5f), nb = -mc * rstd;
      const int ho = WS_H2 + blk * H2_BLK + 2 * t0 * 1024 + l16, ho1 = wave == 0 ? OOB : ho + 2048;
#pragma unroll
      for (int t = 0; t < 2; ++t)
#pragma unroll
        for (int a = 0; a < 2; ++a) {
          v4u o;
#pragma unroll
          for (int hh = 0; hh < 2; ++hh) {
            const int m = 2 * a + hh;
            const v4f hv = fma4(fma4(x2[t][m], splat(rstd), splat(nb)), cbv[t][m], sbv[t][m]);
            o[2 * hh] = pk2(lo2(hv)); o[2 * hh + 1] = pk2(hi2(hv));
          }
          st4c(ws, (t ? ho1 : ho) + a * 1024, o);
        }
      STAMP(6);
      sb();
#pragma unroll
      for (int f = WD_EARLY; f < WD_MID; ++f) wdn[f >> 2][f & 3] = ld4c(ws, (isG ? wl16 : OOB) + (f & 3) * 1024, wdb + (f >> 2) * 4096);
      sb();
#pragma unroll
      for (int e = 0; e < 8; ++e) {
        x2r[e >> 1][2 * (e & 1)] = pk2(lo2(x2[e >> 2][e & 3]) + st.x); x2r[e >> 1][2 * (e & 1) + 1] = pk2(hi2(x2[e >> 2][e & 3]) + st.x);
      }
#pragma unroll
      for (int m = 0; m < 4; ++m) pin(x2r[m]);
    }
    bar();
    if (!isG) {
      x2_store(ws, x2r, blk, wave, l16);
      if (blockIdx.x == 224 && tid == 0) __builtin_amdgcn_raw_buffer_store_b32(epoch + 1u, ws, WS_EPOCH, 0, SC1);
      drain();
      if (lane == 0) st2c(ws, WS_XRDY + blk * 64 + wave * 8, v2u{tag, ~tag});
      DUMP(); return;
    }
  } else {
    const int sj = j >= 30 ? j - 30 : j + 2;
    const int idx = sj * 8 + grp;
    const bool isC = j < 14 || idx == 7;
    const int pair = j < 14 ? grp * 14 + j : 112;
    const int lc = grp + 8 * (j >> 1);
    const bool urgent = j < 4;
    const int qu = urgent ? (lc < 14 ? 2 * lc + (j & 1) : -1) : sj < 2 && grp < 7 ? 28 + grp * 6 + sj : -1;
    const int t1o = WS_T1 + (32 * wave + (lane >> 3)) * T1_BLK + pair * 128 + (lane & 7) * 16;
    ModLoad ml;
    epoch = ld1c(ws, WS_EPOCH);
#pragma unroll
    for (int q = 0; q < 4; ++q) st4c(ws, isC ? t1o + 8 * q * T1_BLK : OOB, sv);
    sb();
    mod_load(ml, cin, wm, qu, tid);
    u32 pf0, pf1, pf2;
    {
      const rsrc_t mr = mkrs(wm, D * 6 * D * 2), xr = mkrs(xin, S * D * 2), fr_ = mkrs((wave & 1) ? wi : wr, S * DF * 2);
      const int row = (sj * 4 + wave) * 7 + (lane >> 1), l7 = lane / 7;
      pf0 = __builtin_amdgcn_raw_buffer_load_b32(mr, lane < 14 && (grp < 6 || !(lane & 1)) ? row * (6 * D * 2) + (grp + 8 * (lane & 1)) * 128 : OOB, 0, PF_AUX);
      pf1 = __builtin_amdgcn_raw_buffer_load_b32(xr, lane < (sj < 6 ? 0 : sj < 10 ? 28 : 56) ? tok_row(16 * grp + (lane < 28 ? sj : sj - 10), 4 * wave + (l7 & 3)) * (D * 2) + (lane - 7 * l7) * 128 : OOB, 0, PF_AUX);
      pf2 = __builtin_amdgcn_raw_buffer_load_b32(fr_, (lane & 31) < 29 && lane < (sj < 6 ? 0 : sj < 10 ? 32 : 64) ? (((16 * grp + (lane < 32 ? sj : sj - 10)) * 7200) & ~127) + ((wave >> 1) * 29 + (lane & 31)) * 128 : OOB, 0, PF_AUX);
    }
    sb();
    asm volatile("s_waitcnt vmcnt(%0)" :: "i"(W_LATE ? 16 : 17) : "memory");
    epoch = __builtin_amdgcn_readfirstlane(epoch);
    tag = mktag(epoch);
    bar();
    if (wave == 1 && lane < 8) st2c(ws, isC ? WS_RDY + lane * 1024 + pair * 8 : OOB, v2u{tag, ~tag});
    if (qu >= 0) {
      mod_part(ml, L, tid);
      bar();
      if (wave == 0) mod_publish(L, ws, qu, lane, tag);
    }
    STAMP(1);
    pin(pf0); pin(pf1); pin(pf2); pin(pfa); pin(pfb);
    const int qn = grp == 7 ? -1 : sj >= 6 && sj < 10 ? 28 + grp * 6 + (sj - 4) : sj < 2 ? 70 + grp * 2 + sj : -1;
    const int tw = sj >= 10 ? (sj - 10) * 8 + grp : sj == 6 && grp == 7 ? 48 : -1;
    v2u dw[2][8];
    if (qn >= 0) mod_load(ml, cin, wm, qn, tid);
    if (tw >= 0) {
      const rsrc_t dr = mkrs(wd, F * D * 2);
      const int u0 = tw * 4 + wave, uc = u0 / 28, kap = 2 * (u0 - uc * 28);
#pragma unroll
      for (int e = 0; e < 16; ++e)
        dw[e >> 3][e & 7] = __builtin_amdgcn_raw_buffer_load_b64(dr, (4 * g * D + 4 * c16) * 2 + (e & 3) * D * 2, ((32 * kap + 16 * (e >> 2)) * D + 64 * uc) * 2, PF_AUX);
    }
    sb();
    u32 pw[4];
    {
      const rsrc_t rg = mkrs(wg, D * F * 2), ru = mkrs(wu, D * F * 2);
      const int nr = sj < 2 ? NR_S : sj < 6 ? NR_U : sj < 10 ? NR_M : NR_T;
      const int r0 = (sj < 2 ? sj * NR_S : sj < 6 ? 2 * NR_S + (sj - 2) * NR_U : sj < 10 ? 2 * NR_S + 4 * NR_U + (sj - 6) * NR_M
                                                                             : 2 * NR_S + 4 * NR_U + 4 * NR_M + (sj - 10) * NR_T) * 4 + wave * nr;
      static_assert(2 * NR_S + 4 * NR_U + 4 * NR_M + 6 * NR_T == 112 && NR_S <= 16 && NR_U <= 16, "all rows");
      const int r7 = lane / 7, po = (r0 + r7) * (F * 2) + ((grp & 3) * 7 + lane - r7 * 7) * 128;
      const int o0 = lane < 56 && r7 < nr ? po : OOB, o1 = lane < 56 && r7 + 8 < nr ? po + 8 * (F * 2) : OOB;
      pw[0] = ld1(rg, o0); pw[1] = ld1(ru, o0); pw[2] = ld1(rg, o1); pw[3] = ld1(ru, o1);
    }
    sb();
    STAMP(20);
    v4f t1c[2], t1s[2], t2c[2], t2s[2];
#pragma unroll
    for (int t = 0; t < 2; ++t) {
#pragma unroll
      for (int r = 0; r < 4; ++r) {
        const float a1 = (float)((16 * t + 4 * g + r) * (16 * wave + c16)) * (1.f / S);
        const float a2 = (float)((16 * t + c16) * (16 * wave + 4 * g + r)) * (1.f / S);
        t1c[t][r] = __builtin_amdgcn_cosf(a1); t1s[t][r] = __builtin_amdgcn_sinf(a1);
        t2c[t][r] = __builtin_amdgcn_cosf(a2); t2s[t][r] = __builtin_amdgcn_sinf(a2);
      }
      pin(t1c[t]); pin(t1s[t]); pin(t2c[t]); pin(t2s[t]);
    }
    if (tw >= 0) {
#pragma unroll
      for (int tt = 0; tt < 8; ++tt) {
        v4u o;
#pragma unroll
        for (int m = 0; m < 4; ++m)
          o[m] = __builtin_amdgcn_perm(dw[tt >> 2][2 * m + 1][(tt & 3) >> 1], dw[tt >> 2][2 * m][(tt & 3) >> 1], (tt & 1) ? 0x07060302u : 0x05040100u);
        st4c(ws, WS_WD + (tw * 4 + wave) * 8192 + tt * 1024 + l16, o);
      }
    }
    if (qn >= 0) {
      bar();
      mod_part(ml, L, tid);
      bar();
      if (wave == 0) mod_publish(L, ws, qn, lane, tag);
    }
    pin(pw[0]); pin(pw[1]); pin(pw[2]); pin(pw[3]);
    STAMP(21);
    if (!isC) { DUMP(); return; }
    v4u c64[2][4], c32[2][2][2];
#pragma unroll
    for (int cp = 0; cp < 2; ++cp)
#pragma unroll
      for (int s = 0; s < 4; ++s) c64[cp][s] = ld4(tab, l16 + s * 1024, (TAB_P2 + wave * 2 + cp) * 4096);
#pragma unroll
    for (int t = 0; t < 2; ++t)
#pragma unroll
      for (int f = 0; f < 4; ++f) c32[t][f >> 1][f & 1] = ld4(tab, l16 + f * 1024, (TAB_P2 + 8 + t) * 4096);
    sb();
    if (tw >= 0) {
      asm volatile("s_waitcnt vmcnt(16)" ::: "memory");
      bar();
      if (tid < 8) st2c(ws, WS_DRDY + tid * 1024 + tw * 8, v2u{tag, ~tag});
    }
    STAMP(2);
    int wfo = WS_WT + pair * 16384 + (16 * g + wave) * 128 + c16 * 8;
    pin(wfo);
    v2u wf[2][4];
    {
      int wl = L_CI + ((lane >> 2) & 1) * 8256 + wave * 2048 + ((lane >> 3) * 4 + (lane & 3)) * 16, lo = t1o;
      pin(wl); pin(lo);
      light_poll(ws, WS_T1 + (32 * wave + (pair & 31)) * T1_BLK + pair * 128);
      STAMP(13);
      v4u z[4];
      _Pragma("nounroll") for (int s = 0; s < SPIN; ++s) {
        lo = fresh(lo);
#pragma unroll
        for (int q = 0; q < 4; ++q) z[q] = ld4c(ws, lo, 8 * q * T1_BLK);
        if (wall(max(max(max4(z[0]), max4(z[1])), max(max4(z[2]), max4(z[3]))) != SENT)) break;
      }
      STAMP(14);
#pragma unroll
      for (int q = 0; q < 4; ++q) *(LAS v4u*)(L + wl + q * 512) = z[q];
    }
    bar();
    v2u fd;
    sb();
    STAMP(4);
    {
      v4u a[2][2][4];
#pragma unroll
      for (int c = 0; c < 2; ++c)
#pragma unroll
        for (int mt = 0; mt < 2; ++mt)
#pragma unroll
          for (int s = 0; s < 4; ++s) a[c][mt][s] = lds4(LB, L_CI + c * 8256 + (s * 2 + mt) * 1024);
      v4u old[2][2];
#pragma unroll
      for (int c = 0; c < 2; ++c)
#pragma unroll
        for (int mt = 0; mt < 2; ++mt) old[c][mt] = lds4(LB + wave * 2048, L_CI + c * 8256 + mt * 1024);
      sb();
#pragma unroll
    for (int t = 0; t < 2; ++t)
#pragma unroll
      for (int r = 0; r < 4; ++r) wf[t][r] = ld2c(ws, wfo + r * 512, t * 8192);
    fd = ld2c(ws, WS_DRDY + grp * 1024 + min(lane, 48) * 8);
      sb();
      v4f re[2][2], im[2][2], ur[2][2], ui[2][2], fr_[2][2], fi_[2][2];
      u32 f2[2][2][4], f3[2][2][4], f4[4];
      char LAS* xw = L + lb + wave * 1024 - L_B;
#define V4(x) v4u{x[0], x[1], x[2], x[3]}
#define S1K(h) c64[0][2 * h], c64[0][2 * h + 1], c64[1][2 * h], c64[1][2 * h + 1]
#define S1X(c, t, h) re[c][t], im[c][t], a[c][t][2 * h], a[c][t][2 * h + 1], S1K(h)
#define S2X(c, t) ur[c][t], ui[c][t], V4(f2[c][0]), V4(f2[c][1]), c32[t][0][0], c32[t][0][1], c32[t][1][0], c32[t][1][1]
#define S3X(c, t) im[c][t], re[c][t], V4(f3[c][0]), V4(f3[c][1]), c32[t][0][0], c32[t][0][1], c32[t][1][0], c32[t][1][1]
#define CM(o, P, Q, C, S_, h) o[2 * h], o[2 * h + 1], P[2 * h], Q[2 * h], C[2 * h], S_[2 * h], P[2 * h + 1], Q[2 * h + 1], C[2 * h + 1], S_[2 * h + 1]
#define TW1(c, t, h) CM(f2[c][t], re[c][t], im[c][t], t1c[t], t1s[t], h)
#define FLT(c, t, h) CM(f3[c][t], ur[c][t], ui[c][t], fr_[c][t], fi_[c][t], h)
#define TW2(c, t, h) CM(f4, re[c][t], im[c][t], t2c[t], t2s[t], h)
#define HV(x, h) (h ? hi2(x) : lo2(x))
#define CP(PL, o, P, Q, C, S_, h) cmul2<PL>(o[2 * h], o[2 * h + 1], HV(P, h), HV(Q, h), HV(C, h), HV(S_, h));
#define XW(c, t) *(LAS v4u*)(xw + L_CX + (c * 2 + t) * 4096) = V4(f4);
#define UNP(c, t) { _Pragma("unroll") for (int r = 0; r < 4; ++r) { fr_[c][t][r] = blo(wf[t][r][c]); fi_[c][t][r] = bhi(wf[t][r][c]); } \
      pin(fr_[c][t]); pin(fi_[c][t]); }
      nop2();
      m4_a0(S1X(0, 0, 0)); m4_a1(S1X(0, 0, 1)); m4_a0(S1X(0, 1, 0)); mfence<3>(re[0][0], im[0][0]);
      h4_a1m(S1X(0, 1, 1), TW1(0, 0, 0)); h4_a0m(S1X(1, 0, 0), TW1(0, 0, 1));
      h4_a1m(S1X(1, 0, 1), TW1(0, 1, 0)); h4_a0m(S1X(1, 1, 0), TW1(0, 1, 1));
      h4_a1m(S1X(1, 1, 1), TW1(1, 0, 0)); h4_b0m(S2X(0, 0), TW1(1, 0, 1));
      h4_b0m(S2X(0, 1), TW1(1, 1, 0));
      sb();
      CP(false, f2[1][1], re[1][1], im[1][1], t1c[1], t1s[1], 1)
      UNP(0, 0) UNP(0, 1)
      sb();
      h4_b0p(S2X(1, 0), FLT(0, 0, 0)); h4_b0p(S2X(1, 1), FLT(0, 0, 1));
      sb();
      CP(true, f3[0][1], ur[0][1], ui[0][1], fr_[0][1], fi_[0][1], 0) CP(true, f3[0][1], ur[0][1], ui[0][1], fr_[0][1], fi_[0][1], 1)
      UNP(1, 0) UNP(1, 1)
      sb();
      h4_a0p(S3X(0, 0), FLT(1, 0, 0)); h4_a0p(S3X(0, 1), FLT(1, 0, 1));
      sb();
      CP(true, f3[1][1], ur[1][1], ui[1][1], fr_[1][1], fi_[1][1], 0) CP(true, f3[1][1], ur[1][1], ui[1][1], fr_[1][1], fi_[1][1], 1)
#pragma unroll
      for (int e = 0; e < 4; ++e) pin(f3[1][1][e]);
      nop2();
      h4_a0p(S3X(1, 0), TW2(0, 0, 0)); h4_a0p(S3X(1, 1), TW2(0, 0, 1));
      XW(0, 0)
      sb();
      CP(true, f4, re[0][1], im[0][1], t2c[1], t2s[1], 0) CP(true, f4, re[0][1], im[0][1], t2c[1], t2s[1], 1) XW(0, 1)
      sb();
      mfence<0>(re[1][0], im[1][0], re[1][1], im[1][1]);
      CP(true, f4, re[1][0], im[1][0], t2c[0], t2s[0], 0) CP(true, f4, re[1][0], im[1][0], t2c[0], t2s[0], 1) XW(1, 0)
      CP(true, f4, re[1][1], im[1][1], t2c[1], t2s[1], 0) CP(true, f4, re[1][1], im[1][1], t2c[1], t2s[1], 1) XW(1, 1)
      bar();
#pragma unroll
      for (int c = 0; c < 2; ++c)
#pragma unroll
        for (int nt = 0; nt < 2; ++nt)
#pragma unroll
          for (int s = 0; s < 4; ++s) a[c][nt][s] = lds4(LB, L_CX + ((c * 2 + nt) * 4 + s) * 1024);
      const int o2 = WS_T1 + (32 * wave + (lane >> 2)) * T1_BLK + pair * 128 + (lane & 3) * 16;
#define S4(c, t) mf8(im[c][t], re[c][t], c64[0], a[c][t], c64[1], a[c][t]);
#define NEQ(x, y) ((x) + ((x) == (y)))
#define OUT(c, t) { const v4u o = {pk(re[c][t].x, im[c][t].x), pk(re[c][t].y, im[c][t].y), pk(re[c][t].z, im[c][t].z), pk(re[c][t].w, im[c][t].w)}; \
      st4c(ws, o2 + c * 64 + t * 16 * T1_BLK, v4u{NEQ(o.x, old[c][t].x), NEQ(o.y, old[c][t].y), NEQ(o.z, old[c][t].z), NEQ(o.w, old[c][t].w)}); sb(); }
      S4(0, 0) S4(0, 1) mfence<0>(re[0][0], im[0][0]); OUT(0, 0)
      S4(1, 0) mfence<0>(re[0][1], im[0][1]); OUT(0, 1)
      S4(1, 1) mfence<0>(re[1][0], im[1][0]); OUT(1, 0)
      mfence<0>(re[1][1], im[1][1]); OUT(1, 1)
    }
    STAMP(5);
    sb();
    if (!isG) { DUMP(); return; }
    if (!wall(fd.x == tag && fd.y == ~tag)) wait_flags(ws, WS_DRDY + grp * 1024 + min(lane, 48) * 8, tag);
    dma_w1(wg, wu, slice, wave, lane, lbase);
#pragma unroll
    for (int f = 0; f < WD_MID; ++f) wdn[f >> 2][f & 3] = ld4c(ws, wl16 + (f & 3) * 1024, wdb + (f >> 2) * 4096);
    fd = ld2c(ws, hro);
    sb();
    ACT_CLEAR(0, 16)
    sb();
    if (!wall(fd.x == tag && fd.y == ~tag)) wait_flags(ws, hro, tag);
    bar();
  }

  STAMP(7);
  const int blk0 = 16 * tile + 4 * (grp & 3);
  const int ab = WS_ACT + blk0 * 57344 + wave * 14 * 1024;
  v4u h[14][2];
  {
    const int hb = WS_H2 + (16 * tile + 4 * wave) * H2_BLK;
    int tb[2][4];
#pragma unroll
    for (int u = 0; u < 8; ++u) {
      tb[u >> 2][u & 3] = (((lane >> 2) * 128 + ((lane >> 3) & 3) * 32 + (lane & 3) * 8) ^ ((u & 3) << 5)) + (u >> 2) * 57344;
      pin(tb[u >> 2][u & 3]);
    }
#define W_RD(dst, k, u) dst = ldtr2(L + tb[k >= 7][u & 3], (k % 7) * 8192 + (u >> 2) * 4096, (k % 7) * 8192 + (u >> 2) * 4096 + 2048)
#define H_LD(v, tt) hh_[v][tt] = ld4c(ws, l16 + (((v) % 14) & 3) * 1024, hb + (2 * ((v) / 14) + (tt)) * H2_BLK + (((v) % 14) >> 2) * 4096)
    const int abx[2] = {ab + (wave >> 1) * 57344, ab + (1 - (wave >> 1)) * 57344};
#define G_LD(e) h[(e) >> 1][(e) & 1] = ld4c(ws, l16 + (((e) >> 1) & 3) * 1024, abx[(e) & 1] + ((e) >> 3) * 4096)
    v4u wfr[2][8], hh_[28][2];
#pragma unroll
    for (int u = 0; u < 8; ++u) W_RD(wfr[0][u], 0, u);
    light_poll(ws, hb + (lane & 1) * H2_BLK + 13 * 1024);
    STAMP(15);
#pragma unroll
    for (int v = 0; v < LEAD; ++v) { H_LD(v, 0); H_LD(v, 1); }
    sb();
    if (!isR && lane < (CLR_MODE == 1 ? 1 : 2))
      st2c(ws, WS_CRDY + ((tile + 2 * lane) * 4 + (grp & 3)) * 256 + (ct * 4 + wave) * 8, v2u{tag, ~tag});
    sb();
#define BAD2(x0, x1) wany(max(max(max(max(x0.x, x0.y), x0.z), max(max(x0.w, x1.x), x1.y)), max(x1.z, x1.w)) == SENT)
#define RETRY2(x0, x1, o0, o1) \
      _Pragma("nounroll") for (int s_ = 0; s_ < SPIN; ++s_) { \
        const int lf_ = fresh(l16); SLOWCNT \
        x0 = ld4c(ws, lf_, o0); x1 = ld4c(ws, lf_, o1); \
        if (wall(max(max4(x0), max4(x1)) != SENT)) break; \
      }
    if (__builtin_expect(BAD2(hh_[0][0], hh_[0][1]), 0)) { RETRY2(hh_[0][0], hh_[0][1], hb, hb + H2_BLK) }
    STAMP(8); CSTAMP(28);
    v4f acc[8][2];
    v2u fa;
    const int fx = WS_CRDY + (tile * 4 + (lane < 32 ? wave : grp & 3)) * 256 + min(lane & 31, 27) * 8;
    const int ao = WS_ACT + (16 * tile + 4 * wave) * 57344 + 2 * slice * 1024 + l16;
#define ACT_OUT(ps, tt, f) { v4u o; \
      _Pragma("unroll") for (int hh = 0; hh < 2; ++hh) { \
        const v4f av = relu4(acc[2 * f + hh][tt]) * acc[4 + 2 * f + hh][tt]; \
        o[2 * hh] = pk2(lo2(av)); o[2 * hh + 1] = pk2(hi2(av)); } \
      st4c(ws, ao + (2 * ps + tt) * 57344 + f * 1024, o); }
#define ACT_ALL(ps) { mfence<0>(acc[0][0], acc[1][0], acc[2][0], acc[3][0]); mfence<0>(acc[4][0], acc[5][0], acc[6][0], acc[7][0]); \
      ACT_OUT(ps, 0, 0) ACT_OUT(ps, 0, 1) sb(); \
      mfence<0>(acc[0][1], acc[1][1], acc[2][1], acc[3][1]); mfence<0>(acc[4][1], acc[5][1], acc[6][1], acc[7][1]); \
      ACT_OUT(ps, 1, 0) ACT_OUT(ps, 1, 1) sb(); }
#define W_RH(dst, k, u, hf) { const v2u t_ = ldtr(L + tb[k >= 7][u & 3], (k % 7) * 8192 + (u >> 2) * 4096 + hf * 2048); \
      dst[2 * hf] = t_.x; dst[2 * hf + 1] = t_.y; }
#pragma unroll
    for (int ps = 0; ps < 2; ++ps)
#pragma unroll
    for (int ks = 0; ks < 14; ++ks) {
      const int v = ps * 14 + ks, cur = v & 1, nk = (v + 1) % 14, vn = v < 27 ? v + 1 : v;
      const bool more = v < 27;
      u32 m0, m1;
      __builtin_amdgcn_s_waitcnt(0xC07F);
      sb();
#pragma unroll
      for (int i = 0; i < 16; ++i) {
        const int u = i & 7, tt = i >> 3;
        if (ks == 0) mv0(acc[u][tt], wfr[cur][u], hh_[v][tt]); else mv(acc[u][tt], wfr[cur][u], hh_[v][tt]);
        if (more) {
          if (i < 4) { W_RH(wfr[cur ^ 1][i], nk, i, 0) W_RH(wfr[cur ^ 1][i], nk, i, 1) }
          else if (i < 12) W_RH(wfr[cur ^ 1][4 + ((i - 4) >> 1)], nk, (4 + ((i - 4) >> 1)), (i & 1))
        }
        if (i == 5 && v + LEAD < 28) H_LD(v + LEAD, 0);
        if (i == 7 && v + LEAD < 28) H_LD(v + LEAD, 1);
        if (i == 9 || i == 11 || i == 13) {
          const int e = (i - 9) >> 1, f = WD_MID + (v < 14 ? v - 2 : 12 + WD_PER * (v - 14) + e);
          if (f < 56 && ((v >= 2 && v < 14 && e == 0) || (v >= 14 && e < WD_PER))) wdn[f >> 2][f & 3] = ld4c(ws, wl16 + (f & 3) * 1024, wdb + (f >> 2) * 4096);
        }
        if (v > A_V && (i & 1) && i >= 5 && ((i - 5) >> 1) < A_PER) G_LD(A_PER * (v - A_V - 1) + ((i - 5) >> 1));
        if (v == 8 && i == 13) fa = ld2c(ws, fx);
        if (more && i == 12) { m0 = max(max(hh_[vn][0].x, hh_[vn][0].y), hh_[vn][0].z); pin(m0); }
        if (more && i == 13) { m1 = max(max(hh_[vn][0].w, hh_[vn][1].x), hh_[vn][1].y); pin(m1); }
        if (more && i == 14) { m0 = max(max(m0, hh_[vn][1].z), hh_[vn][1].w); m0 = max(m0, m1); pin(m0); }
        sb();
      }
      if (v == 12 && !wall(fa.x == tag && fa.y == ~tag)) wait_flags(ws, fx, tag);
      if (v == 19 && isR) {
        static_assert(19 + LEAD < 28, "the count below");
        asm volatile("s_waitcnt vmcnt(12)" ::: "memory");
        if (lane == 0) st2c(ws, WS_XRDY + (16 * grp + j - 14) * 64 + wave * 8, v2u{tag, ~tag});
      }
      if (more && __builtin_expect(wany(m0 == SENT), 0)) {
        const int pb = 2 * ((v + 1) / 14) * H2_BLK + nk * 1024;
        RETRY2(hh_[vn][0], hh_[vn][1], hb + pb, hb + pb + H2_BLK)
      }
      if (v == 13) {
        STAMP(16); CSTAMP(29);
        ACT_ALL(0)
        if (isR) x2_store(ws, x2r, 16 * grp + j - 14, wave, l16);
        nop2();
      }
      sb();
    }
    STAMP(27);
    ACT_ALL(1)
    CSTAMP(30);
#pragma unroll
    for (int e = A_PER * (27 - A_V); e < 28; ++e) G_LD(e);
    sb();
    STAMP(9);
    v4u a[14][2];
    v4f ac2[4][4];
#define A_LD(e) a[(e) >> 1][(e) & 1] = ld4c(ws, l16 + (((e) >> 1) & 3) * 1024, abx[(e) & 1] + 2 * 57344 + ((e) >> 3) * 4096)
    const int po = ab + 3 * 57344 + min(lane, 6) * 2048 + 1024;
#define KN (ks < 13 ? ks + 1 : ks)
    u32 probe;
#pragma unroll
    for (int ks = 0; ks < 14; ++ks) {
      if (ks == 0 && __builtin_expect(BAD2(h[0][0], h[0][1]), 0)) { RETRY2(h[0][0], h[0][1], abx[0], abx[1]) }
#pragma unroll
      for (int tt = 0; tt < 2; ++tt)
#pragma unroll
        for (int n = 0; n < 4; ++n) ac2[tt][n] = mfma(wdn[ks][n], h[ks][tt], ks ? ac2[tt][n] : splat(0.f));
      const bool bad = ks < 13 && BAD2(h[KN][0], h[KN][1]);
      if (ks == P_AT) probe = ld1c(ws, po);
#pragma unroll
      for (int u = 0; u < 8; ++u) { __builtin_amdgcn_sched_group_barrier(0x8, 1, 0); if (u >= 2) __builtin_amdgcn_sched_group_barrier(0x2, 1, 0); }
      sb();
      if (__builtin_expect(bad, 0)) { RETRY2(h[KN][0], h[KN][1], abx[0] + KN * 1024, abx[1] + KN * 1024) }
      if (ks == B_AT2) {
        STAMP(25);
        if (wany(probe == SENT)) light_poll(ws, po);
        STAMP(26);
      }
      if (ks >= B_AT2) {
#pragma unroll
        for (int e = 4 * (ks - B_AT2); e < 4 * (ks - B_AT2 + 1); ++e) A_LD(e);
      }
      sb();
    }
    STAMP(17);
#pragma unroll
    for (int e = 4 * (14 - B_AT2); e < 4 * (14 - B_AT2) + 6; ++e) A_LD(e);
    sb();
    int rw[3], rr_ = L_B + lane * 8 + wave * 6144, oo[2];
#pragma unroll
    for (int x = 1; x < 4; ++x) { rw[x - 1] = L_B + lane * 8 + (wave ^ x) * 6144 + (x - 1) * 2048; pin(rw[x - 1]); }
    pin(rr_);
    const int blk = blk0 + (wave >> 1);
#pragma unroll
    for (int ph = 0; ph < 2; ++ph) { oo[ph] = (tok_row(blk + 2 * ph, c16) * D + ct * 64 + g * 16 + (wave & 1) * 8) * 2; pin(oo[ph]); }
#define RED_W1(ph, x, n) *(LAS v2f*)(L + rw[(x) - 1] + (ph) * 24576 + (n) * 512) = ((x) & 1) ? hi2(ac2[2 * (ph) + ((x) >> 1)][n]) : lo2(ac2[2 * (ph) + ((x) >> 1)][n]);
    const int n0 = ct * 64 + g * 16 + (wave & 1) * 8, reg = (n0 >= 112) + (n0 >= 224) + (n0 >= 336), xm = (0x1320 >> (4 * reg)) & 3;
    const int dz = abs(n0 - ((reg + 1) >> 1) * 224), db = (reg & 1) ? dz - 8 : dz;
#define XSLOT(m, dl) ((((dl) >> 4) * 2 + ((m) >> 1)) * 1024 + (((dl) >> 2) & 3) * 256 + ((m) & 1) * 8)
    int xo = WS_X2 + blk * X2_BLK + c16 * 16 + XSLOT(xm, db);
    int xe = WS_X2 + blk * X2_BLK + c16 * 16 + (n0 == 112 * reg && reg > 0 ? XSLOT(reg, 0) : XSLOT(xm, dz));
    pin(xo); pin(xe);
    const int xf = WS_XRDY + (blk + (lane & 4 ? 2 : 0)) * 64 + (lane & 3) * 8;
    v2u xp[2][2]; u32 x1[2];
    const int go = WS_MOD + grp * MOD_REP + (70 + 2 * ct) * 128 + g * 64 + (wave & 1) * 32;
    v4u xv[2], gv[2];
    STAMP(18);
    sb();
#pragma unroll
    for (int ks = 0; ks < 14; ++ks) {
      if (ks == 0 && __builtin_expect(BAD2(a[0][0], a[0][1]), 0)) { RETRY2(a[0][0], a[0][1], abx[0] + 2 * 57344, abx[1] + 2 * 57344) }
#pragma unroll
      for (int tt = 0; tt < 2; ++tt)
#pragma unroll
        for (int n = 0; n < 4; ++n) ac2[2 + tt][n] = mfma(wdn[ks][n], a[ks][tt], ks ? ac2[2 + tt][n] : splat(0.f));
#pragma unroll
      for (int e = 4 * (14 - B_AT2) + 6 + 3 * ks; e < 4 * (14 - B_AT2) + 9 + 3 * ks; ++e)
        if (e < 28) A_LD(e);
      const bool bad = ks < 13 && BAD2(a[KN][0], a[KN][1]);
      if (ks < 12) RED_W1(0, 1 + (ks >> 2), (ks & 3))
#pragma unroll
      for (int u = 0; u < 4; ++u) {
        __builtin_amdgcn_sched_group_barrier(0x8, 2, 0);
        __builtin_amdgcn_sched_group_barrier(0x20, 1, 0);
        if (u == 2) __builtin_amdgcn_sched_group_barrier(0x200, 1, 0);
        if (u >= 1) __builtin_amdgcn_sched_group_barrier(0x2, 3, 0);
      }
      sb();
      if (__builtin_expect(bad, 0)) { RETRY2(a[KN][0], a[KN][1], abx[0] + 2 * 57344 + KN * 1024, abx[1] + 2 * 57344 + KN * 1024) }
      if (ks == 2) fa = ld2c(ws, xf);
      if (ks == 7) {
        if (!wall(fa.x == tag && fa.y == ~tag)) wait_flags(ws, xf, tag);
#pragma unroll
        for (int ph = 0; ph < 2; ++ph) {
          xp[ph][0] = ld2c(ws, xo, ph * 2 * X2_BLK); xp[ph][1] = ld2c(ws, xo + 256, ph * 2 * X2_BLK);
          x1[ph] = __builtin_amdgcn_raw_buffer_load_b16(ws, xe, ph * 2 * X2_BLK, SC1);
        }
        gv[0] = ld4c(ws, go); gv[1] = ld4c(ws, go + 16);
      }
      sb();
    }
    STAMP(10);
#pragma unroll
    for (int e = 0; e < 12; ++e) RED_W1(1, 3 - (e >> 2), (e & 3))
#define FIN_OK() wall(gv[0].y == tag && gv[0].w == tag && gv[1].y == tag && gv[1].w == tag)
    if (__builtin_expect(!FIN_OK(), 0)) {
      _Pragma("nounroll") for (int s = 0; s < SPIN; ++s) {
        const int gf = fresh(go);
        gv[0] = ld4c(ws, gf); gv[1] = ld4c(ws, gf + 16);
        if (FIN_OK()) break;
      }
    }
#pragma unroll
    for (int ph = 0; ph < 2; ++ph) {
      const v2u p = xp[ph][0], q = xp[ph][1];
      const bool dn = reg & 1;
      xv[ph].x = __builtin_amdgcn_perm(dn ? q.y : p.x, x1[ph], 0x07060100u);
      xv[ph].y = dn ? __builtin_amdgcn_perm(q.x, q.y, 0x07060100u) : p.y;
      xv[ph].z = dn ? __builtin_amdgcn_perm(p.y, q.x, 0x07060100u) : q.x;
      xv[ph].w = dn ? __builtin_amdgcn_perm(p.x, p.y, 0x07060100u) : q.y;
      pin(xv[ph]);
    }
    bar();
#pragma unroll
    for (int ph = 0; ph < 2; ++ph) {
      v2f r[4];
#pragma unroll
      for (int n = 0; n < 4; ++n) {
        r[n] = lo2(ac2[2 * ph][n]);
#pragma unroll
        for (int w = 0; w < 3; ++w) r[n] += *(const LAS v2f*)(L + rr_ + w * 2048 + ph * 24576 + n * 512);
      }
      v4u o;
#pragma unroll
      for (int q = 0; q < 2; ++q) {
        o[2 * q] = pk2(fma2(bun(gv[q].x), v2f{r[0][q], r[1][q]}, bun(xv[ph][2 * q])));
        o[2 * q + 1] = pk2(fma2(bun(gv[q].z), v2f{r[2][q], r[3][q]}, bun(xv[ph][2 * q + 1])));
      }
      __builtin_amdgcn_raw_buffer_store_b128(o, mkrs(out, S * D * 2), oo[ph], 0, OUT_AUX);
    }
  }
  STAMP(11);
  DUMP();
}

extern "C" void kernel_launch(void* const* d_in, const int* in_sizes, int n_in, void* d_out, int out_size, void* d_ws,
                              size_t ws_size, hipStream_t stream) {
  fused<<<256, 256, 0, stream>>>((const u16*)d_in[0], (const u16*)d_in[1], (const u16*)d_in[2], (const u16*)d_in[4],
                                 (const u16*)d_in[5], (const u16*)d_in[6], (const u16*)d_in[7], (const u16*)d_in[8],
                                 (u16*)d_out, (char*)d_ws);
}
```

```cpp
#define PADI 8
#define PADR 0
#define PADB 42
#define PADT 3000
#define ALIAS 0
#define PF_AUX 0
#include <hip/hip_runtime.h>

typedef unsigned short u16;
typedef unsigned int u32;
typedef unsigned long long u64;
typedef u32 v4u __attribute__((ext_vector_type(4)));
typedef u32 v2u __attribute__((ext_vector_type(2)));
typedef float v4f __attribute__((ext_vector_type(4)));
typedef float v2f __attribute__((ext_vector_type(2)));
typedef short v8s __attribute__((ext_vector_type(8)));
typedef short v4s __attribute__((ext_vector_type(4)));
typedef __bf16 v8b __attribute__((ext_vector_type(8)));
typedef __bf16 v2b __attribute__((ext_vector_type(2)));
#define LAS __attribute__((address_space(3)))
#define GAS __attribute__((address_space(1)))
#define DEV __device__ __forceinline__

#define C_W1_EARLY 0
#define CLR_DELAY 0
#define W_DELAY 0
#define CLR2_DELAY 0
#define PF_POS 2
#define SL1 1
#define SL2 2
#define SL4 4
#define X_AFTER 8
#define W_LATE 0
#define W1_PF 1
#define DMA_MOD ""
#define LEAD 6
#define A_V 21
#define A_PER 4
#define WDV(f) ((f) - WD_MID + 2 < 14 ? (f) - WD_MID + 2 : ((f) - WD_MID - 12) / 2 + 16)
#define B_AT2 12
#define RE_PF 0
#define PF2_DELAY 0
#define C_DMA_EARLY 0
#define EVEN_PF 0
#define XBAL 1
#define W1EVEN 1
#define PRE_OUT 0
#define OUT_AUX 16
#define CLR_MODE 0
#define MK 0
#define MKD1 0
#define MKD2 0
#define MKD3 0
#define NR_S 0
#define NR_U 8
#define NR_M 8
#define NR_T 8
#define P_AT 7
#define WD_PER 3
#define A_FIRST 10
#define B_FIRST 10
#define A_AT 9
#define F_AT 4
#define CODE_BYTES 45056
#define WD_EARLY 20
#define WD_MID 20
#define B_AT 9
constexpr int S = 2048, D = 448, DF = 225, F = 1792;

struct alignas(16) Chain { u16 v[2048]; };
struct Cos448 { u16 v[448]; };
constexpr double c_cos_t(double a) {
  double a2 = a * a, t = 1.0, s = 1.0;
  for (int i = 1; i <= 10; ++i) { t = -t * a2 / ((2 * i - 1) * (2 * i)); s += t; }
  return s;
}
constexpr double c_sin_t(double a) {
  double a2 = a * a, t = a, s = a;
  for (int i = 1; i <= 10; ++i) { t = -t * a2 / ((2 * i) * (2 * i + 1)); s += t; }
  return s;
}
constexpr double c_cos448(int m) {
  if (m > 224) m = 448 - m;
  double sg = 1.0;
  if (m > 112) { m = 224 - m; sg = -1.0; }
  const double w = 6.283185307179586476925286766559 / 448.0;
  return m == 112 ? 0.0 : (m <= 56 ? sg * c_cos_t(w * m) : sg * c_sin_t(w * (112 - m)));
}
constexpr u16 c_bf16(double d) {
  const u32 u = __builtin_bit_cast(u32, (float)d);
  return (u16)((u + 0x7FFFu + ((u >> 16) & 1u)) >> 16);
}
constexpr Cos448 mk_cos() { Cos448 c{}; for (int m = 0; m < 448; ++m) c.v[m] = c_bf16(c_cos448(m)); return c; }
constexpr Cos448 CTAB = mk_cos();
constexpr u16 bcos(int m) { return CTAB.v[m % 448]; }
constexpr u16 bsin(int m) { return CTAB.v[(m % 448 + 336) % 448]; }
constexpr u16 bneg(u16 b) { return b ? (u16)(b ^ 0x8000u) : b; }
constexpr u16 bscale(u16 b, int e) { return b ? (u16)(b + e * 0x80) : b; }

constexpr Chain mk_p1(int c) {
  const int tile = c >> 2, par = c & 1, sn = (c >> 1) & 1;
  Chain ch{};
  for (int s = 0; s < 4; ++s)
    for (int l = 0; l < 64; ++l) {
      const int kp = 16 * tile + (l & 15), k = 2 * kp + par;
      if (k > 224) continue;
      for (int j = 0; j < 8; ++j) {
        const int dl = 32 * s + 8 * (l >> 4) + j;
        if (dl > 112) continue;
        u16 b = sn ? bneg(bsin(k * dl)) : bcos(k * dl);
        if (dl == 0 || dl == 112) b = bscale(b, -1);
        ch.v[(s * 64 + l) * 8 + j] = b;
      }
    }
  return ch;
}
constexpr Chain mk_p3(int c) {
  const int tile = c >> 1, sn = c & 1;
  Chain ch{};
  for (int s = 0; s < 4; ++s)
    for (int l = 0; l < 64; ++l) {
      int dl = 16 * tile + (l & 15);
      if (c == 1 && dl == 0) dl = 112;
      for (int j = 0; j < 8; ++j) {
        const int k = 2 * (32 * s + 8 * (l >> 4) + j) + 1;
        if (k < 224) ch.v[(s * 64 + l) * 8 + j] = sn ? bsin(k * dl) : bcos(k * dl);
      }
    }
  return ch;
}
constexpr Chain mk_p2(int c) {
  Chain ch{};
  for (int f = 0; f < 4; ++f)
    for (int l = 0; l < 64; ++l)
      for (int j = 0; j < 8; ++j) {
        const int comp = c < 8 ? (c & 1) : (f >> 1), s = c < 8 ? f : (f & 1);
        const int o = 16 * (c < 8 ? (c >> 1) : (c - 8)) + (l & 15), i = 16 * s + 4 * (l >> 4) + (j >> 1);
        const int m = (c < 8 ? 7 : 14) * ((i * o) % 64);
        u16 b = 0;
        if (comp == 0) b = (j & 1) ? bsin(m) : bcos(m);
        else b = (j & 1) ? bcos(m) : bneg(bsin(m));
        ch.v[(f * 64 + l) * 8 + j] = b;
      }
  return ch;
}
template <int I> constexpr Chain P1C = mk_p1(I);
template <int I> constexpr Chain P3C = mk_p3(I);
template <int I> constexpr Chain P2C = mk_p2(I);
#define L10(T, b) T<b>, T<b + 1>, T<b + 2>, T<b + 3>, T<b + 4>, T<b + 5>, T<b + 6>, T<b + 7>, T<b + 8>, T<b + 9>
#define L29(T) L10(T, 0), L10(T, 10), T<20>, T<21>, T<22>, T<23>, T<24>, T<25>, T<26>, T<27>, T<28>
constexpr int TAB_P1 = 0, TAB_P3 = 29, TAB_P2 = 43, TAB_N = 53;
static __device__ const Chain TAB[TAB_N] = {L29(P1C), L10(P3C, 0), P3C<10>, P3C<11>, P3C<12>, P3C<13>, L10(P2C, 0)};

constexpr int WS_EPOCH = 0;
constexpr int WS_RDY = 4096;
constexpr int WS_HRDY = 4096 + 8192;
constexpr int WS_DRDY = 4096 + 16384;
constexpr int WS_XRDY = 131072 + 65536;
constexpr int WS_ARDY = 131072;
constexpr int WS_CRDY = 131072 + 32768;
constexpr int WS_MK1 = 212992, WS_MK2 = 221184, WS_MK3 = 229376;
constexpr int WS_MOD = 32768, MOD_REP = 12288;
constexpr int T1_BLK = 226 * 64;
constexpr int WS_T1 = 1 << 18;
constexpr int WS_WT = WS_T1 + 128 * T1_BLK;
constexpr int WS_H2 = WS_WT + 113 * 16384, H2_BLK = 14336, WS_X2 = WS_H2 + 128 * 14336, X2_BLK = 14336;
constexpr int WS_WD = WS_X2 + 128 * 14336;
constexpr int WS_ACT = WS_WD + 7 * 56 * 4096;
constexpr int WS_END = WS_ACT + 128 * 57344;
constexpr int WS_DBG = 32 << 20;

constexpr int L_W1 = 0;
constexpr int L_B = 114688;
constexpr int XP = 912;
constexpr int L_XS = L_B;
constexpr int L_ST = L_XS + 16 * XP;
constexpr int L_M1 = L_ST + 128;
constexpr int L_AF = L_M1 + 4 * 1792;
constexpr int L_M2 = L_AF + 16384;
constexpr int L_EX = L_M2 + 3 * 1792;
constexpr int L_REND = L_EX + 512;
constexpr int ZPL = 8192;
constexpr int L_CI = L_B;
constexpr int L_CX = L_CI + 2 * 8256;
constexpr int L_RED = L_CX + 16384;
constexpr int L_DBG = 163840 - 1024;
constexpr int L_SZ = 163840;
static_assert(L_REND <= L_DBG && L_RED + 2048 <= L_DBG && 2 * ZPL <= 16384, "lds");

constexpr u32 SENT = 0xFFFFFFFFu;
constexpr int SPIN = 1 << 14;
constexpr int SC1 = 16;
constexpr int OOB = 0x7FFF0000;

typedef __amdgpu_buffer_rsrc_t rsrc_t;
DEV rsrc_t mkrs(const void* p, int bytes) { return __builtin_amdgcn_make_buffer_rsrc((void*)p, 0, bytes, 0x00020000); }
DEV v4u ld4(rsrc_t r, int off, int so = 0) { return __builtin_amdgcn_raw_buffer_load_b128(r, off, so, 0); }
DEV v2u ld2(rsrc_t r, int off, int so = 0) { return __builtin_amdgcn_raw_buffer_load_b64(r, off, so, 0); }
DEV v4u ld4c(rsrc_t r, int off, int so = 0) { return __builtin_amdgcn_raw_buffer_load_b128(r, off, so, SC1); }
DEV v2u ld2c(rsrc_t r, int off, int so = 0) { return __builtin_amdgcn_raw_buffer_load_b64(r, off, so, SC1); }
DEV u32 ld1c(rsrc_t r, int off, int so = 0) { return __builtin_amdgcn_raw_buffer_load_b32(r, off, so, SC1); }
DEV void st4c(rsrc_t r, int off, v4u v) { __builtin_amdgcn_raw_buffer_store_b128(v, r, off, 0, SC1); }
DEV void st2c(rsrc_t r, int off, v2u v) { __builtin_amdgcn_raw_buffer_store_b64(v, r, off, 0, SC1); }
DEV float blo(u32 v) { return __builtin_bit_cast(float, v << 16); }
DEV float bhi(u32 v) { return __builtin_bit_cast(float, v & 0xFFFF0000u); }
DEV v2f bun(u32 v) { return v2f{blo(v), bhi(v)}; }
DEV u32 pk(float lo, float hi) { return __builtin_bit_cast(u32, __builtin_convertvector(v2f{lo, hi}, v2b)); }
DEV u32 pk2(v2f v) { return __builtin_bit_cast(u32, __builtin_convertvector(v, v2b)); }
DEV float rb(float f) { return blo(pk(f, 0.f)); }
DEV v2f lo2(v4f v) { return __builtin_shufflevector(v, v, 0, 1); }
DEV v2f hi2(v4f v) { return __builtin_shufflevector(v, v, 2, 3); }
DEV v2f fma2(v2f a, v2f b, v2f c) { return __builtin_elementwise_fma(a, b, c); }
DEV v4f fma4(v4f a, v4f b, v4f c) { return __builtin_elementwise_fma(a, b, c); }
typedef int v4i __attribute__((ext_vector_type(4)));
DEV v4f relu4(v4f v) { return __builtin_bit_cast(v4f, __builtin_elementwise_max(__builtin_bit_cast(v4i, v), v4i{0, 0, 0, 0})); }
DEV v4f splat(float f) { return v4f{f, f, f, f}; }
DEV v4f mfma(v4u a, v4u b, v4f c) {
  return __builtin_amdgcn_mfma_f32_16x16x32_bf16(__builtin_bit_cast(v8b, a), __builtin_bit_cast(v8b, b), c, 0, 0, 0);
}
DEV void mfa(v4f& c, v4u a, v4u b) { asm volatile("s_nop 1\n\tv_mfma_f32_16x16x32_bf16 %0, %1, %2, %0" : "+v"(c) : "v"(a), "v"(b)); }
DEV void mf0(v4f& c, v4u a, v4u b) { asm volatile("s_nop 1\n\tv_mfma_f32_16x16x32_bf16 %0, %1, %2, 0" : "=&v"(c) : "v"(a), "v"(b)); }
template <int N> DEV void mfence(v4f& a, v4f& b) { asm volatile("s_nop %2" : "+v"(a), "+v"(b) : "i"(N)); }
template <int N> DEV void mfence(v4f& a, v4f& b, v4f& c, v4f& d) { asm volatile("s_nop %4" : "+v"(a), "+v"(b), "+v"(c), "+v"(d) : "i"(N)); }
#define MF "v_mfma_f32_16x16x32_bf16 "
DEV void mf8(v4f& d0, v4f& d1, const v4u (&a0)[4], const v4u (&b0)[4], const v4u (&a1)[4], const v4u (&b1)[4]) {
  asm volatile("s_nop 1\n\t" MF "%0, %2, %6, 0\n\t" MF "%1, %10, %14, 0\n\t" MF "%0, %3, %7, %0\n\t" MF "%1, %11, %15, %1\n\t"
               MF "%0, %4, %8, %0\n\t" MF "%1, %12, %16, %1\n\t" MF "%0, %5, %9, %0\n\t" MF "%1, %13, %17, %1"
               : "=&v"(d0), "=&v"(d1)
               : "v"(a0[0]), "v"(a0[1]), "v"(a0[2]), "v"(a0[3]), "v"(b0[0]), "v"(b0[1]), "v"(b0[2]), "v"(b0[3]),
                 "v"(a1[0]), "v"(a1[1]), "v"(a1[2]), "v"(a1[3]), "v"(b1[0]), "v"(b1[1]), "v"(b1[2]), "v"(b1[3]));
}
#define H4_M(XA, D, X, K, C) MF D ", " XA(X, K) ", " C "\n\t"
#define H4_XA(X, K) X ", " K
#define H4_XB(X, K) K ", " X
#define H4_DEF(NAME, XA, C0, C1, CONS, N0, N1, PA, PB, PC, PD) \
DEV void NAME(v4f& d0, v4f& d1, v4u x0, v4u x1, v4u k00, v4u k01, v4u k10, v4u k11, \
              u32& o0, u32& o1, float p0, float q0, float c0, float s0, float p1, float q1, float c1, float s1) { \
  float t0, t1, t2, t3; \
  asm volatile(H4_M(XA, "%0", "%8", "%10", C0) "v_mul_f32 %4, %14, %16\n\tv_mul_f32 %5, %15, %16\n\t" \
               H4_M(XA, "%1", "%8", "%12", C1) "v_mul_f32 %6, %18, %20\n\tv_mul_f32 %7, %19, %20\n\t" \
               H4_M(XA, "%0", "%9", "%11", "%0") "v_fma_f32 %4, " N0 "%15, %17, %4\n\tv_fma_f32 %5, " N1 "%14, %17, %5\n\t" \
               H4_M(XA, "%1", "%9", "%13", "%1") "v_fma_f32 %6, " N0 "%19, %21, %6\n\tv_fma_f32 %7, " N1 "%18, %21, %7\n\t" \
               "v_cvt_pk_bf16_f32 %2, " PA ", " PB "\n\tv_cvt_pk_bf16_f32 %3, " PC ", " PD \
               : CONS(d0), CONS(d1), "=&v"(o0), "=&v"(o1), "=&v"(t0), "=&v"(t1), "=&v"(t2), "=&v"(t3) \
               : "v"(x0), "v"(x1), "v"(k00), "v"(k01), "v"(k10), "v"(k11), \
                 "v"(p0), "v"(q0), "v"(c0), "v"(s0), "v"(p1), "v"(q1), "v"(c1), "v"(s1)); \
}
H4_DEF(h4_a0m, H4_XA, "0", "0", "=&v", "", "-", "%4", "%5", "%6", "%7")
H4_DEF(h4_a1m, H4_XA, "%0", "%1", "+v", "", "-", "%4", "%5", "%6", "%7")
H4_DEF(h4_b0m, H4_XB, "0", "0", "=&v", "", "-", "%4", "%5", "%6", "%7")
H4_DEF(h4_b0p, H4_XB, "0", "0", "=&v", "-", "", "%5", "%4", "%7", "%6")
H4_DEF(h4_a0p, H4_XA, "0", "0", "=&v", "-", "", "%5", "%4", "%7", "%6")
#define M4_DEF(NAME, XA, C0, C1, CONS) \
DEV void NAME(v4f& d0, v4f& d1, v4u x0, v4u x1, v4u k00, v4u k01, v4u k10, v4u k11) { \
  asm volatile(H4_M(XA, "%0", "%2", "%4", C0) H4_M(XA, "%1", "%2", "%6", C1) H4_M(XA, "%0", "%3", "%5", "%0") MF "%1, " XA("%3", "%7") ", %1" \
               : CONS(d0), CONS(d1) : "v"(x0), "v"(x1), "v"(k00), "v"(k01), "v"(k10), "v"(k11)); \
}
M4_DEF(m4_a0, H4_XA, "0", "0", "=&v") M4_DEF(m4_a1, H4_XA, "%0", "%1", "+v")
M4_DEF(m4_b0, H4_XB, "0", "0", "=&v") M4_DEF(m4_b1, H4_XB, "%0", "%1", "+v")
DEV void mv(v4f& c, v4u a, v4u b) { asm volatile(MF "%0, %1, %2, %0" : "+v"(c) : "v"(a), "v"(b)); }
DEV void mv0(v4f& c, v4u a, v4u b) { asm volatile(MF "%0, %1, %2, 0" : "=&v"(c) : "v"(a), "v"(b)); }
DEV void nop2() { asm volatile("s_nop 1"); }
template <class T> DEV void pin(T& v) { asm volatile("" : "+v"(v)); }
DEV int fresh(int off) { asm volatile("" : "+v"(off) :: "memory"); return off; }
DEV bool wall(bool ok) { return __builtin_amdgcn_ballot_w64(!ok) == 0; }
DEV bool wany(bool b) { return __builtin_amdgcn_ballot_w64(b) != 0; }
DEV void sb() { __builtin_amdgcn_sched_barrier(0); }
DEV u32 max4(v4u v) { return max(max(v.x, v.y), max(v.z, v.w)); }
DEV void drain() { asm volatile("s_waitcnt vmcnt(0)" ::: "memory"); }
DEV void cfence() { asm volatile("" ::: "memory"); }
DEV void bar() { asm volatile("s_waitcnt lgkmcnt(0)\n\ts_barrier" ::: "memory"); }
template <int CTRL> DEV float dpp(float v) {
  return __builtin_bit_cast(float, __builtin_amdgcn_update_dpp(0, __builtin_bit_cast(int, v), CTRL, 0xF, 0xF, true));
}
DEV float sum16(float v) {
  v += dpp<0x128>(v); v += dpp<0x124>(v); v += dpp<0x122>(v); v += dpp<0x121>(v); return v;
}
DEV void sumg2(float& a, float& b) {
  float c = a, d = b;
  asm volatile("s_nop 1\n\tv_permlane16_swap_b32 %0, %1\n\tv_permlane16_swap_b32 %2, %3" : "+v"(a), "+v"(c), "+v"(b), "+v"(d));
  a += c; b += d; c = a; d = b;
  asm volatile("s_nop 1\n\tv_permlane32_swap_b32 %0, %1\n\tv_permlane32_swap_b32 %2, %3" : "+v"(a), "+v"(c), "+v"(b), "+v"(d));
  a += c; b += d;
}
DEV v4u lds4(const char LAS* L, int off) { return *(const LAS v4u*)(L + off); }
DEV v4f lds4f(const char LAS* L, int off) { return *(const LAS v4f*)(L + off); }
DEV v2u ldtr(const char LAS* L, int off) { return __builtin_bit_cast(v2u, __builtin_amdgcn_ds_read_tr16_b64_v4i16((LAS v4s*)(L + off))); }
DEV v4u ldtr2(const char LAS* L, int o0, int o1) { const v2u a = ldtr(L, o0), b = ldtr(L, o1); return v4u{a.x, a.y, b.x, b.y}; }
DEV void dma16(rsrc_t r, int voff, int soff, u32 lds_dst) {
  u32 keep;
  asm volatile("s_mov_b32 %0, m0\n\ts_mov_b32 m0, %1\n\ts_nop 0\n\tbuffer_load_dwordx4 %2, %3, %4 offen" DMA_MOD " lds\n\ts_mov_b32 m0, %0"
               : "=&s"(keep) : "s"(lds_dst), "v"(voff), "s"(r), "s"(soff) : "memory");
}
DEV void light_poll(rsrc_t ws, int off) {
  _Pragma("nounroll") for (int s = 0; s < SPIN; ++s) {
    if (wall(ld1c(ws, fresh(off)) != SENT)) break;
    __builtin_amdgcn_s_sleep(SL2);
  }
}
DEV int tok_row(int blk, int t) {
  const int n1 = 16 * (blk >> 5) + 4 * ((blk >> 2) & 3) + (t & 3), n2 = 16 * ((blk >> 4) & 1) + 4 * (blk & 3) + (t >> 2);
  return n2 + 32 * n1;
}
template <bool SWAP> DEV v4u cmul_pack(v4f a, v4f b, v4f c, v4f s) {
  const v4f re = fma4(b, s, a * c), im = fma4(b, c, -(a * s));
  return SWAP ? v4u{pk(im.x, re.x), pk(im.y, re.y), pk(im.z, re.z), pk(im.w, re.w)}
              : v4u{pk(re.x, im.x), pk(re.y, im.y), pk(re.z, im.z), pk(re.w, im.w)};
}

template <bool PLUS> DEV void cmul2(u32& o0, u32& o1, v2f p, v2f q, v2f c, v2f s) {
  const v2f re = fma2(PLUS ? -q : q, s, p * c), im = fma2(PLUS ? p : -p, s, q * c);
  o0 = PLUS ? pk(im.x, re.x) : pk(re.x, im.x); o1 = PLUS ? pk(im.y, re.y) : pk(re.y, im.y);
}

#define STAMP(i) if ((i) == PADI && rep_ == PADR) padt = __builtin_amdgcn_s_memrealtime();
#define CSTAMP(i)
#define SLOWCNT
#define DUMP()

DEV void dma_w1(const u16* wg, const u16* wu, int slice, int wave, int lane, u32 lbase) {
  const rsrc_t rg = mkrs(wg, D * F * 2), ru = mkrs(wu, D * F * 2);
  const int r = lane >> 3, ch = ((((lane & 7) >> 1) ^ (lane >> 4)) & 3) * 32 + (lane & 1) * 16 + slice * 128;
  const int vp = r * (F * 2) + ch, vn = (7 - r) * (F * 2) + ch;
#pragma unroll
  for (int n = 0; n < 4; ++n) {
    const int ks = wave + 4 * n;
    if (n == 3 && wave >= 2) break;
    const int t16 = (ks >> 1) * 16, a = ks & 1;
#pragma unroll
    for (int q = 0; q < 4; ++q) {
      const int dl0 = t16 + 8 * (q & 1);
      const bool pos = (q >> 1) == a;
      const int d0 = q < 2 ? (a ? 224 - dl0 - 7 : dl0) : (a ? 224 + dl0 : 448 - dl0 - 7);
      int vo = pos ? vp : vn, so = d0 * (F * 2);
      if (n == 0 && q == 2) {
        vo += so; so = 0;
        if (wave < 2 && lane < 8) vo = (wave ? 336 : 112) * (F * 2) + ch;
      }
      dma16(rg, vo, so, lbase + L_W1 + ks * 8192 + q * 1024);
      dma16(ru, vo, so, lbase + L_W1 + ks * 8192 + 4096 + q * 1024);
    }
  }
}
struct ModLoad { u32 cv[7]; v4u mw[7]; };
DEV void mod_load(ModLoad& m, const u16* cin, const u16* wm, int q, int tid) {
  const rsrc_t cr = mkrs(cin, q >= 0 ? D * 2 : 0), mr = mkrs(wm, q >= 0 ? D * 6 * D * 2 : 0);
#pragma unroll
  for (int i = 0; i < 7; ++i) m.cv[i] = __builtin_amdgcn_raw_buffer_load_b16(cr, (tid >> 2) * 2 + i * 128, 0, 0);
#pragma unroll
  for (int i = 0; i < 7; ++i) m.mw[i] = __builtin_amdgcn_raw_buffer_load_b128(mr, ((tid >> 2) * (6 * D) + q * 32 + (tid & 3) * 8) * 2, i * 64 * 6 * D * 2, PF_AUX);
}
DEV void mod_part(ModLoad& m, char LAS* L, int tid) {
  float sl[7];
#pragma unroll
  for (int i = 0; i < 7; ++i) { const float cc = blo(m.cv[i]); sl[i] = rb(cc * __builtin_amdgcn_rcpf(1.f + __expf(-cc))); pin(sl[i]); }
  v2f a[4];
#pragma unroll
  for (int e = 0; e < 4; ++e) a[e] = v2f{0.f, 0.f};
#pragma unroll
  for (int i = 0; i < 7; ++i)
#pragma unroll
    for (int e = 0; e < 4; ++e) a[e] = fma2(bun(m.mw[i][e]), v2f{sl[i], sl[i]}, a[e]);
#pragma unroll
  for (int e = 0; e < 4; ++e) {
    a[e].x += dpp<0x128>(a[e].x); a[e].y += dpp<0x128>(a[e].y);
    a[e].x += dpp<0x124>(a[e].x); a[e].y += dpp<0x124>(a[e].y);
  }
  if ((tid & 15) < 4) {
    *(LAS v4f*)(L + L_RED + ((tid >> 4) * 32 + (tid & 15) * 8) * 4) = v4f{a[0].x, a[0].y, a[1].x, a[1].y};
    *(LAS v4f*)(L + L_RED + ((tid >> 4) * 32 + (tid & 15) * 8 + 4) * 4) = v4f{a[2].x, a[2].y, a[3].x, a[3].y};
  }
}
DEV void mod_publish(char LAS* L, rsrc_t ws, int q, int lane, u32 tag) {
  v4f s = {0.f, 0.f, 0.f, 0.f};
#pragma unroll
  for (int r = 0; r < 16; ++r) s += lds4f(L, L_RED + (r * 32 + (lane & 7) * 4) * 4);
  const float one = (q / 14 == 1 || q / 14 == 4) ? 1.f : 0.f;
  st4c(ws, WS_MOD + (lane >> 3) * MOD_REP + q * 128 + (lane & 7) * 16,
       v4u{pk(rb(s.x) + one, rb(s.y) + one), tag, pk(rb(s.z) + one, rb(s.w) + one), tag});
}
DEV u32 ld1(rsrc_t r, int off) { return __builtin_amdgcn_raw_buffer_load_b32(r, off, 0, 0); }
DEV u32 ld1n(rsrc_t r, int off) { return __builtin_amdgcn_raw_buffer_load_b32(r, off, 0, PF_AUX); }
DEV u32 mktag(u32 epoch) { return epoch * 0x9E3779B1u + 0x7F4A7C15u; }

DEV void x2_store(rsrc_t ws, const v4u (&r)[4], int blk, int wave, int l16) {
  const int o = WS_X2 + blk * X2_BLK + (wave == 0 ? 0 : 4 * wave - 2) * 1024 + l16;
#pragma unroll
  for (int m = 0; m < 4; ++m) st4c(ws, m < 2 || wave != 0 ? o + m * 1024 : OOB, r[m]);
}
DEV bool flags_ok(v4u rv, u32 tag) { return rv.x == tag && rv.y == ~tag && rv.z == tag && rv.w == ~tag; }
DEV void wait_flags(rsrc_t ws, int off, u32 tag) {
  _Pragma("nounroll") for (int s = 0; s < SPIN; ++s) {
    const v2u rv = ld2c(ws, fresh(off));
    if (wall(rv.x == tag && rv.y == ~tag)) break;
    __builtin_amdgcn_s_sleep(SL1);
  }
}

__global__ void __launch_bounds__(256, 1)
fused(const u16* __restrict__ xin, const u16* __restrict__ wr, const u16* __restrict__ wi, const u16* __restrict__ wg,
      const u16* __restrict__ wu, const u16* __restrict__ wd, const u16* __restrict__ cin, const u16* __restrict__ wm,
      u16* __restrict__ out, char* wsp) {
  __shared__ __attribute__((aligned(16))) char smem[L_SZ];
  char LAS* L = (char LAS*)smem;
  const u32 lbase = (u32)(size_t)L;
  const int tid = threadIdx.x, lane = tid & 63, wave = __builtin_amdgcn_readfirstlane(tid >> 6);
  const int g = lane >> 4, c16 = lane & 15, l16 = lane * 16;
  const int grp = blockIdx.x & 7, j = blockIdx.x >> 3;
  u64 pc, padt = 0; int rep_ = 0;
  asm volatile("s_getpc_b64 %0" : "=s"(pc));
  STAMP(0); CSTAMP(31);
  asm volatile("" :: "s"(xin), "s"(wr), "s"(wi), "s"(wg), "s"(wu), "s"(wd), "s"(cin), "s"(wm), "s"(out), "s"(wsp));
  const rsrc_t ws = mkrs(wsp, WS_END);
  const rsrc_t tab = mkrs(TAB, TAB_N * 4096);
  const rsrc_t code = mkrs((const void*)(pc & ~127ull), CODE_BYTES);
  const bool isR = j >= 14 && j < 30;
  const bool isG = j < 28;
  const int gk = j / 7, ct = j - gk * 7;
  const int tile = 4 * (grp >> 2) + gk, slice = 7 * (grp & 3) + ct;
  const int wdb = WS_WD + (ct * 56 + wave * 14) * 4096;
  const v4u sv = {SENT, SENT, SENT, SENT};
  int lb = L_B + l16;
  pin(lb);
  const char LAS* LB = L + lb - L_B;
  const int pfo = ((j * 4 + wave) * 14 + lane) * 128, pfc = ((j * 4 + wave) * 4 + lane) * 128;
  u32 pfa = ld1(tab, lane < 14 ? pfo : OOB), pfb = ld1(code, lane < 4 ? pfc : OOB);
  const int hro = WS_HRDY + (16 * tile + 4 * wave) * 32 + c16 * 8;
  v4u x2r[4];
  const int wl16 = (lane ^ ((wave & 1) << 1)) * 16;
  v4u wdn[14][4];
  u32 epoch, tag;

#define ACT_CLEAR(lo, hi) _Pragma("unroll") for (int m = lo; m < hi; ++m) \
      st4c(ws, isG ? WS_ACT + (16 * (tile + 2 * (m >> 3)) + 4 * (grp & 3)) * 57344 + ct * 32768 + (m & 7) * 4096 + tid * 16 : OOB, sv);
  if (isR) {
    const int blk = 16 * grp + (j - 14);
    epoch = ld1c(ws, WS_EPOCH);
    const int t0 = wave == 0 ? 0 : 2 * wave - 1;
    v4u cf[8][4];
#define CF_LOAD(q) { const int chn = TAB_P1 + ((wave == 0 && q >= 4) ? 28 : 4 * t0 + q), vo = (q < 5 || wave != 0) ? l16 : OOB; \
      _Pragma("unroll") for (int s = 0; s < 4; ++s) cf[q][s] = ld4(tab, vo + s * 1024, chn * 4096); sb(); }
#pragma unroll
    for (int q = 0; q < X_AFTER; ++q) CF_LOAD(q)
    v2u xv[7];
    {
      const rsrc_t xr = mkrs(xin, S * D * 2);
      const int off = tok_row(blk, 4 * wave + g) * (D * 2) + c16 * 8;
#pragma unroll
      for (int m = 0; m < 7; ++m) xv[m] = ld2(xr, off + m * 128);
    }
    typedef u32 v3u __attribute__((ext_vector_type(3)));
    v2u fa_[2], fb_[2]; v3u fc_[2], fd_[2];
    {
      const rsrc_t rr = mkrs(wr, S * DF * 2), ri = mkrs(wi, S * DF * 2);
      const int fo_ = 16 * wave + c16 < 57 ? 2 * g * 450 + (16 * wave + c16) * 8 : OOB;
#pragma unroll
      for (int hh = 0; hh < 2; ++hh) {
        const int so = blk * 7200 + hh * 3600;
        fa_[hh] = ld2(rr, fo_, so); fb_[hh] = ld2(ri, fo_, so);
        fc_[hh] = __builtin_amdgcn_raw_buffer_load_b96(rr, fo_ + 448, so, 0); fd_[hh] = __builtin_amdgcn_raw_buffer_load_b96(ri, fo_ + 448, so, 0);
      }
    }
    sb();
#pragma unroll
    for (int q = X_AFTER; q < 8; ++q) CF_LOAD(q)
    STAMP(23);
    v2u rdy;
    {
      v2f f[14], s2 = {0.f, 0.f};
#pragma unroll
      for (int m = 0; m < 7; ++m) {
        f[2 * m] = bun(xv[m].x); f[2 * m + 1] = bun(xv[m].y);
        s2 += f[2 * m] + f[2 * m + 1];
        *(LAS v2u*)(L + L_XS + (4 * wave + g) * XP + c16 * 8 + m * 128) = xv[m];
      }
      STAMP(21);
      sb();
      {
        const int q = 16 * wave + c16, o = WS_WT + q * 16384 + blk * 128 + g * 16;
#pragma unroll
        for (int hh = 0; hh < 2; ++hh) {
          const v2u a = fa_[hh], b = fb_[hh]; const v3u c = fc_[hh], d = fd_[hh];
          st4c(ws, q < 57 ? o + hh * 64 : OOB, v4u{__builtin_amdgcn_perm(b.x, a.x, 0x05040100u), __builtin_amdgcn_perm(b.y, a.y, 0x05040100u),
                                                   __builtin_amdgcn_perm(d.x, c.x, 0x07060302u), __builtin_amdgcn_perm(d.y, c.y, 0x07060302u)});
          st4c(ws, q < 56 ? o + hh * 64 + 57 * 16384 : OOB,
               v4u{__builtin_amdgcn_perm(b.x, a.x, 0x07060302u), __builtin_amdgcn_perm(b.y, a.y, 0x07060302u),
                   __builtin_amdgcn_perm(d.y, c.y, 0x05040100u), __builtin_amdgcn_perm(d.z, c.z, 0x05040100u)});
        }
      }
      rdy = ld2c(ws, WS_RDY + grp * 1024 + min(tid >> 1, 112) * 8);
      sb();
      const float mean = sum16(s2.x + s2.y) * (1.f / D);
      v2f q2 = {0.f, 0.f};
#pragma unroll
      for (int m = 0; m < 14; ++m) { const v2f d = f[m] - mean; q2 = fma2(d, d, q2); }
      const float rstd = __builtin_amdgcn_rsqf(sum16(q2.x + q2.y) * (1.f / D) + 1e-5f);
      if (c16 == 0) *(LAS v2f*)(L + L_ST + (4 * wave + g) * 8) = v2f{mean, rstd};
    }
    pin(pfa); pin(pfb);
    epoch = __builtin_amdgcn_readfirstlane(epoch);
    tag = mktag(epoch);
    STAMP(22);
    bar();
    v4f n[4][2];
    const int d0 = 8 * (4 * wave + g);
    const v2f st = *(const LAS v2f*)(L + L_ST + c16 * 8);
    {
      int xr_ = L_XS + c16 * XP + d0 * 2, xb_ = L_XS + c16 * XP + (216 - d0) * 2, x0_ = d0 == 0 ? xr_ : xb_ + 464;
      pin(xr_); pin(xb_); pin(x0_);
      const v4u a = lds4(L + xr_, 0), e = lds4(L + xr_, 448), c = lds4(L + xb_, 0), b = lds4(L + xb_, 448);
      const u32 c0 = *(const LAS u16*)(L + xb_ + 16), b0 = *(const LAS u16*)(L + x0_);
#pragma unroll
      for (int i = 0; i < 8; ++i) {
        n[0][i >> 2][i & 3] = (i & 1) ? bhi(a[i >> 1]) : blo(a[i >> 1]);
        n[3][i >> 2][i & 3] = (i & 1) ? bhi(e[i >> 1]) : blo(e[i >> 1]);
        n[1][i >> 2][i & 3] = i == 0 ? blo(b0) : (i & 1) ? bhi(b[(8 - i) >> 1]) : blo(b[(8 - i) >> 1]);
        n[2][i >> 2][i & 3] = i == 0 ? blo(c0) : (i & 1) ? bhi(c[(8 - i) >> 1]) : blo(c[(8 - i) >> 1]);
      }
      const float nm = -st.x * st.y;
#pragma unroll
      for (int m = 0; m < 4; ++m) {
#pragma unroll
        for (int h = 0; h < 2; ++h) { n[m][h] = fma4(n[m][h], splat(st.y), splat(nm)); pin(n[m][h]); }
      }
    }
    sb();
    STAMP(1);
    int mb = L_M1 + d0 * 4;
    pin(mb);
    v4u xs[4];
    int to[4];
    to[0] = WS_T1 + blk * T1_BLK + (16 * t0 + c16) * 64 + g * 16;
    to[1] = wave != 0 ? to[0] + 1024 : c16 < 2 ? to[0] + 112 * 64 : OOB;
    to[2] = to[0] + 114 * 64;
    to[3] = wave != 0 ? to[0] + 1024 + 114 * 64 : OOB;
#pragma unroll
    for (int q = 0; q < 4; ++q) pin(to[q]);

    {
      const int kind = tid >= 112, d = 4 * (tid - 112 * kind);
      int wb = L_M1 + (kind ? 0 : 2 * 1792) + d * 4, wrv = L_M1 + (kind ? 0 : 2 * 1792) + 1792 + (445 - d) * 4;
      int wr0 = d == 0 ? wrv + 12 - 1792 : wrv + 12;
      const int mo = tid < 224 ? WS_MOD + grp * MOD_REP + tid * 16 : OOB;
      pin(wb); pin(wrv); pin(wr0);
      if (!wall(rdy.x == tag && rdy.y == ~tag)) wait_flags(ws, WS_RDY + grp * 1024 + min(tid >> 1, 112) * 8, tag);
      v4u mv;
      _Pragma("nounroll") for (int s = 0; s < SPIN; ++s) {
        mv = ld4c(ws, fresh(mo));
        if (wall(tid >= 224 || (mv.y == tag && mv.w == tag))) break;
        __builtin_amdgcn_s_sleep(SL1);
      }
      STAMP(2);
      if (tid < 224) {
        const v4f v = {blo(mv.x), bhi(mv.x), blo(mv.z), bhi(mv.z)};
        *(LAS v4f*)(L + wb) = v;
        *(LAS float*)(L + wr0) = v.x;
        *(LAS float*)(L + wrv + 8) = v.y;
        *(LAS float*)(L + wrv + 4) = v.z;
        *(LAS float*)(L + wrv) = v.w;
      }
    }
    bar();
    {
      v4u fr[4];
#pragma unroll
      for (int hh = 0; hh < 2; ++hh) {
        v4f h[4];
#pragma unroll
        for (int m = 0; m < 4; ++m) {
          const int o = ((m == 1 || m == 2) ? 1792 : 0) + (m >= 2 ? 896 : 0) + hh * 16;
          h[m] = fma4(n[m][hh], lds4f(L + mb, o), lds4f(L + mb, 2 * 1792 + o));
        }
        const v4f p = h[0] + h[1], q = h[2] + h[3], u = h[0] - h[1], v = h[3] - h[2];
        const v4f t[4] = {p + q, p - q, u + v, u - v};
#pragma unroll
        for (int m = 0; m < 4; ++m) { fr[m][2 * hh] = pk2(lo2(t[m])); fr[m][2 * hh + 1] = pk2(hi2(t[m])); }
      }
#pragma unroll
      for (int m = 0; m < 4; ++m) *(LAS v4u*)(LB + wave * 1024 + L_AF + m * 4096) = fr[m];
    }
    bar();
    {
      v4u a[4][4];
#pragma unroll
      for (int p = 0; p < 2; ++p)
#pragma unroll
        for (int s = 0; s < 4; ++s) { a[p][s] = lds4(LB, L_AF + (p * 4 + s) * 1024); a[2 + p][s] = lds4(LB, L_AF + ((2 + p) * 4 + s) * 1024); }
      v4f re[4], im[4];
#pragma unroll
      for (int q = 0; q < 5; ++q) {
        if (q < 4) mf8(re[q], im[q], a[q >> 1], cf[4 * (q & 1) + (q >> 1)], a[2 + (q >> 1)], cf[4 * (q & 1) + 2 + (q >> 1)]);
        if (q > 0) {
          if (q < 4) mfence<0>(re[q - 1], im[q - 1]); else mfence<7>(re[3], im[3]);
          xs[q - 1] = v4u{pk(re[q - 1].x, im[q - 1].x), pk(re[q - 1].y, im[q - 1].y), pk(re[q - 1].z, im[q - 1].z), pk(re[q - 1].w, im[q - 1].w)};
          st4c(ws, to[q - 1], xs[q - 1]);
          sb();
        }
      }
    }
    STAMP(3);
    sb();
    sb();
    if (isG) dma_w1(wg, wu, slice, wave, lane, lbase);
#pragma unroll
    for (int q = 0; q < 4; ++q) {
      const int vo = (q < 2 || wave != 0) ? l16 : OOB;
#pragma unroll
      for (int s = 0; s < 4; ++s) cf[2 * q + 1][s] = ld4(tab, vo + s * 1024, (TAB_P3 + 2 * t0 + q) * 4096);
    }
#pragma unroll
    for (int m = 0; m < 4; ++m) {
      st4c(ws, tid + 256 * m < 896 ? WS_H2 + blk * H2_BLK + 4096 * m + tid * 16 : OOB, sv);
    }
    sb();
    STAMP(20);
    v4f xo[2][4];
    int xa = L_XS + c16 * XP + (16 * t0 + 4 * g) * 2, xd = L_XS + c16 * XP + (192 - 16 * t0 - 4 * g) * 2;
    int xs1 = tid < 16 ? L_XS + c16 * XP + 224 : xd + 512, xs3 = tid < 16 ? L_XS + c16 * XP + 672 : xa + 448;
    pin(xa); pin(xd); pin(xs1); pin(xs3);
#define XOFF(t, m, r) ((t) == 0 && (r) == 0 && (m) == 1 ? xs1 : (t) == 0 && (r) == 0 && (m) == 3 ? xs3 : \
    (m) == 0 ? xa + 32 * (t) + 2 * (r) : (m) == 3 ? xa + 448 + 32 * (t) + 2 * (r) : (m) == 1 ? xd + 512 - 32 * (t) - 2 * (r) : xd + 64 - 32 * (t) - 2 * (r))
    {
      v2u w[2][4]; u32 e[2][2], e3;
#pragma unroll
      for (int t = 0; t < 2; ++t) {
        w[t][0] = *(const LAS v2u*)(L + xa + 32 * t); w[t][3] = *(const LAS v2u*)(L + xa + 448 + 32 * t);
        w[t][1] = *(const LAS v2u*)(L + xd + 504 - 32 * t); w[t][2] = *(const LAS v2u*)(L + xd + 56 - 32 * t);
        e[t][0] = *(const LAS u16*)(L + XOFF(t, 1, 0)); e[t][1] = *(const LAS u16*)(L + XOFF(t, 2, 0));
      }
      e3 = *(const LAS u16*)(L + xs3);
      sb();
#pragma unroll
      for (int t = 0; t < 2; ++t) {
        xo[t][0] = v4f{blo(w[t][0].x), bhi(w[t][0].x), blo(w[t][0].y), bhi(w[t][0].y)};
        xo[t][3] = v4f{t == 0 ? blo(e3) : blo(w[t][3].x), bhi(w[t][3].x), blo(w[t][3].y), bhi(w[t][3].y)};
        xo[t][1] = v4f{blo(e[t][0]), bhi(w[t][1].y), blo(w[t][1].y), bhi(w[t][1].x)};
        xo[t][2] = v4f{blo(e[t][1]), bhi(w[t][2].y), blo(w[t][2].y), bhi(w[t][2].x)};
#pragma unroll
        for (int m = 0; m < 4; ++m) { xo[t][m] -= st.x; pin(xo[t][m]); }
      }
    }
    int m0_[2], m1_[2], md_[2];
#pragma unroll
    for (int r = 0; r < 2; ++r) {
      const int q = r == 0 || tid < 80 ? tid + 256 * r : tid;
      const int kind = (q * 586) >> 16, d = 4 * (q - 112 * kind);
      const int reg = (d >= 112) + (d >= 224) + (d >= 336), cc = ((reg + 1) >> 1) * 224, m1 = (0x1320 >> (4 * reg)) & 3;
      const int d1 = abs(d + 1 - cc), d0_ = abs(d - cc), edge = d == 112 * reg && reg > 0;
      m1_[r] = L_M2 + kind * 1792 + (((d1 >> 4) << 6) | (m1 << 4) | (d1 & 15)) * 4;
      m0_[r] = L_M2 + kind * 1792 + (edge ? reg << 4 : ((d0_ >> 4) << 6) | (m1 << 4) | (d0_ & 15)) * 4;
      md_[r] = (reg & 1) ? -4 : 4;
      pin(m0_[r]); pin(m1_[r]); pin(md_[r]);
    }
    {
      v4u mv[2];
      _Pragma("nounroll") for (int s = 0; s < SPIN; ++s) {
        mv[0] = ld4c(ws, fresh(WS_MOD + grp * MOD_REP + (224 + tid) * 16));
        mv[1] = ld4c(ws, fresh(WS_MOD + grp * MOD_REP + (224 + (tid < 80 ? 256 + tid : tid)) * 16));
        const v2u rv = ld2c(ws, fresh(WS_DRDY + grp * 1024 + min(lane, 48) * 8));
        if (wall(mv[0].y == tag && mv[0].w == tag && mv[1].y == tag && mv[1].w == tag && rv.x == tag && rv.y == ~tag)) break;
        __builtin_amdgcn_s_sleep(SL4);
      }
      if (lane < (CLR_MODE == 1 ? 2 : 1)) st2c(ws, lane == 0 ? WS_HRDY + blk * 32 + wave * 8 : isG ? WS_CRDY + (tile * 4 + (grp & 3)) * 256 + (ct * 4 + wave) * 8 : OOB, v2u{tag, ~tag});
      STAMP(12);
#pragma unroll
      for (int f = 0; f < WD_EARLY; ++f) wdn[f >> 2][f & 3] = ld4c(ws, (isG ? wl16 : OOB) + (f & 3) * 1024, wdb + (f >> 2) * 4096);
      sb();
#pragma unroll
      for (int r = 0; r < 2; ++r) {
        const float sc = m0_[r] < L_M2 + 1792 ? 2.f / (float)(S * D) : 1.f;
        *(LAS float*)(L + m0_[r]) = blo(mv[r].x) * sc;
        *(LAS float*)(L + m1_[r]) = bhi(mv[r].x) * sc;
        *(LAS float*)(L + m1_[r] + md_[r]) = blo(mv[r].z) * sc;
        *(LAS float*)(L + m1_[r] + 2 * md_[r]) = bhi(mv[r].z) * sc;
      }
    }
    bar();
    *(LAS v4u*)(L + L_AF + (tid >> 6) * 4096 + 3072 + l16) = v4u{0u, 0u, 0u, 0u};
    STAMP(4);
    int zr = L_AF + lane * 8, ro = L_M2 + (t0 * 64 + 4 * g) * 4, ro1 = wave == 0 ? ro : ro + 256, ex = L_EX + c16 * 8;
    pin(zr); pin(ro); pin(ro1); pin(ex);
    {
      int zl[4];
#pragma unroll
      for (int m = 0; m < 4; ++m) {
        const int kq = (m >> 1) * 128 + (to[m] == OOB ? 127 : wave == 0 && m == 1 ? 112 + c16 : 16 * (t0 + (m & 1)) + c16);
        zl[m] = L_AF + ((kq & ~31) + ((kq & 4) << 2) + ((kq & 24) >> 1) + (kq & 3)) * 32 + g * 8;
        pin(zl[m]);
        if (to[m] == OOB) xs[m] = sv;
      }
      bar();
      if (isG) wait_flags(ws, hro, tag);
      {
        const int src = (blk * 5 + wave * 13) & 63;
        const int po = __builtin_amdgcn_readlane(to[(0)], 0) + (src & 15) * 64 + (src >> 4) * 16;
        const u32 old = __builtin_amdgcn_ds_bpermute(src * 4, xs[0].x);
        _Pragma("nounroll") for (int s = 0; s < SPIN; ++s) {
          if (wall(ld1c(ws, fresh(po)) != old)) break;
          __builtin_amdgcn_s_sleep(SL2);
        }
      }
      STAMP(13);
      v4u z[4];
      _Pragma("nounroll") for (int s = 0; s < SPIN; ++s) {
        u32 mn = 1;
#pragma unroll
        for (int m = 0; m < 4; ++m) z[m] = ld4c(ws, fresh(to[m]));
#pragma unroll
        for (int m = 0; m < 4; ++m) { const v4u d = z[m] ^ xs[m]; mn = min(min(mn, d.x), min(min(d.y, d.z), d.w)); }
        if (wall(mn != 0)) break;
      }
      STAMP(14);
#pragma unroll
      for (int m = 0; m < 4; ++m) {
        char LAS* p = L + zl[m];
        *(LAS v2u*)(p) = v2u{__builtin_amdgcn_perm(z[m].y, z[m].x, 0x05040100u), __builtin_amdgcn_perm(z[m].w, z[m].z, 0x05040100u)};
        *(LAS v2u*)(p + ZPL) = v2u{__builtin_amdgcn_perm(z[m].y, z[m].x, 0x07060302u), __builtin_amdgcn_perm(z[m].w, z[m].z, 0x07060302u)};
      }
    }
    bar();
    STAMP(5);
    {
      v4u zf[4][4];
#pragma unroll
      for (int ty = 0; ty < 4; ++ty)
#pragma unroll
        for (int s = 0; s < 4; ++s) zf[ty][s] = ldtr2(L + zr, ty * 4096 + s * 1024, ty * 4096 + s * 1024 + 512);
      v4f acc[2][4], x2[2][4], gav[2][4];
      v2f s1 = {0.f, 0.f}, s2 = {0.f, 0.f};
#define P3M(t, ty) mf8(acc[t][ty], acc[t][ty + 1], cf[4 * t + ty], zf[ty], cf[4 * t + ty + 1], zf[ty + 1]);
#define P3X(t, m, yv) { x2[t][m] = fma4(gav[t][m], yv, xo[t][m]); \
      if (t == 0 || wave != 0) { s1 += lo2(x2[t][m]) + hi2(x2[t][m]); \
        s2 = fma2(lo2(x2[t][m]), lo2(x2[t][m]), s2); s2 = fma2(hi2(x2[t][m]), hi2(x2[t][m]), s2); } }
      P3M(1, 0) P3M(1, 2)
#pragma unroll
      for (int m = 0; m < 4; ++m) { gav[0][m] = lds4f(L + ro, m * 64); gav[1][m] = lds4f(L + ro1, m * 64); }
      sb();
      P3M(0, 0) P3M(0, 2)
      mfence<0>(acc[1][0], acc[1][1], acc[1][2], acc[1][3]);
      const v4f A1 = acc[1][0] + acc[1][1], B1 = acc[1][0] - acc[1][1], P1 = acc[1][3] - acc[1][2], Q1 = acc[1][2] + acc[1][3];
      P3X(1, 0, A1 - P1) P3X(1, 1, A1 + P1) P3X(1, 2, B1 - Q1) P3X(1, 3, B1 + Q1)
      sb();
      mfence<0>(acc[0][0], acc[0][1], acc[0][2], acc[0][3]);
      {
        const v4f A = acc[0][0] + acc[0][1], B = acc[0][0] - acc[0][1], P = acc[0][3] - acc[0][2], Q = acc[0][2] + acc[0][3];
        v4f y[4] = {A - P, A + P, B - Q, B + Q};
        const bool sp = tid < 16;
        y[0].x = sp ? A.x : y[0].x; y[2].x = sp ? B.x : y[2].x;
        y[1].x = sp ? acc[1][0].x - acc[0][3].x : y[1].x; y[3].x = sp ? acc[1][0].x + acc[0][3].x : y[3].x;
        P3X(0, 0, y[0]) P3X(0, 1, y[1]) P3X(0, 2, y[2]) P3X(0, 3, y[3])
      }
      float sm = s1.x + s1.y, sq = s2.x + s2.y;
      sumg2(sm, sq);
      if (g == 0) *(LAS v2f*)(L + ex + wave * 128) = v2f{sm, sq};
      v4f sbv[2][4], cbv[2][4];
#pragma unroll
      for (int m = 0; m < 4; ++m) {
        sbv[0][m] = lds4f(L + ro, 1792 + m * 64); cbv[0][m] = lds4f(L + ro, 2 * 1792 + m * 64);
        sbv[1][m] = lds4f(L + ro1, 1792 + m * 64); cbv[1][m] = lds4f(L + ro1, 2 * 1792 + m * 64);
      }
      bar();
      v2f e = *(const LAS v2f*)(L + ex);
#pragma unroll
      for (int w = 1; w < 4; ++w) e += *(const LAS v2f*)(L + ex + w * 128);
      const float mc = e.x * (1.f / D), rstd = __builtin_amdgcn_rsqf(fmaxf(e.y * (1.f / D) - mc * mc, 0.f) + 1e-5f), nb = -mc * rstd;
      const int ho = WS_H2 + blk * H2_BLK + 2 * t0 * 1024 + l16, ho1 = wave == 0 ? OOB : ho + 2048;
#pragma unroll
      for (int t = 0; t < 2; ++t)
#pragma unroll
        for (int a = 0; a < 2; ++a) {
          v4u o;
#pragma unroll
          for (int hh = 0; hh < 2; ++hh) {
            const int m = 2 * a + hh;
            const v4f hv = fma4(fma4(x2[t][m], splat(rstd), splat(nb)), cbv[t][m], sbv[t][m]);
            o[2 * hh] = pk2(lo2(hv)); o[2 * hh + 1] = pk2(hi2(hv));
          }
          st4c(ws, (t ? ho1 : ho) + a * 1024, o);
        }
      STAMP(6);
      sb();
#pragma unroll
      for (int f = WD_EARLY; f < WD_MID; ++f) wdn[f >> 2][f & 3] = ld4c(ws, (isG ? wl16 : OOB) + (f & 3) * 1024, wdb + (f >> 2) * 4096);
      sb();
#pragma unroll
      for (int e = 0; e < 8; ++e) {
        x2r[e >> 1][2 * (e & 1)] = pk2(lo2(x2[e >> 2][e & 3]) + st.x); x2r[e >> 1][2 * (e & 1) + 1] = pk2(hi2(x2[e >> 2][e & 3]) + st.x);
      }
#pragma unroll
      for (int m = 0; m < 4; ++m) pin(x2r[m]);
    }
    bar();
    if (!isG) {
      x2_store(ws, x2r, blk, wave, l16);
      if (blockIdx.x == 224 && tid == 0) __builtin_amdgcn_raw_buffer_store_b32(epoch + 1u, ws, WS_EPOCH, 0, SC1);
      drain();
      if (lane == 0) st2c(ws, WS_XRDY + blk * 64 + wave * 8, v2u{tag, ~tag});
      DUMP(); return;
    }
  } else {
    const int sj = j >= 30 ? j - 30 : j + 2;
    const int idx = sj * 8 + grp;
    const bool isC = j < 14 || idx == 7;
    const int pair = j < 14 ? grp * 14 + j : 112;
    const int lc = grp + 8 * (j >> 1);
    const bool urgent = j < 4;
    const int qu = urgent ? (lc < 14 ? 2 * lc + (j & 1) : -1) : sj < 2 && grp < 7 ? 28 + grp * 6 + sj : -1;
    const int t1o = WS_T1 + (32 * wave + (lane >> 3)) * T1_BLK + pair * 128 + (lane & 7) * 16;
    ModLoad ml;
    epoch = ld1c(ws, WS_EPOCH);
#pragma unroll
    for (int q = 0; q < 4; ++q) st4c(ws, isC ? t1o + 8 * q * T1_BLK : OOB, sv);
    sb();
    mod_load(ml, cin, wm, qu, tid);
    u32 pf0, pf1, pf2;
    {
      const rsrc_t mr = mkrs(wm, D * 6 * D * 2), xr = mkrs(xin, S * D * 2), fr_ = mkrs((wave & 1) ? wi : wr, S * DF * 2);
      const int row = (sj * 4 + wave) * 7 + (lane >> 1), l7 = lane / 7;
      pf0 = __builtin_amdgcn_raw_buffer_load_b32(mr, lane < 14 && (grp < 6 || !(lane & 1)) ? row * (6 * D * 2) + (grp + 8 * (lane & 1)) * 128 : OOB, 0, PF_AUX);
      pf1 = __builtin_amdgcn_raw_buffer_load_b32(xr, lane < (sj < 6 ? 0 : sj < 10 ? 28 : 56) ? tok_row(16 * grp + (lane < 28 ? sj : sj - 10), 4 * wave + (l7 & 3)) * (D * 2) + (lane - 7 * l7) * 128 : OOB, 0, PF_AUX);
      pf2 = __builtin_amdgcn_raw_buffer_load_b32(fr_, (lane & 31) < 29 && lane < (sj < 6 ? 0 : sj < 10 ? 32 : 64) ? (((16 * grp + (lane < 32 ? sj : sj - 10)) * 7200) & ~127) + ((wave >> 1) * 29 + (lane & 31)) * 128 : OOB, 0, PF_AUX);
    }
    sb();
    asm volatile("s_waitcnt vmcnt(%0)" :: "i"(W_LATE ? 16 : 17) : "memory");
    epoch = __builtin_amdgcn_readfirstlane(epoch);
    tag = mktag(epoch);
    bar();
    if (wave == 1 && lane < 8) st2c(ws, isC ? WS_RDY + lane * 1024 + pair * 8 : OOB, v2u{tag, ~tag});
    if (qu >= 0) {
      mod_part(ml, L, tid);
      bar();
      if (wave == 0) mod_publish(L, ws, qu, lane, tag);
    }
    STAMP(1);
    pin(pf0); pin(pf1); pin(pf2); pin(pfa); pin(pfb);
    const int qn = grp == 7 ? -1 : sj >= 6 && sj < 10 ? 28 + grp * 6 + (sj - 4) : sj < 2 ? 70 + grp * 2 + sj : -1;
    const int tw = sj >= 10 ? (sj - 10) * 8 + grp : sj == 6 && grp == 7 ? 48 : -1;
    v2u dw[2][8];
    if (qn >= 0) mod_load(ml, cin, wm, qn, tid);
    if (tw >= 0) {
      const rsrc_t dr = mkrs(wd, F * D * 2);
      const int u0 = tw * 4 + wave, uc = u0 / 28, kap = 2 * (u0 - uc * 28);
#pragma unroll
      for (int e = 0; e < 16; ++e)
        dw[e >> 3][e & 7] = __builtin_amdgcn_raw_buffer_load_b64(dr, (4 * g * D + 4 * c16) * 2 + (e & 3) * D * 2, ((32 * kap + 16 * (e >> 2)) * D + 64 * uc) * 2, PF_AUX);
    }
    sb();
    u32 pw[4];
    {
      const rsrc_t rg = mkrs(wg, D * F * 2), ru = mkrs(wu, D * F * 2);
      const int nr = sj < 2 ? NR_S : sj < 6 ? NR_U : sj < 10 ? NR_M : NR_T;
      const int r0 = (sj < 2 ? sj * NR_S : sj < 6 ? 2 * NR_S + (sj - 2) * NR_U : sj < 10 ? 2 * NR_S + 4 * NR_U + (sj - 6) * NR_M
                                                                             : 2 * NR_S + 4 * NR_U + 4 * NR_M + (sj - 10) * NR_T) * 4 + wave * nr;
      static_assert(2 * NR_S + 4 * NR_U + 4 * NR_M + 6 * NR_T == 112 && NR_S <= 16 && NR_U <= 16, "all rows");
      const int r7 = lane / 7, po = (r0 + r7) * (F * 2) + ((grp & 3) * 7 + lane - r7 * 7) * 128;
      const int o0 = lane < 56 && r7 < nr ? po : OOB, o1 = lane < 56 && r7 + 8 < nr ? po + 8 * (F * 2) : OOB;
      pw[0] = ld1(rg, o0); pw[1] = ld1(ru, o0); pw[2] = ld1(rg, o1); pw[3] = ld1(ru, o1);
    }
    sb();
    STAMP(20);
    v4f t1c[2], t1s[2], t2c[2], t2s[2];
#pragma unroll
    for (int t = 0; t < 2; ++t) {
#pragma unroll
      for (int r = 0; r < 4; ++r) {
        const float a1 = (float)((16 * t + 4 * g + r) * (16 * wave + c16)) * (1.f / S);
        const float a2 = (float)((16 * t + c16) * (16 * wave + 4 * g + r)) * (1.f / S);
        t1c[t][r] = __builtin_amdgcn_cosf(a1); t1s[t][r] = __builtin_amdgcn_sinf(a1);
        t2c[t][r] = __builtin_amdgcn_cosf(a2); t2s[t][r] = __builtin_amdgcn_sinf(a2);
      }
      pin(t1c[t]); pin(t1s[t]); pin(t2c[t]); pin(t2s[t]);
    }
    if (tw >= 0) {
#pragma unroll
      for (int tt = 0; tt < 8; ++tt) {
        v4u o;
#pragma unroll
        for (int m = 0; m < 4; ++m)
          o[m] = __builtin_amdgcn_perm(dw[tt >> 2][2 * m + 1][(tt & 3) >> 1], dw[tt >> 2][2 * m][(tt & 3) >> 1], (tt & 1) ? 0x07060302u : 0x05040100u);
        st4c(ws, WS_WD + (tw * 4 + wave) * 8192 + tt * 1024 + l16, o);
      }
    }
    if (qn >= 0) {
      bar();
      mod_part(ml, L, tid);
      bar();
      if (wave == 0) mod_publish(L, ws, qn, lane, tag);
    }
    pin(pw[0]); pin(pw[1]); pin(pw[2]); pin(pw[3]);
    STAMP(21);
    if (!isC) { DUMP(); return; }
    v4u c64[2][4], c32[2][2][2];
#pragma unroll
    for (int cp = 0; cp < 2; ++cp)
#pragma unroll
      for (int s = 0; s < 4; ++s) c64[cp][s] = ld4(tab, l16 + s * 1024, (TAB_P2 + wave * 2 + cp) * 4096);
#pragma unroll
    for (int t = 0; t < 2; ++t)
#pragma unroll
      for (int f = 0; f < 4; ++f) c32[t][f >> 1][f & 1] = ld4(tab, l16 + f * 1024, (TAB_P2 + 8 + t) * 4096);
    sb();
    if (tw >= 0) {
      asm volatile("s_waitcnt vmcnt(16)" ::: "memory");
      bar();
      if (tid < 8) st2c(ws, WS_DRDY + tid * 1024 + tw * 8, v2u{tag, ~tag});
    }
    STAMP(2);
    int wfo = WS_WT + pair * 16384 + (16 * g + wave) * 128 + c16 * 8;
    pin(wfo);
    v2u wf[2][4];
    {
      int wl = L_CI + ((lane >> 2) & 1) * 8256 + wave * 2048 + ((lane >> 3) * 4 + (lane & 3)) * 16, lo = t1o;
      pin(wl); pin(lo);
      light_poll(ws, WS_T1 + (32 * wave + (pair & 31)) * T1_BLK + pair * 128);
      STAMP(13);
      v4u z[4];
      _Pragma("nounroll") for (int s = 0; s < SPIN; ++s) {
        lo = fresh(lo);
#pragma unroll
        for (int q = 0; q < 4; ++q) z[q] = ld4c(ws, lo, 8 * q * T1_BLK);
        if (wall(max(max(max4(z[0]), max4(z[1])), max(max4(z[2]), max4(z[3]))) != SENT)) break;
      }
      STAMP(14);
#pragma unroll
      for (int q = 0; q < 4; ++q) *(LAS v4u*)(L + wl + q * 512) = z[q];
    }
    bar();
    v2u fd;
    sb();
    STAMP(4);
    {
      v4u a[2][2][4];
#pragma unroll
      for (int c = 0; c < 2; ++c)
#pragma unroll
        for (int mt = 0; mt < 2; ++mt)
#pragma unroll
          for (int s = 0; s < 4; ++s) a[c][mt][s] = lds4(LB, L_CI + c * 8256 + (s * 2 + mt) * 1024);
      v4u old[2][2];
#pragma unroll
      for (int c = 0; c < 2; ++c)
#pragma unroll
        for (int mt = 0; mt < 2; ++mt) old[c][mt] = lds4(LB + wave * 2048, L_CI + c * 8256 + mt * 1024);
      sb();
#pragma unroll
    for (int t = 0; t < 2; ++t)
#pragma unroll
      for (int r = 0; r < 4; ++r) wf[t][r] = ld2c(ws, wfo + r * 512, t * 8192);
    fd = ld2c(ws, WS_DRDY + grp * 1024 + min(lane, 48) * 8);
      sb();
      v4f re[2][2], im[2][2], ur[2][2], ui[2][2], fr_[2][2], fi_[2][2];
      u32 f2[2][2][4], f3[2][2][4], f4[4];
      char LAS* xw = L + lb + wave * 1024 - L_B;
#define V4(x) v4u{x[0], x[1], x[2], x[3]}
#define S1K(h) c64[0][2 * h], c64[0][2 * h + 1], c64[1][2 * h], c64[1][2 * h + 1]
#define S1X(c, t, h) re[c][t], im[c][t], a[c][t][2 * h], a[c][t][2 * h + 1], S1K(h)
#define S2X(c, t) ur[c][t], ui[c][t], V4(f2[c][0]), V4(f2[c][1]), c32[t][0][0], c32[t][0][1], c32[t][1][0], c32[t][1][1]
#define S3X(c, t) im[c][t], re[c][t], V4(f3[c][0]), V4(f3[c][1]), c32[t][0][0], c32[t][0][1], c32[t][1][0], c32[t][1][1]
#define CM(o, P, Q, C, S_, h) o[2 * h], o[2 * h + 1], P[2 * h], Q[2 * h], C[2 * h], S_[2 * h], P[2 * h + 1], Q[2 * h + 1], C[2 * h + 1], S_[2 * h + 1]
#define TW1(c, t, h) CM(f2[c][t], re[c][t], im[c][t], t1c[t], t1s[t], h)
#define FLT(c, t, h) CM(f3[c][t], ur[c][t], ui[c][t], fr_[c][t], fi_[c][t], h)
#define TW2(c, t, h) CM(f4, re[c][t], im[c][t], t2c[t], t2s[t], h)
#define HV(x, h) (h ? hi2(x) : lo2(x))
#define CP(PL, o, P, Q, C, S_, h) cmul2<PL>(o[2 * h], o[2 * h + 1], HV(P, h), HV(Q, h), HV(C, h), HV(S_, h));
#define XW(c, t) *(LAS v4u*)(xw + L_CX + (c * 2 + t) * 4096) = V4(f4);
#define UNP(c, t) { _Pragma("unroll") for (int r = 0; r < 4; ++r) { fr_[c][t][r] = blo(wf[t][r][c]); fi_[c][t][r] = bhi(wf[t][r][c]); } \
      pin(fr_[c][t]); pin(fi_[c][t]); }
      nop2();
      m4_a0(S1X(0, 0, 0)); m4_a1(S1X(0, 0, 1)); m4_a0(S1X(0, 1, 0)); mfence<3>(re[0][0], im[0][0]);
      h4_a1m(S1X(0, 1, 1), TW1(0, 0, 0)); h4_a0m(S1X(1, 0, 0), TW1(0, 0, 1));
      h4_a1m(S1X(1, 0, 1), TW1(0, 1, 0)); h4_a0m(S1X(1, 1, 0), TW1(0, 1, 1));
      h4_a1m(S1X(1, 1, 1), TW1(1, 0, 0)); h4_b0m(S2X(0, 0), TW1(1, 0, 1));
      h4_b0m(S2X(0, 1), TW1(1, 1, 0));
      sb();
      CP(false, f2[1][1], re[1][1], im[1][1], t1c[1], t1s[1], 1)
      UNP(0, 0) UNP(0, 1)
      sb();
      h4_b0p(S2X(1, 0), FLT(0, 0, 0)); h4_b0p(S2X(1, 1), FLT(0, 0, 1));
      sb();
      CP(true, f3[0][1], ur[0][1], ui[0][1], fr_[0][1], fi_[0][1], 0) CP(true, f3[0][1], ur[0][1], ui[0][1], fr_[0][1], fi_[0][1], 1)
      UNP(1, 0) UNP(1, 1)
      sb();
      h4_a0p(S3X(0, 0), FLT(1, 0, 0)); h4_a0p(S3X(0, 1), FLT(1, 0, 1));
      sb();
      CP(true, f3[1][1], ur[1][1], ui[1][1], fr_[1][1], fi_[1][1], 0) CP(true, f3[1][1], ur[1][1], ui[1][1], fr_[1][1], fi_[1][1], 1)
#pragma unroll
      for (int e = 0; e < 4; ++e) pin(f3[1][1][e]);
      nop2();
      h4_a0p(S3X(1, 0), TW2(0, 0, 0)); h4_a0p(S3X(1, 1), TW2(0, 0, 1));
      XW(0, 0)
      sb();
      CP(true, f4, re[0][1], im[0][1], t2c[1], t2s[1], 0) CP(true, f4, re[0][1], im[0][1], t2c[1], t2s[1], 1) XW(0, 1)
      sb();
      mfence<0>(re[1][0], im[1][0], re[1][1], im[1][1]);
      CP(true, f4, re[1][0], im[1][0], t2c[0], t2s[0], 0) CP(true, f4, re[1][0], im[1][0], t2c[0], t2s[0], 1) XW(1, 0)
      CP(true, f4, re[1][1], im[1][1], t2c[1], t2s[1], 0) CP(true, f4, re[1][1], im[1][1], t2c[1], t2s[1], 1) XW(1, 1)
      bar();
#pragma unroll
      for (int c = 0; c < 2; ++c)
#pragma unroll
        for (int nt = 0; nt < 2; ++nt)
#pragma unroll
          for (int s = 0; s < 4; ++s) a[c][nt][s] = lds4(LB, L_CX + ((c * 2 + nt) * 4 + s) * 1024);
      const int o2 = WS_T1 + (32 * wave + (lane >> 2)) * T1_BLK + pair * 128 + (lane & 3) * 16;
#define S4(c, t) mf8(im[c][t], re[c][t], c64[0], a[c][t], c64[1], a[c][t]);
#define NEQ(x, y) ((x) + ((x) == (y)))
#define OUT(c, t) { const v4u o = {pk(re[c][t].x, im[c][t].x), pk(re[c][t].y, im[c][t].y), pk(re[c][t].z, im[c][t].z), pk(re[c][t].w, im[c][t].w)}; \
      st4c(ws, o2 + c * 64 + t * 16 * T1_BLK, v4u{NEQ(o.x, old[c][t].x), NEQ(o.y, old[c][t].y), NEQ(o.z, old[c][t].z), NEQ(o.w, old[c][t].w)}); sb(); }
      S4(0, 0) S4(0, 1) mfence<0>(re[0][0], im[0][0]); OUT(0, 0)
      S4(1, 0) mfence<0>(re[0][1], im[0][1]); OUT(0, 1)
      S4(1, 1) mfence<0>(re[1][0], im[1][0]); OUT(1, 0)
      mfence<0>(re[1][1], im[1][1]); OUT(1, 1)
    }
    STAMP(5);
    sb();
    if (!isG) { DUMP(); return; }
    if (!wall(fd.x == tag && fd.y == ~tag)) wait_flags(ws, WS_DRDY + grp * 1024 + min(lane, 48) * 8, tag);
    dma_w1(wg, wu, slice, wave, lane, lbase);
#pragma unroll
    for (int f = 0; f < WD_MID; ++f) wdn[f >> 2][f & 3] = ld4c(ws, wl16 + (f & 3) * 1024, wdb + (f >> 2) * 4096);
    fd = ld2c(ws, hro);
    sb();
    ACT_CLEAR(0, 16)
    sb();
    if (!wall(fd.x == tag && fd.y == ~tag)) wait_flags(ws, hro, tag);
    bar();
  }

  STAMP(7);
  const int blk0 = 16 * tile + 4 * (grp & 3);
  const int ab = WS_ACT + blk0 * 57344 + wave * 14 * 1024;
  v4u h[14][2];
  {
    const int hb = WS_H2 + (16 * tile + 4 * wave) * H2_BLK;
    int tb[2][4];
#pragma unroll
    for (int u = 0; u < 8; ++u) {
      tb[u >> 2][u & 3] = (((lane >> 2) * 128 + ((lane >> 3) & 3) * 32 + (lane & 3) * 8) ^ ((u & 3) << 5)) + (u >> 2) * 57344;
      pin(tb[u >> 2][u & 3]);
    }
#define W_RD(dst, k, u) dst = ldtr2(L + tb[k >= 7][u & 3], (k % 7) * 8192 + (u >> 2) * 4096, (k % 7) * 8192 + (u >> 2) * 4096 + 2048)
#define H_LD(v, tt) hh_[v][tt] = ld4c(ws, l16 + (((v) % 14) & 3) * 1024, hb + (2 * ((v) / 14) + (tt)) * H2_BLK + (((v) % 14) >> 2) * 4096)
    const int abx[2] = {ab + (wave >> 1) * 57344, ab + (1 - (wave >> 1)) * 57344};
#define G_LD(e) h[(e) >> 1][(e) & 1] = ld4c(ws, l16 + (((e) >> 1) & 3) * 1024, abx[(e) & 1] + ((e) >> 3) * 4096)
    v4u wfr[2][8], hh_[28][2];
    v4f acc[8][2];
    v2u fa;
    _Pragma("nounroll") for (rep_ = 0; rep_ < 2; ++rep_) {
#pragma unroll
    for (int u = 0; u < 8; ++u) W_RD(wfr[0][u], 0, u);
    light_poll(ws, hb + (lane & 1) * H2_BLK + 13 * 1024);
    STAMP(15);
#pragma unroll
    for (int v = 0; v < LEAD; ++v) { H_LD(v, 0); H_LD(v, 1); }
    sb();
    if (!isR && lane < (CLR_MODE == 1 ? 1 : 2))
      st2c(ws, WS_CRDY + ((tile + 2 * lane) * 4 + (grp & 3)) * 256 + (ct * 4 + wave) * 8, v2u{tag, ~tag});
    sb();
#define BAD2(x0, x1) wany(max(max(max(max(x0.x, x0.y), x0.z), max(max(x0.w, x1.x), x1.y)), max(x1.z, x1.w)) == SENT)
#define RETRY2(x0, x1, o0, o1) \
      _Pragma("nounroll") for (int s_ = 0; s_ < SPIN; ++s_) { \
        const int lf_ = fresh(l16); SLOWCNT \
        x0 = ld4c(ws, lf_, o0); x1 = ld4c(ws, lf_, o1); \
        if (wall(max(max4(x0), max4(x1)) != SENT)) break; \
      }
    if (__builtin_expect(BAD2(hh_[0][0], hh_[0][1]), 0)) { RETRY2(hh_[0][0], hh_[0][1], hb, hb + H2_BLK) }
    STAMP(8); CSTAMP(28);
    const int fx = WS_CRDY + (tile * 4 + (lane < 32 ? wave : grp & 3)) * 256 + min(lane & 31, 27) * 8;
    const int ao = WS_ACT + (16 * tile + 4 * wave) * 57344 + 2 * slice * 1024 + l16;
#define ACT_OUT(ps, tt, f) { v4u o; \
      _Pragma("unroll") for (int hh = 0; hh < 2; ++hh) { \
        const v4f av = relu4(acc[2 * f + hh][tt]) * acc[4 + 2 * f + hh][tt]; \
        o[2 * hh] = pk2(lo2(av)); o[2 * hh + 1] = pk2(hi2(av)); } \
      st4c(ws, ao + (2 * ps + tt) * 57344 + f * 1024, o); }
#define ACT_ALL(ps) { mfence<0>(acc[0][0], acc[1][0], acc[2][0], acc[3][0]); mfence<0>(acc[4][0], acc[5][0], acc[6][0], acc[7][0]); \
      ACT_OUT(ps, 0, 0) ACT_OUT(ps, 0, 1) sb(); \
      mfence<0>(acc[0][1], acc[1][1], acc[2][1], acc[3][1]); mfence<0>(acc[4][1], acc[5][1], acc[6][1], acc[7][1]); \
      ACT_OUT(ps, 1, 0) ACT_OUT(ps, 1, 1) sb(); }
#define W_RH(dst, k, u, hf) { const v2u t_ = ldtr(L + tb[k >= 7][u & 3], (k % 7) * 8192 + (u >> 2) * 4096 + hf * 2048); \
      dst[2 * hf] = t_.x; dst[2 * hf + 1] = t_.y; }
#pragma unroll
    for (int ps = 0; ps < 2; ++ps)
#pragma unroll
    for (int ks = 0; ks < 14; ++ks) {
      const int v = ps * 14 + ks, cur = v & 1, nk = (v + 1) % 14, vn = v < 27 ? v + 1 : v;
      const bool more = v < 27;
      u32 m0, m1;
      __builtin_amdgcn_s_waitcnt(0xC07F);
      sb();
#pragma unroll
      for (int i = 0; i < 16; ++i) {
        const int u = i & 7, tt = i >> 3;
        if (ks == 0) mv0(acc[u][tt], wfr[cur][u], hh_[v][tt]); else mv(acc[u][tt], wfr[cur][u], hh_[v][tt]);
        if (more) {
          if (i < 4) { W_RH(wfr[cur ^ 1][i], nk, i, 0) W_RH(wfr[cur ^ 1][i], nk, i, 1) }
          else if (i < 12) W_RH(wfr[cur ^ 1][4 + ((i - 4) >> 1)], nk, (4 + ((i - 4) >> 1)), (i & 1))
        }
        if (i == 5 && v + LEAD < 28) H_LD(v + LEAD, 0);
        if (i == 7 && v + LEAD < 28) H_LD(v + LEAD, 1);
        if (i == 9 || i == 11 || i == 13) {
          const int e = (i - 9) >> 1, f = WD_MID + (v < 14 ? v - 2 : 12 + WD_PER * (v - 14) + e);
          if (f < 56 && ((v >= 2 && v < 14 && e == 0) || (v >= 14 && e < WD_PER))) wdn[f >> 2][f & 3] = ld4c(ws, wl16 + (f & 3) * 1024, wdb + (f >> 2) * 4096);
        }
        if (v > A_V && (i & 1) && i >= 5 && ((i - 5) >> 1) < A_PER) G_LD(A_PER * (v - A_V - 1) + ((i - 5) >> 1));
        if (v == 8 && i == 13) fa = ld2c(ws, fx);
        if (more && i == 12) { m0 = max(max(hh_[vn][0].x, hh_[vn][0].y), hh_[vn][0].z); pin(m0); }
        if (more && i == 13) { m1 = max(max(hh_[vn][0].w, hh_[vn][1].x), hh_[vn][1].y); pin(m1); }
        if (more && i == 14) { m0 = max(max(m0, hh_[vn][1].z), hh_[vn][1].w); m0 = max(m0, m1); pin(m0); }
        sb();
      }
      if (v == 12 && !wall(fa.x == tag && fa.y == ~tag)) wait_flags(ws, fx, tag);
      if (v == 19 && isR) {
        static_assert(19 + LEAD < 28, "the count below");
        asm volatile("s_waitcnt vmcnt(12)" ::: "memory");
        if (lane == 0) st2c(ws, WS_XRDY + (16 * grp + j - 14) * 64 + wave * 8, v2u{tag, ~tag});
      }
      if (more && __builtin_expect(wany(m0 == SENT), 0)) {
        const int pb = 2 * ((v + 1) / 14) * H2_BLK + nk * 1024;
        RETRY2(hh_[vn][0], hh_[vn][1], hb + pb, hb + pb + H2_BLK)
      }
      if (v == 13) {
        STAMP(16); CSTAMP(29);
        ACT_ALL(0)
        if (isR) x2_store(ws, x2r, 16 * grp + j - 14, wave, l16);
        nop2();
      }
      sb();
    }
    STAMP(27);
    }
    rep_ = 1;
    const int ao = WS_ACT + (16 * tile + 4 * wave) * 57344 + 2 * slice * 1024 + l16;
    ACT_ALL(1)
    CSTAMP(30);
#pragma unroll
    for (int e = A_PER * (27 - A_V); e < 28; ++e) G_LD(e);
    sb();
    STAMP(9);
    v4u a[14][2];
    v4f ac2[4][4];
#define A_LD(e) a[(e) >> 1][(e) & 1] = ld4c(ws, l16 + (((e) >> 1) & 3) * 1024, abx[(e) & 1] + 2 * 57344 + ((e) >> 3) * 4096)
    const int po = ab + 3 * 57344 + min(lane, 6) * 2048 + 1024;
#define KN (ks < 13 ? ks + 1 : ks)
    u32 probe;
#pragma unroll
    for (int ks = 0; ks < 14; ++ks) {
      if (ks == 0 && __builtin_expect(BAD2(h[0][0], h[0][1]), 0)) { RETRY2(h[0][0], h[0][1], abx[0], abx[1]) }
#pragma unroll
      for (int tt = 0; tt < 2; ++tt)
#pragma unroll
        for (int n = 0; n < 4; ++n) ac2[tt][n] = mfma(wdn[ks][n], h[ks][tt], ks ? ac2[tt][n] : splat(0.f));
      const bool bad = ks < 13 && BAD2(h[KN][0], h[KN][1]);
      if (ks == P_AT) probe = ld1c(ws, po);
#pragma unroll
      for (int u = 0; u < 8; ++u) { __builtin_amdgcn_sched_group_barrier(0x8, 1, 0); if (u >= 2) __builtin_amdgcn_sched_group_barrier(0x2, 1, 0); }
      sb();
      if (__builtin_expect(bad, 0)) { RETRY2(h[KN][0], h[KN][1], abx[0] + KN * 1024, abx[1] + KN * 1024) }
      if (ks == B_AT2) {
        STAMP(25);
        if (wany(probe == SENT)) light_poll(ws, po);
        STAMP(26);
      }
      if (ks >= B_AT2) {
#pragma unroll
        for (int e = 4 * (ks - B_AT2); e < 4 * (ks - B_AT2 + 1); ++e) A_LD(e);
      }
      sb();
    }
    STAMP(17);
#pragma unroll
    for (int e = 4 * (14 - B_AT2); e < 4 * (14 - B_AT2) + 6; ++e) A_LD(e);
    sb();
    int rw[3], rr_ = L_B + lane * 8 + wave * 6144, oo[2];
#pragma unroll
    for (int x = 1; x < 4; ++x) { rw[x - 1] = L_B + lane * 8 + (wave ^ x) * 6144 + (x - 1) * 2048; pin(rw[x - 1]); }
    pin(rr_);
    const int blk = blk0 + (wave >> 1);
#pragma unroll
    for (int ph = 0; ph < 2; ++ph) { oo[ph] = (tok_row(blk + 2 * ph, c16) * D + ct * 64 + g * 16 + (wave & 1) * 8) * 2; pin(oo[ph]); }
#define RED_W1(ph, x, n) *(LAS v2f*)(L + rw[(x) - 1] + (ph) * 24576 + (n) * 512) = ((x) & 1) ? hi2(ac2[2 * (ph) + ((x) >> 1)][n]) : lo2(ac2[2 * (ph) + ((x) >> 1)][n]);
    const int n0 = ct * 64 + g * 16 + (wave & 1) * 8, reg = (n0 >= 112) + (n0 >= 224) + (n0 >= 336), xm = (0x1320 >> (4 * reg)) & 3;
    const int dz = abs(n0 - ((reg + 1) >> 1) * 224), db = (reg & 1) ? dz - 8 : dz;
#define XSLOT(m, dl) ((((dl) >> 4) * 2 + ((m) >> 1)) * 1024 + (((dl) >> 2) & 3) * 256 + ((m) & 1) * 8)
    int xo = WS_X2 + blk * X2_BLK + c16 * 16 + XSLOT(xm, db);
    int xe = WS_X2 + blk * X2_BLK + c16 * 16 + (n0 == 112 * reg && reg > 0 ? XSLOT(reg, 0) : XSLOT(xm, dz));
    pin(xo); pin(xe);
    const int xf = WS_XRDY + (blk + (lane & 4 ? 2 : 0)) * 64 + (lane & 3) * 8;
    v2u xp[2][2]; u32 x1[2];
    const int go = WS_MOD + grp * MOD_REP + (70 + 2 * ct) * 128 + g * 64 + (wave & 1) * 32;
    v4u xv[2], gv[2];
    STAMP(18);
    sb();
#pragma unroll
    for (int ks = 0; ks < 14; ++ks) {
      if (ks == 0 && __builtin_expect(BAD2(a[0][0], a[0][1]), 0)) { RETRY2(a[0][0], a[0][1], abx[0] + 2 * 57344, abx[1] + 2 * 57344) }
#pragma unroll
      for (int tt = 0; tt < 2; ++tt)
#pragma unroll
        for (int n = 0; n < 4; ++n) ac2[2 + tt][n] = mfma(wdn[ks][n], a[ks][tt], ks ? ac2[2 + tt][n] : splat(0.f));
#pragma unroll
      for (int e = 4 * (14 - B_AT2) + 6 + 3 * ks; e < 4 * (14 - B_AT2) + 9 + 3 * ks; ++e)
        if (e < 28) A_LD(e);
      const bool bad = ks < 13 && BAD2(a[KN][0], a[KN][1]);
      if (ks < 12) RED_W1(0, 1 + (ks >> 2), (ks & 3))
#pragma unroll
      for (int u = 0; u < 4; ++u) {
        __builtin_amdgcn_sched_group_barrier(0x8, 2, 0);
        __builtin_amdgcn_sched_group_barrier(0x20, 1, 0);
        if (u == 2) __builtin_amdgcn_sched_group_barrier(0x200, 1, 0);
        if (u >= 1) __builtin_amdgcn_sched_group_barrier(0x2, 3, 0);
      }
      sb();
      if (__builtin_expect(bad, 0)) { RETRY2(a[KN][0], a[KN][1], abx[0] + 2 * 57344 + KN * 1024, abx[1] + 2 * 57344 + KN * 1024) }
      if (ks == 2) fa = ld2c(ws, xf);
      if (ks == 7) {
        if (!wall(fa.x == tag && fa.y == ~tag)) wait_flags(ws, xf, tag);
#pragma unroll
        for (int ph = 0; ph < 2; ++ph) {
          xp[ph][0] = ld2c(ws, xo, ph * 2 * X2_BLK); xp[ph][1] = ld2c(ws, xo + 256, ph * 2 * X2_BLK);
          x1[ph] = __builtin_amdgcn_raw_buffer_load_b16(ws, xe, ph * 2 * X2_BLK, SC1);
        }
        gv[0] = ld4c(ws, go); gv[1] = ld4c(ws, go + 16);
      }
      sb();
    }
    STAMP(10);
#pragma unroll
    for (int e = 0; e < 12; ++e) RED_W1(1, 3 - (e >> 2), (e & 3))
#define FIN_OK() wall(gv[0].y == tag && gv[0].w == tag && gv[1].y == tag && gv[1].w == tag)
    if (__builtin_expect(!FIN_OK(), 0)) {
      _Pragma("nounroll") for (int s = 0; s < SPIN; ++s) {
        const int gf = fresh(go);
        gv[0] = ld4c(ws, gf); gv[1] = ld4c(ws, gf + 16);
        if (FIN_OK()) break;
      }
    }
#pragma unroll
    for (int ph = 0; ph < 2; ++ph) {
      const v2u p = xp[ph][0], q = xp[ph][1];
      const bool dn = reg & 1;
      xv[ph].x = __builtin_amdgcn_perm(dn ? q.y : p.x, x1[ph], 0x07060100u);
      xv[ph].y = dn ? __builtin_amdgcn_perm(q.x, q.y, 0x07060100u) : p.y;
      xv[ph].z = dn ? __builtin_amdgcn_perm(p.y, q.x, 0x07060100u) : q.x;
      xv[ph].w = dn ? __builtin_amdgcn_perm(p.x, p.y, 0x07060100u) : q.y;
      pin(xv[ph]);
    }
    bar();
#pragma unroll
    for (int ph = 0; ph < 2; ++ph) {
      v2f r[4];
#pragma unroll
      for (int n = 0; n < 4; ++n) {
        r[n] = lo2(ac2[2 * ph][n]);
#pragma unroll
        for (int w = 0; w < 3; ++w) r[n] += *(const LAS v2f*)(L + rr_ + w * 2048 + ph * 24576 + n * 512);
      }
      v4u o;
#pragma unroll
      for (int q = 0; q < 2; ++q) {
        o[2 * q] = pk2(fma2(bun(gv[q].x), v2f{r[0][q], r[1][q]}, bun(xv[ph][2 * q])));
        o[2 * q + 1] = pk2(fma2(bun(gv[q].z), v2f{r[2][q], r[3][q]}, bun(xv[ph][2 * q + 1])));
      }
      __builtin_amdgcn_raw_buffer_store_b128(o, mkrs(out, S * D * 2), oo[ph], 0, OUT_AUX);
    }
  }
  if (blockIdx.x == PADB) { while (__builtin_amdgcn_s_memrealtime() < padt + PADT) __builtin_amdgcn_s_sleep(1); }
  DUMP();
}

extern "C" void kernel_launch(void* const* d_in, const int* in_sizes, int n_in, void* d_out, int out_size, void* d_ws,
                              size_t ws_size, hipStream_t stream) {
  fused<<<256, 256, 0, stream>>>((const u16*)d_in[0], (const u16*)d_in[1], (const u16*)d_in[2], (const u16*)d_in[4],
                                 (const u16*)d_in[5], (const u16*)d_in[6], (const u16*)d_in[7], (const u16*)d_in[8],
                                 (u16*)d_out, (char*)d_ws);
}
```

```cpp
#define WD_MID 32
#define WD_EARLY 32
#define WD_PER 2
#include <hip/hip_runtime.h>

typedef unsigned short u16;
typedef unsigned int u32;
typedef unsigned long long u64;
typedef u32 v4u __attribute__((ext_vector_type(4)));
typedef u32 v2u __attribute__((ext_vector_type(2)));
typedef float v4f __attribute__((ext_vector_type(4)));
typedef float v2f __attribute__((ext_vector_type(2)));
typedef short v8s __attribute__((ext_vector_type(8)));
typedef short v4s __attribute__((ext_vector_type(4)));
typedef __bf16 v8b __attribute__((ext_vector_type(8)));
typedef __bf16 v2b __attribute__((ext_vector_type(2)));
#define LAS __attribute__((address_space(3)))
#define GAS __attribute__((address_space(1)))
#define DEV __device__ __forceinline__

#define C_W1_EARLY 0
#define CLR_DELAY 0
#define W_DELAY 0
#define CLR2_DELAY 0
#define PF_POS 2
#define SL1 1
#define SL2 2
#define SL4 4
#define X_AFTER 8
#define PF_AUX 0
#define W_LATE 0
#define W1_PF 1
#define DMA_MOD ""
#define LEAD 6
#define A_V 21
#define A_PER 4
#define WDV(f) ((f) - WD_MID + 2 < 14 ? (f) - WD_MID + 2 : ((f) - WD_MID - 12) / 2 + 16)
#define B_AT2 12
#define RE_PF 0
#define PF2_DELAY 0
#define C_DMA_EARLY 0
#define EVEN_PF 0
#define XBAL 1
#define W1EVEN 1
#define PRE_OUT 0
#define OUT_AUX 16
#define CLR_MODE 0
#define MK 0
#define MKD1 0
#define MKD2 0
#define MKD3 0
#define NR_S 0
#define NR_U 8
#define NR_M 8
#define NR_T 8
#define ALIAS 0
#define HD_V 6
#define P_AT 7
#define A_FIRST 10
#define B_FIRST 10
#define A_AT 9
#define F_AT 4
#define CODE_BYTES 45056
#define B_AT 9
constexpr int S = 2048, D = 448, DF = 225, F = 1792;

struct alignas(16) Chain { u16 v[2048]; };
struct Cos448 { u16 v[448]; };
constexpr double c_cos_t(double a) {
  double a2 = a * a, t = 1.0, s = 1.0;
  for (int i = 1; i <= 10; ++i) { t = -t * a2 / ((2 * i - 1) * (2 * i)); s += t; }
  return s;
}
constexpr double c_sin_t(double a) {
  double a2 = a * a, t = a, s = a;
  for (int i = 1; i <= 10; ++i) { t = -t * a2 / ((2 * i) * (2 * i + 1)); s += t; }
  return s;
}
constexpr double c_cos448(int m) {
  if (m > 224) m = 448 - m;
  double sg = 1.0;
  if (m > 112) { m = 224 - m; sg = -1.0; }
  const double w = 6.283185307179586476925286766559 / 448.0;
  return m == 112 ? 0.0 : (m <= 56 ? sg * c_cos_t(w * m) : sg * c_sin_t(w * (112 - m)));
}
constexpr u16 c_bf16(double d) {
  const u32 u = __builtin_bit_cast(u32, (float)d);
  return (u16)((u + 0x7FFFu + ((u >> 16) & 1u)) >> 16);
}
constexpr Cos448 mk_cos() { Cos448 c{}; for (int m = 0; m < 448; ++m) c.v[m] = c_bf16(c_cos448(m)); return c; }
constexpr Cos448 CTAB = mk_cos();
constexpr u16 bcos(int m) { return CTAB.v[m % 448]; }
constexpr u16 bsin(int m) { return CTAB.v[(m % 448 + 336) % 448]; }
constexpr u16 bneg(u16 b) { return b ? (u16)(b ^ 0x8000u) : b; }
constexpr u16 bscale(u16 b, int e) { return b ? (u16)(b + e * 0x80) : b; }

constexpr Chain mk_p1(int c) {
  const int tile = c >> 2, par = c & 1, sn = (c >> 1) & 1;
  Chain ch{};
  for (int s = 0; s < 4; ++s)
    for (int l = 0; l < 64; ++l) {
      const int kp = 16 * tile + (l & 15), k = 2 * kp + par;
      if (k > 224) continue;
      for (int j = 0; j < 8; ++j) {
        const int dl = 32 * s + 8 * (l >> 4) + j;
        if (dl > 112) continue;
        u16 b = sn ? bneg(bsin(k * dl)) : bcos(k * dl);
        if (dl == 0 || dl == 112) b = bscale(b, -1);
        ch.v[(s * 64 + l) * 8 + j] = b;
      }
    }
  return ch;
}
constexpr Chain mk_p3(int c) {
  const int tile = c >> 1, sn = c & 1;
  Chain ch{};
  for (int s = 0; s < 4; ++s)
    for (int l = 0; l < 64; ++l) {
      int dl = 16 * tile + (l & 15);
      if (c == 1 && dl == 0) dl = 112;
      for (int j = 0; j < 8; ++j) {
        const int k = 2 * (32 * s + 8 * (l >> 4) + j) + 1;
        if (k < 224) ch.v[(s * 64 + l) * 8 + j] = sn ? bsin(k * dl) : bcos(k * dl);
      }
    }
  return ch;
}
constexpr Chain mk_p2(int c) {
  Chain ch{};
  for (int f = 0; f < 4; ++f)
    for (int l = 0; l < 64; ++l)
      for (int j = 0; j < 8; ++j) {
        const int comp = c < 8 ? (c & 1) : (f >> 1), s = c < 8 ? f : (f & 1);
        const int o = 16 * (c < 8 ? (c >> 1) : (c - 8)) + (l & 15), i = 16 * s + 4 * (l >> 4) + (j >> 1);
        const int m = (c < 8 ? 7 : 14) * ((i * o) % 64);
        u16 b = 0;
        if (comp == 0) b = (j & 1) ? bsin(m) : bcos(m);
        else b = (j & 1) ? bcos(m) : bneg(bsin(m));
        ch.v[(f * 64 + l) * 8 + j] = b;
      }
  return ch;
}
template <int I> constexpr Chain P1C = mk_p1(I);
template <int I> constexpr Chain P3C = mk_p3(I);
template <int I> constexpr Chain P2C = mk_p2(I);
#define L10(T, b) T<b>, T<b + 1>, T<b + 2>, T<b + 3>, T<b + 4>, T<b + 5>, T<b + 6>, T<b + 7>, T<b + 8>, T<b + 9>
#define L29(T) L10(T, 0), L10(T, 10), T<20>, T<21>, T<22>, T<23>, T<24>, T<25>, T<26>, T<27>, T<28>
constexpr int TAB_P1 = 0, TAB_P3 = 29, TAB_P2 = 43, TAB_N = 53;
static __device__ const Chain TAB[TAB_N] = {L29(P1C), L10(P3C, 0), P3C<10>, P3C<11>, P3C<12>, P3C<13>, L10(P2C, 0)};

constexpr int WS_EPOCH = 0;
constexpr int WS_RDY = 4096;
constexpr int WS_HRDY = 4096 + 8192;
constexpr int WS_DRDY = 4096 + 16384;
constexpr int WS_XRDY = 131072 + 65536;
constexpr int WS_ARDY = 131072;
constexpr int WS_CRDY = 131072 + 32768;
constexpr int WS_MK1 = 212992, WS_MK2 = 221184, WS_MK3 = 229376;
constexpr int WS_MOD = 32768, MOD_REP = 12288;
constexpr int T1_BLK = 226 * 64;
constexpr int WS_T1 = 1 << 18;
constexpr int WS_WT = WS_T1 + 128 * T1_BLK;
constexpr int WS_H2 = WS_WT + 113 * 16384, H2_BLK = 14336, WS_X2 = WS_H2 + 128 * 14336, X2_BLK = 14336;
constexpr int WS_WD = WS_X2 + 128 * 14336;
constexpr int WS_ACT = WS_WD + 7 * 56 * 4096;
constexpr int WS_END = WS_ACT + 128 * 57344;
constexpr int WS_DBG = 32 << 20;

constexpr int L_W1 = 0;
constexpr int L_B = 114688;
constexpr int XP = 912;
constexpr int L_XS = L_B;
constexpr int L_ST = L_XS + 16 * XP;
constexpr int L_M1 = L_ST + 128;
constexpr int L_AF = L_M1 + 4 * 1792;
constexpr int L_M2 = L_AF + 16384;
constexpr int L_EX = L_M2 + 3 * 1792;
constexpr int L_REND = L_EX + 512;
constexpr int ZPL = 8192;
constexpr int L_CI = L_B;
constexpr int L_CX = L_CI + 2 * 8256;
constexpr int L_RED = L_CX + 16384;
constexpr int L_DBG = 163840 - 1024;
constexpr int L_SZ = 163840;
static_assert(L_REND <= L_DBG && L_RED + 2048 <= L_DBG && 2 * ZPL <= 16384, "lds");

constexpr u32 SENT = 0xFFFFFFFFu;
constexpr int SPIN = 1 << 14;
constexpr int SC1 = 16;
constexpr int OOB = 0x7FFF0000;

typedef __amdgpu_buffer_rsrc_t rsrc_t;
DEV rsrc_t mkrs(const void* p, int bytes) { return __builtin_amdgcn_make_buffer_rsrc((void*)p, 0, bytes, 0x00020000); }
DEV v4u ld4(rsrc_t r, int off, int so = 0) { return __builtin_amdgcn_raw_buffer_load_b128(r, off, so, 0); }
DEV v2u ld2(rsrc_t r, int off, int so = 0) { return __builtin_amdgcn_raw_buffer_load_b64(r, off, so, 0); }
DEV v4u ld4c(rsrc_t r, int off, int so = 0) { return __builtin_amdgcn_raw_buffer_load_b128(r, off, so, SC1); }
DEV v2u ld2c(rsrc_t r, int off, int so = 0) { return __builtin_amdgcn_raw_buffer_load_b64(r, off, so, SC1); }
DEV u32 ld1c(rsrc_t r, int off, int so = 0) { return __builtin_amdgcn_raw_buffer_load_b32(r, off, so, SC1); }
DEV void st4c(rsrc_t r, int off, v4u v) { __builtin_amdgcn_raw_buffer_store_b128(v, r, off, 0, SC1); }
DEV void st2c(rsrc_t r, int off, v2u v) { __builtin_amdgcn_raw_buffer_store_b64(v, r, off, 0, SC1); }
DEV float blo(u32 v) { return __builtin_bit_cast(float, v << 16); }
DEV float bhi(u32 v) { return __builtin_bit_cast(float, v & 0xFFFF0000u); }
DEV v2f bun(u32 v) { return v2f{blo(v), bhi(v)}; }
DEV u32 pk(float lo, float hi) { return __builtin_bit_cast(u32, __builtin_convertvector(v2f{lo, hi}, v2b)); }
DEV u32 pk2(v2f v) { return __builtin_bit_cast(u32, __builtin_convertvector(v, v2b)); }
DEV float rb(float f) { return blo(pk(f, 0.f)); }
DEV v2f lo2(v4f v) { return __builtin_shufflevector(v, v, 0, 1); }
DEV v2f hi2(v4f v) { return __builtin_shufflevector(v, v, 2, 3); }
DEV v2f fma2(v2f a, v2f b, v2f c) { return __builtin_elementwise_fma(a, b, c); }
DEV v4f fma4(v4f a, v4f b, v4f c) { return __builtin_elementwise_fma(a, b, c); }
typedef int v4i __attribute__((ext_vector_type(4)));
DEV v4f relu4(v4f v) { return __builtin_bit_cast(v4f, __builtin_elementwise_max(__builtin_bit_cast(v4i, v), v4i{0, 0, 0, 0})); }
DEV v4f splat(float f) { return v4f{f, f, f, f}; }
DEV v4f mfma(v4u a, v4u b, v4f c) {
  return __builtin_amdgcn_mfma_f32_16x16x32_bf16(__builtin_bit_cast(v8b, a), __builtin_bit_cast(v8b, b), c, 0, 0, 0);
}
DEV void mfa(v4f& c, v4u a, v4u b) { asm volatile("s_nop 1\n\tv_mfma_f32_16x16x32_bf16 %0, %1, %2, %0" : "+v"(c) : "v"(a), "v"(b)); }
DEV void mf0(v4f& c, v4u a, v4u b) { asm volatile("s_nop 1\n\tv_mfma_f32_16x16x32_bf16 %0, %1, %2, 0" : "=&v"(c) : "v"(a), "v"(b)); }
template <int N> DEV void mfence(v4f& a, v4f& b) { asm volatile("s_nop %2" : "+v"(a), "+v"(b) : "i"(N)); }
template <int N> DEV void mfence(v4f& a, v4f& b, v4f& c, v4f& d) { asm volatile("s_nop %4" : "+v"(a), "+v"(b), "+v"(c), "+v"(d) : "i"(N)); }
#define MF "v_mfma_f32_16x16x32_bf16 "
DEV void mf8(v4f& d0, v4f& d1, const v4u (&a0)[4], const v4u (&b0)[4], const v4u (&a1)[4], const v4u (&b1)[4]) {
  asm volatile("s_nop 1\n\t" MF "%0, %2, %6, 0\n\t" MF "%1, %10, %14, 0\n\t" MF "%0, %3, %7, %0\n\t" MF "%1, %11, %15, %1\n\t"
               MF "%0, %4, %8, %0\n\t" MF "%1, %12, %16, %1\n\t" MF "%0, %5, %9, %0\n\t" MF "%1, %13, %17, %1"
               : "=&v"(d0), "=&v"(d1)
               : "v"(a0[0]), "v"(a0[1]), "v"(a0[2]), "v"(a0[3]), "v"(b0[0]), "v"(b0[1]), "v"(b0[2]), "v"(b0[3]),
                 "v"(a1[0]), "v"(a1[1]), "v"(a1[2]), "v"(a1[3]), "v"(b1[0]), "v"(b1[1]), "v"(b1[2]), "v"(b1[3]));
}
#define H4_M(XA, D, X, K, C) MF D ", " XA(X, K) ", " C "\n\t"
#define H4_XA(X, K) X ", " K
#define H4_XB(X, K) K ", " X
#define H4_DEF(NAME, XA, C0, C1, CONS, N0, N1, PA, PB, PC, PD) \
DEV void NAME(v4f& d0, v4f& d1, v4u x0, v4u x1, v4u k00, v4u k01, v4u k10, v4u k11, \
              u32& o0, u32& o1, float p0, float q0, float c0, float s0, float p1, float q1, float c1, float s1) { \
  float t0, t1, t2, t3; \
  asm volatile(H4_M(XA, "%0", "%8", "%10", C0) "v_mul_f32 %4, %14, %16\n\tv_mul_f32 %5, %15, %16\n\t" \
               H4_M(XA, "%1", "%8", "%12", C1) "v_mul_f32 %6, %18, %20\n\tv_mul_f32 %7, %19, %20\n\t" \
               H4_M(XA, "%0", "%9", "%11", "%0") "v_fma_f32 %4, " N0 "%15, %17, %4\n\tv_fma_f32 %5, " N1 "%14, %17, %5\n\t" \
               H4_M(XA, "%1", "%9", "%13", "%1") "v_fma_f32 %6, " N0 "%19, %21, %6\n\tv_fma_f32 %7, " N1 "%18, %21, %7\n\t" \
               "v_cvt_pk_bf16_f32 %2, " PA ", " PB "\n\tv_cvt_pk_bf16_f32 %3, " PC ", " PD \
               : CONS(d0), CONS(d1), "=&v"(o0), "=&v"(o1), "=&v"(t0), "=&v"(t1), "=&v"(t2), "=&v"(t3) \
               : "v"(x0), "v"(x1), "v"(k00), "v"(k01), "v"(k10), "v"(k11), \
                 "v"(p0), "v"(q0), "v"(c0), "v"(s0), "v"(p1), "v"(q1), "v"(c1), "v"(s1)); \
}
H4_DEF(h4_a0m, H4_XA, "0", "0", "=&v", "", "-", "%4", "%5", "%6", "%7")
H4_DEF(h4_a1m, H4_XA, "%0", "%1", "+v", "", "-", "%4", "%5", "%6", "%7")
H4_DEF(h4_b0m, H4_XB, "0", "0", "=&v", "", "-", "%4", "%5", "%6", "%7")
H4_DEF(h4_b0p, H4_XB, "0", "0", "=&v", "-", "", "%5", "%4", "%7", "%6")
H4_DEF(h4_a0p, H4_XA, "0", "0", "=&v", "-", "", "%5", "%4", "%7", "%6")
#define M4_DEF(NAME, XA, C0, C1, CONS) \
DEV void NAME(v4f& d0, v4f& d1, v4u x0, v4u x1, v4u k00, v4u k01, v4u k10, v4u k11) { \
  asm volatile(H4_M(XA, "%0", "%2", "%4", C0) H4_M(XA, "%1", "%2", "%6", C1) H4_M(XA, "%0", "%3", "%5", "%0") MF "%1, " XA("%3", "%7") ", %1" \
               : CONS(d0), CONS(d1) : "v"(x0), "v"(x1), "v"(k00), "v"(k01), "v"(k10), "v"(k11)); \
}
M4_DEF(m4_a0, H4_XA, "0", "0", "=&v") M4_DEF(m4_a1, H4_XA, "%0", "%1", "+v")
M4_DEF(m4_b0, H4_XB, "0", "0", "=&v") M4_DEF(m4_b1, H4_XB, "%0", "%1", "+v")
DEV void mv(v4f& c, v4u a, v4u b) { asm volatile(MF "%0, %1, %2, %0" : "+v"(c) : "v"(a), "v"(b)); }
DEV void mv0(v4f& c, v4u a, v4u b) { asm volatile(MF "%0, %1, %2, 0" : "=&v"(c) : "v"(a), "v"(b)); }
DEV void nop2() { asm volatile("s_nop 1"); }
template <class T> DEV void pin(T& v) { asm volatile("" : "+v"(v)); }
DEV int fresh(int off) { asm volatile("" : "+v"(off) :: "memory"); return off; }
DEV bool wall(bool ok) { return __builtin_amdgcn_ballot_w64(!ok) == 0; }
DEV bool wany(bool b) { return __builtin_amdgcn_ballot_w64(b) != 0; }
DEV void sb() { __builtin_amdgcn_sched_barrier(0); }
DEV u32 max4(v4u v) { return max(max(v.x, v.y), max(v.z, v.w)); }
DEV void drain() { asm volatile("s_waitcnt vmcnt(0)" ::: "memory"); }
DEV void cfence() { asm volatile("" ::: "memory"); }
DEV void bar() { asm volatile("s_waitcnt lgkmcnt(0)\n\ts_barrier" ::: "memory"); }
template <int CTRL> DEV float dpp(float v) {
  return __builtin_bit_cast(float, __builtin_amdgcn_update_dpp(0, __builtin_bit_cast(int, v), CTRL, 0xF, 0xF, true));
}
DEV float sum16(float v) {
  v += dpp<0x128>(v); v += dpp<0x124>(v); v += dpp<0x122>(v); v += dpp<0x121>(v); return v;
}
DEV void sumg2(float& a, float& b) {
  float c = a, d = b;
  asm volatile("s_nop 1\n\tv_permlane16_swap_b32 %0, %1\n\tv_permlane16_swap_b32 %2, %3" : "+v"(a), "+v"(c), "+v"(b), "+v"(d));
  a += c; b += d; c = a; d = b;
  asm volatile("s_nop 1\n\tv_permlane32_swap_b32 %0, %1\n\tv_permlane32_swap_b32 %2, %3" : "+v"(a), "+v"(c), "+v"(b), "+v"(d));
  a += c; b += d;
}
DEV v4u lds4(const char LAS* L, int off) { return *(const LAS v4u*)(L + off); }
DEV v4f lds4f(const char LAS* L, int off) { return *(const LAS v4f*)(L + off); }
DEV v2u ldtr(const char LAS* L, int off) { return __builtin_bit_cast(v2u, __builtin_amdgcn_ds_read_tr16_b64_v4i16((LAS v4s*)(L + off))); }
DEV v4u ldtr2(const char LAS* L, int o0, int o1) { const v2u a = ldtr(L, o0), b = ldtr(L, o1); return v4u{a.x, a.y, b.x, b.y}; }
DEV void dma16(rsrc_t r, int voff, int soff, u32 lds_dst) {
  u32 keep;
  asm volatile("s_mov_b32 %0, m0\n\ts_mov_b32 m0, %1\n\ts_nop 0\n\tbuffer_load_dwordx4 %2, %3, %4 offen" DMA_MOD " lds\n\ts_mov_b32 m0, %0"
               : "=&s"(keep) : "s"(lds_dst), "v"(voff), "s"(r), "s"(soff) : "memory");
}
DEV void light_poll(rsrc_t ws, int off) {
  _Pragma("nounroll") for (int s = 0; s < SPIN; ++s) {
    if (wall(ld1c(ws, fresh(off)) != SENT)) break;
    __builtin_amdgcn_s_sleep(SL2);
  }
}
DEV int tok_row(int blk, int t) {
  const int n1 = 16 * (blk >> 5) + 4 * ((blk >> 2) & 3) + (t & 3), n2 = 16 * ((blk >> 4) & 1) + 4 * (blk & 3) + (t >> 2);
  return n2 + 32 * n1;
}
template <bool SWAP> DEV v4u cmul_pack(v4f a, v4f b, v4f c, v4f s) {
  const v4f re = fma4(b, s, a * c), im = fma4(b, c, -(a * s));
  return SWAP ? v4u{pk(im.x, re.x), pk(im.y, re.y), pk(im.z, re.z), pk(im.w, re.w)}
              : v4u{pk(re.x, im.x), pk(re.y, im.y), pk(re.z, im.z), pk(re.w, im.w)};
}

template <bool PLUS> DEV void cmul2(u32& o0, u32& o1, v2f p, v2f q, v2f c, v2f s) {
  const v2f re = fma2(PLUS ? -q : q, s, p * c), im = fma2(PLUS ? p : -p, s, q * c);
  o0 = PLUS ? pk(im.x, re.x) : pk(re.x, im.x); o1 = PLUS ? pk(im.y, re.y) : pk(re.y, im.y);
}

#define STAMP(i)
#define CSTAMP(i)
#define SLOWCNT
#define DUMP()

DEV void dma_w1(const u16* wg, const u16* wu, int slice, int wave, int lane, u32 lbase) {
  const rsrc_t rg = mkrs(wg, D * F * 2), ru = mkrs(wu, D * F * 2);
  const int r = lane >> 3, ch = ((((lane & 7) >> 1) ^ (lane >> 4)) & 3) * 32 + (lane & 1) * 16 + slice * 128;
  const int vp = r * (F * 2) + ch, vn = (7 - r) * (F * 2) + ch;
#pragma unroll
  for (int n = 0; n < 4; ++n) {
    const int ks = wave + 4 * n;
    if (n == 3 && wave >= 2) break;
    const int t16 = (ks >> 1) * 16, a = ks & 1;
#pragma unroll
    for (int q = 0; q < 4; ++q) {
      const int dl0 = t16 + 8 * (q & 1);
      const bool pos = (q >> 1) == a;
      const int d0 = q < 2 ? (a ? 224 - dl0 - 7 : dl0) : (a ? 224 + dl0 : 448 - dl0 - 7);
      int vo = pos ? vp : vn, so = d0 * (F * 2);
      if (n == 0 && q == 2) {
        vo += so; so = 0;
        if (wave < 2 && lane < 8) vo = (wave ? 336 : 112) * (F * 2) + ch;
      }
      dma16(rg, vo, so, lbase + L_W1 + ks * 8192 + q * 1024);
      dma16(ru, vo, so, lbase + L_W1 + ks * 8192 + 4096 + q * 1024);
    }
  }
}
struct ModLoad { u32 cv[7]; v4u mw[7]; };
DEV void mod_load(ModLoad& m, const u16* cin, const u16* wm, int q, int tid) {
  const rsrc_t cr = mkrs(cin, q >= 0 ? D * 2 : 0), mr = mkrs(wm, q >= 0 ? D * 6 * D * 2 : 0);
#pragma unroll
  for (int i = 0; i < 7; ++i) m.cv[i] = __builtin_amdgcn_raw_buffer_load_b16(cr, (tid >> 2) * 2 + i * 128, 0, 0);
#pragma unroll
  for (int i = 0; i < 7; ++i) m.mw[i] = __builtin_amdgcn_raw_buffer_load_b128(mr, ((tid >> 2) * (6 * D) + q * 32 + (tid & 3) * 8) * 2, i * 64 * 6 * D * 2, PF_AUX);
}
DEV void mod_part(ModLoad& m, char LAS* L, int tid) {
  float sl[7];
#pragma unroll
  for (int i = 0; i < 7; ++i) { const float cc = blo(m.cv[i]); sl[i] = rb(cc * __builtin_amdgcn_rcpf(1.f + __expf(-cc))); pin(sl[i]); }
  v2f a[4];
#pragma unroll
  for (int e = 0; e < 4; ++e) a[e] = v2f{0.f, 0.f};
#pragma unroll
  for (int i = 0; i < 7; ++i)
#pragma unroll
    for (int e = 0; e < 4; ++e) a[e] = fma2(bun(m.mw[i][e]), v2f{sl[i], sl[i]}, a[e]);
#pragma unroll
  for (int e = 0; e < 4; ++e) {
    a[e].x += dpp<0x128>(a[e].x); a[e].y += dpp<0x128>(a[e].y);
    a[e].x += dpp<0x124>(a[e].x); a[e].y += dpp<0x124>(a[e].y);
  }
  if ((tid & 15) < 4) {
    *(LAS v4f*)(L + L_RED + ((tid >> 4) * 32 + (tid & 15) * 8) * 4) = v4f{a[0].x, a[0].y, a[1].x, a[1].y};
    *(LAS v4f*)(L + L_RED + ((tid >> 4) * 32 + (tid & 15) * 8 + 4) * 4) = v4f{a[2].x, a[2].y, a[3].x, a[3].y};
  }
}
DEV void mod_publish(char LAS* L, rsrc_t ws, int q, int lane, u32 tag) {
  v4f s = {0.f, 0.f, 0.f, 0.f};
#pragma unroll
  for (int r = 0; r < 16; ++r) s += lds4f(L, L_RED + (r * 32 + (lane & 7) * 4) * 4);
  const float one = (q / 14 == 1 || q / 14 == 4) ? 1.f : 0.f;
  st4c(ws, WS_MOD + (lane >> 3) * MOD_REP + q * 128 + (lane & 7) * 16,
       v4u{pk(rb(s.x) + one, rb(s.y) + one), tag, pk(rb(s.z) + one, rb(s.w) + one), tag});
}
DEV u32 ld1(rsrc_t r, int off) { return __builtin_amdgcn_raw_buffer_load_b32(r, off, 0, 0); }
DEV u32 ld1n(rsrc_t r, int off) { return __builtin_amdgcn_raw_buffer_load_b32(r, off, 0, PF_AUX); }
DEV u32 mktag(u32 epoch) { return epoch * 0x9E3779B1u + 0x7F4A7C15u; }

DEV void x2_store(rsrc_t ws, const v4u (&r)[4], int blk, int wave, int l16) {
  const int o = WS_X2 + blk * X2_BLK + (wave == 0 ? 0 : 4 * wave - 2) * 1024 + l16;
#pragma unroll
  for (int m = 0; m < 4; ++m) st4c(ws, m < 2 || wave != 0 ? o + m * 1024 : OOB, r[m]);
}
DEV bool flags_ok(v4u rv, u32 tag) { return rv.x == tag && rv.y == ~tag && rv.z == tag && rv.w == ~tag; }
DEV void wait_flags(rsrc_t ws, int off, u32 tag) {
  _Pragma("nounroll") for (int s = 0; s < SPIN; ++s) {
    const v2u rv = ld2c(ws, fresh(off));
    if (wall(rv.x == tag && rv.y == ~tag)) break;
    __builtin_amdgcn_s_sleep(SL1);
  }
}

__global__ void __launch_bounds__(256, 1)
fused(const u16* __restrict__ xin, const u16* __restrict__ wr, const u16* __restrict__ wi, const u16* __restrict__ wg,
      const u16* __restrict__ wu, const u16* __restrict__ wd, const u16* __restrict__ cin, const u16* __restrict__ wm,
      u16* __restrict__ out, char* wsp) {
  __shared__ __attribute__((aligned(16))) char smem[L_SZ];
  char LAS* L = (char LAS*)smem;
  const u32 lbase = (u32)(size_t)L;
  const int tid = threadIdx.x, lane = tid & 63, wave = __builtin_amdgcn_readfirstlane(tid >> 6);
  const int g = lane >> 4, c16 = lane & 15, l16 = lane * 16;
  const int grp = blockIdx.x & 7, j = blockIdx.x >> 3;
  u64 pc;
  asm volatile("s_getpc_b64 %0" : "=s"(pc));
  STAMP(0); CSTAMP(31);
  asm volatile("" :: "s"(xin), "s"(wr), "s"(wi), "s"(wg), "s"(wu), "s"(wd), "s"(cin), "s"(wm), "s"(out), "s"(wsp));
  const rsrc_t ws = mkrs(wsp, WS_END);
  const rsrc_t tab = mkrs(TAB, TAB_N * 4096);
  const rsrc_t code = mkrs((const void*)(pc & ~127ull), CODE_BYTES);
  const bool isR = j >= 14 && j < 30;
  const bool isG = j < 28;
  const int gk = j / 7, ct = j - gk * 7;
  const int tile = 4 * (grp >> 2) + gk, slice = 7 * (grp & 3) + ct;
  const int wdb = WS_WD + (ct * 56 + wave * 14) * 4096;
  const v4u sv = {SENT, SENT, SENT, SENT};
  int lb = L_B + l16;
  pin(lb);
  const char LAS* LB = L + lb - L_B;
  const int pfo = ((j * 4 + wave) * 14 + lane) * 128, pfc = ((j * 4 + wave) * 4 + lane) * 128;
  u32 pfa = ld1(tab, lane < 14 ? pfo : OOB), pfb = ld1(code, lane < 4 ? pfc : OOB);
  const int hro = WS_HRDY + (16 * tile + 4 * wave) * 32 + c16 * 8;
  v4u x2r[4];
  const int wl16 = (lane ^ ((wave & 1) << 1)) * 16;
  v4u wdn[14][4];
  u32 epoch, tag;

#define ACT_CLEAR(lo, hi) _Pragma("unroll") for (int m = lo; m < hi; ++m) \
      st4c(ws, isG ? WS_ACT + (16 * (tile + 2 * (m >> 3)) + 4 * (grp & 3)) * 57344 + ct * 32768 + (m & 7) * 4096 + tid * 16 : OOB, sv);
  if (isR) {
    const int blk = 16 * grp + (j - 14);
    epoch = ld1c(ws, WS_EPOCH);
    const int t0 = wave == 0 ? 0 : 2 * wave - 1;
    v4u cf[8][4];
#define CF_LOAD(q) { const int chn = TAB_P1 + ((wave == 0 && q >= 4) ? 28 : 4 * t0 + q), vo = (q < 5 || wave != 0) ? l16 : OOB; \
      _Pragma("unroll") for (int s = 0; s < 4; ++s) cf[q][s] = ld4(tab, vo + s * 1024, chn * 4096); sb(); }
#pragma unroll
    for (int q = 0; q < X_AFTER; ++q) CF_LOAD(q)
    v2u xv[7];
    {
      const rsrc_t xr = mkrs(xin, S * D * 2);
      const int off = tok_row(blk, 4 * wave + g) * (D * 2) + c16 * 8;
#pragma unroll
      for (int m = 0; m < 7; ++m) xv[m] = ld2(xr, off + m * 128);
    }
    typedef u32 v3u __attribute__((ext_vector_type(3)));
    v2u fa_[2], fb_[2]; v3u fc_[2], fd_[2];
    {
      const rsrc_t rr = mkrs(wr, S * DF * 2), ri = mkrs(wi, S * DF * 2);
      const int fo_ = 16 * wave + c16 < 57 ? 2 * g * 450 + (16 * wave + c16) * 8 : OOB;
#pragma unroll
      for (int hh = 0; hh < 2; ++hh) {
        const int so = blk * 7200 + hh * 3600;
        fa_[hh] = ld2(rr, fo_, so); fb_[hh] = ld2(ri, fo_, so);
        fc_[hh] = __builtin_amdgcn_raw_buffer_load_b96(rr, fo_ + 448, so, 0); fd_[hh] = __builtin_amdgcn_raw_buffer_load_b96(ri, fo_ + 448, so, 0);
      }
    }
    sb();
#pragma unroll
    for (int q = X_AFTER; q < 8; ++q) CF_LOAD(q)
    STAMP(23);
    v2u rdy;
    {
      v2f f[14], s2 = {0.f, 0.f};
#pragma unroll
      for (int m = 0; m < 7; ++m) {
        f[2 * m] = bun(xv[m].x); f[2 * m + 1] = bun(xv[m].y);
        s2 += f[2 * m] + f[2 * m + 1];
        *(LAS v2u*)(L + L_XS + (4 * wave + g) * XP + c16 * 8 + m * 128) = xv[m];
      }
      STAMP(21);
      sb();
      {
        const int q = 16 * wave + c16, o = WS_WT + q * 16384 + blk * 128 + g * 16;
#pragma unroll
        for (int hh = 0; hh < 2; ++hh) {
          const v2u a = fa_[hh], b = fb_[hh]; const v3u c = fc_[hh], d = fd_[hh];
          st4c(ws, q < 57 ? o + hh * 64 : OOB, v4u{__builtin_amdgcn_perm(b.x, a.x, 0x05040100u), __builtin_amdgcn_perm(b.y, a.y, 0x05040100u),
                                                   __builtin_amdgcn_perm(d.x, c.x, 0x07060302u), __builtin_amdgcn_perm(d.y, c.y, 0x07060302u)});
          st4c(ws, q < 56 ? o + hh * 64 + 57 * 16384 : OOB,
               v4u{__builtin_amdgcn_perm(b.x, a.x, 0x07060302u), __builtin_amdgcn_perm(b.y, a.y, 0x07060302u),
                   __builtin_amdgcn_perm(d.y, c.y, 0x05040100u), __builtin_amdgcn_perm(d.z, c.z, 0x05040100u)});
        }
      }
      rdy = ld2c(ws, WS_RDY + grp * 1024 + min(tid >> 1, 112) * 8);
      sb();
      const float mean = sum16(s2.x + s2.y) * (1.f / D);
      v2f q2 = {0.f, 0.f};
#pragma unroll
      for (int m = 0; m < 14; ++m) { const v2f d = f[m] - mean; q2 = fma2(d, d, q2); }
      const float rstd = __builtin_amdgcn_rsqf(sum16(q2.x + q2.y) * (1.f / D) + 1e-5f);
      if (c16 == 0) *(LAS v2f*)(L + L_ST + (4 * wave + g) * 8) = v2f{mean, rstd};
    }
    pin(pfa); pin(pfb);
    epoch = __builtin_amdgcn_readfirstlane(epoch);
    tag = mktag(epoch);
    STAMP(22);
    bar();
    v4f n[4][2];
    const int d0 = 8 * (4 * wave + g);
    const v2f st = *(const LAS v2f*)(L + L_ST + c16 * 8);
    {
      int xr_ = L_XS + c16 * XP + d0 * 2, xb_ = L_XS + c16 * XP + (216 - d0) * 2, x0_ = d0 == 0 ? xr_ : xb_ + 464;
      pin(xr_); pin(xb_); pin(x0_);
      const v4u a = lds4(L + xr_, 0), e = lds4(L + xr_, 448), c = lds4(L + xb_, 0), b = lds4(L + xb_, 448);
      const u32 c0 = *(const LAS u16*)(L + xb_ + 16), b0 = *(const LAS u16*)(L + x0_);
#pragma unroll
      for (int i = 0; i < 8; ++i) {
        n[0][i >> 2][i & 3] = (i & 1) ? bhi(a[i >> 1]) : blo(a[i >> 1]);
        n[3][i >> 2][i & 3] = (i & 1) ? bhi(e[i >> 1]) : blo(e[i >> 1]);
        n[1][i >> 2][i & 3] = i == 0 ? blo(b0) : (i & 1) ? bhi(b[(8 - i) >> 1]) : blo(b[(8 - i) >> 1]);
        n[2][i >> 2][i & 3] = i == 0 ? blo(c0) : (i & 1) ? bhi(c[(8 - i) >> 1]) : blo(c[(8 - i) >> 1]);
      }
      const float nm = -st.x * st.y;
#pragma unroll
      for (int m = 0; m < 4; ++m) {
#pragma unroll
        for (int h = 0; h < 2; ++h) { n[m][h] = fma4(n[m][h], splat(st.y), splat(nm)); pin(n[m][h]); }
      }
    }
    sb();
    STAMP(1);
    int mb = L_M1 + d0 * 4;
    pin(mb);
    v4u xs[4];
    int to[4];
    to[0] = WS_T1 + blk * T1_BLK + (16 * t0 + c16) * 64 + g * 16;
    to[1] = wave != 0 ? to[0] + 1024 : c16 < 2 ? to[0] + 112 * 64 : OOB;
    to[2] = to[0] + 114 * 64;
    to[3] = wave != 0 ? to[0] + 1024 + 114 * 64 : OOB;
#pragma unroll
    for (int q = 0; q < 4; ++q) pin(to[q]);

    {
      const int kind = tid >= 112, d = 4 * (tid - 112 * kind);
      int wb = L_M1 + (kind ? 0 : 2 * 1792) + d * 4, wrv = L_M1 + (kind ? 0 : 2 * 1792) + 1792 + (445 - d) * 4;
      int wr0 = d == 0 ? wrv + 12 - 1792 : wrv + 12;
      const int mo = tid < 224 ? WS_MOD + grp * MOD_REP + tid * 16 : OOB;
      pin(wb); pin(wrv); pin(wr0);
      if (!wall(rdy.x == tag && rdy.y == ~tag)) wait_flags(ws, WS_RDY + grp * 1024 + min(tid >> 1, 112) * 8, tag);
      v4u mv;
      _Pragma("nounroll") for (int s = 0; s < SPIN; ++s) {
        mv = ld4c(ws, fresh(mo));
        if (wall(tid >= 224 || (mv.y == tag && mv.w == tag))) break;
        __builtin_amdgcn_s_sleep(SL1);
      }
      STAMP(2);
      if (tid < 224) {
        const v4f v = {blo(mv.x), bhi(mv.x), blo(mv.z), bhi(mv.z)};
        *(LAS v4f*)(L + wb) = v;
        *(LAS float*)(L + wr0) = v.x;
        *(LAS float*)(L + wrv + 8) = v.y;
        *(LAS float*)(L + wrv + 4) = v.z;
        *(LAS float*)(L + wrv) = v.w;
      }
    }
    bar();
    {
      v4u fr[4];
#pragma unroll
      for (int hh = 0; hh < 2; ++hh) {
        v4f h[4];
#pragma unroll
        for (int m = 0; m < 4; ++m) {
          const int o = ((m == 1 || m == 2) ? 1792 : 0) + (m >= 2 ? 896 : 0) + hh * 16;
          h[m] = fma4(n[m][hh], lds4f(L + mb, o), lds4f(L + mb, 2 * 1792 + o));
        }
        const v4f p = h[0] + h[1], q = h[2] + h[3], u = h[0] - h[1], v = h[3] - h[2];
        const v4f t[4] = {p + q, p - q, u + v, u - v};
#pragma unroll
        for (int m = 0; m < 4; ++m) { fr[m][2 * hh] = pk2(lo2(t[m])); fr[m][2 * hh + 1] = pk2(hi2(t[m])); }
      }
#pragma unroll
      for (int m = 0; m < 4; ++m) *(LAS v4u*)(LB + wave * 1024 + L_AF + m * 4096) = fr[m];
    }
    bar();
    {
      v4u a[4][4];
#pragma unroll
      for (int p = 0; p < 2; ++p)
#pragma unroll
        for (int s = 0; s < 4; ++s) { a[p][s] = lds4(LB, L_AF + (p * 4 + s) * 1024); a[2 + p][s] = lds4(LB, L_AF + ((2 + p) * 4 + s) * 1024); }
      v4f re[4], im[4];
#pragma unroll
      for (int q = 0; q < 5; ++q) {
        if (q < 4) mf8(re[q], im[q], a[q >> 1], cf[4 * (q & 1) + (q >> 1)], a[2 + (q >> 1)], cf[4 * (q & 1) + 2 + (q >> 1)]);
        if (q > 0) {
          if (q < 4) mfence<0>(re[q - 1], im[q - 1]); else mfence<7>(re[3], im[3]);
          xs[q - 1] = v4u{pk(re[q - 1].x, im[q - 1].x), pk(re[q - 1].y, im[q - 1].y), pk(re[q - 1].z, im[q - 1].z), pk(re[q - 1].w, im[q - 1].w)};
          st4c(ws, to[q - 1], xs[q - 1]);
          sb();
        }
      }
    }
    STAMP(3);
    sb();
    sb();
    if (isG) dma_w1(wg, wu, slice, wave, lane, lbase);
#pragma unroll
    for (int q = 0; q < 4; ++q) {
      const int vo = (q < 2 || wave != 0) ? l16 : OOB;
#pragma unroll
      for (int s = 0; s < 4; ++s) cf[2 * q + 1][s] = ld4(tab, vo + s * 1024, (TAB_P3 + 2 * t0 + q) * 4096);
    }
#pragma unroll
    for (int m = 0; m < 4; ++m) {
      st4c(ws, tid + 256 * m < 896 ? WS_H2 + blk * H2_BLK + 4096 * m + tid * 16 : OOB, sv);
    }
    sb();
    STAMP(20);
    v4f xo[2][4];
    int xa = L_XS + c16 * XP + (16 * t0 + 4 * g) * 2, xd = L_XS + c16 * XP + (192 - 16 * t0 - 4 * g) * 2;
    int xs1 = tid < 16 ? L_XS + c16 * XP + 224 : xd + 512, xs3 = tid < 16 ? L_XS + c16 * XP + 672 : xa + 448;
    pin(xa); pin(xd); pin(xs1); pin(xs3);
#define XOFF(t, m, r) ((t) == 0 && (r) == 0 && (m) == 1 ? xs1 : (t) == 0 && (r) == 0 && (m) == 3 ? xs3 : \
    (m) == 0 ? xa + 32 * (t) + 2 * (r) : (m) == 3 ? xa + 448 + 32 * (t) + 2 * (r) : (m) == 1 ? xd + 512 - 32 * (t) - 2 * (r) : xd + 64 - 32 * (t) - 2 * (r))
    {
      v2u w[2][4]; u32 e[2][2], e3;
#pragma unroll
      for (int t = 0; t < 2; ++t) {
        w[t][0] = *(const LAS v2u*)(L + xa + 32 * t); w[t][3] = *(const LAS v2u*)(L + xa + 448 + 32 * t);
        w[t][1] = *(const LAS v2u*)(L + xd + 504 - 32 * t); w[t][2] = *(const LAS v2u*)(L + xd + 56 - 32 * t);
        e[t][0] = *(const LAS u16*)(L + XOFF(t, 1, 0)); e[t][1] = *(const LAS u16*)(L + XOFF(t, 2, 0));
      }
      e3 = *(const LAS u16*)(L + xs3);
      sb();
#pragma unroll
      for (int t = 0; t < 2; ++t) {
        xo[t][0] = v4f{blo(w[t][0].x), bhi(w[t][0].x), blo(w[t][0].y), bhi(w[t][0].y)};
        xo[t][3] = v4f{t == 0 ? blo(e3) : blo(w[t][3].x), bhi(w[t][3].x), blo(w[t][3].y), bhi(w[t][3].y)};
        xo[t][1] = v4f{blo(e[t][0]), bhi(w[t][1].y), blo(w[t][1].y), bhi(w[t][1].x)};
        xo[t][2] = v4f{blo(e[t][1]), bhi(w[t][2].y), blo(w[t][2].y), bhi(w[t][2].x)};
#pragma unroll
        for (int m = 0; m < 4; ++m) { xo[t][m] -= st.x; pin(xo[t][m]); }
      }
    }
    int m0_[2], m1_[2], md_[2];
#pragma unroll
    for (int r = 0; r < 2; ++r) {
      const int q = r == 0 || tid < 80 ? tid + 256 * r : tid;
      const int kind = (q * 586) >> 16, d = 4 * (q - 112 * kind);
      const int reg = (d >= 112) + (d >= 224) + (d >= 336), cc = ((reg + 1) >> 1) * 224, m1 = (0x1320 >> (4 * reg)) & 3;
      const int d1 = abs(d + 1 - cc), d0_ = abs(d - cc), edge = d == 112 * reg && reg > 0;
      m1_[r] = L_M2 + kind * 1792 + (((d1 >> 4) << 6) | (m1 << 4) | (d1 & 15)) * 4;
      m0_[r] = L_M2 + kind * 1792 + (edge ? reg << 4 : ((d0_ >> 4) << 6) | (m1 << 4) | (d0_ & 15)) * 4;
      md_[r] = (reg & 1) ? -4 : 4;
      pin(m0_[r]); pin(m1_[r]); pin(md_[r]);
    }
    {
      v4u mv[2];
      _Pragma("nounroll") for (int s = 0; s < SPIN; ++s) {
        mv[0] = ld4c(ws, fresh(WS_MOD + grp * MOD_REP + (224 + tid) * 16));
        mv[1] = ld4c(ws, fresh(WS_MOD + grp * MOD_REP + (224 + (tid < 80 ? 256 + tid : tid)) * 16));
        const v2u rv = ld2c(ws, fresh(WS_DRDY + grp * 1024 + min(lane, 48) * 8));
        if (wall(mv[0].y == tag && mv[0].w == tag && mv[1].y == tag && mv[1].w == tag && rv.x == tag && rv.y == ~tag)) break;
        __builtin_amdgcn_s_sleep(SL4);
      }
      if (lane < (CLR_MODE == 1 ? 2 : 1)) st2c(ws, lane == 0 ? WS_HRDY + blk * 32 + wave * 8 : isG ? WS_CRDY + (tile * 4 + (grp & 3)) * 256 + (ct * 4 + wave) * 8 : OOB, v2u{tag, ~tag});
      STAMP(12);
#pragma unroll
      for (int f = 0; f < WD_EARLY; ++f) wdn[f >> 2][f & 3] = ld4c(ws, (isG ? wl16 : OOB) + (f & 3) * 1024, wdb + (f >> 2) * 4096);
      sb();
#pragma unroll
      for (int r = 0; r < 2; ++r) {
        const float sc = m0_[r] < L_M2 + 1792 ? 2.f / (float)(S * D) : 1.f;
        *(LAS float*)(L + m0_[r]) = blo(mv[r].x) * sc;
        *(LAS float*)(L + m1_[r]) = bhi(mv[r].x) * sc;
        *(LAS float*)(L + m1_[r] + md_[r]) = blo(mv[r].z) * sc;
        *(LAS float*)(L + m1_[r] + 2 * md_[r]) = bhi(mv[r].z) * sc;
      }
    }
    bar();
    *(LAS v4u*)(L + L_AF + (tid >> 6) * 4096 + 3072 + l16) = v4u{0u, 0u, 0u, 0u};
    STAMP(4);
    int zr = L_AF + lane * 8, ro = L_M2 + (t0 * 64 + 4 * g) * 4, ro1 = wave == 0 ? ro : ro + 256, ex = L_EX + c16 * 8;
    pin(zr); pin(ro); pin(ro1); pin(ex);
    {
      int zl[4];
#pragma unroll
      for (int m = 0; m < 4; ++m) {
        const int kq = (m >> 1) * 128 + (to[m] == OOB ? 127 : wave == 0 && m == 1 ? 112 + c16 : 16 * (t0 + (m & 1)) + c16);
        zl[m] = L_AF + ((kq & ~31) + ((kq & 4) << 2) + ((kq & 24) >> 1) + (kq & 3)) * 32 + g * 8;
        pin(zl[m]);
        if (to[m] == OOB) xs[m] = sv;
      }
      bar();
      if (isG) wait_flags(ws, hro, tag);
      {
        const int src = (blk * 5 + wave * 13) & 63;
        const int po = __builtin_amdgcn_readlane(to[(0)], 0) + (src & 15) * 64 + (src >> 4) * 16;
        const u32 old = __builtin_amdgcn_ds_bpermute(src * 4, xs[0].x);
        _Pragma("nounroll") for (int s = 0; s < SPIN; ++s) {
          if (wall(ld1c(ws, fresh(po)) != old)) break;
          __builtin_amdgcn_s_sleep(SL2);
        }
      }
      STAMP(13);
      v4u z[4];
      _Pragma("nounroll") for (int s = 0; s < SPIN; ++s) {
        u32 mn = 1;
#pragma unroll
        for (int m = 0; m < 4; ++m) z[m] = ld4c(ws, fresh(to[m]));
#pragma unroll
        for (int m = 0; m < 4; ++m) { const v4u d = z[m] ^ xs[m]; mn = min(min(mn, d.x), min(min(d.y, d.z), d.w)); }
        if (wall(mn != 0)) break;
      }
      STAMP(14);
#pragma unroll
      for (int m = 0; m < 4; ++m) {
        char LAS* p = L + zl[m];
        *(LAS v2u*)(p) = v2u{__builtin_amdgcn_perm(z[m].y, z[m].x, 0x05040100u), __builtin_amdgcn_perm(z[m].w, z[m].z, 0x05040100u)};
        *(LAS v2u*)(p + ZPL) = v2u{__builtin_amdgcn_perm(z[m].y, z[m].x, 0x07060302u), __builtin_amdgcn_perm(z[m].w, z[m].z, 0x07060302u)};
      }
    }
    bar();
    STAMP(5);
    {
      v4u zf[4][4];
#pragma unroll
      for (int ty = 0; ty < 4; ++ty)
#pragma unroll
        for (int s = 0; s < 4; ++s) zf[ty][s] = ldtr2(L + zr, ty * 4096 + s * 1024, ty * 4096 + s * 1024 + 512);
      v4f acc[2][4], x2[2][4], gav[2][4];
      v2f s1 = {0.f, 0.f}, s2 = {0.f, 0.f};
#define P3M(t, ty) mf8(acc[t][ty], acc[t][ty + 1], cf[4 * t + ty], zf[ty], cf[4 * t + ty + 1], zf[ty + 1]);
#define P3X(t, m, yv) { x2[t][m] = fma4(gav[t][m], yv, xo[t][m]); \
      if (t == 0 || wave != 0) { s1 += lo2(x2[t][m]) + hi2(x2[t][m]); \
        s2 = fma2(lo2(x2[t][m]), lo2(x2[t][m]), s2); s2 = fma2(hi2(x2[t][m]), hi2(x2[t][m]), s2); } }
      P3M(1, 0) P3M(1, 2)
#pragma unroll
      for (int m = 0; m < 4; ++m) { gav[0][m] = lds4f(L + ro, m * 64); gav[1][m] = lds4f(L + ro1, m * 64); }
      sb();
      P3M(0, 0) P3M(0, 2)
      mfence<0>(acc[1][0], acc[1][1], acc[1][2], acc[1][3]);
      const v4f A1 = acc[1][0] + acc[1][1], B1 = acc[1][0] - acc[1][1], P1 = acc[1][3] - acc[1][2], Q1 = acc[1][2] + acc[1][3];
      P3X(1, 0, A1 - P1) P3X(1, 1, A1 + P1) P3X(1, 2, B1 - Q1) P3X(1, 3, B1 + Q1)
      sb();
      mfence<0>(acc[0][0], acc[0][1], acc[0][2], acc[0][3]);
      {
        const v4f A = acc[0][0] + acc[0][1], B = acc[0][0] - acc[0][1], P = acc[0][3] - acc[0][2], Q = acc[0][2] + acc[0][3];
        v4f y[4] = {A - P, A + P, B - Q, B + Q};
        const bool sp = tid < 16;
        y[0].x = sp ? A.x : y[0].x; y[2].x = sp ? B.x : y[2].x;
        y[1].x = sp ? acc[1][0].x - acc[0][3].x : y[1].x; y[3].x = sp ? acc[1][0].x + acc[0][3].x : y[3].x;
        P3X(0, 0, y[0]) P3X(0, 1, y[1]) P3X(0, 2, y[2]) P3X(0, 3, y[3])
      }
      float sm = s1.x + s1.y, sq = s2.x + s2.y;
      sumg2(sm, sq);
      if (g == 0) *(LAS v2f*)(L + ex + wave * 128) = v2f{sm, sq};
      v4f sbv[2][4], cbv[2][4];
#pragma unroll
      for (int m = 0; m < 4; ++m) {
        sbv[0][m] = lds4f(L + ro, 1792 + m * 64); cbv[0][m] = lds4f(L + ro, 2 * 1792 + m * 64);
        sbv[1][m] = lds4f(L + ro1, 1792 + m * 64); cbv[1][m] = lds4f(L + ro1, 2 * 1792 + m * 64);
      }
      bar();
      v2f e = *(const LAS v2f*)(L + ex);
#pragma unroll
      for (int w = 1; w < 4; ++w) e += *(const LAS v2f*)(L + ex + w * 128);
      const float mc = e.x * (1.f / D), rstd = __builtin_amdgcn_rsqf(fmaxf(e.y * (1.f / D) - mc * mc, 0.f) + 1e-5f), nb = -mc * rstd;
      const int ho = WS_H2 + blk * H2_BLK + 2 * t0 * 1024 + l16, ho1 = wave == 0 ? OOB : ho + 2048;
#pragma unroll
      for (int t = 0; t < 2; ++t)
#pragma unroll
        for (int a = 0; a < 2; ++a) {
          v4u o;
#pragma unroll
          for (int hh = 0; hh < 2; ++hh) {
            const int m = 2 * a + hh;
            const v4f hv = fma4(fma4(x2[t][m], splat(rstd), splat(nb)), cbv[t][m], sbv[t][m]);
            o[2 * hh] = pk2(lo2(hv)); o[2 * hh + 1] = pk2(hi2(hv));
          }
          st4c(ws, (t ? ho1 : ho) + a * 1024, o);
        }
      STAMP(6);
      sb();
#pragma unroll
      for (int f = WD_EARLY; f < WD_MID; ++f) wdn[f >> 2][f & 3] = ld4c(ws, (isG ? wl16 : OOB) + (f & 3) * 1024, wdb + (f >> 2) * 4096);
      sb();
#pragma unroll
      for (int e = 0; e < 8; ++e) {
        x2r[e >> 1][2 * (e & 1)] = pk2(lo2(x2[e >> 2][e & 3]) + st.x); x2r[e >> 1][2 * (e & 1) + 1] = pk2(hi2(x2[e >> 2][e & 3]) + st.x);
      }
#pragma unroll
      for (int m = 0; m < 4; ++m) pin(x2r[m]);
    }
    bar();
    if (!isG) {
      x2_store(ws, x2r, blk, wave, l16);
      if (blockIdx.x == 224 && tid == 0) __builtin_amdgcn_raw_buffer_store_b32(epoch + 1u, ws, WS_EPOCH, 0, SC1);
      drain();
      if (lane < 2) st2c(ws, (lane ? WS_MK3 + blk * 32 : WS_XRDY + blk * 64) + wave * 8, v2u{tag, ~tag});
      DUMP(); return;
    }
  } else {
    const int sj = j >= 30 ? j - 30 : j + 2;
    const int idx = sj * 8 + grp;
    const bool isC = j < 14 || idx == 7;
    const int pair = j < 14 ? grp * 14 + j : 112;
    const int lc = grp + 8 * (j >> 1);
    const bool urgent = j < 4;
    const int qu = urgent ? (lc < 14 ? 2 * lc + (j & 1) : -1) : sj < 2 && grp < 7 ? 28 + grp * 6 + sj : -1;
    const int t1o = WS_T1 + (32 * wave + (lane >> 3)) * T1_BLK + pair * 128 + (lane & 7) * 16;
    ModLoad ml;
    epoch = ld1c(ws, WS_EPOCH);
#pragma unroll
    for (int q = 0; q < 4; ++q) st4c(ws, isC ? t1o + 8 * q * T1_BLK : OOB, sv);
    sb();
    mod_load(ml, cin, wm, qu, tid);
    u32 pf0, pf1, pf2;
    {
      const rsrc_t mr = mkrs(wm, D * 6 * D * 2), xr = mkrs(xin, S * D * 2), fr_ = mkrs((wave & 1) ? wi : wr, S * DF * 2);
      const int row = (sj * 4 + wave) * 7 + (lane >> 1), l7 = lane / 7;
      pf0 = __builtin_amdgcn_raw_buffer_load_b32(mr, lane < 14 && (grp < 6 || !(lane & 1)) ? row * (6 * D * 2) + (grp + 8 * (lane & 1)) * 128 : OOB, 0, PF_AUX);
      pf1 = __builtin_amdgcn_raw_buffer_load_b32(xr, lane < (sj < 6 ? 0 : sj < 10 ? 28 : 56) ? tok_row(16 * grp + (lane < 28 ? sj : sj - 10), 4 * wave + (l7 & 3)) * (D * 2) + (lane - 7 * l7) * 128 : OOB, 0, PF_AUX);
      pf2 = __builtin_amdgcn_raw_buffer_load_b32(fr_, (lane & 31) < 29 && lane < (sj < 6 ? 0 : sj < 10 ? 32 : 64) ? (((16 * grp + (lane < 32 ? sj : sj - 10)) * 7200) & ~127) + ((wave >> 1) * 29 + (lane & 31)) * 128 : OOB, 0, PF_AUX);
    }
    sb();
    asm volatile("s_waitcnt vmcnt(%0)" :: "i"(W_LATE ? 16 : 17) : "memory");
    epoch = __builtin_amdgcn_readfirstlane(epoch);
    tag = mktag(epoch);
    bar();
    if (wave == 1 && lane < 8) st2c(ws, isC ? WS_RDY + lane * 1024 + pair * 8 : OOB, v2u{tag, ~tag});
    if (qu >= 0) {
      mod_part(ml, L, tid);
      bar();
      if (wave == 0) mod_publish(L, ws, qu, lane, tag);
    }
    STAMP(1);
    pin(pf0); pin(pf1); pin(pf2); pin(pfa); pin(pfb);
    const int qn = grp == 7 ? -1 : sj >= 6 && sj < 10 ? 28 + grp * 6 + (sj - 4) : sj < 2 ? 70 + grp * 2 + sj : -1;
    const int tw = sj >= 10 ? (sj - 10) * 8 + grp : sj == 6 && grp == 7 ? 48 : -1;
    v2u dw[2][8];
    if (qn >= 0) mod_load(ml, cin, wm, qn, tid);
    if (tw >= 0) {
      const rsrc_t dr = mkrs(wd, F * D * 2);
      const int u0 = tw * 4 + wave, uc = u0 / 28, kap = 2 * (u0 - uc * 28);
#pragma unroll
      for (int e = 0; e < 16; ++e)
        dw[e >> 3][e & 7] = __builtin_amdgcn_raw_buffer_load_b64(dr, (4 * g * D + 4 * c16) * 2 + (e & 3) * D * 2, ((32 * kap + 16 * (e >> 2)) * D + 64 * uc) * 2, PF_AUX);
    }
    sb();
    u32 pw[4];
    {
      const rsrc_t rg = mkrs(wg, D * F * 2), ru = mkrs(wu, D * F * 2);
      const int nr = sj < 2 ? NR_S : sj < 6 ? NR_U : sj < 10 ? NR_M : NR_T;
      const int r0 = (sj < 2 ? sj * NR_S : sj < 6 ? 2 * NR_S + (sj - 2) * NR_U : sj < 10 ? 2 * NR_S + 4 * NR_U + (sj - 6) * NR_M
                                                                             : 2 * NR_S + 4 * NR_U + 4 * NR_M + (sj - 10) * NR_T) * 4 + wave * nr;
      static_assert(2 * NR_S + 4 * NR_U + 4 * NR_M + 6 * NR_T == 112 && NR_S <= 16 && NR_U <= 16, "all rows");
      const int r7 = lane / 7, po = (r0 + r7) * (F * 2) + ((grp & 3) * 7 + lane - r7 * 7) * 128;
      const int o0 = lane < 56 && r7 < nr ? po : OOB, o1 = lane < 56 && r7 + 8 < nr ? po + 8 * (F * 2) : OOB;
      pw[0] = ld1(rg, o0); pw[1] = ld1(ru, o0); pw[2] = ld1(rg, o1); pw[3] = ld1(ru, o1);
    }
    sb();
    STAMP(20);
    v4f t1c[2], t1s[2], t2c[2], t2s[2];
#pragma unroll
    for (int t = 0; t < 2; ++t) {
#pragma unroll
      for (int r = 0; r < 4; ++r) {
        const float a1 = (float)((16 * t + 4 * g + r) * (16 * wave + c16)) * (1.f / S);
        const float a2 = (float)((16 * t + c16) * (16 * wave + 4 * g + r)) * (1.f / S);
        t1c[t][r] = __builtin_amdgcn_cosf(a1); t1s[t][r] = __builtin_amdgcn_sinf(a1);
        t2c[t][r] = __builtin_amdgcn_cosf(a2); t2s[t][r] = __builtin_amdgcn_sinf(a2);
      }
      pin(t1c[t]); pin(t1s[t]); pin(t2c[t]); pin(t2s[t]);
    }
    if (tw >= 0) {
#pragma unroll
      for (int tt = 0; tt < 8; ++tt) {
        v4u o;
#pragma unroll
        for (int m = 0; m < 4; ++m)
          o[m] = __builtin_amdgcn_perm(dw[tt >> 2][2 * m + 1][(tt & 3) >> 1], dw[tt >> 2][2 * m][(tt & 3) >> 1], (tt & 1) ? 0x07060302u : 0x05040100u);
        st4c(ws, WS_WD + (tw * 4 + wave) * 8192 + tt * 1024 + l16, o);
      }
    }
    if (qn >= 0) {
      bar();
      mod_part(ml, L, tid);
      bar();
      if (wave == 0) mod_publish(L, ws, qn, lane, tag);
    }
    pin(pw[0]); pin(pw[1]); pin(pw[2]); pin(pw[3]);
    STAMP(21);
    if (!isC) { DUMP(); return; }
    v4u c64[2][4], c32[2][2][2];
#pragma unroll
    for (int cp = 0; cp < 2; ++cp)
#pragma unroll
      for (int s = 0; s < 4; ++s) c64[cp][s] = ld4(tab, l16 + s * 1024, (TAB_P2 + wave * 2 + cp) * 4096);
#pragma unroll
    for (int t = 0; t < 2; ++t)
#pragma unroll
      for (int f = 0; f < 4; ++f) c32[t][f >> 1][f & 1] = ld4(tab, l16 + f * 1024, (TAB_P2 + 8 + t) * 4096);
    sb();
    if (tw >= 0) {
      asm volatile("s_waitcnt vmcnt(16)" ::: "memory");
      bar();
      if (tid < 8) st2c(ws, WS_DRDY + tid * 1024 + tw * 8, v2u{tag, ~tag});
    }
    STAMP(2);
    int wfo = WS_WT + pair * 16384 + (16 * g + wave) * 128 + c16 * 8;
    pin(wfo);
    v2u wf[2][4];
    {
      int wl = L_CI + ((lane >> 2) & 1) * 8256 + wave * 2048 + ((lane >> 3) * 4 + (lane & 3)) * 16, lo = t1o;
      pin(wl); pin(lo);
      light_poll(ws, WS_T1 + (32 * wave + (pair & 31)) * T1_BLK + pair * 128);
      STAMP(13);
      v4u z[4];
      _Pragma("nounroll") for (int s = 0; s < SPIN; ++s) {
        lo = fresh(lo);
#pragma unroll
        for (int q = 0; q < 4; ++q) z[q] = ld4c(ws, lo, 8 * q * T1_BLK);
        if (wall(max(max(max4(z[0]), max4(z[1])), max(max4(z[2]), max4(z[3]))) != SENT)) break;
      }
      STAMP(14);
#pragma unroll
      for (int q = 0; q < 4; ++q) *(LAS v4u*)(L + wl + q * 512) = z[q];
    }
    bar();
    v2u fd;
    sb();
    STAMP(4);
    {
      v4u a[2][2][4];
#pragma unroll
      for (int c = 0; c < 2; ++c)
#pragma unroll
        for (int mt = 0; mt < 2; ++mt)
#pragma unroll
          for (int s = 0; s < 4; ++s) a[c][mt][s] = lds4(LB, L_CI + c * 8256 + (s * 2 + mt) * 1024);
      v4u old[2][2];
#pragma unroll
      for (int c = 0; c < 2; ++c)
#pragma unroll
        for (int mt = 0; mt < 2; ++mt) old[c][mt] = lds4(LB + wave * 2048, L_CI + c * 8256 + mt * 1024);
      sb();
#pragma unroll
    for (int t = 0; t < 2; ++t)
#pragma unroll
      for (int r = 0; r < 4; ++r) wf[t][r] = ld2c(ws, wfo + r * 512, t * 8192);
    fd = ld2c(ws, WS_DRDY + grp * 1024 + min(lane, 48) * 8);
      sb();
      v4f re[2][2], im[2][2], ur[2][2], ui[2][2], fr_[2][2], fi_[2][2];
      u32 f2[2][2][4], f3[2][2][4], f4[4];
      char LAS* xw = L + lb + wave * 1024 - L_B;
#define V4(x) v4u{x[0], x[1], x[2], x[3]}
#define S1K(h) c64[0][2 * h], c64[0][2 * h + 1], c64[1][2 * h], c64[1][2 * h + 1]
#define S1X(c, t, h) re[c][t], im[c][t], a[c][t][2 * h], a[c][t][2 * h + 1], S1K(h)
#define S2X(c, t) ur[c][t], ui[c][t], V4(f2[c][0]), V4(f2[c][1]), c32[t][0][0], c32[t][0][1], c32[t][1][0], c32[t][1][1]
#define S3X(c, t) im[c][t], re[c][t], V4(f3[c][0]), V4(f3[c][1]), c32[t][0][0], c32[t][0][1], c32[t][1][0], c32[t][1][1]
#define CM(o, P, Q, C, S_, h) o[2 * h], o[2 * h + 1], P[2 * h], Q[2 * h], C[2 * h], S_[2 * h], P[2 * h + 1], Q[2 * h + 1], C[2 * h + 1], S_[2 * h + 1]
#define TW1(c, t, h) CM(f2[c][t], re[c][t], im[c][t], t1c[t], t1s[t], h)
#define FLT(c, t, h) CM(f3[c][t], ur[c][t], ui[c][t], fr_[c][t], fi_[c][t], h)
#define TW2(c, t, h) CM(f4, re[c][t], im[c][t], t2c[t], t2s[t], h)
#define HV(x, h) (h ? hi2(x) : lo2(x))
#define CP(PL, o, P, Q, C, S_, h) cmul2<PL>(o[2 * h], o[2 * h + 1], HV(P, h), HV(Q, h), HV(C, h), HV(S_, h));
#define XW(c, t) *(LAS v4u*)(xw + L_CX + (c * 2 + t) * 4096) = V4(f4);
#define UNP(c, t) { _Pragma("unroll") for (int r = 0; r < 4; ++r) { fr_[c][t][r] = blo(wf[t][r][c]); fi_[c][t][r] = bhi(wf[t][r][c]); } \
      pin(fr_[c][t]); pin(fi_[c][t]); }
      nop2();
      m4_a0(S1X(0, 0, 0)); m4_a1(S1X(0, 0, 1)); m4_a0(S1X(0, 1, 0)); mfence<3>(re[0][0], im[0][0]);
      h4_a1m(S1X(0, 1, 1), TW1(0, 0, 0)); h4_a0m(S1X(1, 0, 0), TW1(0, 0, 1));
      h4_a1m(S1X(1, 0, 1), TW1(0, 1, 0)); h4_a0m(S1X(1, 1, 0), TW1(0, 1, 1));
      h4_a1m(S1X(1, 1, 1), TW1(1, 0, 0)); h4_b0m(S2X(0, 0), TW1(1, 0, 1));
      h4_b0m(S2X(0, 1), TW1(1, 1, 0));
      sb();
      CP(false, f2[1][1], re[1][1], im[1][1], t1c[1], t1s[1], 1)
      UNP(0, 0) UNP(0, 1)
      sb();
      h4_b0p(S2X(1, 0), FLT(0, 0, 0)); h4_b0p(S2X(1, 1), FLT(0, 0, 1));
      sb();
      CP(true, f3[0][1], ur[0][1], ui[0][1], fr_[0][1], fi_[0][1], 0) CP(true, f3[0][1], ur[0][1], ui[0][1], fr_[0][1], fi_[0][1], 1)
      UNP(1, 0) UNP(1, 1)
      sb();
      h4_a0p(S3X(0, 0), FLT(1, 0, 0)); h4_a0p(S3X(0, 1), FLT(1, 0, 1));
      sb();
      CP(true, f3[1][1], ur[1][1], ui[1][1], fr_[1][1], fi_[1][1], 0) CP(true, f3[1][1], ur[1][1], ui[1][1], fr_[1][1], fi_[1][1], 1)
#pragma unroll
      for (int e = 0; e < 4; ++e) pin(f3[1][1][e]);
      nop2();
      h4_a0p(S3X(1, 0), TW2(0, 0, 0)); h4_a0p(S3X(1, 1), TW2(0, 0, 1));
      XW(0, 0)
      sb();
      CP(true, f4, re[0][1], im[0][1], t2c[1], t2s[1], 0) CP(true, f4, re[0][1], im[0][1], t2c[1], t2s[1], 1) XW(0, 1)
      sb();
      mfence<0>(re[1][0], im[1][0], re[1][1], im[1][1]);
      CP(true, f4, re[1][0], im[1][0], t2c[0], t2s[0], 0) CP(true, f4, re[1][0], im[1][0], t2c[0], t2s[0], 1) XW(1, 0)
      CP(true, f4, re[1][1], im[1][1], t2c[1], t2s[1], 0) CP(true, f4, re[1][1], im[1][1], t2c[1], t2s[1], 1) XW(1, 1)
      bar();
#pragma unroll
      for (int c = 0; c < 2; ++c)
#pragma unroll
        for (int nt = 0; nt < 2; ++nt)
#pragma unroll
          for (int s = 0; s < 4; ++s) a[c][nt][s] = lds4(LB, L_CX + ((c * 2 + nt) * 4 + s) * 1024);
      const int o2 = WS_T1 + (32 * wave + (lane >> 2)) * T1_BLK + pair * 128 + (lane & 3) * 16;
#define S4(c, t) mf8(im[c][t], re[c][t], c64[0], a[c][t], c64[1], a[c][t]);
#define NEQ(x, y) ((x) + ((x) == (y)))
#define OUT(c, t) { const v4u o = {pk(re[c][t].x, im[c][t].x), pk(re[c][t].y, im[c][t].y), pk(re[c][t].z, im[c][t].z), pk(re[c][t].w, im[c][t].w)}; \
      st4c(ws, o2 + c * 64 + t * 16 * T1_BLK, v4u{NEQ(o.x, old[c][t].x), NEQ(o.y, old[c][t].y), NEQ(o.z, old[c][t].z), NEQ(o.w, old[c][t].w)}); sb(); }
      S4(0, 0) S4(0, 1) mfence<0>(re[0][0], im[0][0]); OUT(0, 0)
      S4(1, 0) mfence<0>(re[0][1], im[0][1]); OUT(0, 1)
      S4(1, 1) mfence<0>(re[1][0], im[1][0]); OUT(1, 0)
      mfence<0>(re[1][1], im[1][1]); OUT(1, 1)
    }
    STAMP(5);
    sb();
    if (!isG) { DUMP(); return; }
    if (!wall(fd.x == tag && fd.y == ~tag)) wait_flags(ws, WS_DRDY + grp * 1024 + min(lane, 48) * 8, tag);
    dma_w1(wg, wu, slice, wave, lane, lbase);
#pragma unroll
    for (int f = 0; f < WD_MID; ++f) wdn[f >> 2][f & 3] = ld4c(ws, wl16 + (f & 3) * 1024, wdb + (f >> 2) * 4096);
    fd = ld2c(ws, hro);
    sb();
    ACT_CLEAR(0, 16)
    sb();
    if (!wall(fd.x == tag && fd.y == ~tag)) wait_flags(ws, hro, tag);
    bar();
  }

  STAMP(7);
  const int blk0 = 16 * tile + 4 * (grp & 3);
  const int ab = WS_ACT + blk0 * 57344 + wave * 14 * 1024;
  v4u h[14][2];
  {
    const int hb = WS_H2 + (16 * tile + 4 * wave) * H2_BLK;
    int tb[2][4];
#pragma unroll
    for (int u = 0; u < 8; ++u) {
      tb[u >> 2][u & 3] = (((lane >> 2) * 128 + ((lane >> 3) & 3) * 32 + (lane & 3) * 8) ^ ((u & 3) << 5)) + (u >> 2) * 57344;
      pin(tb[u >> 2][u & 3]);
    }
#define W_RD(dst, k, u) dst = ldtr2(L + tb[k >= 7][u & 3], (k % 7) * 8192 + (u >> 2) * 4096, (k % 7) * 8192 + (u >> 2) * 4096 + 2048)
#define H_LD(v, tt) hh_[v][tt] = ld4c(ws, l16 + (((v) % 14) & 3) * 1024, hb + (2 * ((v) / 14) + (tt)) * H2_BLK + (((v) % 14) >> 2) * 4096)
    const int abx[2] = {ab + (wave >> 1) * 57344, ab + (1 - (wave >> 1)) * 57344};
#define G_LD(e) h[(e) >> 1][(e) & 1] = ld4c(ws, l16 + (((e) >> 1) & 3) * 1024, abx[(e) & 1] + ((e) >> 3) * 4096)
    v4u wfr[2][8], hh_[28][2];
#pragma unroll
    for (int u = 0; u < 8; ++u) W_RD(wfr[0][u], 0, u);
    light_poll(ws, hb + (lane & 1) * H2_BLK + 13 * 1024);
    STAMP(15);
#pragma unroll
    for (int v = 0; v < LEAD; ++v) { H_LD(v, 0); H_LD(v, 1); }
    sb();
    if (lane < (isR ? 1 : 2))
      st2c(ws, isR ? WS_MK3 + (16 * grp + j - 14) * 32 + wave * 8 : WS_CRDY + ((tile + 2 * lane) * 4 + (grp & 3)) * 256 + (ct * 4 + wave) * 8, v2u{tag, ~tag});
    sb();
#define BAD2(x0, x1) wany(max(max(max(max(x0.x, x0.y), x0.z), max(max(x0.w, x1.x), x1.y)), max(x1.z, x1.w)) == SENT)
#define RETRY2(x0, x1, o0, o1) \
      _Pragma("nounroll") for (int s_ = 0; s_ < SPIN; ++s_) { \
        const int lf_ = fresh(l16); SLOWCNT \
        x0 = ld4c(ws, lf_, o0); x1 = ld4c(ws, lf_, o1); \
        if (wall(max(max4(x0), max4(x1)) != SENT)) break; \
      }
    if (__builtin_expect(BAD2(hh_[0][0], hh_[0][1]), 0)) { RETRY2(hh_[0][0], hh_[0][1], hb, hb + H2_BLK) }
    STAMP(8); CSTAMP(28);
    v4f acc[8][2];
    v2u fa;
    const int fx = WS_CRDY + (tile * 4 + (lane < 32 ? wave : grp & 3)) * 256 + min(lane & 31, 27) * 8;
    const int ao = WS_ACT + (16 * tile + 4 * wave) * 57344 + 2 * slice * 1024 + l16;
#define ACT_OUT(ps, tt, f) { v4u o; \
      _Pragma("unroll") for (int hh = 0; hh < 2; ++hh) { \
        const v4f av = relu4(acc[2 * f + hh][tt]) * acc[4 + 2 * f + hh][tt]; \
        o[2 * hh] = pk2(lo2(av)); o[2 * hh + 1] = pk2(hi2(av)); } \
      st4c(ws, ao + (2 * ps + tt) * 57344 + f * 1024, o); }
#define ACT_ALL(ps) { mfence<0>(acc[0][0], acc[1][0], acc[2][0], acc[3][0]); mfence<0>(acc[4][0], acc[5][0], acc[6][0], acc[7][0]); \
      ACT_OUT(ps, 0, 0) ACT_OUT(ps, 0, 1) sb(); \
      mfence<0>(acc[0][1], acc[1][1], acc[2][1], acc[3][1]); mfence<0>(acc[4][1], acc[5][1], acc[6][1], acc[7][1]); \
      ACT_OUT(ps, 1, 0) ACT_OUT(ps, 1, 1) sb(); }
#define W_RH(dst, k, u, hf) { const v2u t_ = ldtr(L + tb[k >= 7][u & 3], (k % 7) * 8192 + (u >> 2) * 4096 + hf * 2048); \
      dst[2 * hf] = t_.x; dst[2 * hf + 1] = t_.y; }
#pragma unroll
    for (int ps = 0; ps < 2; ++ps)
#pragma unroll
    for (int ks = 0; ks < 14; ++ks) {
      const int v = ps * 14 + ks, cur = v & 1, nk = (v + 1) % 14, vn = v < 27 ? v + 1 : v;
      const bool more = v < 27, chk = v + 1 <= HD_V + LEAD;
      u32 m0, m1;
      __builtin_amdgcn_s_waitcnt(0xC07F);
      sb();
#pragma unroll
      for (int i = 0; i < 16; ++i) {
        const int u = i & 7, tt = i >> 3;
        if (ks == 0) mv0(acc[u][tt], wfr[cur][u], hh_[v][tt]); else mv(acc[u][tt], wfr[cur][u], hh_[v][tt]);
        if (more) {
          if (i < 4) { W_RH(wfr[cur ^ 1][i], nk, i, 0) W_RH(wfr[cur ^ 1][i], nk, i, 1) }
          else if (i < 12) W_RH(wfr[cur ^ 1][4 + ((i - 4) >> 1)], nk, (4 + ((i - 4) >> 1)), (i & 1))
        }
        if (i == 5 && v + LEAD < 28) H_LD(v + LEAD, 0);
        if (i == 7 && v + LEAD < 28) H_LD(v + LEAD, 1);
        if (i == 9 || i == 11 || i == 13) {
          const int e = (i - 9) >> 1, f = WD_MID + (v < 14 ? v - 2 : 12 + WD_PER * (v - 14) + e);
          if (f < 56 && ((v >= 2 && v < 14 && e == 0) || (v >= 14 && e < WD_PER))) wdn[f >> 2][f & 3] = ld4c(ws, wl16 + (f & 3) * 1024, wdb + (f >> 2) * 4096);
        }
        if (v > A_V && (i & 1) && i >= 5 && ((i - 5) >> 1) < A_PER) G_LD(A_PER * (v - A_V - 1) + ((i - 5) >> 1));
        if (v == HD_V - 2 && i == 13) fa = ld2c(ws, WS_MK3 + (16 * tile + 4 * wave) * 32 + c16 * 8);
        if (v == 9 && i == 13) fa = ld2c(ws, fx);
        if (chk && i == 12) { m0 = max(max(hh_[vn][0].x, hh_[vn][0].y), hh_[vn][0].z); pin(m0); }
        if (chk && i == 13) { m1 = max(max(hh_[vn][0].w, hh_[vn][1].x), hh_[vn][1].y); pin(m1); }
        if (chk && i == 14) { m0 = max(max(m0, hh_[vn][1].z), hh_[vn][1].w); m0 = max(m0, m1); pin(m0); }
        sb();
      }
      if (v == 12 && !wall(fa.x == tag && fa.y == ~tag)) wait_flags(ws, fx, tag);
      if (v == 19 && isR) {
        static_assert(19 + LEAD < 28, "the count below");
        asm volatile("s_waitcnt vmcnt(12)" ::: "memory");
        if (lane == 0) st2c(ws, WS_XRDY + (16 * grp + j - 14) * 64 + wave * 8, v2u{tag, ~tag});
      }
      if (v == HD_V && !wall(fa.x == tag && fa.y == ~tag)) wait_flags(ws, WS_MK3 + (16 * tile + 4 * wave) * 32 + c16 * 8, tag);
      if (chk && __builtin_expect(wany(m0 == SENT), 0)) {
        const int pb = 2 * ((v + 1) / 14) * H2_BLK + nk * 1024;
        RETRY2(hh_[vn][0], hh_[vn][1], hb + pb, hb + pb + H2_BLK)
      }
      if (v == 13) {
        STAMP(16); CSTAMP(29);
        ACT_ALL(0)
        if (isR) x2_store(ws, x2r, 16 * grp + j - 14, wave, l16);
        nop2();
      }
      sb();
    }
    STAMP(27);
    ACT_ALL(1)
    CSTAMP(30);
#pragma unroll
    for (int e = A_PER * (27 - A_V); e < 28; ++e) G_LD(e);
    sb();
    STAMP(9);
    v4u a[14][2];
    v4f ac2[4][4];
#define A_LD(e) a[(e) >> 1][(e) & 1] = ld4c(ws, l16 + (((e) >> 1) & 3) * 1024, abx[(e) & 1] + 2 * 57344 + ((e) >> 3) * 4096)
    const int po = ab + 3 * 57344 + min(lane, 6) * 2048 + 1024;
#define KN (ks < 13 ? ks + 1 : ks)
    u32 probe;
#pragma unroll
    for (int ks = 0; ks < 14; ++ks) {
      if (ks == 0 && __builtin_expect(BAD2(h[0][0], h[0][1]), 0)) { RETRY2(h[0][0], h[0][1], abx[0], abx[1]) }
#pragma unroll
      for (int tt = 0; tt < 2; ++tt)
#pragma unroll
        for (int n = 0; n < 4; ++n) ac2[tt][n] = mfma(wdn[ks][n], h[ks][tt], ks ? ac2[tt][n] : splat(0.f));
      const bool bad = ks < 13 && BAD2(h[KN][0], h[KN][1]);
      if (ks == P_AT) probe = ld1c(ws, po);
#pragma unroll
      for (int u = 0; u < 8; ++u) { __builtin_amdgcn_sched_group_barrier(0x8, 1, 0); if (u >= 2) __builtin_amdgcn_sched_group_barrier(0x2, 1, 0); }
      sb();
      if (__builtin_expect(bad, 0)) { RETRY2(h[KN][0], h[KN][1], abx[0] + KN * 1024, abx[1] + KN * 1024) }
      if (ks == B_AT2) {
        STAMP(25);
        if (wany(probe == SENT)) light_poll(ws, po);
        STAMP(26);
      }
      if (ks >= B_AT2) {
#pragma unroll
        for (int e = 4 * (ks - B_AT2); e < 4 * (ks - B_AT2 + 1); ++e) A_LD(e);
      }
      sb();
    }
    STAMP(17);
#pragma unroll
    for (int e = 4 * (14 - B_AT2); e < 4 * (14 - B_AT2) + 6; ++e) A_LD(e);
    sb();
    int rw[3], rr_ = L_B + lane * 8 + wave * 6144, oo[2];
#pragma unroll
    for (int x = 1; x < 4; ++x) { rw[x - 1] = L_B + lane * 8 + (wave ^ x) * 6144 + (x - 1) * 2048; pin(rw[x - 1]); }
    pin(rr_);
    const int blk = blk0 + (wave >> 1);
#pragma unroll
    for (int ph = 0; ph < 2; ++ph) { oo[ph] = (tok_row(blk + 2 * ph, c16) * D + ct * 64 + g * 16 + (wave & 1) * 8) * 2; pin(oo[ph]); }
#define RED_W1(ph, x, n) *(LAS v2f*)(L + rw[(x) - 1] + (ph) * 24576 + (n) * 512) = ((x) & 1) ? hi2(ac2[2 * (ph) + ((x) >> 1)][n]) : lo2(ac2[2 * (ph) + ((x) >> 1)][n]);
    const int n0 = ct * 64 + g * 16 + (wave & 1) * 8, reg = (n0 >= 112) + (n0 >= 224) + (n0 >= 336), xm = (0x1320 >> (4 * reg)) & 3;
    const int dz = abs(n0 - ((reg + 1) >> 1) * 224), db = (reg & 1) ? dz - 8 : dz;
#define XSLOT(m, dl) ((((dl) >> 4) * 2 + ((m) >> 1)) * 1024 + (((dl) >> 2) & 3) * 256 + ((m) & 1) * 8)
    int xo = WS_X2 + blk * X2_BLK + c16 * 16 + XSLOT(xm, db);
    int xe = WS_X2 + blk * X2_BLK + c16 * 16 + (n0 == 112 * reg && reg > 0 ? XSLOT(reg, 0) : XSLOT(xm, dz));
    pin(xo); pin(xe);
    const int xf = WS_XRDY + (blk + (lane & 4 ? 2 : 0)) * 64 + (lane & 3) * 8;
    v2u xp[2][2]; u32 x1[2];
    const int go = WS_MOD + grp * MOD_REP + (70 + 2 * ct) * 128 + g * 64 + (wave & 1) * 32;
    v4u xv[2], gv[2];
    STAMP(18);
    sb();
#pragma unroll
    for (int ks = 0; ks < 14; ++ks) {
      if (ks == 0 && __builtin_expect(BAD2(a[0][0], a[0][1]), 0)) { RETRY2(a[0][0], a[0][1], abx[0] + 2 * 57344, abx[1] + 2 * 57344) }
#pragma unroll
      for (int tt = 0; tt < 2; ++tt)
#pragma unroll
        for (int n = 0; n < 4; ++n) ac2[2 + tt][n] = mfma(wdn[ks][n], a[ks][tt], ks ? ac2[2 + tt][n] : splat(0.f));
#pragma unroll
      for (int e = 4 * (14 - B_AT2) + 6 + 3 * ks; e < 4 * (14 - B_AT2) + 9 + 3 * ks; ++e)
        if (e < 28) A_LD(e);
      const bool bad = ks < 13 && BAD2(a[KN][0], a[KN][1]);
      if (ks < 12) RED_W1(0, 1 + (ks >> 2), (ks & 3))
#pragma unroll
      for (int u = 0; u < 4; ++u) {
        __builtin_amdgcn_sched_group_barrier(0x8, 2, 0);
        __builtin_amdgcn_sched_group_barrier(0x20, 1, 0);
        if (u == 2) __builtin_amdgcn_sched_group_barrier(0x200, 1, 0);
        if (u >= 1) __builtin_amdgcn_sched_group_barrier(0x2, 3, 0);
      }
      sb();
      if (__builtin_expect(bad, 0)) { RETRY2(a[KN][0], a[KN][1], abx[0] + 2 * 57344 + KN * 1024, abx[1] + 2 * 57344 + KN * 1024) }
      if (ks == 2) fa = ld2c(ws, xf);
      if (ks == 7) {
        if (!wall(fa.x == tag && fa.y == ~tag)) wait_flags(ws, xf, tag);
#pragma unroll
        for (int ph = 0; ph < 2; ++ph) {
          xp[ph][0] = ld2c(ws, xo, ph * 2 * X2_BLK); xp[ph][1] = ld2c(ws, xo + 256, ph * 2 * X2_BLK);
          x1[ph] = __builtin_amdgcn_raw_buffer_load_b16(ws, xe, ph * 2 * X2_BLK, SC1);
        }
        gv[0] = ld4c(ws, go); gv[1] = ld4c(ws, go + 16);
      }
      sb();
    }
    STAMP(10);
#pragma unroll
    for (int e = 0; e < 12; ++e) RED_W1(1, 3 - (e >> 2), (e & 3))
#define FIN_OK() wall(gv[0].y == tag && gv[0].w == tag && gv[1].y == tag && gv[1].w == tag)
    if (__builtin_expect(!FIN_OK(), 0)) {
      _Pragma("nounroll") for (int s = 0; s < SPIN; ++s) {
        const int gf = fresh(go);
        gv[0] = ld4c(ws, gf); gv[1] = ld4c(ws, gf + 16);
        if (FIN_OK()) break;
      }
    }
#pragma unroll
    for (int ph = 0; ph < 2; ++ph) {
      const v2u p = xp[ph][0], q = xp[ph][1];
      const bool dn = reg & 1;
      xv[ph].x = __builtin_amdgcn_perm(dn ? q.y : p.x, x1[ph], 0x07060100u);
      xv[ph].y = dn ? __builtin_amdgcn_perm(q.x, q.y, 0x07060100u) : p.y;
      xv[ph].z = dn ? __builtin_amdgcn_perm(p.y, q.x, 0x07060100u) : q.x;
      xv[ph].w = dn ? __builtin_amdgcn_perm(p.x, p.y, 0x07060100u) : q.y;
      pin(xv[ph]);
    }
    bar();
#pragma unroll
    for (int ph = 0; ph < 2; ++ph) {
      v2f r[4];
#pragma unroll
      for (int n = 0; n < 4; ++n) {
        r[n] = lo2(ac2[2 * ph][n]);
#pragma unroll
        for (int w = 0; w < 3; ++w) r[n] += *(const LAS v2f*)(L + rr_ + w * 2048 + ph * 24576 + n * 512);
      }
      v4u o;
#pragma unroll
      for (int q = 0; q < 2; ++q) {
        o[2 * q] = pk2(fma2(bun(gv[q].x), v2f{r[0][q], r[1][q]}, bun(xv[ph][2 * q])));
        o[2 * q + 1] = pk2(fma2(bun(gv[q].z), v2f{r[2][q], r[3][q]}, bun(xv[ph][2 * q + 1])));
      }
      __builtin_amdgcn_raw_buffer_store_b128(o, mkrs(out, S * D * 2), oo[ph], 0, OUT_AUX);
    }
  }
  STAMP(11);
  DUMP();
}

extern "C" void kernel_launch(void* const* d_in, const int* in_sizes, int n_in, void* d_out, int out_size, void* d_ws,
                              size_t ws_size, hipStream_t stream) {
  fused<<<256, 256, 0, stream>>>((const u16*)d_in[0], (const u16*)d_in[1], (const u16*)d_in[2], (const u16*)d_in[4],
                                 (const u16*)d_in[5], (const u16*)d_in[6], (const u16*)d_in[7], (const u16*)d_in[8],
                                 (u16*)d_out, (char*)d_ws);
}
```

```cpp
#define NR_S 0
#define NR_U 8
#define NR_M 8
#define NR_T 8
#include <hip/hip_runtime.h>

typedef unsigned short u16;
typedef unsigned int u32;
typedef unsigned long long u64;
typedef u32 v4u __attribute__((ext_vector_type(4)));
typedef u32 v2u __attribute__((ext_vector_type(2)));
typedef float v4f __attribute__((ext_vector_type(4)));
typedef float v2f __attribute__((ext_vector_type(2)));
typedef short v8s __attribute__((ext_vector_type(8)));
typedef short v4s __attribute__((ext_vector_type(4)));
typedef __bf16 v8b __attribute__((ext_vector_type(8)));
typedef __bf16 v2b __attribute__((ext_vector_type(2)));
#define LAS __attribute__((address_space(3)))
#define GAS __attribute__((address_space(1)))
#define DEV __device__ __forceinline__

#define C_W1_EARLY 0
#define CLR_DELAY 0
#define W_DELAY 0
#define CLR2_DELAY 0
#define PF_POS 2
#define SL1 1
#define SL2 2
#define SL4 4
#define X_AFTER 8
#define PF_AUX 2
#define W_LATE 0
#define W1_PF 1
#define DMA_MOD ""
#define LEAD 6
#define A_V 21
#define A_PER 4
#define WDV(f) ((f) - WD_MID + 2 < 14 ? (f) - WD_MID + 2 : ((f) - WD_MID - 12) / 2 + 16)
#define B_AT2 12
#define RE_PF 0
#define PF2_DELAY 0
#define C_DMA_EARLY 0
#define EVEN_PF 0
#define XBAL 1
#define W1EVEN 1
#define PRE_OUT 0
#define OUT_AUX 16
#define CLR_MODE 0
#define MK 0
#define MKD1 0
#define MKD2 0
#define MKD3 0
#define P_AT 7
#define WD_PER 3
#define A_FIRST 10
#define B_FIRST 10
#define A_AT 9
#define F_AT 4
#define CODE_BYTES 45056
#define WD_EARLY 20
#define WD_MID 20
#define B_AT 9
constexpr int S = 2048, D = 448, DF = 225, F = 1792;

struct alignas(16) Chain { u16 v[2048]; };
struct Cos448 { u16 v[448]; };
constexpr double c_cos_t(double a) {
  double a2 = a * a, t = 1.0, s = 1.0;
  for (int i = 1; i <= 10; ++i) { t = -t * a2 / ((2 * i - 1) * (2 * i)); s += t; }
  return s;
}
constexpr double c_sin_t(double a) {
  double a2 = a * a, t = a, s = a;
  for (int i = 1; i <= 10; ++i) { t = -t * a2 / ((2 * i) * (2 * i + 1)); s += t; }
  return s;
}
constexpr double c_cos448(int m) {
  if (m > 224) m = 448 - m;
  double sg = 1.0;
  if (m > 112) { m = 224 - m; sg = -1.0; }
  const double w = 6.283185307179586476925286766559 / 448.0;
  return m == 112 ? 0.0 : (m <= 56 ? sg * c_cos_t(w * m) : sg * c_sin_t(w * (112 - m)));
}
constexpr u16 c_bf16(double d) {
  const u32 u = __builtin_bit_cast(u32, (float)d);
  return (u16)((u + 0x7FFFu + ((u >> 16) & 1u)) >> 16);
}
constexpr Cos448 mk_cos() { Cos448 c{}; for (int m = 0; m < 448; ++m) c.v[m] = c_bf16(c_cos448(m)); return c; }
constexpr Cos448 CTAB = mk_cos();
constexpr u16 bcos(int m) { return CTAB.v[m % 448]; }
constexpr u16 bsin(int m) { return CTAB.v[(m % 448 + 336) % 448]; }
constexpr u16 bneg(u16 b) { return b ? (u16)(b ^ 0x8000u) : b; }
constexpr u16 bscale(u16 b, int e) { return b ? (u16)(b + e * 0x80) : b; }

constexpr Chain mk_p1(int c) {
  const int tile = c >> 2, par = c & 1, sn = (c >> 1) & 1;
  Chain ch{};
  for (int s = 0; s < 4; ++s)
    for (int l = 0; l < 64; ++l) {
      const int kp = 16 * tile + (l & 15), k = 2 * kp + par;
      if (k > 224) continue;
      for (int j = 0; j < 8; ++j) {
        const int dl = 32 * s + 8 * (l >> 4) + j;
        if (dl > 112) continue;
        u16 b = sn ? bneg(bsin(k * dl)) : bcos(k * dl);
        if (dl == 0 || dl == 112) b = bscale(b, -1);
        ch.v[(s * 64 + l) * 8 + j] = b;
      }
    }
  return ch;
}
constexpr Chain mk_p3(int c) {
  const int tile = c >> 1, sn = c & 1;
  Chain ch{};
  for (int s = 0; s < 4; ++s)
    for (int l = 0; l < 64; ++l) {
      int dl = 16 * tile + (l & 15);
      if (c == 1 && dl == 0) dl = 112;
      for (int j = 0; j < 8; ++j) {
        const int k = 2 * (32 * s + 8 * (l >> 4) + j) + 1;
        if (k < 224) ch.v[(s * 64 + l) * 8 + j] = sn ? bsin(k * dl) : bcos(k * dl);
      }
    }
  return ch;
}
constexpr Chain mk_p2(int c) {
  Chain ch{};
  for (int f = 0; f < 4; ++f)
    for (int l = 0; l < 64; ++l)
      for (int j = 0; j < 8; ++j) {
        const int comp = c < 8 ? (c & 1) : (f >> 1), s = c < 8 ? f : (f & 1);
        const int o = 16 * (c < 8 ? (c >> 1) : (c - 8)) + (l & 15), i = 16 * s + 4 * (l >> 4) + (j >> 1);
        const int m = (c < 8 ? 7 : 14) * ((i * o) % 64);
        u16 b = 0;
        if (comp == 0) b = (j & 1) ? bsin(m) : bcos(m);
        else b = (j & 1) ? bcos(m) : bneg(bsin(m));
        ch.v[(f * 64 + l) * 8 + j] = b;
      }
  return ch;
}
template <int I> constexpr Chain P1C = mk_p1(I);
template <int I> constexpr Chain P3C = mk_p3(I);
template <int I> constexpr Chain P2C = mk_p2(I);
#define L10(T, b) T<b>, T<b + 1>, T<b + 2>, T<b + 3>, T<b + 4>, T<b + 5>, T<b + 6>, T<b + 7>, T<b + 8>, T<b + 9>
#define L29(T) L10(T, 0), L10(T, 10), T<20>, T<21>, T<22>, T<23>, T<24>, T<25>, T<26>, T<27>, T<28>
constexpr int TAB_P1 = 0, TAB_P3 = 29, TAB_P2 = 43, TAB_N = 53;
static __device__ const Chain TAB[TAB_N] = {L29(P1C), L10(P3C, 0), P3C<10>, P3C<11>, P3C<12>, P3C<13>, L10(P2C, 0)};

constexpr int WS_EPOCH = 0;
constexpr int WS_RDY = 4096;
constexpr int WS_HRDY = 4096 + 8192;
constexpr int WS_DRDY = 4096 + 16384;
constexpr int WS_XRDY = 131072 + 65536;
constexpr int WS_ARDY = 131072;
constexpr int WS_CRDY = 131072 + 32768;
constexpr int WS_MK1 = 212992, WS_MK2 = 221184, WS_MK3 = 229376;
constexpr int WS_MOD = 32768, MOD_REP = 12288;
constexpr int T1_BLK = 226 * 64;
constexpr int WS_T1 = 1 << 18;
constexpr int WS_WT = WS_T1 + 128 * T1_BLK;
constexpr int WS_H2 = WS_WT + 113 * 16384;
constexpr int WS_X2 = WS_H2 + 128 * 14336;
constexpr int WS_WD = WS_X2 + 128 * 14336;
constexpr int WS_ACT = WS_WD + 7 * 56 * 4096;
constexpr int WS_END = WS_ACT + 128 * 57344;
constexpr int WS_DBG = 32 << 20;

constexpr int L_W1 = 0;
constexpr int L_B = 114688;
constexpr int XP = 912;
constexpr int L_XS = L_B;
constexpr int L_ST = L_XS + 16 * XP;
constexpr int L_M1 = L_ST + 128;
constexpr int L_AF = L_M1 + 4 * 1792;
constexpr int L_M2 = L_AF + 16384;
constexpr int L_EX = L_M2 + 3 * 1792;
constexpr int L_REND = L_EX + 512;
constexpr int ZPL = 8192;
constexpr int L_CI = L_B;
constexpr int L_CX = L_CI + 2 * 8256;
constexpr int L_RED = L_CX + 16384;
constexpr int L_DBG = 163840 - 1024;
constexpr int L_SZ = 163840;
static_assert(L_REND <= L_DBG && L_RED + 2048 <= L_DBG && 2 * ZPL <= 16384, "lds");

constexpr u32 SENT = 0xFFFFFFFFu;
constexpr int SPIN = 1 << 14;
constexpr int SC1 = 16;
constexpr int OOB = 0x7FFF0000;

typedef __amdgpu_buffer_rsrc_t rsrc_t;
DEV rsrc_t mkrs(const void* p, int bytes) { return __builtin_amdgcn_make_buffer_rsrc((void*)p, 0, bytes, 0x00020000); }
DEV v4u ld4(rsrc_t r, int off, int so = 0) { return __builtin_amdgcn_raw_buffer_load_b128(r, off, so, 0); }
DEV v2u ld2(rsrc_t r, int off, int so = 0) { return __builtin_amdgcn_raw_buffer_load_b64(r, off, so, 0); }
DEV v4u ld4c(rsrc_t r, int off, int so = 0) { return __builtin_amdgcn_raw_buffer_load_b128(r, off, so, SC1); }
DEV v2u ld2c(rsrc_t r, int off, int so = 0) { return __builtin_amdgcn_raw_buffer_load_b64(r, off, so, SC1); }
DEV u32 ld1c(rsrc_t r, int off, int so = 0) { return __builtin_amdgcn_raw_buffer_load_b32(r, off, so, SC1); }
DEV void st4c(rsrc_t r, int off, v4u v) { __builtin_amdgcn_raw_buffer_store_b128(v, r, off, 0, SC1); }
DEV void st2c(rsrc_t r, int off, v2u v) { __builtin_amdgcn_raw_buffer_store_b64(v, r, off, 0, SC1); }
DEV float blo(u32 v) { return __builtin_bit_cast(float, v << 16); }
DEV float bhi(u32 v) { return __builtin_bit_cast(float, v & 0xFFFF0000u); }
DEV v2f bun(u32 v) { return v2f{blo(v), bhi(v)}; }
DEV u32 pk(float lo, float hi) { return __builtin_bit_cast(u32, __builtin_convertvector(v2f{lo, hi}, v2b)); }
DEV u32 pk2(v2f v) { return __builtin_bit_cast(u32, __builtin_convertvector(v, v2b)); }
DEV float rb(float f) { return blo(pk(f, 0.f)); }
DEV v2f lo2(v4f v) { return __builtin_shufflevector(v, v, 0, 1); }
DEV v2f hi2(v4f v) { return __builtin_shufflevector(v, v, 2, 3); }
DEV v2f fma2(v2f a, v2f b, v2f c) { return __builtin_elementwise_fma(a, b, c); }
DEV v4f fma4(v4f a, v4f b, v4f c) { return __builtin_elementwise_fma(a, b, c); }
typedef int v4i __attribute__((ext_vector_type(4)));
DEV v4f relu4(v4f v) { return __builtin_bit_cast(v4f, __builtin_elementwise_max(__builtin_bit_cast(v4i, v), v4i{0, 0, 0, 0})); }
DEV v4f splat(float f) { return v4f{f, f, f, f}; }
DEV v4f mfma(v4u a, v4u b, v4f c) {
  return __builtin_amdgcn_mfma_f32_16x16x32_bf16(__builtin_bit_cast(v8b, a), __builtin_bit_cast(v8b, b), c, 0, 0, 0);
}
DEV void mfa(v4f& c, v4u a, v4u b) { asm volatile("s_nop 1\n\tv_mfma_f32_16x16x32_bf16 %0, %1, %2, %0" : "+v"(c) : "v"(a), "v"(b)); }
DEV void mf0(v4f& c, v4u a, v4u b) { asm volatile("s_nop 1\n\tv_mfma_f32_16x16x32_bf16 %0, %1, %2, 0" : "=&v"(c) : "v"(a), "v"(b)); }
template <int N> DEV void mfence(v4f& a, v4f& b) { asm volatile("s_nop %2" : "+v"(a), "+v"(b) : "i"(N)); }
template <int N> DEV void mfence(v4f& a, v4f& b, v4f& c, v4f& d) { asm volatile("s_nop %4" : "+v"(a), "+v"(b), "+v"(c), "+v"(d) : "i"(N)); }
#define MF "v_mfma_f32_16x16x32_bf16 "
DEV void mf8(v4f& d0, v4f& d1, const v4u (&a0)[4], const v4u (&b0)[4], const v4u (&a1)[4], const v4u (&b1)[4]) {
  asm volatile("s_nop 1\n\t" MF "%0, %2, %6, 0\n\t" MF "%1, %10, %14, 0\n\t" MF "%0, %3, %7, %0\n\t" MF "%1, %11, %15, %1\n\t"
               MF "%0, %4, %8, %0\n\t" MF "%1, %12, %16, %1\n\t" MF "%0, %5, %9, %0\n\t" MF "%1, %13, %17, %1"
               : "=&v"(d0), "=&v"(d1)
               : "v"(a0[0]), "v"(a0[1]), "v"(a0[2]), "v"(a0[3]), "v"(b0[0]), "v"(b0[1]), "v"(b0[2]), "v"(b0[3]),
                 "v"(a1[0]), "v"(a1[1]), "v"(a1[2]), "v"(a1[3]), "v"(b1[0]), "v"(b1[1]), "v"(b1[2]), "v"(b1[3]));
}
#define H4_M(XA, D, X, K, C) MF D ", " XA(X, K) ", " C "\n\t"
#define H4_XA(X, K) X ", " K
#define H4_XB(X, K) K ", " X
#define H4_DEF(NAME, XA, C0, C1, CONS, N0, N1, PA, PB, PC, PD) \
DEV void NAME(v4f& d0, v4f& d1, v4u x0, v4u x1, v4u k00, v4u k01, v4u k10, v4u k11, \
              u32& o0, u32& o1, float p0, float q0, float c0, float s0, float p1, float q1, float c1, float s1) { \
  float t0, t1, t2, t3; \
  asm volatile(H4_M(XA, "%0", "%8", "%10", C0) "v_mul_f32 %4, %14, %16\n\tv_mul_f32 %5, %15, %16\n\t" \
               H4_M(XA, "%1", "%8", "%12", C1) "v_mul_f32 %6, %18, %20\n\tv_mul_f32 %7, %19, %20\n\t" \
               H4_M(XA, "%0", "%9", "%11", "%0") "v_fma_f32 %4, " N0 "%15, %17, %4\n\tv_fma_f32 %5, " N1 "%14, %17, %5\n\t" \
               H4_M(XA, "%1", "%9", "%13", "%1") "v_fma_f32 %6, " N0 "%19, %21, %6\n\tv_fma_f32 %7, " N1 "%18, %21, %7\n\t" \
               "v_cvt_pk_bf16_f32 %2, " PA ", " PB "\n\tv_cvt_pk_bf16_f32 %3, " PC ", " PD \
               : CONS(d0), CONS(d1), "=&v"(o0), "=&v"(o1), "=&v"(t0), "=&v"(t1), "=&v"(t2), "=&v"(t3) \
               : "v"(x0), "v"(x1), "v"(k00), "v"(k01), "v"(k10), "v"(k11), \
                 "v"(p0), "v"(q0), "v"(c0), "v"(s0), "v"(p1), "v"(q1), "v"(c1), "v"(s1)); \
}
H4_DEF(h4_a0m, H4_XA, "0", "0", "=&v", "", "-", "%4", "%5", "%6", "%7")
H4_DEF(h4_a1m, H4_XA, "%0", "%1", "+v", "", "-", "%4", "%5", "%6", "%7")
H4_DEF(h4_b0m, H4_XB, "0", "0", "=&v", "", "-", "%4", "%5", "%6", "%7")
H4_DEF(h4_b0p, H4_XB, "0", "0", "=&v", "-", "", "%5", "%4", "%7", "%6")
H4_DEF(h4_a0p, H4_XA, "0", "0", "=&v", "-", "", "%5", "%4", "%7", "%6")
#define M4_DEF(NAME, XA, C0, C1, CONS) \
DEV void NAME(v4f& d0, v4f& d1, v4u x0, v4u x1, v4u k00, v4u k01, v4u k10, v4u k11) { \
  asm volatile(H4_M(XA, "%0", "%2", "%4", C0) H4_M(XA, "%1", "%2", "%6", C1) H4_M(XA, "%0", "%3", "%5", "%0") MF "%1, " XA("%3", "%7") ", %1" \
               : CONS(d0), CONS(d1) : "v"(x0), "v"(x1), "v"(k00), "v"(k01), "v"(k10), "v"(k11)); \
}
M4_DEF(m4_a0, H4_XA, "0", "0", "=&v") M4_DEF(m4_a1, H4_XA, "%0", "%1", "+v")
M4_DEF(m4_b0, H4_XB, "0", "0", "=&v") M4_DEF(m4_b1, H4_XB, "%0", "%1", "+v")
DEV void mv(v4f& c, v4u a, v4u b) { asm volatile(MF "%0, %1, %2, %0" : "+v"(c) : "v"(a), "v"(b)); }
DEV void mv0(v4f& c, v4u a, v4u b) { asm volatile(MF "%0, %1, %2, 0" : "=&v"(c) : "v"(a), "v"(b)); }
DEV void nop2() { asm volatile("s_nop 1"); }
template <class T> DEV void pin(T& v) { asm volatile("" : "+v"(v)); }
DEV int fresh(int off) { asm volatile("" : "+v"(off) :: "memory"); return off; }
DEV bool wall(bool ok) { return __builtin_amdgcn_ballot_w64(!ok) == 0; }
DEV bool wany(bool b) { return __builtin_amdgcn_ballot_w64(b) != 0; }
DEV void sb() { __builtin_amdgcn_sched_barrier(0); }
DEV u32 max4(v4u v) { return max(max(v.x, v.y), max(v.z, v.w)); }
DEV void drain() { asm volatile("s_waitcnt vmcnt(0)" ::: "memory"); }
DEV void cfence() { asm volatile("" ::: "memory"); }
DEV void bar() { asm volatile("s_waitcnt lgkmcnt(0)\n\ts_barrier" ::: "memory"); }
template <int CTRL> DEV float dpp(float v) {
  return __builtin_bit_cast(float, __builtin_amdgcn_update_dpp(0, __builtin_bit_cast(int, v), CTRL, 0xF, 0xF, true));
}
DEV float sum16(float v) {
  v += dpp<0x128>(v); v += dpp<0x124>(v); v += dpp<0x122>(v); v += dpp<0x121>(v); return v;
}
DEV void sumg2(float& a, float& b) {
  float c = a, d = b;
  asm volatile("s_nop 1\n\tv_permlane16_swap_b32 %0, %1\n\tv_permlane16_swap_b32 %2, %3" : "+v"(a), "+v"(c), "+v"(b), "+v"(d));
  a += c; b += d; c = a; d = b;
  asm volatile("s_nop 1\n\tv_permlane32_swap_b32 %0, %1\n\tv_permlane32_swap_b32 %2, %3" : "+v"(a), "+v"(c), "+v"(b), "+v"(d));
  a += c; b += d;
}
DEV v4u lds4(const char LAS* L, int off) { return *(const LAS v4u*)(L + off); }
DEV v4f lds4f(const char LAS* L, int off) { return *(const LAS v4f*)(L + off); }
DEV v2u ldtr(const char LAS* L, int off) { return __builtin_bit_cast(v2u, __builtin_amdgcn_ds_read_tr16_b64_v4i16((LAS v4s*)(L + off))); }
DEV v4u ldtr2(const char LAS* L, int o0, int o1) { const v2u a = ldtr(L, o0), b = ldtr(L, o1); return v4u{a.x, a.y, b.x, b.y}; }
DEV void dma16(rsrc_t r, int voff, int soff, u32 lds_dst) {
  u32 keep;
  asm volatile("s_mov_b32 %0, m0\n\ts_mov_b32 m0, %1\n\ts_nop 0\n\tbuffer_load_dwordx4 %2, %3, %4 offen" DMA_MOD " lds\n\ts_mov_b32 m0, %0"
               : "=&s"(keep) : "s"(lds_dst), "v"(voff), "s"(r), "s"(soff) : "memory");
}
DEV void light_poll(rsrc_t ws, int off) {
  _Pragma("nounroll") for (int s = 0; s < SPIN; ++s) {
    if (wall(ld1c(ws, fresh(off)) != SENT)) break;
    __builtin_amdgcn_s_sleep(SL2);
  }
}
DEV int tok_row(int blk, int t) {
  const int n1 = 16 * (blk >> 5) + 4 * ((blk >> 2) & 3) + (t & 3), n2 = 16 * ((blk >> 4) & 1) + 4 * (blk & 3) + (t >> 2);
  return n2 + 32 * n1;
}
template <bool SWAP> DEV v4u cmul_pack(v4f a, v4f b, v4f c, v4f s) {
  const v4f re = fma4(b, s, a * c), im = fma4(b, c, -(a * s));
  return SWAP ? v4u{pk(im.x, re.x), pk(im.y, re.y), pk(im.z, re.z), pk(im.w, re.w)}
              : v4u{pk(re.x, im.x), pk(re.y, im.y), pk(re.z, im.z), pk(re.w, im.w)};
}

template <bool PLUS> DEV void cmul2(u32& o0, u32& o1, v2f p, v2f q, v2f c, v2f s) {
  const v2f re = fma2(PLUS ? -q : q, s, p * c), im = fma2(PLUS ? p : -p, s, q * c);
  o0 = PLUS ? pk(im.x, re.x) : pk(re.x, im.x); o1 = PLUS ? pk(im.y, re.y) : pk(re.y, im.y);
}

#define STAMP(i)
#define CSTAMP(i)
#define SLOWCNT
#define DUMP()

DEV void dma_w1(const u16* wg, const u16* wu, int slice, int wave, int lane, u32 lbase) {
  const rsrc_t rg = mkrs(wg, D * F * 2), ru = mkrs(wu, D * F * 2);
  const int r = lane >> 3, ch = ((((lane & 7) >> 1) ^ (lane >> 4)) & 3) * 32 + (lane & 1) * 16 + slice * 128;
  const int vp = r * (F * 2) + ch, vn = (7 - r) * (F * 2) + ch;
#pragma unroll
  for (int n = 0; n < 4; ++n) {
    const int ks = wave + 4 * n;
    if (n == 3 && wave >= 2) break;
    const int t16 = (ks >> 1) * 16, a = ks & 1;
#pragma unroll
    for (int q = 0; q < 4; ++q) {
      const int dl0 = t16 + 8 * (q & 1);
      const bool pos = (q >> 1) == a;
      const int d0 = q < 2 ? (a ? 224 - dl0 - 7 : dl0) : (a ? 224 + dl0 : 448 - dl0 - 7);
      int vo = pos ? vp : vn, so = d0 * (F * 2);
      if (n == 0 && q == 2) {
        vo += so; so = 0;
        if (wave < 2 && lane < 8) vo = (wave ? 336 : 112) * (F * 2) + ch;
      }
      dma16(rg, vo, so, lbase + L_W1 + ks * 8192 + q * 1024);
      dma16(ru, vo, so, lbase + L_W1 + ks * 8192 + 4096 + q * 1024);
    }
  }
}
struct ModLoad { u32 cv[7]; v4u mw[7]; };
DEV void mod_load(ModLoad& m, const u16* cin, const u16* wm, int q, int tid) {
  const rsrc_t cr = mkrs(cin, q >= 0 ? D * 2 : 0), mr = mkrs(wm, q >= 0 ? D * 6 * D * 2 : 0);
#pragma unroll
  for (int i = 0; i < 7; ++i) m.cv[i] = __builtin_amdgcn_raw_buffer_load_b16(cr, (tid >> 2) * 2 + i * 128, 0, 0);
#pragma unroll
  for (int i = 0; i < 7; ++i) m.mw[i] = __builtin_amdgcn_raw_buffer_load_b128(mr, ((tid >> 2) * (6 * D) + q * 32 + (tid & 3) * 8) * 2, i * 64 * 6 * D * 2, PF_AUX);
}
DEV void mod_part(ModLoad& m, char LAS* L, int tid) {
  float sl[7];
#pragma unroll
  for (int i = 0; i < 7; ++i) { const float cc = blo(m.cv[i]); sl[i] = rb(cc * __builtin_amdgcn_rcpf(1.f + __expf(-cc))); pin(sl[i]); }
  v2f a[4];
#pragma unroll
  for (int e = 0; e < 4; ++e) a[e] = v2f{0.f, 0.f};
#pragma unroll
  for (int i = 0; i < 7; ++i)
#pragma unroll
    for (int e = 0; e < 4; ++e) a[e] = fma2(bun(m.mw[i][e]), v2f{sl[i], sl[i]}, a[e]);
#pragma unroll
  for (int e = 0; e < 4; ++e) {
    a[e].x += dpp<0x128>(a[e].x); a[e].y += dpp<0x128>(a[e].y);
    a[e].x += dpp<0x124>(a[e].x); a[e].y += dpp<0x124>(a[e].y);
  }
  if ((tid & 15) < 4) {
    *(LAS v4f*)(L + L_RED + ((tid >> 4) * 32 + (tid & 15) * 8) * 4) = v4f{a[0].x, a[0].y, a[1].x, a[1].y};
    *(LAS v4f*)(L + L_RED + ((tid >> 4) * 32 + (tid & 15) * 8 + 4) * 4) = v4f{a[2].x, a[2].y, a[3].x, a[3].y};
  }
}
DEV void mod_publish(char LAS* L, rsrc_t ws, int q, int lane, u32 tag) {
  v4f s = {0.f, 0.f, 0.f, 0.f};
#pragma unroll
  for (int r = 0; r < 16; ++r) s += lds4f(L, L_RED + (r * 32 + (lane & 7) * 4) * 4);
  const float one = (q / 14 == 1 || q / 14 == 4) ? 1.f : 0.f;
  st4c(ws, WS_MOD + (lane >> 3) * MOD_REP + q * 128 + (lane & 7) * 16,
       v4u{pk(rb(s.x) + one, rb(s.y) + one), tag, pk(rb(s.z) + one, rb(s.w) + one), tag});
}
DEV u32 ld1(rsrc_t r, int off) { return __builtin_amdgcn_raw_buffer_load_b32(r, off, 0, 0); }
DEV u32 ld1n(rsrc_t r, int off) { return __builtin_amdgcn_raw_buffer_load_b32(r, off, 0, PF_AUX); }
DEV u32 mktag(u32 epoch) { return epoch * 0x9E3779B1u + 0x7F4A7C15u; }

DEV void x2_store(rsrc_t ws, const v4u (&r)[4], int blk, int wave, int l16) {
  const int o = WS_X2 + blk * 14336 + (wave == 0 ? 0 : 4 * wave - 2) * 1024 + l16;
#pragma unroll
  for (int m = 0; m < 4; ++m) st4c(ws, m < 2 || wave != 0 ? o + m * 1024 : OOB, r[m]);
}
DEV bool flags_ok(v4u rv, u32 tag) { return rv.x == tag && rv.y == ~tag && rv.z == tag && rv.w == ~tag; }
DEV void wait_flags(rsrc_t ws, int off, u32 tag) {
  _Pragma("nounroll") for (int s = 0; s < SPIN; ++s) {
    const v2u rv = ld2c(ws, fresh(off));
    if (wall(rv.x == tag && rv.y == ~tag)) break;
    __builtin_amdgcn_s_sleep(SL1);
  }
}

__global__ void __launch_bounds__(256, 1)
fused(const u16* __restrict__ xin, const u16* __restrict__ wr, const u16* __restrict__ wi, const u16* __restrict__ wg,
      const u16* __restrict__ wu, const u16* __restrict__ wd, const u16* __restrict__ cin, const u16* __restrict__ wm,
      u16* __restrict__ out, char* wsp) {
  __shared__ __attribute__((aligned(16))) char smem[L_SZ];
  char LAS* L = (char LAS*)smem;
  const u32 lbase = (u32)(size_t)L;
  const int tid = threadIdx.x, lane = tid & 63, wave = __builtin_amdgcn_readfirstlane(tid >> 6);
  const int g = lane >> 4, c16 = lane & 15, l16 = lane * 16;
  const int grp = blockIdx.x & 7, j = blockIdx.x >> 3;
  u64 pc;
  asm volatile("s_getpc_b64 %0" : "=s"(pc));
  STAMP(0); CSTAMP(31);
  asm volatile("" :: "s"(xin), "s"(wr), "s"(wi), "s"(wg), "s"(wu), "s"(wd), "s"(cin), "s"(wm), "s"(out), "s"(wsp));
  const rsrc_t ws = mkrs(wsp, WS_END);
  const rsrc_t tab = mkrs(TAB, TAB_N * 4096);
  const rsrc_t code = mkrs((const void*)(pc & ~127ull), CODE_BYTES);
  const bool isR = j >= 14 && j < 30;
  const bool isG = j < 28;
  const int gk = j / 7, ct = j - gk * 7;
  const int tile = 4 * (grp >> 2) + gk, slice = 7 * (grp & 3) + ct;
  const int wdb = WS_WD + (ct * 56 + wave * 14) * 4096;
  const v4u sv = {SENT, SENT, SENT, SENT};
  int lb = L_B + l16;
  pin(lb);
  const char LAS* LB = L + lb - L_B;
  const int pfo = ((j * 4 + wave) * 14 + lane) * 128, pfc = ((j * 4 + wave) * 4 + lane) * 128;
  u32 pfa = ld1(tab, lane < 14 ? pfo : OOB), pfb = ld1(code, lane < 4 ? pfc : OOB);
  const int hro = WS_HRDY + (16 * tile + 4 * wave) * 32 + c16 * 8;
  v4u x2r[4];
  const int wl16 = (lane ^ ((wave & 1) << 1)) * 16;
  v4u wdn[14][4];
  u32 epoch, tag;

#define ACT_CLEAR(lo, hi) _Pragma("unroll") for (int m = lo; m < hi; ++m) \
      st4c(ws, isG ? WS_ACT + (16 * (tile + 2 * (m >> 3)) + 4 * (grp & 3)) * 57344 + ct * 32768 + (m & 7) * 4096 + tid * 16 : OOB, sv);
  if (isR) {
    const int blk = 16 * grp + (j - 14);
    epoch = ld1c(ws, WS_EPOCH);
    const int t0 = wave == 0 ? 0 : 2 * wave - 1;
    v4u cf[8][4];
#define CF_LOAD(q) { const int chn = TAB_P1 + ((wave == 0 && q >= 4) ? 28 : 4 * t0 + q), vo = (q < 5 || wave != 0) ? l16 : OOB; \
      _Pragma("unroll") for (int s = 0; s < 4; ++s) cf[q][s] = ld4(tab, vo + s * 1024, chn * 4096); sb(); }
#pragma unroll
    for (int q = 0; q < X_AFTER; ++q) CF_LOAD(q)
    v2u xv[7];
    {
      const rsrc_t xr = mkrs(xin, S * D * 2);
      const int off = tok_row(blk, 4 * wave + g) * (D * 2) + c16 * 8;
#pragma unroll
      for (int m = 0; m < 7; ++m) xv[m] = ld2(xr, off + m * 128);
    }
    typedef u32 v3u __attribute__((ext_vector_type(3)));
    v2u fa_[2], fb_[2]; v3u fc_[2], fd_[2];
    {
      const rsrc_t rr = mkrs(wr, S * DF * 2), ri = mkrs(wi, S * DF * 2);
      const int fo_ = 16 * wave + c16 < 57 ? 2 * g * 450 + (16 * wave + c16) * 8 : OOB;
#pragma unroll
      for (int hh = 0; hh < 2; ++hh) {
        const int so = blk * 7200 + hh * 3600;
        fa_[hh] = ld2(rr, fo_, so); fb_[hh] = ld2(ri, fo_, so);
        fc_[hh] = __builtin_amdgcn_raw_buffer_load_b96(rr, fo_ + 448, so, 0); fd_[hh] = __builtin_amdgcn_raw_buffer_load_b96(ri, fo_ + 448, so, 0);
      }
    }
    sb();
#pragma unroll
    for (int q = X_AFTER; q < 8; ++q) CF_LOAD(q)
    STAMP(23);
    v2u rdy;
    {
      v2f f[14], s2 = {0.f, 0.f};
#pragma unroll
      for (int m = 0; m < 7; ++m) {
        f[2 * m] = bun(xv[m].x); f[2 * m + 1] = bun(xv[m].y);
        s2 += f[2 * m] + f[2 * m + 1];
        *(LAS v2u*)(L + L_XS + (4 * wave + g) * XP + c16 * 8 + m * 128) = xv[m];
      }
      STAMP(21);
      sb();
      {
        const int q = 16 * wave + c16, o = WS_WT + q * 16384 + blk * 128 + g * 16;
#pragma unroll
        for (int hh = 0; hh < 2; ++hh) {
          const v2u a = fa_[hh], b = fb_[hh]; const v3u c = fc_[hh], d = fd_[hh];
          st4c(ws, q < 57 ? o + hh * 64 : OOB, v4u{__builtin_amdgcn_perm(b.x, a.x, 0x05040100u), __builtin_amdgcn_perm(b.y, a.y, 0x05040100u),
                                                   __builtin_amdgcn_perm(d.x, c.x, 0x07060302u), __builtin_amdgcn_perm(d.y, c.y, 0x07060302u)});
          st4c(ws, q < 56 ? o + hh * 64 + 57 * 16384 : OOB,
               v4u{__builtin_amdgcn_perm(b.x, a.x, 0x07060302u), __builtin_amdgcn_perm(b.y, a.y, 0x07060302u),
                   __builtin_amdgcn_perm(d.y, c.y, 0x05040100u), __builtin_amdgcn_perm(d.z, c.z, 0x05040100u)});
        }
      }
      rdy = ld2c(ws, WS_RDY + grp * 1024 + min(tid >> 1, 112) * 8);
      sb();
      const float mean = sum16(s2.x + s2.y) * (1.f / D);
      v2f q2 = {0.f, 0.f};
#pragma unroll
      for (int m = 0; m < 14; ++m) { const v2f d = f[m] - mean; q2 = fma2(d, d, q2); }
      const float rstd = __builtin_amdgcn_rsqf(sum16(q2.x + q2.y) * (1.f / D) + 1e-5f);
      if (c16 == 0) *(LAS v2f*)(L + L_ST + (4 * wave + g) * 8) = v2f{mean, rstd};
    }
    pin(pfa); pin(pfb);
    epoch = __builtin_amdgcn_readfirstlane(epoch);
    tag = mktag(epoch);
    STAMP(22);
    bar();
    v4f n[4][2];
    const int d0 = 8 * (4 * wave + g);
    const v2f st = *(const LAS v2f*)(L + L_ST + c16 * 8);
    {
      int xr_ = L_XS + c16 * XP + d0 * 2, xb_ = L_XS + c16 * XP + (216 - d0) * 2, x0_ = d0 == 0 ? xr_ : xb_ + 464;
      pin(xr_); pin(xb_); pin(x0_);
      const v4u a = lds4(L + xr_, 0), e = lds4(L + xr_, 448), c = lds4(L + xb_, 0), b = lds4(L + xb_, 448);
      const u32 c0 = *(const LAS u16*)(L + xb_ + 16), b0 = *(const LAS u16*)(L + x0_);
#pragma unroll
      for (int i = 0; i < 8; ++i) {
        n[0][i >> 2][i & 3] = (i & 1) ? bhi(a[i >> 1]) : blo(a[i >> 1]);
        n[3][i >> 2][i & 3] = (i & 1) ? bhi(e[i >> 1]) : blo(e[i >> 1]);
        n[1][i >> 2][i & 3] = i == 0 ? blo(b0) : (i & 1) ? bhi(b[(8 - i) >> 1]) : blo(b[(8 - i) >> 1]);
        n[2][i >> 2][i & 3] = i == 0 ? blo(c0) : (i & 1) ? bhi(c[(8 - i) >> 1]) : blo(c[(8 - i) >> 1]);
      }
      const float nm = -st.x * st.y;
#pragma unroll
      for (int m = 0; m < 4; ++m) {
#pragma unroll
        for (int h = 0; h < 2; ++h) { n[m][h] = fma4(n[m][h], splat(st.y), splat(nm)); pin(n[m][h]); }
      }
    }
    sb();
    STAMP(1);
    int mb = L_M1 + d0 * 4;
    pin(mb);
    v4u xs[4];
    int to[4];
    to[0] = WS_T1 + blk * T1_BLK + (16 * t0 + c16) * 64 + g * 16;
    to[1] = wave != 0 ? to[0] + 1024 : c16 < 2 ? to[0] + 112 * 64 : OOB;
    to[2] = to[0] + 114 * 64;
    to[3] = wave != 0 ? to[0] + 1024 + 114 * 64 : OOB;
#pragma unroll
    for (int q = 0; q < 4; ++q) pin(to[q]);

    {
      const int kind = tid >= 112, d = 4 * (tid - 112 * kind);
      int wb = L_M1 + (kind ? 0 : 2 * 1792) + d * 4, wrv = L_M1 + (kind ? 0 : 2 * 1792) + 1792 + (445 - d) * 4;
      int wr0 = d == 0 ? wrv + 12 - 1792 : wrv + 12;
      const int mo = tid < 224 ? WS_MOD + grp * MOD_REP + tid * 16 : OOB;
      pin(wb); pin(wrv); pin(wr0);
      if (!wall(rdy.x == tag && rdy.y == ~tag)) wait_flags(ws, WS_RDY + grp * 1024 + min(tid >> 1, 112) * 8, tag);
      v4u mv;
      _Pragma("nounroll") for (int s = 0; s < SPIN; ++s) {
        mv = ld4c(ws, fresh(mo));
        if (wall(tid >= 224 || (mv.y == tag && mv.w == tag))) break;
        __builtin_amdgcn_s_sleep(SL1);
      }
      STAMP(2);
      if (tid < 224) {
        const v4f v = {blo(mv.x), bhi(mv.x), blo(mv.z), bhi(mv.z)};
        *(LAS v4f*)(L + wb) = v;
        *(LAS float*)(L + wr0) = v.x;
        *(LAS float*)(L + wrv + 8) = v.y;
        *(LAS float*)(L + wrv + 4) = v.z;
        *(LAS float*)(L + wrv) = v.w;
      }
    }
    bar();
    {
      v4u fr[4];
#pragma unroll
      for (int hh = 0; hh < 2; ++hh) {
        v4f h[4];
#pragma unroll
        for (int m = 0; m < 4; ++m) {
          const int o = ((m == 1 || m == 2) ? 1792 : 0) + (m >= 2 ? 896 : 0) + hh * 16;
          h[m] = fma4(n[m][hh], lds4f(L + mb, o), lds4f(L + mb, 2 * 1792 + o));
        }
        const v4f p = h[0] + h[1], q = h[2] + h[3], u = h[0] - h[1], v = h[3] - h[2];
        const v4f t[4] = {p + q, p - q, u + v, u - v};
#pragma unroll
        for (int m = 0; m < 4; ++m) { fr[m][2 * hh] = pk2(lo2(t[m])); fr[m][2 * hh + 1] = pk2(hi2(t[m])); }
      }
#pragma unroll
      for (int m = 0; m < 4; ++m) *(LAS v4u*)(LB + wave * 1024 + L_AF + m * 4096) = fr[m];
    }
    bar();
    {
      v4u a[4][4];
#pragma unroll
      for (int p = 0; p < 2; ++p)
#pragma unroll
        for (int s = 0; s < 4; ++s) { a[p][s] = lds4(LB, L_AF + (p * 4 + s) * 1024); a[2 + p][s] = lds4(LB, L_AF + ((2 + p) * 4 + s) * 1024); }
      v4f re[4], im[4];
#pragma unroll
      for (int q = 0; q < 5; ++q) {
        if (q < 4) mf8(re[q], im[q], a[q >> 1], cf[4 * (q & 1) + (q >> 1)], a[2 + (q >> 1)], cf[4 * (q & 1) + 2 + (q >> 1)]);
        if (q > 0) {
          if (q < 4) mfence<0>(re[q - 1], im[q - 1]); else mfence<7>(re[3], im[3]);
          xs[q - 1] = v4u{pk(re[q - 1].x, im[q - 1].x), pk(re[q - 1].y, im[q - 1].y), pk(re[q - 1].z, im[q - 1].z), pk(re[q - 1].w, im[q - 1].w)};
          st4c(ws, to[q - 1], xs[q - 1]);
          sb();
        }
      }
    }
    STAMP(3);
    sb();
    sb();
    if (isG) dma_w1(wg, wu, slice, wave, lane, lbase);
#pragma unroll
    for (int q = 0; q < 4; ++q) {
      const int vo = (q < 2 || wave != 0) ? l16 : OOB;
#pragma unroll
      for (int s = 0; s < 4; ++s) cf[2 * q + 1][s] = ld4(tab, vo + s * 1024, (TAB_P3 + 2 * t0 + q) * 4096);
    }
#pragma unroll
    for (int m = 0; m < 4; ++m) {
      st4c(ws, tid + 256 * m < 896 ? WS_H2 + blk * 14336 + 4096 * m + tid * 16 : OOB, sv);
    }
    sb();
    STAMP(20);
    v4f xo[2][4];
    int xa = L_XS + c16 * XP + (16 * t0 + 4 * g) * 2, xd = L_XS + c16 * XP + (192 - 16 * t0 - 4 * g) * 2;
    int xs1 = tid < 16 ? L_XS + c16 * XP + 224 : xd + 512, xs3 = tid < 16 ? L_XS + c16 * XP + 672 : xa + 448;
    pin(xa); pin(xd); pin(xs1); pin(xs3);
#define XOFF(t, m, r) ((t) == 0 && (r) == 0 && (m) == 1 ? xs1 : (t) == 0 && (r) == 0 && (m) == 3 ? xs3 : \
    (m) == 0 ? xa + 32 * (t) + 2 * (r) : (m) == 3 ? xa + 448 + 32 * (t) + 2 * (r) : (m) == 1 ? xd + 512 - 32 * (t) - 2 * (r) : xd + 64 - 32 * (t) - 2 * (r))
    {
      v2u w[2][4]; u32 e[2][2], e3;
#pragma unroll
      for (int t = 0; t < 2; ++t) {
        w[t][0] = *(const LAS v2u*)(L + xa + 32 * t); w[t][3] = *(const LAS v2u*)(L + xa + 448 + 32 * t);
        w[t][1] = *(const LAS v2u*)(L + xd + 504 - 32 * t); w[t][2] = *(const LAS v2u*)(L + xd + 56 - 32 * t);
        e[t][0] = *(const LAS u16*)(L + XOFF(t, 1, 0)); e[t][1] = *(const LAS u16*)(L + XOFF(t, 2, 0));
      }
      e3 = *(const LAS u16*)(L + xs3);
      sb();
#pragma unroll
      for (int t = 0; t < 2; ++t) {
        xo[t][0] = v4f{blo(w[t][0].x), bhi(w[t][0].x), blo(w[t][0].y), bhi(w[t][0].y)};
        xo[t][3] = v4f{t == 0 ? blo(e3) : blo(w[t][3].x), bhi(w[t][3].x), blo(w[t][3].y), bhi(w[t][3].y)};
        xo[t][1] = v4f{blo(e[t][0]), bhi(w[t][1].y), blo(w[t][1].y), bhi(w[t][1].x)};
        xo[t][2] = v4f{blo(e[t][1]), bhi(w[t][2].y), blo(w[t][2].y), bhi(w[t][2].x)};
#pragma unroll
        for (int m = 0; m < 4; ++m) { xo[t][m] -= st.x; pin(xo[t][m]); }
      }
    }
    int m0_[2], m1_[2], md_[2];
#pragma unroll
    for (int r = 0; r < 2; ++r) {
      const int q = r == 0 || tid < 80 ? tid + 256 * r : tid;
      const int kind = (q * 586) >> 16, d = 4 * (q - 112 * kind);
      const int reg = (d >= 112) + (d >= 224) + (d >= 336), cc = ((reg + 1) >> 1) * 224, m1 = (0x1320 >> (4 * reg)) & 3;
      const int d1 = abs(d + 1 - cc), d0_ = abs(d - cc), edge = d == 112 * reg && reg > 0;
      m1_[r] = L_M2 + kind * 1792 + (((d1 >> 4) << 6) | (m1 << 4) | (d1 & 15)) * 4;
      m0_[r] = L_M2 + kind * 1792 + (edge ? reg << 4 : ((d0_ >> 4) << 6) | (m1 << 4) | (d0_ & 15)) * 4;
      md_[r] = (reg & 1) ? -4 : 4;
      pin(m0_[r]); pin(m1_[r]); pin(md_[r]);
    }
    {
      v4u mv[2];
      _Pragma("nounroll") for (int s = 0; s < SPIN; ++s) {
        mv[0] = ld4c(ws, fresh(WS_MOD + grp * MOD_REP + (224 + tid) * 16));
        mv[1] = ld4c(ws, fresh(WS_MOD + grp * MOD_REP + (224 + (tid < 80 ? 256 + tid : tid)) * 16));
        const v2u rv = ld2c(ws, fresh(WS_DRDY + grp * 1024 + min(lane, 48) * 8));
        if (wall(mv[0].y == tag && mv[0].w == tag && mv[1].y == tag && mv[1].w == tag && rv.x == tag && rv.y == ~tag)) break;
        __builtin_amdgcn_s_sleep(SL4);
      }
      if (lane < (CLR_MODE == 1 ? 2 : 1)) st2c(ws, lane == 0 ? WS_HRDY + blk * 32 + wave * 8 : isG ? WS_CRDY + (tile * 4 + (grp & 3)) * 256 + (ct * 4 + wave) * 8 : OOB, v2u{tag, ~tag});
      STAMP(12);
#pragma unroll
      for (int f = 0; f < WD_EARLY; ++f) wdn[f >> 2][f & 3] = ld4c(ws, (isG ? wl16 : OOB) + (f & 3) * 1024, wdb + (f >> 2) * 4096);
      sb();
#pragma unroll
      for (int r = 0; r < 2; ++r) {
        const float sc = m0_[r] < L_M2 + 1792 ? 2.f / (float)(S * D) : 1.f;
        *(LAS float*)(L + m0_[r]) = blo(mv[r].x) * sc;
        *(LAS float*)(L + m1_[r]) = bhi(mv[r].x) * sc;
        *(LAS float*)(L + m1_[r] + md_[r]) = blo(mv[r].z) * sc;
        *(LAS float*)(L + m1_[r] + 2 * md_[r]) = bhi(mv[r].z) * sc;
      }
    }
    bar();
    *(LAS v4u*)(L + L_AF + (tid >> 6) * 4096 + 3072 + l16) = v4u{0u, 0u, 0u, 0u};
    STAMP(4);
    int zr = L_AF + lane * 8, ro = L_M2 + (t0 * 64 + 4 * g) * 4, ro1 = wave == 0 ? ro : ro + 256, ex = L_EX + c16 * 8;
    pin(zr); pin(ro); pin(ro1); pin(ex);
    {
      int zl[4];
#pragma unroll
      for (int m = 0; m < 4; ++m) {
        const int kq = (m >> 1) * 128 + (to[m] == OOB ? 127 : wave == 0 && m == 1 ? 112 + c16 : 16 * (t0 + (m & 1)) + c16);
        zl[m] = L_AF + ((kq & ~31) + ((kq & 4) << 2) + ((kq & 24) >> 1) + (kq & 3)) * 32 + g * 8;
        pin(zl[m]);
        if (to[m] == OOB) xs[m] = sv;
      }
      bar();
      if (isG) wait_flags(ws, hro, tag);
      {
        const int src = (blk * 5 + wave * 13) & 63;
        const int po = __builtin_amdgcn_readlane(to[(0)], 0) + (src & 15) * 64 + (src >> 4) * 16;
        const u32 old = __builtin_amdgcn_ds_bpermute(src * 4, xs[0].x);
        _Pragma("nounroll") for (int s = 0; s < SPIN; ++s) {
          if (wall(ld1c(ws, fresh(po)) != old)) break;
          __builtin_amdgcn_s_sleep(SL2);
        }
      }
      STAMP(13);
      v4u z[4];
      _Pragma("nounroll") for (int s = 0; s < SPIN; ++s) {
        u32 mn = 1;
#pragma unroll
        for (int m = 0; m < 4; ++m) z[m] = ld4c(ws, fresh(to[m]));
#pragma unroll
        for (int m = 0; m < 4; ++m) { const v4u d = z[m] ^ xs[m]; mn = min(min(mn, d.x), min(min(d.y, d.z), d.w)); }
        if (wall(mn != 0)) break;
      }
      STAMP(14);
#pragma unroll
      for (int m = 0; m < 4; ++m) {
        char LAS* p = L + zl[m];
        *(LAS v2u*)(p) = v2u{__builtin_amdgcn_perm(z[m].y, z[m].x, 0x05040100u), __builtin_amdgcn_perm(z[m].w, z[m].z, 0x05040100u)};
        *(LAS v2u*)(p + ZPL) = v2u{__builtin_amdgcn_perm(z[m].y, z[m].x, 0x07060302u), __builtin_amdgcn_perm(z[m].w, z[m].z, 0x07060302u)};
      }
    }
    bar();
    STAMP(5);
    {
      v4u zf[4][4];
#pragma unroll
      for (int ty = 0; ty < 4; ++ty)
#pragma unroll
        for (int s = 0; s < 4; ++s) zf[ty][s] = ldtr2(L + zr, ty * 4096 + s * 1024, ty * 4096 + s * 1024 + 512);
      v4f acc[2][4], x2[2][4], gav[2][4];
      v2f s1 = {0.f, 0.f}, s2 = {0.f, 0.f};
#define P3M(t, ty) mf8(acc[t][ty], acc[t][ty + 1], cf[4 * t + ty], zf[ty], cf[4 * t + ty + 1], zf[ty + 1]);
#define P3X(t, m, yv) { x2[t][m] = fma4(gav[t][m], yv, xo[t][m]); \
      if (t == 0 || wave != 0) { s1 += lo2(x2[t][m]) + hi2(x2[t][m]); \
        s2 = fma2(lo2(x2[t][m]), lo2(x2[t][m]), s2); s2 = fma2(hi2(x2[t][m]), hi2(x2[t][m]), s2); } }
      P3M(1, 0) P3M(1, 2)
#pragma unroll
      for (int m = 0; m < 4; ++m) { gav[0][m] = lds4f(L + ro, m * 64); gav[1][m] = lds4f(L + ro1, m * 64); }
      sb();
      P3M(0, 0) P3M(0, 2)
      mfence<0>(acc[1][0], acc[1][1], acc[1][2], acc[1][3]);
      const v4f A1 = acc[1][0] + acc[1][1], B1 = acc[1][0] - acc[1][1], P1 = acc[1][3] - acc[1][2], Q1 = acc[1][2] + acc[1][3];
      P3X(1, 0, A1 - P1) P3X(1, 1, A1 + P1) P3X(1, 2, B1 - Q1) P3X(1, 3, B1 + Q1)
      sb();
      mfence<0>(acc[0][0], acc[0][1], acc[0][2], acc[0][3]);
      {
        const v4f A = acc[0][0] + acc[0][1], B = acc[0][0] - acc[0][1], P = acc[0][3] - acc[0][2], Q = acc[0][2] + acc[0][3];
        v4f y[4] = {A - P, A + P, B - Q, B + Q};
        const bool sp = tid < 16;
        y[0].x = sp ? A.x : y[0].x; y[2].x = sp ? B.x : y[2].x;
        y[1].x = sp ? acc[1][0].x - acc[0][3].x : y[1].x; y[3].x = sp ? acc[1][0].x + acc[0][3].x : y[3].x;
        P3X(0, 0, y[0]) P3X(0, 1, y[1]) P3X(0, 2, y[2]) P3X(0, 3, y[3])
      }
      float sm = s1.x + s1.y, sq = s2.x + s2.y;
      sumg2(sm, sq);
      if (g == 0) *(LAS v2f*)(L + ex + wave * 128) = v2f{sm, sq};
      v4f sbv[2][4], cbv[2][4];
#pragma unroll
      for (int m = 0; m < 4; ++m) {
        sbv[0][m] = lds4f(L + ro, 1792 + m * 64); cbv[0][m] = lds4f(L + ro, 2 * 1792 + m * 64);
        sbv[1][m] = lds4f(L + ro1, 1792 + m * 64); cbv[1][m] = lds4f(L + ro1, 2 * 1792 + m * 64);
      }
      bar();
      v2f e = *(const LAS v2f*)(L + ex);
#pragma unroll
      for (int w = 1; w < 4; ++w) e += *(const LAS v2f*)(L + ex + w * 128);
      const float mc = e.x * (1.f / D), rstd = __builtin_amdgcn_rsqf(fmaxf(e.y * (1.f / D) - mc * mc, 0.f) + 1e-5f), nb = -mc * rstd;
      const int ho = WS_H2 + blk * 14336 + 2 * t0 * 1024 + l16, ho1 = wave == 0 ? OOB : ho + 2048;
#pragma unroll
      for (int t = 0; t < 2; ++t)
#pragma unroll
        for (int a = 0; a < 2; ++a) {
          v4u o;
#pragma unroll
          for (int hh = 0; hh < 2; ++hh) {
            const int m = 2 * a + hh;
            const v4f hv = fma4(fma4(x2[t][m], splat(rstd), splat(nb)), cbv[t][m], sbv[t][m]);
            o[2 * hh] = pk2(lo2(hv)); o[2 * hh + 1] = pk2(hi2(hv));
          }
          st4c(ws, (t ? ho1 : ho) + a * 1024, o);
        }
      STAMP(6);
      sb();
#pragma unroll
      for (int f = WD_EARLY; f < WD_MID; ++f) wdn[f >> 2][f & 3] = ld4c(ws, (isG ? wl16 : OOB) + (f & 3) * 1024, wdb + (f >> 2) * 4096);
      sb();
#pragma unroll
      for (int e = 0; e < 8; ++e) {
        x2r[e >> 1][2 * (e & 1)] = pk2(lo2(x2[e >> 2][e & 3]) + st.x); x2r[e >> 1][2 * (e & 1) + 1] = pk2(hi2(x2[e >> 2][e & 3]) + st.x);
      }
#pragma unroll
      for (int m = 0; m < 4; ++m) pin(x2r[m]);
    }
    bar();
    if (!isG) {
      x2_store(ws, x2r, blk, wave, l16);
      if (blockIdx.x == 224 && tid == 0) __builtin_amdgcn_raw_buffer_store_b32(epoch + 1u, ws, WS_EPOCH, 0, SC1);
      drain();
      if (lane == 0) st2c(ws, WS_XRDY + blk * 64 + wave * 8, v2u{tag, ~tag});
      DUMP(); return;
    }
  } else {
    const int sj = j >= 30 ? j - 30 : j + 2;
    const int idx = sj * 8 + grp;
    const bool isC = j < 14 || idx == 7;
    const int pair = j < 14 ? grp * 14 + j : 112;
    const int lc = grp + 8 * (j >> 1);
    const bool urgent = j < 4;
    const int qu = urgent ? (lc < 14 ? 2 * lc + (j & 1) : -1) : sj < 2 && grp < 7 ? 28 + grp * 6 + sj : -1;
    const int t1o = WS_T1 + (32 * wave + (lane >> 3)) * T1_BLK + pair * 128 + (lane & 7) * 16;
    ModLoad ml;
    epoch = ld1c(ws, WS_EPOCH);
#pragma unroll
    for (int q = 0; q < 4; ++q) st4c(ws, isC ? t1o + 8 * q * T1_BLK : OOB, sv);
    sb();
    mod_load(ml, cin, wm, qu, tid);
    u32 pf0, pf1, pf2;
    {
      const rsrc_t mr = mkrs(wm, D * 6 * D * 2), xr = mkrs(xin, S * D * 2), fr_ = mkrs((wave & 1) ? wi : wr, S * DF * 2);
      const int row = (sj * 4 + wave) * 7 + (lane >> 1), l7 = lane / 7;
      pf0 = __builtin_amdgcn_raw_buffer_load_b32(mr, lane < 14 && (grp < 6 || !(lane & 1)) ? row * (6 * D * 2) + (grp + 8 * (lane & 1)) * 128 : OOB, 0, PF_AUX);
      pf1 = __builtin_amdgcn_raw_buffer_load_b32(xr, lane < (sj < 6 ? 0 : sj < 10 ? 28 : 56) ? tok_row(16 * grp + (lane < 28 ? sj : sj - 10), 4 * wave + (l7 & 3)) * (D * 2) + (lane - 7 * l7) * 128 : OOB, 0, PF_AUX);
      pf2 = __builtin_amdgcn_raw_buffer_load_b32(fr_, (lane & 31) < 29 && lane < (sj < 6 ? 0 : sj < 10 ? 32 : 64) ? (((16 * grp + (lane < 32 ? sj : sj - 10)) * 7200) & ~127) + ((wave >> 1) * 29 + (lane & 31)) * 128 : OOB, 0, PF_AUX);
    }
    sb();
    asm volatile("s_waitcnt vmcnt(%0)" :: "i"(W_LATE ? 16 : 17) : "memory");
    epoch = __builtin_amdgcn_readfirstlane(epoch);
    tag = mktag(epoch);
    bar();
    if (wave == 1 && lane < 8) st2c(ws, isC ? WS_RDY + lane * 1024 + pair * 8 : OOB, v2u{tag, ~tag});
    if (qu >= 0) {
      mod_part(ml, L, tid);
      bar();
      if (wave == 0) mod_publish(L, ws, qu, lane, tag);
    }
    STAMP(1);
    pin(pf0); pin(pf1); pin(pf2); pin(pfa); pin(pfb);
    const int qn = grp == 7 ? -1 : sj >= 6 && sj < 10 ? 28 + grp * 6 + (sj - 4) : sj < 2 ? 70 + grp * 2 + sj : -1;
    const int tw = sj >= 10 ? (sj - 10) * 8 + grp : sj == 6 && grp == 7 ? 48 : -1;
    v2u dw[2][8];
    if (qn >= 0) mod_load(ml, cin, wm, qn, tid);
    if (tw >= 0) {
      const rsrc_t dr = mkrs(wd, F * D * 2);
      const int u0 = tw * 4 + wave, uc = u0 / 28, kap = 2 * (u0 - uc * 28);
#pragma unroll
      for (int e = 0; e < 16; ++e)
        dw[e >> 3][e & 7] = __builtin_amdgcn_raw_buffer_load_b64(dr, (4 * g * D + 4 * c16) * 2 + (e & 3) * D * 2, ((32 * kap + 16 * (e >> 2)) * D + 64 * uc) * 2, PF_AUX);
    }
    sb();
    u32 pw[4];
    {
      const rsrc_t rg = mkrs(wg, D * F * 2), ru = mkrs(wu, D * F * 2);
      const int nr = sj < 2 ? NR_S : sj < 6 ? NR_U : sj < 10 ? NR_M : NR_T;
      const int r0 = (sj < 2 ? sj * NR_S : sj < 6 ? 2 * NR_S + (sj - 2) * NR_U : sj < 10 ? 2 * NR_S + 4 * NR_U + (sj - 6) * NR_M
                                                                             : 2 * NR_S + 4 * NR_U + 4 * NR_M + (sj - 10) * NR_T) * 4 + wave * nr;
      static_assert(2 * NR_S + 4 * NR_U + 4 * NR_M + 6 * NR_T == 112 && NR_S <= 16 && NR_U <= 16, "all rows");
      const int r7 = lane / 7, po = (r0 + r7) * (F * 2) + ((grp & 3) * 7 + lane - r7 * 7) * 128;
      const int o0 = lane < 56 && r7 < nr ? po : OOB, o1 = lane < 56 && r7 + 8 < nr ? po + 8 * (F * 2) : OOB;
      pw[0] = ld1(rg, o0); pw[1] = ld1(ru, o0); pw[2] = ld1(rg, o1); pw[3] = ld1(ru, o1);
    }
    sb();
    STAMP(20);
    v4f t1c[2], t1s[2], t2c[2], t2s[2];
#pragma unroll
    for (int t = 0; t < 2; ++t) {
#pragma unroll
      for (int r = 0; r < 4; ++r) {
        const float a1 = (float)((16 * t + 4 * g + r) * (16 * wave + c16)) * (1.f / S);
        const float a2 = (float)((16 * t + c16) * (16 * wave + 4 * g + r)) * (1.f / S);
        t1c[t][r] = __builtin_amdgcn_cosf(a1); t1s[t][r] = __builtin_amdgcn_sinf(a1);
        t2c[t][r] = __builtin_amdgcn_cosf(a2); t2s[t][r] = __builtin_amdgcn_sinf(a2);
      }
      pin(t1c[t]); pin(t1s[t]); pin(t2c[t]); pin(t2s[t]);
    }
    if (tw >= 0) {
#pragma unroll
      for (int tt = 0; tt < 8; ++tt) {
        v4u o;
#pragma unroll
        for (int m = 0; m < 4; ++m)
          o[m] = __builtin_amdgcn_perm(dw[tt >> 2][2 * m + 1][(tt & 3) >> 1], dw[tt >> 2][2 * m][(tt & 3) >> 1], (tt & 1) ? 0x07060302u : 0x05040100u);
        st4c(ws, WS_WD + (tw * 4 + wave) * 8192 + tt * 1024 + l16, o);
      }
    }
    if (qn >= 0) {
      bar();
      mod_part(ml, L, tid);
      bar();
      if (wave == 0) mod_publish(L, ws, qn, lane, tag);
    }
    pin(pw[0]); pin(pw[1]); pin(pw[2]); pin(pw[3]);
    STAMP(21);
    if (!isC) { DUMP(); return; }
    v4u c64[2][4], c32[2][2][2];
#pragma unroll
    for (int cp = 0; cp < 2; ++cp)
#pragma unroll
      for (int s = 0; s < 4; ++s) c64[cp][s] = ld4(tab, l16 + s * 1024, (TAB_P2 + wave * 2 + cp) * 4096);
#pragma unroll
    for (int t = 0; t < 2; ++t)
#pragma unroll
      for (int f = 0; f < 4; ++f) c32[t][f >> 1][f & 1] = ld4(tab, l16 + f * 1024, (TAB_P2 + 8 + t) * 4096);
    sb();
    if (tw >= 0) {
      asm volatile("s_waitcnt vmcnt(16)" ::: "memory");
      bar();
      if (tid < 8) st2c(ws, WS_DRDY + tid * 1024 + tw * 8, v2u{tag, ~tag});
    }
    STAMP(2);
    int wfo = WS_WT + pair * 16384 + (16 * g + wave) * 128 + c16 * 8;
    pin(wfo);
    v2u wf[2][4];
    {
      int wl = L_CI + ((lane >> 2) & 1) * 8256 + wave * 2048 + ((lane >> 3) * 4 + (lane & 3)) * 16, lo = t1o;
      pin(wl); pin(lo);
      light_poll(ws, WS_T1 + (32 * wave + (pair & 31)) * T1_BLK + pair * 128);
      STAMP(13);
      v4u z[4];
      _Pragma("nounroll") for (int s = 0; s < SPIN; ++s) {
        lo = fresh(lo);
#pragma unroll
        for (int q = 0; q < 4; ++q) z[q] = ld4c(ws, lo, 8 * q * T1_BLK);
        if (wall(max(max(max4(z[0]), max4(z[1])), max(max4(z[2]), max4(z[3]))) != SENT)) break;
      }
      STAMP(14);
#pragma unroll
      for (int q = 0; q < 4; ++q) *(LAS v4u*)(L + wl + q * 512) = z[q];
    }
    bar();
    v2u fd;
    sb();
    STAMP(4);
    {
      v4u a[2][2][4];
#pragma unroll
      for (int c = 0; c < 2; ++c)
#pragma unroll
        for (int mt = 0; mt < 2; ++mt)
#pragma unroll
          for (int s = 0; s < 4; ++s) a[c][mt][s] = lds4(LB, L_CI + c * 8256 + (s * 2 + mt) * 1024);
      v4u old[2][2];
#pragma unroll
      for (int c = 0; c < 2; ++c)
#pragma unroll
        for (int mt = 0; mt < 2; ++mt) old[c][mt] = lds4(LB + wave * 2048, L_CI + c * 8256 + mt * 1024);
      sb();
#pragma unroll
    for (int t = 0; t < 2; ++t)
#pragma unroll
      for (int r = 0; r < 4; ++r) wf[t][r] = ld2c(ws, wfo + r * 512, t * 8192);
    fd = ld2c(ws, WS_DRDY + grp * 1024 + min(lane, 48) * 8);
      sb();
      v4f re[2][2], im[2][2], ur[2][2], ui[2][2], fr_[2][2], fi_[2][2];
      u32 f2[2][2][4], f3[2][2][4], f4[4];
      char LAS* xw = L + lb + wave * 1024 - L_B;
#define V4(x) v4u{x[0], x[1], x[2], x[3]}
#define S1K(h) c64[0][2 * h], c64[0][2 * h + 1], c64[1][2 * h], c64[1][2 * h + 1]
#define S1X(c, t, h) re[c][t], im[c][t], a[c][t][2 * h], a[c][t][2 * h + 1], S1K(h)
#define S2X(c, t) ur[c][t], ui[c][t], V4(f2[c][0]), V4(f2[c][1]), c32[t][0][0], c32[t][0][1], c32[t][1][0], c32[t][1][1]
#define S3X(c, t) im[c][t], re[c][t], V4(f3[c][0]), V4(f3[c][1]), c32[t][0][0], c32[t][0][1], c32[t][1][0], c32[t][1][1]
#define CM(o, P, Q, C, S_, h) o[2 * h], o[2 * h + 1], P[2 * h], Q[2 * h], C[2 * h], S_[2 * h], P[2 * h + 1], Q[2 * h + 1], C[2 * h + 1], S_[2 * h + 1]
#define TW1(c, t, h) CM(f2[c][t], re[c][t], im[c][t], t1c[t], t1s[t], h)
#define FLT(c, t, h) CM(f3[c][t], ur[c][t], ui[c][t], fr_[c][t], fi_[c][t], h)
#define TW2(c, t, h) CM(f4, re[c][t], im[c][t], t2c[t], t2s[t], h)
#define HV(x, h) (h ? hi2(x) : lo2(x))
#define CP(PL, o, P, Q, C, S_, h) cmul2<PL>(o[2 * h], o[2 * h + 1], HV(P, h), HV(Q, h), HV(C, h), HV(S_, h));
#define XW(c, t) *(LAS v4u*)(xw + L_CX + (c * 2 + t) * 4096) = V4(f4);
#define UNP(c, t) { _Pragma("unroll") for (int r = 0; r < 4; ++r) { fr_[c][t][r] = blo(wf[t][r][c]); fi_[c][t][r] = bhi(wf[t][r][c]); } \
      pin(fr_[c][t]); pin(fi_[c][t]); }
      nop2();
      m4_a0(S1X(0, 0, 0)); m4_a1(S1X(0, 0, 1)); m4_a0(S1X(0, 1, 0)); mfence<3>(re[0][0], im[0][0]);
      h4_a1m(S1X(0, 1, 1), TW1(0, 0, 0)); h4_a0m(S1X(1, 0, 0), TW1(0, 0, 1));
      h4_a1m(S1X(1, 0, 1), TW1(0, 1, 0)); h4_a0m(S1X(1, 1, 0), TW1(0, 1, 1));
      h4_a1m(S1X(1, 1, 1), TW1(1, 0, 0)); h4_b0m(S2X(0, 0), TW1(1, 0, 1));
      h4_b0m(S2X(0, 1), TW1(1, 1, 0));
      sb();
      CP(false, f2[1][1], re[1][1], im[1][1], t1c[1], t1s[1], 1)
      UNP(0, 0) UNP(0, 1)
      sb();
      h4_b0p(S2X(1, 0), FLT(0, 0, 0)); h4_b0p(S2X(1, 1), FLT(0, 0, 1));
      sb();
      CP(true, f3[0][1], ur[0][1], ui[0][1], fr_[0][1], fi_[0][1], 0) CP(true, f3[0][1], ur[0][1], ui[0][1], fr_[0][1], fi_[0][1], 1)
      UNP(1, 0) UNP(1, 1)
      sb();
      h4_a0p(S3X(0, 0), FLT(1, 0, 0)); h4_a0p(S3X(0, 1), FLT(1, 0, 1));
      sb();
      CP(true, f3[1][1], ur[1][1], ui[1][1], fr_[1][1], fi_[1][1], 0) CP(true, f3[1][1], ur[1][1], ui[1][1], fr_[1][1], fi_[1][1], 1)
#pragma unroll
      for (int e = 0; e < 4; ++e) pin(f3[1][1][e]);
      nop2();
      h4_a0p(S3X(1, 0), TW2(0, 0, 0)); h4_a0p(S3X(1, 1), TW2(0, 0, 1));
      XW(0, 0)
      sb();
      CP(true, f4, re[0][1], im[0][1], t2c[1], t2s[1], 0) CP(true, f4, re[0][1], im[0][1], t2c[1], t2s[1], 1) XW(0, 1)
      sb();
      mfence<0>(re[1][0], im[1][0], re[1][1], im[1][1]);
      CP(true, f4, re[1][0], im[1][0], t2c[0], t2s[0], 0) CP(true, f4, re[1][0], im[1][0], t2c[0], t2s[0], 1) XW(1, 0)
      CP(true, f4, re[1][1], im[1][1], t2c[1], t2s[1], 0) CP(true, f4, re[1][1], im[1][1], t2c[1], t2s[1], 1) XW(1, 1)
      bar();
#pragma unroll
      for (int c = 0; c < 2; ++c)
#pragma unroll
        for (int nt = 0; nt < 2; ++nt)
#pragma unroll
          for (int s = 0; s < 4; ++s) a[c][nt][s] = lds4(LB, L_CX + ((c * 2 + nt) * 4 + s) * 1024);
      const int o2 = WS_T1 + (32 * wave + (lane >> 2)) * T1_BLK + pair * 128 + (lane & 3) * 16;
#define S4(c, t) mf8(im[c][t], re[c][t], c64[0], a[c][t], c64[1], a[c][t]);
#define NEQ(x, y) ((x) + ((x) == (y)))
#define OUT(c, t) { const v4u o = {pk(re[c][t].x, im[c][t].x), pk(re[c][t].y, im[c][t].y), pk(re[c][t].z, im[c][t].z), pk(re[c][t].w, im[c][t].w)}; \
      st4c(ws, o2 + c * 64 + t * 16 * T1_BLK, v4u{NEQ(o.x, old[c][t].x), NEQ(o.y, old[c][t].y), NEQ(o.z, old[c][t].z), NEQ(o.w, old[c][t].w)}); sb(); }
      S4(0, 0) S4(0, 1) mfence<0>(re[0][0], im[0][0]); OUT(0, 0)
      S4(1, 0) mfence<0>(re[0][1], im[0][1]); OUT(0, 1)
      S4(1, 1) mfence<0>(re[1][0], im[1][0]); OUT(1, 0)
      mfence<0>(re[1][1], im[1][1]); OUT(1, 1)
    }
    STAMP(5);
    sb();
    if (!isG) { DUMP(); return; }
    if (!wall(fd.x == tag && fd.y == ~tag)) wait_flags(ws, WS_DRDY + grp * 1024 + min(lane, 48) * 8, tag);
    dma_w1(wg, wu, slice, wave, lane, lbase);
#pragma unroll
    for (int f = 0; f < WD_MID; ++f) wdn[f >> 2][f & 3] = ld4c(ws, wl16 + (f & 3) * 1024, wdb + (f >> 2) * 4096);
    fd = ld2c(ws, hro);
    sb();
    ACT_CLEAR(0, 16)
    sb();
    if (!wall(fd.x == tag && fd.y == ~tag)) wait_flags(ws, hro, tag);
    bar();
  }

  STAMP(7);
  const int blk0 = 16 * tile + 4 * (grp & 3);
  const int ab = WS_ACT + blk0 * 57344 + wave * 14 * 1024;
  v4u h[14][2];
  {
    const int hb = WS_H2 + (16 * tile + 4 * wave) * 14336;
    int tb[2][4];
#pragma unroll
    for (int u = 0; u < 8; ++u) {
      tb[u >> 2][u & 3] = (((lane >> 2) * 128 + ((lane >> 3) & 3) * 32 + (lane & 3) * 8) ^ ((u & 3) << 5)) + (u >> 2) * 57344;
      pin(tb[u >> 2][u & 3]);
    }
#define W_RD(dst, k, u) dst = ldtr2(L + tb[k >= 7][u & 3], (k % 7) * 8192 + (u >> 2) * 4096, (k % 7) * 8192 + (u >> 2) * 4096 + 2048)
#define H_LD(v, tt) hh_[v][tt] = ld4c(ws, l16 + (((v) % 14) & 3) * 1024, hb + (2 * ((v) / 14) + (tt)) * 14336 + (((v) % 14) >> 2) * 4096)
    const int abx[2] = {ab + (wave >> 1) * 57344, ab + (1 - (wave >> 1)) * 57344};
#define G_LD(e) h[(e) >> 1][(e) & 1] = ld4c(ws, l16 + (((e) >> 1) & 3) * 1024, abx[(e) & 1] + ((e) >> 3) * 4096)
    v4u wfr[2][8], hh_[28][2];
#pragma unroll
    for (int u = 0; u < 8; ++u) W_RD(wfr[0][u], 0, u);
    light_poll(ws, hb + (lane & 1) * 14336 + 13 * 1024);
    STAMP(15);
#pragma unroll
    for (int v = 0; v < LEAD; ++v) { H_LD(v, 0); H_LD(v, 1); }
    sb();
    if (!isR && lane < (CLR_MODE == 1 ? 1 : 2))
      st2c(ws, WS_CRDY + ((tile + 2 * lane) * 4 + (grp & 3)) * 256 + (ct * 4 + wave) * 8, v2u{tag, ~tag});
    sb();
#define BAD2(x0, x1) wany(max(max(max(max(x0.x, x0.y), x0.z), max(max(x0.w, x1.x), x1.y)), max(x1.z, x1.w)) == SENT)
#define RETRY2(x0, x1, o0, o1) \
      _Pragma("nounroll") for (int s_ = 0; s_ < SPIN; ++s_) { \
        const int lf_ = fresh(l16); SLOWCNT \
        x0 = ld4c(ws, lf_, o0); x1 = ld4c(ws, lf_, o1); \
        if (wall(max(max4(x0), max4(x1)) != SENT)) break; \
      }
    if (__builtin_expect(BAD2(hh_[0][0], hh_[0][1]), 0)) { RETRY2(hh_[0][0], hh_[0][1], hb, hb + 14336) }
    STAMP(8); CSTAMP(28);
    v4f acc[8][2];
    v2u fa;
    const int fx = WS_CRDY + (tile * 4 + (lane < 32 ? wave : grp & 3)) * 256 + min(lane & 31, 27) * 8;
    const int ao = WS_ACT + (16 * tile + 4 * wave) * 57344 + 2 * slice * 1024 + l16;
#define ACT_OUT(ps, tt, f) { v4u o; \
      _Pragma("unroll") for (int hh = 0; hh < 2; ++hh) { \
        const v4f av = relu4(acc[2 * f + hh][tt]) * acc[4 + 2 * f + hh][tt]; \
        o[2 * hh] = pk2(lo2(av)); o[2 * hh + 1] = pk2(hi2(av)); } \
      st4c(ws, ao + (2 * ps + tt) * 57344 + f * 1024, o); }
#define ACT_ALL(ps) { mfence<0>(acc[0][0], acc[1][0], acc[2][0], acc[3][0]); mfence<0>(acc[4][0], acc[5][0], acc[6][0], acc[7][0]); \
      ACT_OUT(ps, 0, 0) ACT_OUT(ps, 0, 1) sb(); \
      mfence<0>(acc[0][1], acc[1][1], acc[2][1], acc[3][1]); mfence<0>(acc[4][1], acc[5][1], acc[6][1], acc[7][1]); \
      ACT_OUT(ps, 1, 0) ACT_OUT(ps, 1, 1) sb(); }
#define W_RH(dst, k, u, hf) { const v2u t_ = ldtr(L + tb[k >= 7][u & 3], (k % 7) * 8192 + (u >> 2) * 4096 + hf * 2048); \
      dst[2 * hf] = t_.x; dst[2 * hf + 1] = t_.y; }
#pragma unroll
    for (int ps = 0; ps < 2; ++ps)
#pragma unroll
    for (int ks = 0; ks < 14; ++ks) {
      const int v = ps * 14 + ks, cur = v & 1, nk = (v + 1) % 14, vn = v < 27 ? v + 1 : v;
      const bool more = v < 27;
      u32 m0, m1;
      __builtin_amdgcn_s_waitcnt(0xC07F);
      sb();
#pragma unroll
      for (int i = 0; i < 16; ++i) {
        const int u = i & 7, tt = i >> 3;
        if (ks == 0) mv0(acc[u][tt], wfr[cur][u], hh_[v][tt]); else mv(acc[u][tt], wfr[cur][u], hh_[v][tt]);
        if (more) {
          if (i < 4) { W_RH(wfr[cur ^ 1][i], nk, i, 0) W_RH(wfr[cur ^ 1][i], nk, i, 1) }
          else if (i < 12) W_RH(wfr[cur ^ 1][4 + ((i - 4) >> 1)], nk, (4 + ((i - 4) >> 1)), (i & 1))
        }
        if (i == 5 && v + LEAD < 28) H_LD(v + LEAD, 0);
        if (i == 7 && v + LEAD < 28) H_LD(v + LEAD, 1);
        if (i == 9 || i == 11 || i == 13) {
          const int e = (i - 9) >> 1, f = WD_MID + (v < 14 ? v - 2 : 12 + WD_PER * (v - 14) + e);
          if (f < 56 && ((v >= 2 && v < 14 && e == 0) || (v >= 14 && e < WD_PER))) wdn[f >> 2][f & 3] = ld4c(ws, wl16 + (f & 3) * 1024, wdb + (f >> 2) * 4096);
        }
        if (v > A_V && (i & 1) && i >= 5 && ((i - 5) >> 1) < A_PER) G_LD(A_PER * (v - A_V - 1) + ((i - 5) >> 1));
        if (v == 8 && i == 13) fa = ld2c(ws, fx);
        if (more && i == 12) { m0 = max(max(hh_[vn][0].x, hh_[vn][0].y), hh_[vn][0].z); pin(m0); }
        if (more && i == 13) { m1 = max(max(hh_[vn][0].w, hh_[vn][1].x), hh_[vn][1].y); pin(m1); }
        if (more && i == 14) { m0 = max(max(m0, hh_[vn][1].z), hh_[vn][1].w); m0 = max(m0, m1); pin(m0); }
        sb();
      }
      if (v == 12 && !wall(fa.x == tag && fa.y == ~tag)) wait_flags(ws, fx, tag);
      if (v == 19 && isR) {
        static_assert(19 + LEAD < 28, "the count below");
        asm volatile("s_waitcnt vmcnt(12)" ::: "memory");
        if (lane == 0) st2c(ws, WS_XRDY + (16 * grp + j - 14) * 64 + wave * 8, v2u{tag, ~tag});
      }
      if (more && __builtin_expect(wany(m0 == SENT), 0)) {
        const int pb = 2 * ((v + 1) / 14) * 14336 + nk * 1024;
        RETRY2(hh_[vn][0], hh_[vn][1], hb + pb, hb + pb + 14336)
      }
      if (v == 13) {
        STAMP(16); CSTAMP(29);
        ACT_ALL(0)
        if (isR) x2_store(ws, x2r, 16 * grp + j - 14, wave, l16);
        nop2();
      }
      sb();
    }
    STAMP(27);
    ACT_ALL(1)
    CSTAMP(30);
#pragma unroll
    for (int e = A_PER * (27 - A_V); e < 28; ++e) G_LD(e);
    sb();
    STAMP(9);
    v4u a[14][2];
    v4f ac2[4][4];
#define A_LD(e) a[(e) >> 1][(e) & 1] = ld4c(ws, l16 + (((e) >> 1) & 3) * 1024, abx[(e) & 1] + 2 * 57344 + ((e) >> 3) * 4096)
    const int po = ab + 3 * 57344 + min(lane, 6) * 2048 + 1024;
#define KN (ks < 13 ? ks + 1 : ks)
    u32 probe;
#pragma unroll
    for (int ks = 0; ks < 14; ++ks) {
      if (ks == 0 && __builtin_expect(BAD2(h[0][0], h[0][1]), 0)) { RETRY2(h[0][0], h[0][1], abx[0], abx[1]) }
#pragma unroll
      for (int tt = 0; tt < 2; ++tt)
#pragma unroll
        for (int n = 0; n < 4; ++n) ac2[tt][n] = mfma(wdn[ks][n], h[ks][tt], ks ? ac2[tt][n] : splat(0.f));
      const bool bad = ks < 13 && BAD2(h[KN][0], h[KN][1]);
      if (ks == P_AT) probe = ld1c(ws, po);
#pragma unroll
      for (int u = 0; u < 8; ++u) { __builtin_amdgcn_sched_group_barrier(0x8, 1, 0); if (u >= 2) __builtin_amdgcn_sched_group_barrier(0x2, 1, 0); }
      sb();
      if (__builtin_expect(bad, 0)) { RETRY2(h[KN][0], h[KN][1], abx[0] + KN * 1024, abx[1] + KN * 1024) }
      if (ks == B_AT2) {
        STAMP(25);
        if (wany(probe == SENT)) light_poll(ws, po);
        STAMP(26);
      }
      if (ks >= B_AT2) {
#pragma unroll
        for (int e = 4 * (ks - B_AT2); e < 4 * (ks - B_AT2 + 1); ++e) A_LD(e);
      }
      sb();
    }
    STAMP(17);
#pragma unroll
    for (int e = 4 * (14 - B_AT2); e < 4 * (14 - B_AT2) + 6; ++e) A_LD(e);
    sb();
    int rw[3], rr_ = L_B + lane * 8 + wave * 6144, oo[2];
#pragma unroll
    for (int x = 1; x < 4; ++x) { rw[x - 1] = L_B + lane * 8 + (wave ^ x) * 6144 + (x - 1) * 2048; pin(rw[x - 1]); }
    pin(rr_);
    const int blk = blk0 + (wave >> 1);
#pragma unroll
    for (int ph = 0; ph < 2; ++ph) { oo[ph] = (tok_row(blk + 2 * ph, c16) * D + ct * 64 + g * 16 + (wave & 1) * 8) * 2; pin(oo[ph]); }
#define RED_W1(ph, x, n) *(LAS v2f*)(L + rw[(x) - 1] + (ph) * 24576 + (n) * 512) = ((x) & 1) ? hi2(ac2[2 * (ph) + ((x) >> 1)][n]) : lo2(ac2[2 * (ph) + ((x) >> 1)][n]);
    const int n0 = ct * 64 + g * 16 + (wave & 1) * 8, reg = (n0 >= 112) + (n0 >= 224) + (n0 >= 336), xm = (0x1320 >> (4 * reg)) & 3;
    const int dz = abs(n0 - ((reg + 1) >> 1) * 224), db = (reg & 1) ? dz - 8 : dz;
#define XSLOT(m, dl) ((((dl) >> 4) * 2 + ((m) >> 1)) * 1024 + (((dl) >> 2) & 3) * 256 + ((m) & 1) * 8)
    int xo = WS_X2 + blk * 14336 + c16 * 16 + XSLOT(xm, db);
    int xe = WS_X2 + blk * 14336 + c16 * 16 + (n0 == 112 * reg && reg > 0 ? XSLOT(reg, 0) : XSLOT(xm, dz));
    pin(xo); pin(xe);
    const int xf = WS_XRDY + (blk + (lane & 4 ? 2 : 0)) * 64 + (lane & 3) * 8;
    v2u xp[2][2]; u32 x1[2];
    const int go = WS_MOD + grp * MOD_REP + (70 + 2 * ct) * 128 + g * 64 + (wave & 1) * 32;
    v4u xv[2], gv[2];
    STAMP(18);
    sb();
#pragma unroll
    for (int ks = 0; ks < 14; ++ks) {
      if (ks == 0 && __builtin_expect(BAD2(a[0][0], a[0][1]), 0)) { RETRY2(a[0][0], a[0][1], abx[0] + 2 * 57344, abx[1] + 2 * 57344) }
#pragma unroll
      for (int tt = 0; tt < 2; ++tt)
#pragma unroll
        for (int n = 0; n < 4; ++n) ac2[2 + tt][n] = mfma(wdn[ks][n], a[ks][tt], ks ? ac2[2 + tt][n] : splat(0.f));
#pragma unroll
      for (int e = 4 * (14 - B_AT2) + 6 + 3 * ks; e < 4 * (14 - B_AT2) + 9 + 3 * ks; ++e)
        if (e < 28) A_LD(e);
      const bool bad = ks < 13 && BAD2(a[KN][0], a[KN][1]);
      if (ks < 12) RED_W1(0, 1 + (ks >> 2), (ks & 3))
#pragma unroll
      for (int u = 0; u < 4; ++u) {
        __builtin_amdgcn_sched_group_barrier(0x8, 2, 0);
        __builtin_amdgcn_sched_group_barrier(0x20, 1, 0);
        if (u == 2) __builtin_amdgcn_sched_group_barrier(0x200, 1, 0);
        if (u >= 1) __builtin_amdgcn_sched_group_barrier(0x2, 3, 0);
      }
      sb();
      if (__builtin_expect(bad, 0)) { RETRY2(a[KN][0], a[KN][1], abx[0] + 2 * 57344 + KN * 1024, abx[1] + 2 * 57344 + KN * 1024) }
      if (ks == 2) fa = ld2c(ws, xf);
      if (ks == 7) {
        if (!wall(fa.x == tag && fa.y == ~tag)) wait_flags(ws, xf, tag);
#pragma unroll
        for (int ph = 0; ph < 2; ++ph) {
          xp[ph][0] = ld2c(ws, xo, ph * 2 * 14336); xp[ph][1] = ld2c(ws, xo + 256, ph * 2 * 14336);
          x1[ph] = __builtin_amdgcn_raw_buffer_load_b16(ws, xe, ph * 2 * 14336, SC1);
        }
        gv[0] = ld4c(ws, go); gv[1] = ld4c(ws, go + 16);
      }
      sb();
    }
    STAMP(10);
#pragma unroll
    for (int e = 0; e < 12; ++e) RED_W1(1, 3 - (e >> 2), (e & 3))
#define FIN_OK() wall(gv[0].y == tag && gv[0].w == tag && gv[1].y == tag && gv[1].w == tag)
    if (__builtin_expect(!FIN_OK(), 0)) {
      _Pragma("nounroll") for (int s = 0; s < SPIN; ++s) {
        const int gf = fresh(go);
        gv[0] = ld4c(ws, gf); gv[1] = ld4c(ws, gf + 16);
        if (FIN_OK()) break;
      }
    }
#pragma unroll
    for (int ph = 0; ph < 2; ++ph) {
      const v2u p = xp[ph][0], q = xp[ph][1];
      const bool dn = reg & 1;
      xv[ph].x = __builtin_amdgcn_perm(dn ? q.y : p.x, x1[ph], 0x07060100u);
      xv[ph].y = dn ? __builtin_amdgcn_perm(q.x, q.y, 0x07060100u) : p.y;
      xv[ph].z = dn ? __builtin_amdgcn_perm(p.y, q.x, 0x07060100u) : q.x;
      xv[ph].w = dn ? __builtin_amdgcn_perm(p.x, p.y, 0x07060100u) : q.y;
      pin(xv[ph]);
    }
    bar();
#pragma unroll
    for (int ph = 0; ph < 2; ++ph) {
      v2f r[4];
#pragma unroll
      for (int n = 0; n < 4; ++n) {
        r[n] = lo2(ac2[2 * ph][n]);
#pragma unroll
        for (int w = 0; w < 3; ++w) r[n] += *(const LAS v2f*)(L + rr_ + w * 2048 + ph * 24576 + n * 512);
      }
      v4u o;
#pragma unroll
      for (int q = 0; q < 2; ++q) {
        o[2 * q] = pk2(fma2(bun(gv[q].x), v2f{r[0][q], r[1][q]}, bun(xv[ph][2 * q])));
        o[2 * q + 1] = pk2(fma2(bun(gv[q].z), v2f{r[2][q], r[3][q]}, bun(xv[ph][2 * q + 1])));
      }
      __builtin_amdgcn_raw_buffer_store_b128(o, mkrs(out, S * D * 2), oo[ph], 0, OUT_AUX);
    }
  }
  STAMP(11);
  DUMP();
}

extern "C" void kernel_launch(void* const* d_in, const int* in_sizes, int n_in, void* d_out, int out_size, void* d_ws,
                              size_t ws_size, hipStream_t stream) {
  fused<<<256, 256, 0, stream>>>((const u16*)d_in[0], (const u16*)d_in[1], (const u16*)d_in[2], (const u16*)d_in[4],
                                 (const u16*)d_in[5], (const u16*)d_in[6], (const u16*)d_in[7], (const u16*)d_in[8],
                                 (u16*)d_out, (char*)d_ws);
}
```
